```python
import torch

def _softmax(x, axis=-1):
    return torch.softmax(x, dim=axis)
def _gelu(x):
    return torch.nn.functional.gelu(x, approximate="tanh")
def _silu(x):
    return torch.nn.functional.silu(x)
def _sigmoid(x):
    return torch.sigmoid(x)
def _layernorm(x, axis=-1, eps=1e-5):
    m = x.mean(dim=axis, keepdim=True)
    v = x.var(dim=axis, keepdim=True, unbiased=False)
    return (x - m) / torch.sqrt(v + eps)
def _scatter_add(tgt, idx, src, axis=0):
    return tgt.clone().index_add_(axis, idx, src)
def _segment_sum(src, seg, n):
    out = torch.zeros((n,) + src.shape[1:], dtype=src.dtype)
    return out.index_add_(0, seg, src)
def _pad(x, pw):
    flat = tuple(v for pair in reversed(pw) for v in pair)
    return torch.nn.functional.pad(x, flat)

kernel_name = 'concat_heads__gqa_block_cached__mha_block_cached__near_ridge'

def setup_inputs():

    _w = (lambda a: (lambda t: t.to(torch.bfloat16) if t.is_floating_point() else t)(torch.as_tensor(a)))
    def _A_setup():
        B, Sq, D = 16, 48, 512
        Hq, Hkv, Sk, Dh = 32, 8, 512, 128
        assert Hq % Hkv == 0, (Hq, Hkv)
        return {
            "x": _w((lambda *s: torch.randn(*s, dtype=torch.bfloat16) * 0.8)(B, Sq, D)),
            "k_cache": _w((lambda *s: torch.randn(*s, dtype=torch.bfloat16) * 0.8)(B, Hkv, Sk, Dh)),
            "v_cache": _w((lambda *s: torch.randn(*s, dtype=torch.bfloat16) * 0.05)(B, Hkv, Sk, Dh)),
            "wq": _w((lambda *s: torch.randn(*s, dtype=torch.bfloat16) * 0.8)(D, Hq * Dh) * (D ** -0.5)),
            "wo": _w((lambda *s: torch.randn(*s, dtype=torch.bfloat16) * 0.05)(Hq * Dh, D) * ((Hq * Dh) ** -0.5)),
        }
    _A_in = _A_setup()

    def _B_setup():
        B, Sq, D = 16, 48, 512
        H, Sk, Dh = 32, 512, 128
        return {
            "x": _w((lambda *s: torch.randn(*s, dtype=torch.bfloat16) * 0.8)(B, Sq, D)),
            "k_cache": _w((lambda *s: torch.randn(*s, dtype=torch.bfloat16) * 0.8)(B, H, Sk, Dh)),
            "v_cache": _w((lambda *s: torch.randn(*s, dtype=torch.bfloat16) * 0.05)(B, H, Sk, Dh)),
            "wq": _w((lambda *s: torch.randn(*s, dtype=torch.bfloat16) * 0.8)(D, H * Dh) * (D ** -0.5)),
            "wo": _w((lambda *s: torch.randn(*s, dtype=torch.bfloat16) * 0.05)(H * Dh, D) * ((H * Dh) ** -0.5)),
        }
    _B_in = _B_setup()

    return {
        "A_x": _A_in["x"],
        "A_k_cache": _A_in["k_cache"],
        "A_v_cache": _A_in["v_cache"],
        "A_wq": _A_in["wq"],
        "A_wo": _A_in["wo"],
        "B_x": _B_in["x"],
        "B_k_cache": _B_in["k_cache"],
        "B_v_cache": _B_in["v_cache"],
        "B_wq": _B_in["wq"],
        "B_wo": _B_in["wo"],
    }

def reference(A_x, A_k_cache, A_v_cache, A_wq, A_wo, B_x, B_k_cache, B_v_cache, B_wq, B_wo):
    def _A_ref(x, k_cache, v_cache, wq, wo):
        B, Sq, D = x.shape
        Hq, Dh = 32, 128
        Hkv = k_cache.shape[1]
        rep = Hq // Hkv
        q = (lambda x, *d: x.permute(*d))((x @ wq).reshape(B, Sq, Hq, Dh), 0, 2, 1, 3)
        k = (lambda x, n, axis: torch.repeat_interleave(x, n, dim=axis))(k_cache, rep, axis=1)
        v = (lambda x, n, axis: torch.repeat_interleave(x, n, dim=axis))(v_cache, rep, axis=1)
        scores = torch.einsum("bhqd,bhkd->bhqk", q, k) * (Dh ** -0.5)
        p = _softmax(scores, axis=-1)
        o = torch.einsum("bhqk,bhkd->bhqd", p, v)
        return (lambda x, *d: x.permute(*d))(o, 0, 2, 1, 3).reshape(B, Sq, Hq * Dh) @ wo

    def _B_ref(x, k_cache, v_cache, wq, wo):
        B, Sq, D = x.shape
        H, Dh = 32, 128
        q = (lambda x, *d: x.permute(*d))((x @ wq).reshape(B, Sq, H, Dh), 0, 2, 1, 3)
        scores = torch.einsum("bhqd,bhkd->bhqk", q, k_cache) * (Dh ** -0.5)
        p = _softmax(scores, axis=-1)
        o = torch.einsum("bhqk,bhkd->bhqd", p, v_cache)
        return (lambda x, *d: x.permute(*d))(o, 0, 2, 1, 3).reshape(B, Sq, H * Dh) @ wo

    a = _A_ref(A_x, A_k_cache, A_v_cache, A_wq, A_wo)
    b = _B_ref(B_x, B_k_cache, B_v_cache, B_wq, B_wo)
    return torch.concatenate([a, b], axis=-1)
```

```cpp
#include <hip/hip_runtime.h>
#include <hip/hip_bf16.h>
#include <stdint.h>
typedef unsigned short u16;
__device__ __forceinline__ float b2f(u16 x){ return __uint_as_float(((unsigned)x)<<16); }
__device__ __forceinline__ u16 f2b(float f){ unsigned u=__float_as_uint(f); u += 0x7FFF + ((u>>16)&1); return (u16)(u>>16); }

__global__ void gemm_naive(const u16* A, const u16* B, u16* C, int M, int N, int K, int ldc){
  __shared__ float As[16][17], Bs[16][17];
  int tx=threadIdx.x, ty=threadIdx.y;
  int row=blockIdx.y*16+ty, col=blockIdx.x*16+tx;
  float acc=0;
  for(int k0=0;k0<K;k0+=16){
    As[ty][tx]=b2f(A[(size_t)row*K+k0+tx]);
    Bs[ty][tx]=b2f(B[(size_t)(k0+ty)*N+col]);
    __syncthreads();
    for(int k=0;k<16;k++) acc+=As[ty][k]*Bs[k][tx];
    __syncthreads();
  }
  C[(size_t)row*ldc+col]=f2b(acc);
}
__global__ void attn_naive(const u16* Q, const u16* Kc, const u16* Vc, u16* O, int Hkv){
  int q=blockIdx.x, h=blockIdx.y, b=blockIdx.z;
  int hk = h/(32/Hkv);
  __shared__ float qs[128]; __shared__ float s[512]; __shared__ float red[128];
  int t=threadIdx.x;
  qs[t]=b2f(Q[((size_t)(b*48+q))*4096+h*128+t]);
  __syncthreads();
  const u16* Kp=Kc+((size_t)(b*Hkv+hk))*512*128; const u16* Vp=Vc+((size_t)(b*Hkv+hk))*512*128;
  float mx=-1e30f;
  for(int k=t;k<512;k+=128){ float a=0; for(int d=0;d<128;d++) a+=qs[d]*b2f(Kp[k*128+d]); a*=0.08838834764831845f; s[k]=a; mx=fmaxf(mx,a);}
  red[t]=mx; __syncthreads();
  for(int o=64;o>0;o>>=1){ if(t<o) red[t]=fmaxf(red[t],red[t+o]); __syncthreads(); }
  mx=red[0]; __syncthreads();
  float sm=0; for(int k=t;k<512;k+=128){ float e=__expf(s[k]-mx); s[k]=e; sm+=e; }
  red[t]=sm; __syncthreads();
  for(int o=64;o>0;o>>=1){ if(t<o) red[t]+=red[t+o]; __syncthreads(); }
  sm=red[0];
  float acc=0; for(int k=0;k<512;k++) acc+=s[k]*b2f(Vp[k*128+t]);
  O[((size_t)(b*48+q))*4096+h*128+t]=f2b(acc/sm);
}
extern "C" void kernel_launch(void* const* d_in, const int* in_sizes, int n_in, void* d_out, int out_size, void* d_ws, size_t ws_size, hipStream_t stream){
  u16* out=(u16*)d_out; u16* q=(u16*)d_ws; u16* o=q+768*4096;
  for(int br=0;br<2;br++){
    const u16* x=(const u16*)d_in[br*5+0]; const u16* k=(const u16*)d_in[br*5+1]; const u16* v=(const u16*)d_in[br*5+2];
    const u16* wq=(const u16*)d_in[br*5+3]; const u16* wo=(const u16*)d_in[br*5+4];
    gemm_naive<<<dim3(4096/16,768/16),dim3(16,16),0,stream>>>(x,wq,q,768,4096,512,4096);
    attn_naive<<<dim3(48,32,16),128,0,stream>>>(q,k,v,o,br==0?8:32);
    gemm_naive<<<dim3(512/16,768/16),dim3(16,16),0,stream>>>(o,wo,out+br*512,768,512,4096,1024);
  }
}
```

```cpp
#include <hip/hip_runtime.h>
#include <hip/hip_bf16.h>
#include <stdint.h>

typedef unsigned short u16;
typedef unsigned int u32;
typedef unsigned long long u64;
typedef float f32x4 __attribute__((ext_vector_type(4)));
typedef float f32x2 __attribute__((ext_vector_type(2)));
typedef u32 u32x4 __attribute__((ext_vector_type(4)));
typedef u32 u32x2 __attribute__((ext_vector_type(2)));
typedef short s16x4 __attribute__((ext_vector_type(4)));
typedef __bf16 bf16x8 __attribute__((ext_vector_type(8)));
typedef __bf16 bf16x2 __attribute__((ext_vector_type(2)));
#define LAS __attribute__((address_space(3)))
#define DI __device__ __forceinline__
typedef __amdgpu_buffer_rsrc_t srd_t;

constexpr int LDS_BYTES = 160 * 1024;
constexpr float CSC = 0.08838834764831845f * 1.4426950408889634f;
constexpr float THR = 8.0f;

DI u32 rfl(u32 x) { return __builtin_amdgcn_readfirstlane(x); }
DI u32 cvtpk(float lo, float hi) { f32x2 v = {lo, hi}; bf16x2 b = __builtin_convertvector(v, bf16x2); return __builtin_bit_cast(u32, b); }
DI f32x4 mfma(u32x4 a, u32x4 b, f32x4 c) { return __builtin_amdgcn_mfma_f32_16x16x32_bf16(__builtin_bit_cast(bf16x8, a), __builtin_bit_cast(bf16x8, b), c, 0, 0, 0); }
DI u32x4 lds_rd128(u32 a) { return *(LAS u32x4*)(a); }
DI void lds_wr128(u32 a, u32x4 v) { *(LAS u32x4*)(a) = v; }
DI u32x2 lds_rdtr(u32 a) { return __builtin_bit_cast(u32x2, __builtin_amdgcn_ds_read_tr16_b64_v4i16((LAS s16x4*)(a))); }
DI srd_t mksrd(const void* p, u32 bytes) { return __builtin_amdgcn_make_buffer_rsrc((void*)p, (short)0, (int)bytes, 0x00020000); }
template <int N> DI void waitv() { asm volatile("s_waitcnt vmcnt(%0)" ::"i"(N) : "memory"); }
DI void barrier() { asm volatile("s_waitcnt lgkmcnt(0)\n\ts_barrier" ::: "memory"); }

template <int NT> DI void dma4(u32 m0, srd_t s, u32 soff, u32 v0, u32 v1, u32 v2, u32 v3) {
  if constexpr (NT)
    asm volatile("s_mov_b32 m0, %0\n\ts_nop 0\n\t"
               "buffer_load_dwordx4 %3, %1, %2 offen nt lds\n\t"
               "buffer_load_dwordx4 %4, %1, %2 offen offset:1024 nt lds\n\t"
               "buffer_load_dwordx4 %5, %1, %2 offen offset:2048 nt lds\n\t"
               "buffer_load_dwordx4 %6, %1, %2 offen offset:3072 nt lds"
               ::"s"(m0), "s"(s), "s"(soff), "v"(v0), "v"(v1), "v"(v2), "v"(v3) : "memory");
  else
    asm volatile("s_mov_b32 m0, %0\n\ts_nop 0\n\t"
               "buffer_load_dwordx4 %3, %1, %2 offen lds\n\t"
               "buffer_load_dwordx4 %4, %1, %2 offen offset:1024 lds\n\t"
               "buffer_load_dwordx4 %5, %1, %2 offen offset:2048 lds\n\t"
               "buffer_load_dwordx4 %6, %1, %2 offen offset:3072 lds"
               ::"s"(m0), "s"(s), "s"(soff), "v"(v0), "v"(v1), "v"(v2), "v"(v3) : "memory");
}
template <int NT> DI void dma2(u32 m0, srd_t s, u32 soff, u32 v0, u32 v1) {
  if constexpr (NT)
    asm volatile("s_mov_b32 m0, %0\n\ts_nop 0\n\t"
               "buffer_load_dwordx4 %3, %1, %2 offen nt lds\n\t"
               "buffer_load_dwordx4 %4, %1, %2 offen offset:1024 nt lds"
               ::"s"(m0), "s"(s), "s"(soff), "v"(v0), "v"(v1) : "memory");
  else
    asm volatile("s_mov_b32 m0, %0\n\ts_nop 0\n\t"
               "buffer_load_dwordx4 %3, %1, %2 offen lds\n\t"
               "buffer_load_dwordx4 %4, %1, %2 offen offset:1024 lds"
               ::"s"(m0), "s"(s), "s"(soff), "v"(v0), "v"(v1) : "memory");
}
struct WReg { u32x4 r[8]; };
DI void ldw_q(WReg& w, srd_t s, u32 soff, const u32 (&v)[8]) {
  asm volatile("buffer_load_dwordx4 %0, %8, %16, %17 offen\n\t"
               "buffer_load_dwordx4 %1, %9, %16, %17 offen\n\t"
               "buffer_load_dwordx4 %2, %10, %16, %17 offen\n\t"
               "buffer_load_dwordx4 %3, %11, %16, %17 offen\n\t"
               "buffer_load_dwordx4 %4, %12, %16, %17 offen\n\t"
               "buffer_load_dwordx4 %5, %13, %16, %17 offen\n\t"
               "buffer_load_dwordx4 %6, %14, %16, %17 offen\n\t"
               "buffer_load_dwordx4 %7, %15, %16, %17 offen"
               : "+v"(w.r[0]), "+v"(w.r[1]), "+v"(w.r[2]), "+v"(w.r[3]), "+v"(w.r[4]), "+v"(w.r[5]), "+v"(w.r[6]), "+v"(w.r[7])
               : "v"(v[0]), "v"(v[1]), "v"(v[2]), "v"(v[3]), "v"(v[4]), "v"(v[5]), "v"(v[6]), "v"(v[7]), "s"(s), "s"(soff) : "memory");
}
DI void ldw_o(WReg& w, srd_t s, u32 soff, u32 v0, u32 v1) {
  asm volatile("buffer_load_dwordx4 %0, %8, %10, %11 offen\n\t"
               "buffer_load_dwordx4 %1, %8, %10, %11 offen offset:1024\n\t"
               "buffer_load_dwordx4 %2, %8, %10, %11 offen offset:2048\n\t"
               "buffer_load_dwordx4 %3, %8, %10, %11 offen offset:3072\n\t"
               "buffer_load_dwordx4 %4, %9, %10, %11 offen\n\t"
               "buffer_load_dwordx4 %5, %9, %10, %11 offen offset:1024\n\t"
               "buffer_load_dwordx4 %6, %9, %10, %11 offen offset:2048\n\t"
               "buffer_load_dwordx4 %7, %9, %10, %11 offen offset:3072"
               : "+v"(w.r[0]), "+v"(w.r[1]), "+v"(w.r[2]), "+v"(w.r[3]), "+v"(w.r[4]), "+v"(w.r[5]), "+v"(w.r[6]), "+v"(w.r[7])
               : "v"(v0), "v"(v1), "s"(s), "s"(soff) : "memory");
}
template <int N> DI void wait_w(WReg& w) {
  asm volatile("s_waitcnt vmcnt(%8)"
               : "+v"(w.r[0]), "+v"(w.r[1]), "+v"(w.r[2]), "+v"(w.r[3]), "+v"(w.r[4]), "+v"(w.r[5]), "+v"(w.r[6]), "+v"(w.r[7])
               : "i"(N) : "memory");
}
DI void zero_w(WReg& w) {
#pragma unroll
  for (int j = 0; j < 8; j++) w.r[j] = u32x4{0, 0, 0, 0};
}
DI void proj_step(f32x4 (&acc)[8][3], const WReg& w, const u32x4 (&bf)[3]) {
#pragma unroll
  for (int c = 0; c < 8; c++) {
    const u32 sel = (c & 1) ? 0x07060302u : 0x05040100u;
    u32x4 a;
#pragma unroll
    for (int jj = 0; jj < 4; jj++) a[jj] = __builtin_amdgcn_perm(w.r[2 * jj + 1][c >> 1], w.r[2 * jj][c >> 1], sel);
#pragma unroll
    for (int t = 0; t < 3; t++) acc[c][t] = mfma(a, bf[t], acc[c][t]);
  }
}
DI float xmax(float v) {
  auto a = __builtin_amdgcn_permlane32_swap(__float_as_uint(v), __float_as_uint(v), false, false);
  v = fmaxf(__uint_as_float(a[0]), __uint_as_float(a[1]));
  auto b = __builtin_amdgcn_permlane16_swap(__float_as_uint(v), __float_as_uint(v), false, false);
  return fmaxf(__uint_as_float(b[0]), __uint_as_float(b[1]));
}
DI float xsum(float v) {
  auto a = __builtin_amdgcn_permlane32_swap(__float_as_uint(v), __float_as_uint(v), false, false);
  v = __uint_as_float(a[0]) + __uint_as_float(a[1]);
  auto b = __builtin_amdgcn_permlane16_swap(__float_as_uint(v), __float_as_uint(v), false, false);
  return __uint_as_float(b[0]) + __uint_as_float(b[1]);
}

DI u32 kswz(u32 r) { return (r & 10) | ((r & 1) << 2) | ((r >> 2) & 1); }
struct Attn {
  u32x4 q[4][3];
  f32x4 o[8][3];
  float m[3], l[3];
};
DI void tile_qk(f32x4 (&s)[2][3], const Attn& st, const u32 (&ka)[4], u32 base) {
#pragma unroll
  for (int u = 0; u < 2; u++)
#pragma unroll
    for (int t = 0; t < 3; t++) s[u][t] = f32x4{0, 0, 0, 0};
#pragma unroll
  for (int ks = 0; ks < 4; ks++)
#pragma unroll
    for (int u = 0; u < 2; u++) {
      u32x4 kf = lds_rd128(ka[ks] + base + u * 4096);
#pragma unroll
      for (int t = 0; t < 3; t++) s[u][t] = mfma(kf, st.q[ks][t], s[u][t]);
    }
}
DI void tile_sm(u32x4 (&p)[3], f32x4 (&s)[2][3], Attn& st) {
  float mx[3];
  bool need = false;
#pragma unroll
  for (int t = 0; t < 3; t++) {
    float a = fmaxf(fmaxf(s[0][t][0], s[0][t][1]), fmaxf(s[0][t][2], s[0][t][3]));
    float b = fmaxf(fmaxf(s[1][t][0], s[1][t][1]), fmaxf(s[1][t][2], s[1][t][3]));
    mx[t] = fmaxf(a, b) * CSC;
    need |= (mx[t] - st.m[t] > THR);
  }
  if (__any(need)) {
#pragma unroll
    for (int t = 0; t < 3; t++) {
      float mn = fmaxf(st.m[t], xmax(mx[t]));
      float al = __builtin_amdgcn_exp2f(st.m[t] - mn);
      st.m[t] = mn;
      st.l[t] *= al;
#pragma unroll
      for (int d = 0; d < 8; d++) st.o[d][t] *= al;
    }
  }
#pragma unroll
  for (int t = 0; t < 3; t++) {
    float e[8];
#pragma unroll
    for (int i = 0; i < 8; i++) e[i] = __builtin_amdgcn_exp2f(fmaf(s[i >> 2][t][i & 3], CSC, -st.m[t]));
    st.l[t] += ((e[0] + e[1]) + (e[2] + e[3])) + ((e[4] + e[5]) + (e[6] + e[7]));
    p[t] = u32x4{cvtpk(e[0], e[1]), cvtpk(e[2], e[3]), cvtpk(e[4], e[5]), cvtpk(e[6], e[7])};
  }
}
DI void tile_pv(Attn& st, const u32x4 (&p)[3], const u32 (&va)[8], u32 base) {
#pragma unroll
  for (int d = 0; d < 8; d++) {
    u32x2 lo = lds_rdtr(va[d] + base), hi = lds_rdtr(va[d] + base + 4096);
    u32x4 vf = {lo[0], lo[1], hi[0], hi[1]};
#pragma unroll
    for (int t = 0; t < 3; t++) st.o[d][t] = mfma(vf, p[t], st.o[d][t]);
  }
}

#define STAMP_AT(i)

template <int BR>
DI void body(const u16* x, const u16* kc, const u16* vc, const u16* wq, const u16* wo, u16* part, u64* stamps) {
  const u32 wg = blockIdx.x, j = wg & 7, b = wg >> 4;
  const u32 lane = threadIdx.x & 63, w = rfl(threadIdx.x >> 6);
  const u32 r = lane & 15, g = lane >> 4;
  STAMP_AT(0);
  constexpr u32 KVB = 512 * 128 * 2;
  const u32 kvi = BR == 0 ? b * 8 + j : (b * 32 + j * 4 + w);
  const srd_t sK = mksrd((const char*)kc + (size_t)kvi * KVB, KVB), sV = mksrd((const char*)vc + (size_t)kvi * KVB, KVB);
  const srd_t sX = mksrd((const char*)x + (size_t)b * 49152, 49152);
  const srd_t sWq = mksrd(wq, 512 * 4096 * 2), sWo = mksrd(wo, 4096 * 512 * 2);
  constexpr int KVNT = 1;
  u32 kv_[4], vv_[2];
#pragma unroll
  for (int n = 0; n < 4; n++) kv_[n] = g * 256 + ((r ^ kswz(4 * n + g)) << 4);
#pragma unroll
  for (int n = 0; n < 2; n++) vv_[n] = g * 256 + ((r ^ ((4 * n + g) << 1)) << 4);
  auto dmaK = [&](u32 lds, u32 tile) { dma4<KVNT>(lds, sK, tile * 8192, kv_[0], kv_[1], kv_[2], kv_[3]); dma4<KVNT>(lds + 4096, sK, tile * 8192 + 4096, kv_[0], kv_[1], kv_[2], kv_[3]); };
  auto dmaV = [&](u32 lds, u32 tile) { dma4<KVNT>(lds, sV, tile * 8192, vv_[0], vv_[1], vv_[0], vv_[1]); dma4<KVNT>(lds + 4096, sV, tile * 8192 + 4096, vv_[0], vv_[1], vv_[0], vv_[1]); };
  const u32 ka0 = (w & 1) ? kv_[2] : kv_[0], ka1 = (w & 1) ? kv_[3] : kv_[1];
  auto dmaA = [&](u32 slot, u32 tile) {
    dma2<KVNT>(slot * 16384 + w * 2048, sK, tile * 8192 + w * 2048, ka0, ka1);
    dma2<KVNT>(slot * 16384 + 8192 + w * 2048, sV, tile * 8192 + w * 2048, vv_[0], vv_[1]);
  };
  if constexpr (BR == 0) {
#pragma unroll
    for (int t = 0; t < 7; t++) dmaA(t, t);
  }
  const u32 xlds = BR == 0 ? 114688 + w * 12288 : w * 40960 + 28672;
#pragma unroll
  for (int i4 = 0; i4 < 3; i4++) {
    u32 vo[4];
#pragma unroll
    for (int n = 0; n < 4; n++) { u32 row = 12 * w + 4 * i4 + n; vo[n] = ((lane & 48) | (r ^ (row & 15))) << 4; }
    dma4<0>(xlds + i4 * 4096, sX, (12 * w + 4 * i4) * 1024, vo[0], vo[1], vo[2], vo[3]);
  }
  const u32 n0 = 8 * r;
  const u32 rot = b;
  f32x4 acc[8][3];
  Attn st;
  {
    u32 wv[8];
#pragma unroll
    for (int i = 0; i < 8; i++) wv[i] = (8 * g + i) * 8192 + (j * 512 + w * 128 + n0) * 2;
    WReg w0, w1, w2;
    zero_w(w0); zero_w(w1); zero_w(w2);
    ldw_q(w0, sWq, ((rot + 0) & 15) * 262144, wv);
    ldw_q(w1, sWq, ((rot + 1) & 15) * 262144, wv);
    ldw_q(w2, sWq, ((rot + 2) & 15) * 262144, wv);
    u32 xb[3];
#pragma unroll
    for (int t = 0; t < 3; t++) {
      u32 q = 16 * t + r;
      u32 rowb = BR == 0 ? 114688 + q * 1024 : (q / 12) * 40960 + 28672 + (q % 12) * 1024;
      xb[t] = rowb + ((g ^ r) << 4);
    }
#pragma unroll
    for (int c = 0; c < 8; c++)
#pragma unroll
      for (int t = 0; t < 3; t++) acc[c][t] = f32x4{0, 0, 0, 0};
    waitv<24>();
    barrier();
    STAMP_AT(1);
    auto xfr = [&](u32x4 (&bf)[3], u32 it) {
      u32 s6 = ((rot + it) & 15) << 6;
#pragma unroll
      for (int t = 0; t < 3; t++) bf[t] = lds_rd128(xb[t] ^ s6);
    };
    u32x4 bf[3];
    for (u32 it = 0; it < 12; it += 3) {
      xfr(bf, it); wait_w<16>(w0); proj_step(acc, w0, bf); ldw_q(w0, sWq, ((rot + it + 3) & 15) * 262144, wv);
      xfr(bf, it + 1); wait_w<16>(w1); proj_step(acc, w1, bf); ldw_q(w1, sWq, ((rot + it + 4) & 15) * 262144, wv);
      xfr(bf, it + 2); wait_w<16>(w2); proj_step(acc, w2, bf); ldw_q(w2, sWq, ((rot + it + 5) & 15) * 262144, wv);
      if (it == 0) STAMP_AT(2);
    }
    STAMP_AT(3);
    xfr(bf, 12); wait_w<16>(w0); proj_step(acc, w0, bf); ldw_q(w0, sWq, ((rot + 15) & 15) * 262144, wv);
    if constexpr (BR == 1) {
      dmaK(w * 40960, 0);
      xfr(bf, 13); wait_w<24>(w1); proj_step(acc, w1, bf); dmaV(w * 40960 + 8192, 0);
      xfr(bf, 14); wait_w<24>(w2); proj_step(acc, w2, bf); dmaK(w * 40960 + 16384, 1);
      xfr(bf, 15); wait_w<24>(w0); proj_step(acc, w0, bf);
    } else {
      xfr(bf, 13); wait_w<16>(w1); proj_step(acc, w1, bf);
      xfr(bf, 14); wait_w<8>(w2); proj_step(acc, w2, bf);
      xfr(bf, 15); wait_w<0>(w0); proj_step(acc, w0, bf);
    }
    barrier();
    if constexpr (BR == 1) dmaV(w * 40960 + 24576, 1);
    else { dmaA(7, 7); dmaA(8, 8); dmaA(9, 9); }
#pragma unroll
    for (int ks = 0; ks < 4; ks++)
#pragma unroll
      for (int t = 0; t < 3; t++)
        st.q[ks][t] = u32x4{cvtpk(acc[0][t][ks], acc[1][t][ks]), cvtpk(acc[2][t][ks], acc[3][t][ks]), cvtpk(acc[4][t][ks], acc[5][t][ks]), cvtpk(acc[6][t][ks], acc[7][t][ks])};
  }
  STAMP_AT(4);
#pragma unroll
  for (int t = 0; t < 3; t++) {
    st.m[t] = -__builtin_inff(); st.l[t] = 0.f;
#pragma unroll
    for (int d = 0; d < 8; d++) st.o[d][t] = f32x4{0, 0, 0, 0};
  }
  u32 ka[4], va[8];
  {
    const u32 wb = BR == 0 ? 0 : w * 40960;
#pragma unroll
    for (int ks = 0; ks < 4; ks++) ka[ks] = wb + r * 256 + (((4 * g + ks) ^ kswz(r)) << 4);
    const u32 row = 4 * g + (r >> 2), p = r & 3;
#pragma unroll
    for (int d = 0; d < 8; d++) va[d] = wb + 8192 + row * 256 + ((d ^ (row & 7)) << 5) + p * 8;
  }
  f32x4 s[2][3];
  u32x4 p[3];
  if constexpr (BR == 1) {
    for (u32 t = 0; t < 14; t++) {
      const u32 sb = (t & 1) * 16384;
      waitv<24>();
      tile_qk(s, st, ka, sb);
      asm volatile("s_waitcnt lgkmcnt(0)" ::: "memory");
      dmaK(w * 40960 + sb, t + 2);
      tile_sm(p, s, st);
      waitv<24>();
      tile_pv(st, p, va, sb);
      asm volatile("s_waitcnt lgkmcnt(0)" ::: "memory");
      dmaV(w * 40960 + sb + 8192, t + 2);
    }
    waitv<24>(); tile_qk(s, st, ka, 0); tile_sm(p, s, st); waitv<16>(); tile_pv(st, p, va, 0);
    waitv<8>(); tile_qk(s, st, ka, 16384); tile_sm(p, s, st); waitv<0>(); tile_pv(st, p, va, 16384);
  } else {
    u32 slot = 0;
    for (u32 t = 0; t < 16; t++) {
      if (t == 7) waitv<24>();
      if (t == 10) waitv<0>();
      if (t > 0) barrier();
      if (t >= 1 && t <= 6) dmaA(t - 1, t + 9);
      const u32 sb = slot * 16384;
      tile_qk(s, st, ka, sb);
      tile_sm(p, s, st);
      tile_pv(st, p, va, sb);
      slot = slot == 9 ? 0 : slot + 1;
    }
    barrier();
  }
  STAMP_AT(5);
  {
    const u32 wv0 = 4 * g * 1024 + (w * 128 + n0) * 2, wv1 = wv0 + 16384;
    const u32 so = j * 524288;
    WReg w0, w1, w2;
    zero_w(w0); zero_w(w1); zero_w(w2);
    ldw_o(w0, sWo, so + ((rot + 0) & 15) * 32768, wv0, wv1);
    ldw_o(w1, sWo, so + ((rot + 1) & 15) * 32768, wv0, wv1);
    ldw_o(w2, sWo, so + ((rot + 2) & 15) * 32768, wv0, wv1);
#pragma unroll
    for (int t = 0; t < 3; t++) {
      const float inv = 1.0f / xsum(st.l[t]);
#pragma unroll
      for (int ks = 0; ks < 4; ks++) {
        f32x4 a = st.o[2 * ks][t] * inv, c = st.o[2 * ks + 1][t] * inv;
        lds_wr128(w * 40960 + (ks * 3 + t) * 1024 + lane * 16, u32x4{cvtpk(a[0], a[1]), cvtpk(a[2], a[3]), cvtpk(c[0], c[1]), cvtpk(c[2], c[3])});
      }
    }
#pragma unroll
    for (int c = 0; c < 8; c++)
#pragma unroll
      for (int t = 0; t < 3; t++) acc[c][t] = f32x4{0, 0, 0, 0};
    barrier();
    STAMP_AT(6);
    auto ofr = [&](u32x4 (&bf)[3], u32 it) {
      u32 ks = (rot + it) & 15;
      u32 a = (ks >> 2) * 40960 + (ks & 3) * 3072 + lane * 16;
#pragma unroll
      for (int t = 0; t < 3; t++) bf[t] = lds_rd128(a + t * 1024);
    };
    u32x4 bf[3];
    for (u32 it = 0; it < 12; it += 3) {
      ofr(bf, it); wait_w<16>(w0); proj_step(acc, w0, bf); ldw_o(w0, sWo, so + ((rot + it + 3) & 15) * 32768, wv0, wv1);
      ofr(bf, it + 1); wait_w<16>(w1); proj_step(acc, w1, bf); ldw_o(w1, sWo, so + ((rot + it + 4) & 15) * 32768, wv0, wv1);
      ofr(bf, it + 2); wait_w<16>(w2); proj_step(acc, w2, bf); ldw_o(w2, sWo, so + ((rot + it + 5) & 15) * 32768, wv0, wv1);
    }
    ofr(bf, 12); wait_w<16>(w0); proj_step(acc, w0, bf); ldw_o(w0, sWo, so + ((rot + 15) & 15) * 32768, wv0, wv1);
    ofr(bf, 13); wait_w<16>(w1); proj_step(acc, w1, bf);
    ofr(bf, 14); wait_w<8>(w2); proj_step(acc, w2, bf);
    ofr(bf, 15); wait_w<0>(w0); proj_step(acc, w0, bf);
  }
  STAMP_AT(7);
  {
    char* pp = (char*)part + ((size_t)(((BR * 16 + b) * 8 + j) * 4 + w) * 12) * 1024 + lane * 16;
#pragma unroll
    for (int t = 0; t < 3; t++)
#pragma unroll
      for (int i = 0; i < 4; i++)
        *(u32x4*)(pp + (t * 4 + i) * 1024) = u32x4{cvtpk(acc[0][t][i], acc[1][t][i]), cvtpk(acc[2][t][i], acc[3][t][i]), cvtpk(acc[4][t][i], acc[5][t][i]), cvtpk(acc[6][t][i], acc[7][t][i])};
  }
  STAMP_AT(8);
}

__global__ __launch_bounds__(256, 2) void attn_main(const u16* xA, const u16* kA, const u16* vA, const u16* wqA, const u16* woA,
                                                    const u16* xB, const u16* kB, const u16* vB, const u16* wqB, const u16* woB, u16* part
) {
  u64* stamps = nullptr;
  if ((blockIdx.x >> 3) & 1) body<1>(xB, kB, vB, wqB, woB, part, stamps);
  else body<0>(xA, kA, vA, wqA, woA, part, stamps);
}

__global__ __launch_bounds__(384) void reduce_out(const u16* part, u16* out) {
  const u32 task = blockIdx.x * 6 + (threadIdx.x >> 6), lane = threadIdx.x & 63;
  const u32 grp = task / 48, blk = task % 48;
  const char* pp = (const char*)part + ((size_t)grp * 8 * 48 + blk) * 1024 + lane * 16;
  u32x4 v[8];
#pragma unroll
  for (int s = 0; s < 8; s++) v[s] = *(const u32x4*)(pp + (size_t)s * 49152);
  float a[8] = {0, 0, 0, 0, 0, 0, 0, 0};
#pragma unroll
  for (int s = 0; s < 8; s++)
#pragma unroll
    for (int c = 0; c < 4; c++) { a[2 * c] += __uint_as_float(v[s][c] << 16); a[2 * c + 1] += __uint_as_float(v[s][c] & 0xffff0000u); }
  const u32 br = grp >> 4, b = grp & 15, w = blk / 12, t = (blk % 12) >> 2, i = blk & 3;
  u16* o = out + (size_t)(b * 48 + 16 * t + (lane & 15)) * 1024 + br * 512 + w * 128 + 8 * i + 32 * (lane >> 4);
  *(u32x4*)o = u32x4{cvtpk(a[0], a[1]), cvtpk(a[2], a[3]), cvtpk(a[4], a[5]), cvtpk(a[6], a[7])};
}

extern "C" void kernel_launch(void* const* d_in, const int* in_sizes, int n_in, void* d_out, int out_size, void* d_ws, size_t ws_size, hipStream_t stream) {
  (void)hipFuncSetAttribute((const void*)attn_main, hipFuncAttributeMaxDynamicSharedMemorySize, LDS_BYTES);
  u16* part = (u16*)d_ws;
  attn_main<<<256, 256, LDS_BYTES, stream>>>((const u16*)d_in[0], (const u16*)d_in[1], (const u16*)d_in[2], (const u16*)d_in[3], (const u16*)d_in[4],
                                             (const u16*)d_in[5], (const u16*)d_in[6], (const u16*)d_in[7], (const u16*)d_in[8], (const u16*)d_in[9], part
  );
  reduce_out<<<256, 384, 0, stream>>>(part, (u16*)d_out);
}
```

```cpp
#include <hip/hip_runtime.h>
#include <hip/hip_bf16.h>
#include <stdint.h>

typedef unsigned short u16;
typedef unsigned int u32;
typedef unsigned long long u64;
typedef float f32x4 __attribute__((ext_vector_type(4)));
typedef float f32x2 __attribute__((ext_vector_type(2)));
typedef u32 u32x4 __attribute__((ext_vector_type(4)));
typedef u32 u32x2 __attribute__((ext_vector_type(2)));
typedef short s16x4 __attribute__((ext_vector_type(4)));
typedef __bf16 bf16x8 __attribute__((ext_vector_type(8)));
typedef __bf16 bf16x2 __attribute__((ext_vector_type(2)));
#define LAS __attribute__((address_space(3)))
#define DI __device__ __forceinline__
typedef __amdgpu_buffer_rsrc_t srd_t;

constexpr int LDS_BYTES = 160 * 1024;
constexpr float CSC = 0.08838834764831845f * 1.4426950408889634f;
constexpr float THR = 8.0f;

DI u32 rfl(u32 x) { return __builtin_amdgcn_readfirstlane(x); }
DI u32 cvtpk(float lo, float hi) { f32x2 v = {lo, hi}; bf16x2 b = __builtin_convertvector(v, bf16x2); return __builtin_bit_cast(u32, b); }
DI f32x4 mfma(u32x4 a, u32x4 b, f32x4 c) { return __builtin_amdgcn_mfma_f32_16x16x32_bf16(__builtin_bit_cast(bf16x8, a), __builtin_bit_cast(bf16x8, b), c, 0, 0, 0); }
DI u32x4 lds_rd128(u32 a) { return *(LAS u32x4*)(a); }
DI void lds_wr128(u32 a, u32x4 v) { *(LAS u32x4*)(a) = v; }
DI u32x2 lds_rdtr(u32 a) { return __builtin_bit_cast(u32x2, __builtin_amdgcn_ds_read_tr16_b64_v4i16((LAS s16x4*)(a))); }
DI srd_t mksrd(const void* p, u32 bytes) { return __builtin_amdgcn_make_buffer_rsrc((void*)p, (short)0, (int)bytes, 0x00020000); }
template <int N> DI void waitv() { asm volatile("s_waitcnt vmcnt(%0)" ::"i"(N) : "memory"); }
DI void barrier() { asm volatile("s_waitcnt lgkmcnt(0)\n\ts_barrier" ::: "memory"); }

template <int NT> DI void dma4(u32 m0, srd_t s, u32 soff, u32 v0, u32 v1, u32 v2, u32 v3) {
  if constexpr (NT)
    asm volatile("s_mov_b32 m0, %0\n\ts_nop 0\n\t"
               "buffer_load_dwordx4 %3, %1, %2 offen nt lds\n\t"
               "buffer_load_dwordx4 %4, %1, %2 offen offset:1024 nt lds\n\t"
               "buffer_load_dwordx4 %5, %1, %2 offen offset:2048 nt lds\n\t"
               "buffer_load_dwordx4 %6, %1, %2 offen offset:3072 nt lds"
               ::"s"(m0), "s"(s), "s"(soff), "v"(v0), "v"(v1), "v"(v2), "v"(v3) : "memory");
  else
    asm volatile("s_mov_b32 m0, %0\n\ts_nop 0\n\t"
               "buffer_load_dwordx4 %3, %1, %2 offen lds\n\t"
               "buffer_load_dwordx4 %4, %1, %2 offen offset:1024 lds\n\t"
               "buffer_load_dwordx4 %5, %1, %2 offen offset:2048 lds\n\t"
               "buffer_load_dwordx4 %6, %1, %2 offen offset:3072 lds"
               ::"s"(m0), "s"(s), "s"(soff), "v"(v0), "v"(v1), "v"(v2), "v"(v3) : "memory");
}
template <int NT> DI void dma2(u32 m0, srd_t s, u32 soff, u32 v0, u32 v1) {
  if constexpr (NT)
    asm volatile("s_mov_b32 m0, %0\n\ts_nop 0\n\t"
               "buffer_load_dwordx4 %3, %1, %2 offen nt lds\n\t"
               "buffer_load_dwordx4 %4, %1, %2 offen offset:1024 nt lds"
               ::"s"(m0), "s"(s), "s"(soff), "v"(v0), "v"(v1) : "memory");
  else
    asm volatile("s_mov_b32 m0, %0\n\ts_nop 0\n\t"
               "buffer_load_dwordx4 %3, %1, %2 offen lds\n\t"
               "buffer_load_dwordx4 %4, %1, %2 offen offset:1024 lds"
               ::"s"(m0), "s"(s), "s"(soff), "v"(v0), "v"(v1) : "memory");
}
struct WReg { u32x4 r[8]; };
DI void ldw_q(WReg& w, srd_t s, u32 soff, const u32 (&v)[8]) {
  asm volatile("buffer_load_dwordx4 %0, %8, %16, %17 offen\n\t"
               "buffer_load_dwordx4 %1, %9, %16, %17 offen\n\t"
               "buffer_load_dwordx4 %2, %10, %16, %17 offen\n\t"
               "buffer_load_dwordx4 %3, %11, %16, %17 offen\n\t"
               "buffer_load_dwordx4 %4, %12, %16, %17 offen\n\t"
               "buffer_load_dwordx4 %5, %13, %16, %17 offen\n\t"
               "buffer_load_dwordx4 %6, %14, %16, %17 offen\n\t"
               "buffer_load_dwordx4 %7, %15, %16, %17 offen"
               : "+v"(w.r[0]), "+v"(w.r[1]), "+v"(w.r[2]), "+v"(w.r[3]), "+v"(w.r[4]), "+v"(w.r[5]), "+v"(w.r[6]), "+v"(w.r[7])
               : "v"(v[0]), "v"(v[1]), "v"(v[2]), "v"(v[3]), "v"(v[4]), "v"(v[5]), "v"(v[6]), "v"(v[7]), "s"(s), "s"(soff) : "memory");
}
DI void ldw_o(WReg& w, srd_t s, u32 soff, u32 v0, u32 v1) {
  asm volatile("buffer_load_dwordx4 %0, %8, %10, %11 offen\n\t"
               "buffer_load_dwordx4 %1, %8, %10, %11 offen offset:1024\n\t"
               "buffer_load_dwordx4 %2, %8, %10, %11 offen offset:2048\n\t"
               "buffer_load_dwordx4 %3, %8, %10, %11 offen offset:3072\n\t"
               "buffer_load_dwordx4 %4, %9, %10, %11 offen\n\t"
               "buffer_load_dwordx4 %5, %9, %10, %11 offen offset:1024\n\t"
               "buffer_load_dwordx4 %6, %9, %10, %11 offen offset:2048\n\t"
               "buffer_load_dwordx4 %7, %9, %10, %11 offen offset:3072"
               : "+v"(w.r[0]), "+v"(w.r[1]), "+v"(w.r[2]), "+v"(w.r[3]), "+v"(w.r[4]), "+v"(w.r[5]), "+v"(w.r[6]), "+v"(w.r[7])
               : "v"(v0), "v"(v1), "s"(s), "s"(soff) : "memory");
}
template <int N> DI void wait_w(WReg& w) {
  asm volatile("s_waitcnt vmcnt(%8)"
               : "+v"(w.r[0]), "+v"(w.r[1]), "+v"(w.r[2]), "+v"(w.r[3]), "+v"(w.r[4]), "+v"(w.r[5]), "+v"(w.r[6]), "+v"(w.r[7])
               : "i"(N) : "memory");
}
DI void zero_w(WReg& w) {
#pragma unroll
  for (int j = 0; j < 8; j++) w.r[j] = u32x4{0, 0, 0, 0};
}
DI void proj_step(f32x4 (&acc)[8][3], const WReg& w, const u32x4 (&bf)[3]) {
#pragma unroll
  for (int c = 0; c < 8; c++) {
    const u32 sel = (c & 1) ? 0x07060302u : 0x05040100u;
    u32x4 a;
#pragma unroll
    for (int jj = 0; jj < 4; jj++) a[jj] = __builtin_amdgcn_perm(w.r[2 * jj + 1][c >> 1], w.r[2 * jj][c >> 1], sel);
#pragma unroll
    for (int t = 0; t < 3; t++) acc[c][t] = mfma(a, bf[t], acc[c][t]);
  }
}
DI float xmax(float v) {
  auto a = __builtin_amdgcn_permlane32_swap(__float_as_uint(v), __float_as_uint(v), false, false);
  v = fmaxf(__uint_as_float(a[0]), __uint_as_float(a[1]));
  auto b = __builtin_amdgcn_permlane16_swap(__float_as_uint(v), __float_as_uint(v), false, false);
  return fmaxf(__uint_as_float(b[0]), __uint_as_float(b[1]));
}
DI float xsum(float v) {
  auto a = __builtin_amdgcn_permlane32_swap(__float_as_uint(v), __float_as_uint(v), false, false);
  v = __uint_as_float(a[0]) + __uint_as_float(a[1]);
  auto b = __builtin_amdgcn_permlane16_swap(__float_as_uint(v), __float_as_uint(v), false, false);
  return __uint_as_float(b[0]) + __uint_as_float(b[1]);
}

DI u32 kswz(u32 r) { return (r & 10) | ((r & 1) << 2) | ((r >> 2) & 1); }
struct Attn {
  u32x4 q[4][3];
  f32x4 o[8][3];
  float m[3], l[3];
};
DI void tile_qk(f32x4 (&s)[2][3], const Attn& st, const u32 (&ka)[4], u32 base) {
#pragma unroll
  for (int u = 0; u < 2; u++)
#pragma unroll
    for (int t = 0; t < 3; t++) s[u][t] = f32x4{0, 0, 0, 0};
#pragma unroll
  for (int ks = 0; ks < 4; ks++)
#pragma unroll
    for (int u = 0; u < 2; u++) {
      u32x4 kf = lds_rd128(ka[ks] + base + u * 4096);
#pragma unroll
      for (int t = 0; t < 3; t++) s[u][t] = mfma(kf, st.q[ks][t], s[u][t]);
    }
}
DI void tile_sm(u32x4 (&p)[3], f32x4 (&s)[2][3], Attn& st) {
  float mx[3];
  bool need = false;
#pragma unroll
  for (int t = 0; t < 3; t++) {
    float a = fmaxf(fmaxf(s[0][t][0], s[0][t][1]), fmaxf(s[0][t][2], s[0][t][3]));
    float b = fmaxf(fmaxf(s[1][t][0], s[1][t][1]), fmaxf(s[1][t][2], s[1][t][3]));
    mx[t] = fmaxf(a, b) * CSC;
    need |= (mx[t] - st.m[t] > THR);
  }
  if (__any(need)) {
#pragma unroll
    for (int t = 0; t < 3; t++) {
      float mn = fmaxf(st.m[t], xmax(mx[t]));
      float al = __builtin_amdgcn_exp2f(st.m[t] - mn);
      st.m[t] = mn;
      st.l[t] *= al;
#pragma unroll
      for (int d = 0; d < 8; d++) st.o[d][t] *= al;
    }
  }
#pragma unroll
  for (int t = 0; t < 3; t++) {
    float e[8];
#pragma unroll
    for (int i = 0; i < 8; i++) e[i] = __builtin_amdgcn_exp2f(fmaf(s[i >> 2][t][i & 3], CSC, -st.m[t]));
    st.l[t] += ((e[0] + e[1]) + (e[2] + e[3])) + ((e[4] + e[5]) + (e[6] + e[7]));
    p[t] = u32x4{cvtpk(e[0], e[1]), cvtpk(e[2], e[3]), cvtpk(e[4], e[5]), cvtpk(e[6], e[7])};
  }
}
DI void tile_pv(Attn& st, const u32x4 (&p)[3], const u32 (&va)[8], u32 base) {
#pragma unroll
  for (int d = 0; d < 8; d++) {
    u32x2 lo = lds_rdtr(va[d] + base), hi = lds_rdtr(va[d] + base + 4096);
    u32x4 vf = {lo[0], lo[1], hi[0], hi[1]};
#pragma unroll
    for (int t = 0; t < 3; t++) st.o[d][t] = mfma(vf, p[t], st.o[d][t]);
  }
}

#define STAMP_AT(i)

template <int BR>
DI void body(const u16* x, const u16* kc, const u16* vc, const u16* wq, const u16* wo, u16* part, u32* sync, u16* out, u64* stamps) {
  const u32 wg = blockIdx.x, j = wg & 7, b = wg >> 4;
  const u32 lane = threadIdx.x & 63, w = rfl(threadIdx.x >> 6);
  const u32 r = lane & 15, g = lane >> 4;
  STAMP_AT(0);
  typedef __attribute__((address_space(1))) u32 gu32;
  gu32* const Rw = (gu32*)sync + (BR * 16 + b) * 512;
  gu32* const Fw = Rw + 256;
  u32 nonce = 0, oldf = 0;
  if (w == 3) asm volatile("global_load_dword %0, %1, off sc1" : "+v"(oldf) : "v"(Fw + j * 32 + (lane & 7)) : "memory");
  constexpr u32 KVB = 512 * 128 * 2;
  const u32 kvi = BR == 0 ? b * 8 + j : (b * 32 + j * 4 + w);
  const srd_t sK = mksrd((const char*)kc + (size_t)kvi * KVB, KVB), sV = mksrd((const char*)vc + (size_t)kvi * KVB, KVB);
  const srd_t sX = mksrd((const char*)x + (size_t)b * 49152, 49152);
  const srd_t sWq = mksrd(wq, 512 * 4096 * 2), sWo = mksrd(wo, 4096 * 512 * 2);
  constexpr int KVNT = 1;
  u32 kv_[4], vv_[2];
#pragma unroll
  for (int n = 0; n < 4; n++) kv_[n] = g * 256 + ((r ^ kswz(4 * n + g)) << 4);
#pragma unroll
  for (int n = 0; n < 2; n++) vv_[n] = g * 256 + ((r ^ ((4 * n + g) << 1)) << 4);
  auto dmaK = [&](u32 lds, u32 tile) { dma4<KVNT>(lds, sK, tile * 8192, kv_[0], kv_[1], kv_[2], kv_[3]); dma4<KVNT>(lds + 4096, sK, tile * 8192 + 4096, kv_[0], kv_[1], kv_[2], kv_[3]); };
  auto dmaV = [&](u32 lds, u32 tile) { dma4<KVNT>(lds, sV, tile * 8192, vv_[0], vv_[1], vv_[0], vv_[1]); dma4<KVNT>(lds + 4096, sV, tile * 8192 + 4096, vv_[0], vv_[1], vv_[0], vv_[1]); };
  const u32 ka0 = (w & 1) ? kv_[2] : kv_[0], ka1 = (w & 1) ? kv_[3] : kv_[1];
  auto dmaA = [&](u32 slot, u32 tile) {
    dma2<KVNT>(slot * 16384 + w * 2048, sK, tile * 8192 + w * 2048, ka0, ka1);
    dma2<KVNT>(slot * 16384 + 8192 + w * 2048, sV, tile * 8192 + w * 2048, vv_[0], vv_[1]);
  };
  if constexpr (BR == 0) {
#pragma unroll
    for (int t = 0; t < 7; t++) dmaA(t, t);
  }
  const u32 n0 = 8 * r;
  const u32 rot = b;
  f32x4 acc[8][3];
  Attn st;
  {
    u32 wv[8];
#pragma unroll
    for (int i = 0; i < 8; i++) wv[i] = (8 * g + i) * 8192 + (j * 512 + w * 128 + n0) * 2;
    WReg w0, w1, w2;
    zero_w(w0); zero_w(w1); zero_w(w2);
    ldw_q(w0, sWq, ((rot + 0) & 15) * 262144, wv);
  const u32 xlds = BR == 0 ? 114688 + w * 12288 : w * 40960 + 28672;
#pragma unroll
  for (int i4 = 0; i4 < 3; i4++) {
    u32 vo[4];
#pragma unroll
    for (int n = 0; n < 4; n++) { u32 row = 12 * w + 4 * i4 + n; vo[n] = ((lane & 48) | (r ^ (row & 15))) << 4; }
    dma4<0>(xlds + i4 * 4096, sX, (12 * w + 4 * i4) * 1024, vo[0], vo[1], vo[2], vo[3]);
  }
    ldw_q(w1, sWq, ((rot + 1) & 15) * 262144, wv);
    ldw_q(w2, sWq, ((rot + 2) & 15) * 262144, wv);
    u32 xb[3];
#pragma unroll
    for (int t = 0; t < 3; t++) {
      u32 q = 16 * t + r;
      u32 rowb = BR == 0 ? 114688 + q * 1024 : (q / 12) * 40960 + 28672 + (q % 12) * 1024;
      xb[t] = rowb + ((g ^ r) << 4);
    }
#pragma unroll
    for (int c = 0; c < 8; c++)
#pragma unroll
      for (int t = 0; t < 3; t++) acc[c][t] = f32x4{0, 0, 0, 0};
    asm volatile("s_waitcnt vmcnt(16)" : "+v"(oldf) :: "memory");
    barrier();
    STAMP_AT(1);
    auto xfr = [&](u32x4 (&bf)[3], u32 it) {
      u32 s6 = ((rot + it) & 15) << 6;
#pragma unroll
      for (int t = 0; t < 3; t++) bf[t] = lds_rd128(xb[t] ^ s6);
    };
    u32x4 bf[3];
    for (u32 it = 0; it < 12; it += 3) {
      xfr(bf, it); wait_w<16>(w0); proj_step(acc, w0, bf); ldw_q(w0, sWq, ((rot + it + 3) & 15) * 262144, wv);
      xfr(bf, it + 1); wait_w<16>(w1); proj_step(acc, w1, bf); ldw_q(w1, sWq, ((rot + it + 4) & 15) * 262144, wv);
      xfr(bf, it + 2); wait_w<16>(w2); proj_step(acc, w2, bf); ldw_q(w2, sWq, ((rot + it + 5) & 15) * 262144, wv);
      if (it == 0) STAMP_AT(2);
    }
    STAMP_AT(3);
    xfr(bf, 12); wait_w<16>(w0); proj_step(acc, w0, bf);
    if (w == 3) {
      u32 used = 0;
#pragma unroll
      for (int c = 0; c < 9; c++) used |= __any(oldf == (u32)c) ? (1u << c) : 0u;
      nonce = rfl(__builtin_ctz(~used));
      if (lane == 0) __hip_atomic_store(Rw + j * 32, nonce, __ATOMIC_RELAXED, __HIP_MEMORY_SCOPE_AGENT);
    }
    ldw_q(w0, sWq, ((rot + 15) & 15) * 262144, wv);
    if constexpr (BR == 1) {
      dmaK(w * 40960, 0);
      xfr(bf, 13); wait_w<24>(w1); proj_step(acc, w1, bf); dmaV(w * 40960 + 8192, 0);
      xfr(bf, 14); wait_w<24>(w2); proj_step(acc, w2, bf); dmaK(w * 40960 + 16384, 1);
      xfr(bf, 15); wait_w<24>(w0); proj_step(acc, w0, bf);
    } else {
      xfr(bf, 13); wait_w<16>(w1); proj_step(acc, w1, bf);
      xfr(bf, 14); wait_w<8>(w2); proj_step(acc, w2, bf);
      xfr(bf, 15); wait_w<0>(w0); proj_step(acc, w0, bf);
    }
    barrier();
    if constexpr (BR == 1) dmaV(w * 40960 + 24576, 1);
    else { dmaA(7, 7); dmaA(8, 8); dmaA(9, 9); }
#pragma unroll
    for (int ks = 0; ks < 4; ks++)
#pragma unroll
      for (int t = 0; t < 3; t++)
        st.q[ks][t] = u32x4{cvtpk(acc[0][t][ks], acc[1][t][ks]), cvtpk(acc[2][t][ks], acc[3][t][ks]), cvtpk(acc[4][t][ks], acc[5][t][ks]), cvtpk(acc[6][t][ks], acc[7][t][ks])};
  }
  STAMP_AT(4);
#pragma unroll
  for (int t = 0; t < 3; t++) {
    st.m[t] = -__builtin_inff(); st.l[t] = 0.f;
#pragma unroll
    for (int d = 0; d < 8; d++) st.o[d][t] = f32x4{0, 0, 0, 0};
  }
  u32 ka[4], va[8];
  {
    const u32 wb = BR == 0 ? 0 : w * 40960;
#pragma unroll
    for (int ks = 0; ks < 4; ks++) ka[ks] = wb + r * 256 + (((4 * g + ks) ^ kswz(r)) << 4);
    const u32 row = 4 * g + (r >> 2), p = r & 3;
#pragma unroll
    for (int d = 0; d < 8; d++) va[d] = wb + 8192 + row * 256 + ((d ^ (row & 7)) << 5) + p * 8;
  }
  f32x4 s[2][3];
  u32x4 p[3];
  if constexpr (BR == 1) {
    for (u32 t = 0; t < 14; t++) {
      const u32 sb = (t & 1) * 16384;
      waitv<24>();
      tile_qk(s, st, ka, sb);
      asm volatile("s_waitcnt lgkmcnt(0)" ::: "memory");
      dmaK(w * 40960 + sb, t + 2);
      tile_sm(p, s, st);
      waitv<24>();
      tile_pv(st, p, va, sb);
      asm volatile("s_waitcnt lgkmcnt(0)" ::: "memory");
      dmaV(w * 40960 + sb + 8192, t + 2);
    }
    waitv<24>(); tile_qk(s, st, ka, 0); tile_sm(p, s, st); waitv<16>(); tile_pv(st, p, va, 0);
    waitv<8>(); tile_qk(s, st, ka, 16384); tile_sm(p, s, st); waitv<0>(); tile_pv(st, p, va, 16384);
  } else {
    u32 slot = 0;
    for (u32 t = 0; t < 16; t++) {
      if (t == 7) waitv<24>();
      if (t == 10) waitv<0>();
      if (t > 0) barrier();
      if (t >= 1 && t <= 6) dmaA(t - 1, t + 9);
      const u32 sb = slot * 16384;
      tile_qk(s, st, ka, sb);
      tile_sm(p, s, st);
      tile_pv(st, p, va, sb);
      slot = slot == 9 ? 0 : slot + 1;
    }
    barrier();
  }
  STAMP_AT(5);
  {
    const u32 wv0 = 4 * g * 1024 + (w * 128 + n0) * 2, wv1 = wv0 + 16384;
    const u32 so = j * 524288;
    WReg w0, w1, w2;
    zero_w(w0); zero_w(w1); zero_w(w2);
    ldw_o(w0, sWo, so + ((rot + 0) & 15) * 32768, wv0, wv1);
    ldw_o(w1, sWo, so + ((rot + 1) & 15) * 32768, wv0, wv1);
    ldw_o(w2, sWo, so + ((rot + 2) & 15) * 32768, wv0, wv1);
#pragma unroll
    for (int t = 0; t < 3; t++) {
      const float inv = 1.0f / xsum(st.l[t]);
#pragma unroll
      for (int ks = 0; ks < 4; ks++) {
        f32x4 a = st.o[2 * ks][t] * inv, c = st.o[2 * ks + 1][t] * inv;
        lds_wr128(w * 40960 + (ks * 3 + t) * 1024 + lane * 16, u32x4{cvtpk(a[0], a[1]), cvtpk(a[2], a[3]), cvtpk(c[0], c[1]), cvtpk(c[2], c[3])});
      }
    }
#pragma unroll
    for (int c = 0; c < 8; c++)
#pragma unroll
      for (int t = 0; t < 3; t++) acc[c][t] = f32x4{0, 0, 0, 0};
    barrier();
    STAMP_AT(6);
    auto ofr = [&](u32x4 (&bf)[3], u32 it) {
      u32 ks = (rot + it) & 15;
      u32 a = (ks >> 2) * 40960 + (ks & 3) * 3072 + lane * 16;
#pragma unroll
      for (int t = 0; t < 3; t++) bf[t] = lds_rd128(a + t * 1024);
    };
    u32x4 bf[3];
    for (u32 it = 0; it < 12; it += 3) {
      ofr(bf, it); wait_w<16>(w0); proj_step(acc, w0, bf); ldw_o(w0, sWo, so + ((rot + it + 3) & 15) * 32768, wv0, wv1);
      ofr(bf, it + 1); wait_w<16>(w1); proj_step(acc, w1, bf); ldw_o(w1, sWo, so + ((rot + it + 4) & 15) * 32768, wv0, wv1);
      ofr(bf, it + 2); wait_w<16>(w2); proj_step(acc, w2, bf); ldw_o(w2, sWo, so + ((rot + it + 5) & 15) * 32768, wv0, wv1);
    }
    ofr(bf, 12); wait_w<16>(w0); proj_step(acc, w0, bf); ldw_o(w0, sWo, so + ((rot + 15) & 15) * 32768, wv0, wv1);
    ofr(bf, 13); wait_w<16>(w1); proj_step(acc, w1, bf);
    ofr(bf, 14); wait_w<8>(w2); proj_step(acc, w2, bf);
    ofr(bf, 15); wait_w<0>(w0); proj_step(acc, w0, bf);
  }
  STAMP_AT(7);
  {
    const srd_t sP = mksrd((const char*)part + (size_t)(BR * 16 + b) * 8 * 49152, 8 * 49152);
#pragma unroll
    for (int t = 0; t < 3; t++)
#pragma unroll
      for (int i = 0; i < 4; i++)
        __builtin_amdgcn_raw_buffer_store_b128(u32x4{cvtpk(acc[0][t][i], acc[1][t][i]), cvtpk(acc[2][t][i], acc[3][t][i]), cvtpk(acc[4][t][i], acc[5][t][i]), cvtpk(acc[6][t][i], acc[7][t][i])},
                                               sP, lane * 16 + (t * 4 + i) * 1024, j * 49152 + w * 12288, 16);
    u32 echoed = 0;
    if (w == 3) echoed = __hip_atomic_load(Rw + (lane & 7) * 32, __ATOMIC_RELAXED, __HIP_MEMORY_SCOPE_AGENT);
    waitv<0>();
    barrier();
    STAMP_AT(8);
    if (w == 3) {
      if (lane < 8) __hip_atomic_store(Fw + lane * 32 + j, echoed, __ATOMIC_RELAXED, __HIP_MEMORY_SCOPE_AGENT);
      for (u32 spin = 0; spin < (1u << 20); spin++) {
        const u32 f = __hip_atomic_load(Fw + j * 32 + (lane & 7), __ATOMIC_RELAXED, __HIP_MEMORY_SCOPE_AGENT);
        if (__all(f == nonce)) break;
        if ((spin & 15) == 15) {
          const u32 rq = __hip_atomic_load(Rw + (lane & 7) * 32, __ATOMIC_RELAXED, __HIP_MEMORY_SCOPE_AGENT);
          if (rq != echoed && lane < 8) __hip_atomic_store(Fw + lane * 32 + j, rq, __ATOMIC_RELAXED, __HIP_MEMORY_SCOPE_AGENT);
          echoed = rq;
        }
      }
    }
    barrier();
    STAMP_AT(9);
    if (w == 3) {
      const u32 rq = __hip_atomic_load(Rw + (lane & 7) * 32, __ATOMIC_RELAXED, __HIP_MEMORY_SCOPE_AGENT);
      if (rq != echoed && lane < 8) __hip_atomic_store(Fw + lane * 32 + j, rq, __ATOMIC_RELAXED, __HIP_MEMORY_SCOPE_AGENT);
    } else {
      const u32 blk = 6 * j + 2 * w;
      u32x4 v[2][8];
#pragma unroll
      for (int h = 0; h < 2; h++)
#pragma unroll
        for (int s = 0; s < 8; s++) v[h][s] = __builtin_amdgcn_raw_buffer_load_b128(sP, lane * 16 + h * 1024, s * 49152 + blk * 1024, 16);
      const u32 ww = blk / 12, t = (blk % 12) >> 2, i = blk & 3;
      u16* o = out + (size_t)(b * 48 + 16 * t + r) * 1024 + BR * 512 + ww * 128 + 8 * i + 32 * g;
#pragma unroll
      for (int h = 0; h < 2; h++) {
        float a[8] = {0, 0, 0, 0, 0, 0, 0, 0};
#pragma unroll
        for (int s = 0; s < 8; s++)
#pragma unroll
          for (int c = 0; c < 4; c++) { a[2 * c] += __uint_as_float(v[h][s][c] << 16); a[2 * c + 1] += __uint_as_float(v[h][s][c] & 0xffff0000u); }
        *(u32x4*)(o + 8 * h) = u32x4{cvtpk(a[0], a[1]), cvtpk(a[2], a[3]), cvtpk(a[4], a[5]), cvtpk(a[6], a[7])};
      }
    }
  }
  STAMP_AT(10);
}

__global__ __launch_bounds__(256, 2) void attn_main(const u16* xA, const u16* kA, const u16* vA, const u16* wqA, const u16* woA,
                                                    const u16* xB, const u16* kB, const u16* vB, const u16* wqB, const u16* woB, u16* part, u32* sync, u16* out
) {
  u64* stamps = nullptr;
  if ((blockIdx.x >> 3) & 1) body<1>(xB, kB, vB, wqB, woB, part, sync, out, stamps);
  else body<0>(xA, kA, vA, wqA, woA, part, sync, out, stamps);
}

extern "C" void kernel_launch(void* const* d_in, const int* in_sizes, int n_in, void* d_out, int out_size, void* d_ws, size_t ws_size, hipStream_t stream) {
  (void)hipFuncSetAttribute((const void*)attn_main, hipFuncAttributeMaxDynamicSharedMemorySize, LDS_BYTES);
  u16* part = (u16*)d_ws;
  attn_main<<<256, 256, LDS_BYTES, stream>>>((const u16*)d_in[0], (const u16*)d_in[1], (const u16*)d_in[2], (const u16*)d_in[3], (const u16*)d_in[4],
                                             (const u16*)d_in[5], (const u16*)d_in[6], (const u16*)d_in[7], (const u16*)d_in[8], (const u16*)d_in[9], part, (u32*)((char*)d_ws + (32 << 20)), (u16*)d_out
  );
}
```

```cpp
#define AMPI 0
#define AMPJ 1
#define AMPBR 1
#include <hip/hip_runtime.h>
#include <hip/hip_bf16.h>
#include <stdint.h>

typedef unsigned short u16;
typedef unsigned int u32;
typedef unsigned long long u64;
typedef float f32x4 __attribute__((ext_vector_type(4)));
typedef float f32x2 __attribute__((ext_vector_type(2)));
typedef u32 u32x4 __attribute__((ext_vector_type(4)));
typedef u32 u32x2 __attribute__((ext_vector_type(2)));
typedef short s16x4 __attribute__((ext_vector_type(4)));
typedef __bf16 bf16x8 __attribute__((ext_vector_type(8)));
typedef __bf16 bf16x2 __attribute__((ext_vector_type(2)));
#define LAS __attribute__((address_space(3)))
#define DI __device__ __forceinline__
typedef __amdgpu_buffer_rsrc_t srd_t;

constexpr int LDS_BYTES = 160 * 1024;
constexpr float CSC = 0.08838834764831845f * 1.4426950408889634f;
constexpr float THR = 8.0f;

DI u32 rfl(u32 x) { return __builtin_amdgcn_readfirstlane(x); }
DI u32 cvtpk(float lo, float hi) { f32x2 v = {lo, hi}; bf16x2 b = __builtin_convertvector(v, bf16x2); return __builtin_bit_cast(u32, b); }
DI f32x4 mfma(u32x4 a, u32x4 b, f32x4 c) { return __builtin_amdgcn_mfma_f32_16x16x32_bf16(__builtin_bit_cast(bf16x8, a), __builtin_bit_cast(bf16x8, b), c, 0, 0, 0); }
DI u32x4 lds_rd128(u32 a) { return *(LAS u32x4*)(a); }
DI void lds_wr128(u32 a, u32x4 v) { *(LAS u32x4*)(a) = v; }
DI u32x2 lds_rdtr(u32 a) { return __builtin_bit_cast(u32x2, __builtin_amdgcn_ds_read_tr16_b64_v4i16((LAS s16x4*)(a))); }
DI srd_t mksrd(const void* p, u32 bytes) { return __builtin_amdgcn_make_buffer_rsrc((void*)p, (short)0, (int)bytes, 0x00020000); }
template <int N> DI void waitv() { asm volatile("s_waitcnt vmcnt(%0)" ::"i"(N) : "memory"); }
DI void barrier() { asm volatile("s_waitcnt lgkmcnt(0)\n\ts_barrier" ::: "memory"); }

template <int NT> DI void dma4(u32 m0, srd_t s, u32 soff, u32 v0, u32 v1, u32 v2, u32 v3) {
  if constexpr (NT)
    asm volatile("s_mov_b32 m0, %0\n\ts_nop 0\n\t"
               "buffer_load_dwordx4 %3, %1, %2 offen nt lds\n\t"
               "buffer_load_dwordx4 %4, %1, %2 offen offset:1024 nt lds\n\t"
               "buffer_load_dwordx4 %5, %1, %2 offen offset:2048 nt lds\n\t"
               "buffer_load_dwordx4 %6, %1, %2 offen offset:3072 nt lds"
               ::"s"(m0), "s"(s), "s"(soff), "v"(v0), "v"(v1), "v"(v2), "v"(v3) : "memory");
  else
    asm volatile("s_mov_b32 m0, %0\n\ts_nop 0\n\t"
               "buffer_load_dwordx4 %3, %1, %2 offen lds\n\t"
               "buffer_load_dwordx4 %4, %1, %2 offen offset:1024 lds\n\t"
               "buffer_load_dwordx4 %5, %1, %2 offen offset:2048 lds\n\t"
               "buffer_load_dwordx4 %6, %1, %2 offen offset:3072 lds"
               ::"s"(m0), "s"(s), "s"(soff), "v"(v0), "v"(v1), "v"(v2), "v"(v3) : "memory");
}
template <int NT> DI void dma2(u32 m0, srd_t s, u32 soff, u32 v0, u32 v1) {
  if constexpr (NT)
    asm volatile("s_mov_b32 m0, %0\n\ts_nop 0\n\t"
               "buffer_load_dwordx4 %3, %1, %2 offen nt lds\n\t"
               "buffer_load_dwordx4 %4, %1, %2 offen offset:1024 nt lds"
               ::"s"(m0), "s"(s), "s"(soff), "v"(v0), "v"(v1) : "memory");
  else
    asm volatile("s_mov_b32 m0, %0\n\ts_nop 0\n\t"
               "buffer_load_dwordx4 %3, %1, %2 offen lds\n\t"
               "buffer_load_dwordx4 %4, %1, %2 offen offset:1024 lds"
               ::"s"(m0), "s"(s), "s"(soff), "v"(v0), "v"(v1) : "memory");
}
struct WReg { u32x4 r[8]; };
DI void ldw_q(WReg& w, srd_t s, u32 soff, const u32 (&v)[8]) {
  asm volatile("buffer_load_dwordx4 %0, %8, %16, %17 offen\n\t"
               "buffer_load_dwordx4 %1, %9, %16, %17 offen\n\t"
               "buffer_load_dwordx4 %2, %10, %16, %17 offen\n\t"
               "buffer_load_dwordx4 %3, %11, %16, %17 offen\n\t"
               "buffer_load_dwordx4 %4, %12, %16, %17 offen\n\t"
               "buffer_load_dwordx4 %5, %13, %16, %17 offen\n\t"
               "buffer_load_dwordx4 %6, %14, %16, %17 offen\n\t"
               "buffer_load_dwordx4 %7, %15, %16, %17 offen"
               : "+v"(w.r[0]), "+v"(w.r[1]), "+v"(w.r[2]), "+v"(w.r[3]), "+v"(w.r[4]), "+v"(w.r[5]), "+v"(w.r[6]), "+v"(w.r[7])
               : "v"(v[0]), "v"(v[1]), "v"(v[2]), "v"(v[3]), "v"(v[4]), "v"(v[5]), "v"(v[6]), "v"(v[7]), "s"(s), "s"(soff) : "memory");
}
DI void ldw_o(WReg& w, srd_t s, u32 soff, u32 v0, u32 v1) {
  asm volatile("buffer_load_dwordx4 %0, %8, %10, %11 offen\n\t"
               "buffer_load_dwordx4 %1, %8, %10, %11 offen offset:1024\n\t"
               "buffer_load_dwordx4 %2, %8, %10, %11 offen offset:2048\n\t"
               "buffer_load_dwordx4 %3, %8, %10, %11 offen offset:3072\n\t"
               "buffer_load_dwordx4 %4, %9, %10, %11 offen\n\t"
               "buffer_load_dwordx4 %5, %9, %10, %11 offen offset:1024\n\t"
               "buffer_load_dwordx4 %6, %9, %10, %11 offen offset:2048\n\t"
               "buffer_load_dwordx4 %7, %9, %10, %11 offen offset:3072"
               : "+v"(w.r[0]), "+v"(w.r[1]), "+v"(w.r[2]), "+v"(w.r[3]), "+v"(w.r[4]), "+v"(w.r[5]), "+v"(w.r[6]), "+v"(w.r[7])
               : "v"(v0), "v"(v1), "s"(s), "s"(soff) : "memory");
}
template <int N> DI void wait_w(WReg& w) {
  asm volatile("s_waitcnt vmcnt(%8)"
               : "+v"(w.r[0]), "+v"(w.r[1]), "+v"(w.r[2]), "+v"(w.r[3]), "+v"(w.r[4]), "+v"(w.r[5]), "+v"(w.r[6]), "+v"(w.r[7])
               : "i"(N) : "memory");
}
DI void zero_w(WReg& w) {
#pragma unroll
  for (int j = 0; j < 8; j++) w.r[j] = u32x4{0, 0, 0, 0};
}
DI void proj_step(f32x4 (&acc)[8][3], const WReg& w, const u32x4 (&bf)[3]) {
#pragma unroll
  for (int c = 0; c < 8; c++) {
    const u32 sel = (c & 1) ? 0x07060302u : 0x05040100u;
    u32x4 a;
#pragma unroll
    for (int jj = 0; jj < 4; jj++) a[jj] = __builtin_amdgcn_perm(w.r[2 * jj + 1][c >> 1], w.r[2 * jj][c >> 1], sel);
#pragma unroll
    for (int t = 0; t < 3; t++) acc[c][t] = mfma(a, bf[t], acc[c][t]);
  }
}
DI float xmax(float v) {
  auto a = __builtin_amdgcn_permlane32_swap(__float_as_uint(v), __float_as_uint(v), false, false);
  v = fmaxf(__uint_as_float(a[0]), __uint_as_float(a[1]));
  auto b = __builtin_amdgcn_permlane16_swap(__float_as_uint(v), __float_as_uint(v), false, false);
  return fmaxf(__uint_as_float(b[0]), __uint_as_float(b[1]));
}
DI float xsum(float v) {
  auto a = __builtin_amdgcn_permlane32_swap(__float_as_uint(v), __float_as_uint(v), false, false);
  v = __uint_as_float(a[0]) + __uint_as_float(a[1]);
  auto b = __builtin_amdgcn_permlane16_swap(__float_as_uint(v), __float_as_uint(v), false, false);
  return __uint_as_float(b[0]) + __uint_as_float(b[1]);
}

DI u32 kswz(u32 r) { return (r & 10) | ((r & 1) << 2) | ((r >> 2) & 1); }
struct Attn {
  u32x4 q[4][3];
  f32x4 o[8][3];
  float m[3], l[3];
};
DI void tile_qk(f32x4 (&s)[2][3], const Attn& st, const u32 (&ka)[4], u32 base) {
#pragma unroll
  for (int u = 0; u < 2; u++)
#pragma unroll
    for (int t = 0; t < 3; t++) s[u][t] = f32x4{0, 0, 0, 0};
#pragma unroll
  for (int ks = 0; ks < 4; ks++)
#pragma unroll
    for (int u = 0; u < 2; u++) {
      u32x4 kf = lds_rd128(ka[ks] + base + u * 4096);
#pragma unroll
      for (int t = 0; t < 3; t++) s[u][t] = mfma(kf, st.q[ks][t], s[u][t]);
    }
}
DI void tile_sm(u32x4 (&p)[3], f32x4 (&s)[2][3], Attn& st) {
  float mx[3];
  bool need = false;
#pragma unroll
  for (int t = 0; t < 3; t++) {
    float a = fmaxf(fmaxf(s[0][t][0], s[0][t][1]), fmaxf(s[0][t][2], s[0][t][3]));
    float b = fmaxf(fmaxf(s[1][t][0], s[1][t][1]), fmaxf(s[1][t][2], s[1][t][3]));
    mx[t] = fmaxf(a, b) * CSC;
    need |= (mx[t] - st.m[t] > THR);
  }
  if (__any(need)) {
#pragma unroll
    for (int t = 0; t < 3; t++) {
      float mn = fmaxf(st.m[t], xmax(mx[t]));
      float al = __builtin_amdgcn_exp2f(st.m[t] - mn);
      st.m[t] = mn;
      st.l[t] *= al;
#pragma unroll
      for (int d = 0; d < 8; d++) st.o[d][t] *= al;
    }
  }
#pragma unroll
  for (int t = 0; t < 3; t++) {
    float e[8];
#pragma unroll
    for (int i = 0; i < 8; i++) e[i] = __builtin_amdgcn_exp2f(fmaf(s[i >> 2][t][i & 3], CSC, -st.m[t]));
    st.l[t] += ((e[0] + e[1]) + (e[2] + e[3])) + ((e[4] + e[5]) + (e[6] + e[7]));
    p[t] = u32x4{cvtpk(e[0], e[1]), cvtpk(e[2], e[3]), cvtpk(e[4], e[5]), cvtpk(e[6], e[7])};
  }
}
DI void tile_pv(Attn& st, const u32x4 (&p)[3], const u32 (&va)[8], u32 base) {
#pragma unroll
  for (int d = 0; d < 8; d++) {
    u32x2 lo = lds_rdtr(va[d] + base), hi = lds_rdtr(va[d] + base + 4096);
    u32x4 vf = {lo[0], lo[1], hi[0], hi[1]};
#pragma unroll
    for (int t = 0; t < 3; t++) st.o[d][t] = mfma(vf, p[t], st.o[d][t]);
  }
}

#define STAMP_AT(i) do { tst[i] = __builtin_amdgcn_s_memrealtime(); asm volatile("s_waitcnt lgkmcnt(0)" ::: "memory"); } while (0)

template <int BR>
DI void body(const u16* x, const u16* kc, const u16* vc, const u16* wq, const u16* wo, u16* part, u32* sync, u16* out, u64* stamps) {
  const u32 wg = blockIdx.x, j = wg & 7, b = wg >> 4;
  const u32 lane = threadIdx.x & 63, w = rfl(threadIdx.x >> 6);
  const u32 r = lane & 15, g = lane >> 4;
  u64 tst[11];
  STAMP_AT(0);
  u64 c0_ = __builtin_amdgcn_s_memtime();
  typedef __attribute__((address_space(1))) u32 gu32;
  gu32* const Rw = (gu32*)sync + (BR * 16 + b) * 512;
  gu32* const Fw = Rw + 256;
  u32 nonce = 0, oldf = 0;
  if (w == 3) asm volatile("global_load_dword %0, %1, off sc1" : "+v"(oldf) : "v"(Fw + j * 32 + (lane & 7)) : "memory");
  constexpr u32 KVB = 512 * 128 * 2;
  const u32 kvi = BR == 0 ? b * 8 + j : (b * 32 + j * 4 + w);
  const srd_t sK = mksrd((const char*)kc + (size_t)kvi * KVB, KVB), sV = mksrd((const char*)vc + (size_t)kvi * KVB, KVB);
  const srd_t sX = mksrd((const char*)x + (size_t)b * 49152, 49152);
  const srd_t sWq = mksrd(wq, 512 * 4096 * 2), sWo = mksrd(wo, 4096 * 512 * 2);
  constexpr int KVNT = 1;
  u32 kv_[4], vv_[2];
#pragma unroll
  for (int n = 0; n < 4; n++) kv_[n] = g * 256 + ((r ^ kswz(4 * n + g)) << 4);
#pragma unroll
  for (int n = 0; n < 2; n++) vv_[n] = g * 256 + ((r ^ ((4 * n + g) << 1)) << 4);
  auto dmaK = [&](u32 lds, u32 tile) { dma4<KVNT>(lds, sK, tile * 8192, kv_[0], kv_[1], kv_[2], kv_[3]); dma4<KVNT>(lds + 4096, sK, tile * 8192 + 4096, kv_[0], kv_[1], kv_[2], kv_[3]); };
  auto dmaV = [&](u32 lds, u32 tile) { dma4<KVNT>(lds, sV, tile * 8192, vv_[0], vv_[1], vv_[0], vv_[1]); dma4<KVNT>(lds + 4096, sV, tile * 8192 + 4096, vv_[0], vv_[1], vv_[0], vv_[1]); };
  const u32 ka0 = (w & 1) ? kv_[2] : kv_[0], ka1 = (w & 1) ? kv_[3] : kv_[1];
  auto dmaA = [&](u32 slot, u32 tile) {
    dma2<KVNT>(slot * 16384 + w * 2048, sK, tile * 8192 + w * 2048, ka0, ka1);
    dma2<KVNT>(slot * 16384 + 8192 + w * 2048, sV, tile * 8192 + w * 2048, vv_[0], vv_[1]);
  };
  if constexpr (BR == 0) {
#pragma unroll
    for (int t = 0; t < 7; t++) dmaA(t, t);
  }
  const u32 n0 = 8 * r;
  const u32 rot = b;
  f32x4 acc[8][3];
  Attn st;
  {
    u32 wv[8];
#pragma unroll
    for (int i = 0; i < 8; i++) wv[i] = (8 * g + i) * 8192 + (j * 512 + w * 128 + n0) * 2;
    WReg w0, w1, w2;
    zero_w(w0); zero_w(w1); zero_w(w2);
    ldw_q(w0, sWq, ((rot + 0) & 15) * 262144, wv);
  const u32 xlds = BR == 0 ? 114688 + w * 12288 : w * 40960 + 28672;
#pragma unroll
  for (int i4 = 0; i4 < 3; i4++) {
    u32 vo[4];
#pragma unroll
    for (int n = 0; n < 4; n++) { u32 row = 12 * w + 4 * i4 + n; vo[n] = ((lane & 48) | (r ^ (row & 15))) << 4; }
    dma4<0>(xlds + i4 * 4096, sX, (12 * w + 4 * i4) * 1024, vo[0], vo[1], vo[2], vo[3]);
  }
    ldw_q(w1, sWq, ((rot + 1) & 15) * 262144, wv);
    ldw_q(w2, sWq, ((rot + 2) & 15) * 262144, wv);
    u32 xb[3];
#pragma unroll
    for (int t = 0; t < 3; t++) {
      u32 q = 16 * t + r;
      u32 rowb = BR == 0 ? 114688 + q * 1024 : (q / 12) * 40960 + 28672 + (q % 12) * 1024;
      xb[t] = rowb + ((g ^ r) << 4);
    }
#pragma unroll
    for (int c = 0; c < 8; c++)
#pragma unroll
      for (int t = 0; t < 3; t++) acc[c][t] = f32x4{0, 0, 0, 0};
    asm volatile("s_waitcnt vmcnt(16)" : "+v"(oldf) :: "memory");
    barrier();
    STAMP_AT(1);
    auto xfr = [&](u32x4 (&bf)[3], u32 it) {
      u32 s6 = ((rot + it) & 15) << 6;
#pragma unroll
      for (int t = 0; t < 3; t++) bf[t] = lds_rd128(xb[t] ^ s6);
    };
    u32x4 bf[3];
    for (u32 it = 0; it < 12; it += 3) {
      xfr(bf, it); wait_w<16>(w0); proj_step(acc, w0, bf); ldw_q(w0, sWq, ((rot + it + 3) & 15) * 262144, wv);
      xfr(bf, it + 1); wait_w<16>(w1); proj_step(acc, w1, bf); ldw_q(w1, sWq, ((rot + it + 4) & 15) * 262144, wv);
      xfr(bf, it + 2); wait_w<16>(w2); proj_step(acc, w2, bf); ldw_q(w2, sWq, ((rot + it + 5) & 15) * 262144, wv);
      if (it == 0) STAMP_AT(2);
    }
    STAMP_AT(3);
    xfr(bf, 12); wait_w<16>(w0); proj_step(acc, w0, bf);
    if (w == 3) {
      u32 used = 0;
#pragma unroll
      for (int c = 0; c < 9; c++) used |= __any(oldf == (u32)c) ? (1u << c) : 0u;
      nonce = rfl(__builtin_ctz(~used));
      if (lane == 0) __hip_atomic_store(Rw + j * 32, nonce, __ATOMIC_RELAXED, __HIP_MEMORY_SCOPE_AGENT);
    }
    ldw_q(w0, sWq, ((rot + 15) & 15) * 262144, wv);
    if constexpr (BR == 1) {
      dmaK(w * 40960, 0);
      xfr(bf, 13); wait_w<24>(w1); proj_step(acc, w1, bf); dmaV(w * 40960 + 8192, 0);
      xfr(bf, 14); wait_w<24>(w2); proj_step(acc, w2, bf); dmaK(w * 40960 + 16384, 1);
      xfr(bf, 15); wait_w<24>(w0); proj_step(acc, w0, bf);
    } else {
      xfr(bf, 13); wait_w<16>(w1); proj_step(acc, w1, bf);
      xfr(bf, 14); wait_w<8>(w2); proj_step(acc, w2, bf);
      xfr(bf, 15); wait_w<0>(w0); proj_step(acc, w0, bf);
    }
    barrier();
    if constexpr (BR == 1) dmaV(w * 40960 + 24576, 1);
    else { dmaA(7, 7); dmaA(8, 8); dmaA(9, 9); }
#pragma unroll
    for (int ks = 0; ks < 4; ks++)
#pragma unroll
      for (int t = 0; t < 3; t++)
        st.q[ks][t] = u32x4{cvtpk(acc[0][t][ks], acc[1][t][ks]), cvtpk(acc[2][t][ks], acc[3][t][ks]), cvtpk(acc[4][t][ks], acc[5][t][ks]), cvtpk(acc[6][t][ks], acc[7][t][ks])};
  }
  STAMP_AT(4);
#pragma unroll
  for (int t = 0; t < 3; t++) {
    st.m[t] = -__builtin_inff(); st.l[t] = 0.f;
#pragma unroll
    for (int d = 0; d < 8; d++) st.o[d][t] = f32x4{0, 0, 0, 0};
  }
  u32 ka[4], va[8];
  {
    const u32 wb = BR == 0 ? 0 : w * 40960;
#pragma unroll
    for (int ks = 0; ks < 4; ks++) ka[ks] = wb + r * 256 + (((4 * g + ks) ^ kswz(r)) << 4);
    const u32 row = 4 * g + (r >> 2), p = r & 3;
#pragma unroll
    for (int d = 0; d < 8; d++) va[d] = wb + 8192 + row * 256 + ((d ^ (row & 7)) << 5) + p * 8;
  }
  f32x4 s[2][3];
  u32x4 p[3];
  if constexpr (BR == 1) {
    for (u32 t = 0; t < 14; t++) {
      const u32 sb = (t & 1) * 16384;
      waitv<24>();
      tile_qk(s, st, ka, sb);
      asm volatile("s_waitcnt lgkmcnt(0)" ::: "memory");
      dmaK(w * 40960 + sb, t + 2);
      tile_sm(p, s, st);
      waitv<24>();
      tile_pv(st, p, va, sb);
      asm volatile("s_waitcnt lgkmcnt(0)" ::: "memory");
      dmaV(w * 40960 + sb + 8192, t + 2);
    }
    waitv<24>(); tile_qk(s, st, ka, 0); tile_sm(p, s, st); waitv<16>(); tile_pv(st, p, va, 0);
    waitv<8>(); tile_qk(s, st, ka, 16384); tile_sm(p, s, st); waitv<0>(); tile_pv(st, p, va, 16384);
  } else {
    u32 slot = 0;
    for (u32 t = 0; t < 16; t++) {
      if (t == 7) waitv<24>();
      if (t == 10) waitv<0>();
      if (t > 0) barrier();
      if (t >= 1 && t <= 6) dmaA(t - 1, t + 9);
      const u32 sb = slot * 16384;
      tile_qk(s, st, ka, sb);
      tile_sm(p, s, st);
      tile_pv(st, p, va, sb);
      slot = slot == 9 ? 0 : slot + 1;
    }
    barrier();
  }
  STAMP_AT(5);
  {
    const u32 wv0 = 4 * g * 1024 + (w * 128 + n0) * 2, wv1 = wv0 + 16384;
    const u32 so = j * 524288;
    WReg w0, w1, w2;
    zero_w(w0); zero_w(w1); zero_w(w2);
    ldw_o(w0, sWo, so + ((rot + 0) & 15) * 32768, wv0, wv1);
    ldw_o(w1, sWo, so + ((rot + 1) & 15) * 32768, wv0, wv1);
    ldw_o(w2, sWo, so + ((rot + 2) & 15) * 32768, wv0, wv1);
#pragma unroll
    for (int t = 0; t < 3; t++) {
      const float inv = 1.0f / xsum(st.l[t]);
#pragma unroll
      for (int ks = 0; ks < 4; ks++) {
        f32x4 a = st.o[2 * ks][t] * inv, c = st.o[2 * ks + 1][t] * inv;
        lds_wr128(w * 40960 + (ks * 3 + t) * 1024 + lane * 16, u32x4{cvtpk(a[0], a[1]), cvtpk(a[2], a[3]), cvtpk(c[0], c[1]), cvtpk(c[2], c[3])});
      }
    }
#pragma unroll
    for (int c = 0; c < 8; c++)
#pragma unroll
      for (int t = 0; t < 3; t++) acc[c][t] = f32x4{0, 0, 0, 0};
    barrier();
    STAMP_AT(6);
    auto ofr = [&](u32x4 (&bf)[3], u32 it) {
      u32 ks = (rot + it) & 15;
      u32 a = (ks >> 2) * 40960 + (ks & 3) * 3072 + lane * 16;
#pragma unroll
      for (int t = 0; t < 3; t++) bf[t] = lds_rd128(a + t * 1024);
    };
    u32x4 bf[3];
    for (u32 it = 0; it < 12; it += 3) {
      ofr(bf, it); wait_w<16>(w0); proj_step(acc, w0, bf); ldw_o(w0, sWo, so + ((rot + it + 3) & 15) * 32768, wv0, wv1);
      ofr(bf, it + 1); wait_w<16>(w1); proj_step(acc, w1, bf); ldw_o(w1, sWo, so + ((rot + it + 4) & 15) * 32768, wv0, wv1);
      ofr(bf, it + 2); wait_w<16>(w2); proj_step(acc, w2, bf); ldw_o(w2, sWo, so + ((rot + it + 5) & 15) * 32768, wv0, wv1);
    }
    ofr(bf, 12); wait_w<16>(w0); proj_step(acc, w0, bf); ldw_o(w0, sWo, so + ((rot + 15) & 15) * 32768, wv0, wv1);
    ofr(bf, 13); wait_w<16>(w1); proj_step(acc, w1, bf);
    ofr(bf, 14); wait_w<8>(w2); proj_step(acc, w2, bf);
    ofr(bf, 15); wait_w<0>(w0); proj_step(acc, w0, bf);
  }
  STAMP_AT(7);
  {
    const srd_t sP = mksrd((const char*)part + (size_t)(BR * 16 + b) * 8 * 49152, 8 * 49152);
#pragma unroll
    for (int t = 0; t < 3; t++)
#pragma unroll
      for (int i = 0; i < 4; i++)
        __builtin_amdgcn_raw_buffer_store_b128(u32x4{cvtpk(acc[0][t][i], acc[1][t][i]), cvtpk(acc[2][t][i], acc[3][t][i]), cvtpk(acc[4][t][i], acc[5][t][i]), cvtpk(acc[6][t][i], acc[7][t][i])},
                                               sP, lane * 16 + (t * 4 + i) * 1024, j * 49152 + w * 12288, 16);
    u32 echoed = 0;
    if (w == 3) echoed = __hip_atomic_load(Rw + (lane & 7) * 32, __ATOMIC_RELAXED, __HIP_MEMORY_SCOPE_AGENT);
    waitv<0>();
    barrier();
    STAMP_AT(8);
    if (w == 3) {
      if (lane < 8) __hip_atomic_store(Fw + lane * 32 + j, echoed, __ATOMIC_RELAXED, __HIP_MEMORY_SCOPE_AGENT);
      for (u32 spin = 0; spin < (1u << 20); spin++) {
        const u32 f = __hip_atomic_load(Fw + j * 32 + (lane & 7), __ATOMIC_RELAXED, __HIP_MEMORY_SCOPE_AGENT);
        if (__all(f == nonce)) break;
        if ((spin & 15) == 15) {
          const u32 rq = __hip_atomic_load(Rw + (lane & 7) * 32, __ATOMIC_RELAXED, __HIP_MEMORY_SCOPE_AGENT);
          if (rq != echoed && lane < 8) __hip_atomic_store(Fw + lane * 32 + j, rq, __ATOMIC_RELAXED, __HIP_MEMORY_SCOPE_AGENT);
          echoed = rq;
        }
      }
    }
    barrier();
    STAMP_AT(9);
    if (w == 3) {
      const u32 rq = __hip_atomic_load(Rw + (lane & 7) * 32, __ATOMIC_RELAXED, __HIP_MEMORY_SCOPE_AGENT);
      if (rq != echoed && lane < 8) __hip_atomic_store(Fw + lane * 32 + j, rq, __ATOMIC_RELAXED, __HIP_MEMORY_SCOPE_AGENT);
    } else {
      const u32 blk = 6 * j + 2 * w;
      u32x4 v[2][8];
#pragma unroll
      for (int h = 0; h < 2; h++)
#pragma unroll
        for (int s = 0; s < 8; s++) v[h][s] = __builtin_amdgcn_raw_buffer_load_b128(sP, lane * 16 + h * 1024, s * 49152 + blk * 1024, 16);
      const u32 ww = blk / 12, t = (blk % 12) >> 2, i = blk & 3;
      u16* o = out + (size_t)(b * 48 + 16 * t + r) * 1024 + BR * 512 + ww * 128 + 8 * i + 32 * g;
#pragma unroll
      for (int h = 0; h < 2; h++) {
        float a[8] = {0, 0, 0, 0, 0, 0, 0, 0};
#pragma unroll
        for (int s = 0; s < 8; s++)
#pragma unroll
          for (int c = 0; c < 4; c++) { a[2 * c] += __uint_as_float(v[h][s][c] << 16); a[2 * c + 1] += __uint_as_float(v[h][s][c] & 0xffff0000u); }
        *(u32x4*)(o + 8 * h) = u32x4{cvtpk(a[0], a[1]), cvtpk(a[2], a[3]), cvtpk(a[4], a[5]), cvtpk(a[6], a[7])};
      }
    }
  }
  waitv<0>();
  STAMP_AT(10);
  if (BR == AMPBR) {
    u64 end_ = tst[10] + (tst[AMPJ] - tst[AMPI]) * 20;
    while (__builtin_amdgcn_s_memrealtime() < end_) __builtin_amdgcn_s_sleep(4);
  }
}

__global__ __launch_bounds__(256, 2) void attn_main(const u16* xA, const u16* kA, const u16* vA, const u16* wqA, const u16* woA,
                                                    const u16* xB, const u16* kB, const u16* vB, const u16* wqB, const u16* woB, u16* part, u32* sync, u16* out
) {
  u64* stamps = nullptr;
  if ((blockIdx.x >> 3) & 1) body<1>(xB, kB, vB, wqB, woB, part, sync, out, stamps);
  else body<0>(xA, kA, vA, wqA, woA, part, sync, out, stamps);
}

extern "C" void kernel_launch(void* const* d_in, const int* in_sizes, int n_in, void* d_out, int out_size, void* d_ws, size_t ws_size, hipStream_t stream) {
  (void)hipFuncSetAttribute((const void*)attn_main, hipFuncAttributeMaxDynamicSharedMemorySize, LDS_BYTES);
  u16* part = (u16*)d_ws;
  attn_main<<<256, 256, LDS_BYTES, stream>>>((const u16*)d_in[0], (const u16*)d_in[1], (const u16*)d_in[2], (const u16*)d_in[3], (const u16*)d_in[4],
                                             (const u16*)d_in[5], (const u16*)d_in[6], (const u16*)d_in[7], (const u16*)d_in[8], (const u16*)d_in[9], part, (u32*)((char*)d_ws + (32 << 20)), (u16*)d_out
  );
}
```

```cpp
#define AMPI 0
#define AMPJ 0
#define AMPBR 0
#include <hip/hip_runtime.h>
#include <hip/hip_bf16.h>
#include <stdint.h>

typedef unsigned short u16;
typedef unsigned int u32;
typedef unsigned long long u64;
typedef float f32x4 __attribute__((ext_vector_type(4)));
typedef float f32x2 __attribute__((ext_vector_type(2)));
typedef u32 u32x4 __attribute__((ext_vector_type(4)));
typedef u32 u32x2 __attribute__((ext_vector_type(2)));
typedef short s16x4 __attribute__((ext_vector_type(4)));
typedef __bf16 bf16x8 __attribute__((ext_vector_type(8)));
typedef __bf16 bf16x2 __attribute__((ext_vector_type(2)));
#define LAS __attribute__((address_space(3)))
#define DI __device__ __forceinline__
typedef __amdgpu_buffer_rsrc_t srd_t;

constexpr int LDS_BYTES = 160 * 1024;
constexpr float CSC = 0.08838834764831845f * 1.4426950408889634f;
constexpr float THR = 8.0f;

DI u32 rfl(u32 x) { return __builtin_amdgcn_readfirstlane(x); }
DI u32 cvtpk(float lo, float hi) { f32x2 v = {lo, hi}; bf16x2 b = __builtin_convertvector(v, bf16x2); return __builtin_bit_cast(u32, b); }
DI f32x4 mfma(u32x4 a, u32x4 b, f32x4 c) { return __builtin_amdgcn_mfma_f32_16x16x32_bf16(__builtin_bit_cast(bf16x8, a), __builtin_bit_cast(bf16x8, b), c, 0, 0, 0); }
DI u32x4 lds_rd128(u32 a) { return *(LAS u32x4*)(a); }
DI void lds_wr128(u32 a, u32x4 v) { *(LAS u32x4*)(a) = v; }
DI u32x2 lds_rdtr(u32 a) { return __builtin_bit_cast(u32x2, __builtin_amdgcn_ds_read_tr16_b64_v4i16((LAS s16x4*)(a))); }
DI srd_t mksrd(const void* p, u32 bytes) { return __builtin_amdgcn_make_buffer_rsrc((void*)p, (short)0, (int)bytes, 0x00020000); }
template <int N> DI void waitv() { asm volatile("s_waitcnt vmcnt(%0)" ::"i"(N) : "memory"); }
DI void barrier() { asm volatile("s_waitcnt lgkmcnt(0)\n\ts_barrier" ::: "memory"); }

template <int NT> DI void dma4(u32 m0, srd_t s, u32 soff, u32 v0, u32 v1, u32 v2, u32 v3) {
  if constexpr (NT)
    asm volatile("s_mov_b32 m0, %0\n\ts_nop 0\n\t"
               "buffer_load_dwordx4 %3, %1, %2 offen nt lds\n\t"
               "buffer_load_dwordx4 %4, %1, %2 offen offset:1024 nt lds\n\t"
               "buffer_load_dwordx4 %5, %1, %2 offen offset:2048 nt lds\n\t"
               "buffer_load_dwordx4 %6, %1, %2 offen offset:3072 nt lds"
               ::"s"(m0), "s"(s), "s"(soff), "v"(v0), "v"(v1), "v"(v2), "v"(v3) : "memory");
  else
    asm volatile("s_mov_b32 m0, %0\n\ts_nop 0\n\t"
               "buffer_load_dwordx4 %3, %1, %2 offen lds\n\t"
               "buffer_load_dwordx4 %4, %1, %2 offen offset:1024 lds\n\t"
               "buffer_load_dwordx4 %5, %1, %2 offen offset:2048 lds\n\t"
               "buffer_load_dwordx4 %6, %1, %2 offen offset:3072 lds"
               ::"s"(m0), "s"(s), "s"(soff), "v"(v0), "v"(v1), "v"(v2), "v"(v3) : "memory");
}
template <int NT> DI void dma2(u32 m0, srd_t s, u32 soff, u32 v0, u32 v1) {
  if constexpr (NT)
    asm volatile("s_mov_b32 m0, %0\n\ts_nop 0\n\t"
               "buffer_load_dwordx4 %3, %1, %2 offen nt lds\n\t"
               "buffer_load_dwordx4 %4, %1, %2 offen offset:1024 nt lds"
               ::"s"(m0), "s"(s), "s"(soff), "v"(v0), "v"(v1) : "memory");
  else
    asm volatile("s_mov_b32 m0, %0\n\ts_nop 0\n\t"
               "buffer_load_dwordx4 %3, %1, %2 offen lds\n\t"
               "buffer_load_dwordx4 %4, %1, %2 offen offset:1024 lds"
               ::"s"(m0), "s"(s), "s"(soff), "v"(v0), "v"(v1) : "memory");
}
struct WReg { u32x4 r[8]; };
DI void ldw_q(WReg& w, srd_t s, u32 soff, const u32 (&v)[8]) {
  asm volatile("buffer_load_dwordx4 %0, %8, %16, %17 offen\n\t"
               "buffer_load_dwordx4 %1, %9, %16, %17 offen\n\t"
               "buffer_load_dwordx4 %2, %10, %16, %17 offen\n\t"
               "buffer_load_dwordx4 %3, %11, %16, %17 offen\n\t"
               "buffer_load_dwordx4 %4, %12, %16, %17 offen\n\t"
               "buffer_load_dwordx4 %5, %13, %16, %17 offen\n\t"
               "buffer_load_dwordx4 %6, %14, %16, %17 offen\n\t"
               "buffer_load_dwordx4 %7, %15, %16, %17 offen"
               : "+v"(w.r[0]), "+v"(w.r[1]), "+v"(w.r[2]), "+v"(w.r[3]), "+v"(w.r[4]), "+v"(w.r[5]), "+v"(w.r[6]), "+v"(w.r[7])
               : "v"(v[0]), "v"(v[1]), "v"(v[2]), "v"(v[3]), "v"(v[4]), "v"(v[5]), "v"(v[6]), "v"(v[7]), "s"(s), "s"(soff) : "memory");
}
DI void ldw_o(WReg& w, srd_t s, u32 soff, u32 v0, u32 v1) {
  asm volatile("buffer_load_dwordx4 %0, %8, %10, %11 offen\n\t"
               "buffer_load_dwordx4 %1, %8, %10, %11 offen offset:1024\n\t"
               "buffer_load_dwordx4 %2, %8, %10, %11 offen offset:2048\n\t"
               "buffer_load_dwordx4 %3, %8, %10, %11 offen offset:3072\n\t"
               "buffer_load_dwordx4 %4, %9, %10, %11 offen\n\t"
               "buffer_load_dwordx4 %5, %9, %10, %11 offen offset:1024\n\t"
               "buffer_load_dwordx4 %6, %9, %10, %11 offen offset:2048\n\t"
               "buffer_load_dwordx4 %7, %9, %10, %11 offen offset:3072"
               : "+v"(w.r[0]), "+v"(w.r[1]), "+v"(w.r[2]), "+v"(w.r[3]), "+v"(w.r[4]), "+v"(w.r[5]), "+v"(w.r[6]), "+v"(w.r[7])
               : "v"(v0), "v"(v1), "s"(s), "s"(soff) : "memory");
}
template <int N> DI void wait_w(WReg& w) {
  asm volatile("s_waitcnt vmcnt(%8)"
               : "+v"(w.r[0]), "+v"(w.r[1]), "+v"(w.r[2]), "+v"(w.r[3]), "+v"(w.r[4]), "+v"(w.r[5]), "+v"(w.r[6]), "+v"(w.r[7])
               : "i"(N) : "memory");
}
DI void zero_w(WReg& w) {
#pragma unroll
  for (int j = 0; j < 8; j++) w.r[j] = u32x4{0, 0, 0, 0};
}
DI void proj_step(f32x4 (&acc)[8][3], const WReg& w, const u32x4 (&bf)[3]) {
#pragma unroll
  for (int c = 0; c < 8; c++) {
    const u32 sel = (c & 1) ? 0x07060302u : 0x05040100u;
    u32x4 a;
#pragma unroll
    for (int jj = 0; jj < 4; jj++) a[jj] = __builtin_amdgcn_perm(w.r[2 * jj + 1][c >> 1], w.r[2 * jj][c >> 1], sel);
#pragma unroll
    for (int t = 0; t < 3; t++) acc[c][t] = mfma(a, bf[t], acc[c][t]);
  }
}
DI float xmax(float v) {
  auto a = __builtin_amdgcn_permlane32_swap(__float_as_uint(v), __float_as_uint(v), false, false);
  v = fmaxf(__uint_as_float(a[0]), __uint_as_float(a[1]));
  auto b = __builtin_amdgcn_permlane16_swap(__float_as_uint(v), __float_as_uint(v), false, false);
  return fmaxf(__uint_as_float(b[0]), __uint_as_float(b[1]));
}
DI float xsum(float v) {
  auto a = __builtin_amdgcn_permlane32_swap(__float_as_uint(v), __float_as_uint(v), false, false);
  v = __uint_as_float(a[0]) + __uint_as_float(a[1]);
  auto b = __builtin_amdgcn_permlane16_swap(__float_as_uint(v), __float_as_uint(v), false, false);
  return __uint_as_float(b[0]) + __uint_as_float(b[1]);
}

DI u32 kswz(u32 r) { return (r & 10) | ((r & 1) << 2) | ((r >> 2) & 1); }
struct Attn {
  u32x4 q[4][3];
  f32x4 o[8][3];
  float m[3], l[3];
};
DI void tile_qk(f32x4 (&s)[2][3], const Attn& st, const u32 (&ka)[4], u32 base) {
#pragma unroll
  for (int u = 0; u < 2; u++)
#pragma unroll
    for (int t = 0; t < 3; t++) s[u][t] = f32x4{0, 0, 0, 0};
#pragma unroll
  for (int ks = 0; ks < 4; ks++)
#pragma unroll
    for (int u = 0; u < 2; u++) {
      u32x4 kf = lds_rd128(ka[ks] + base + u * 4096);
#pragma unroll
      for (int t = 0; t < 3; t++) s[u][t] = mfma(kf, st.q[ks][t], s[u][t]);
    }
}
DI void tile_sm(u32x4 (&p)[3], f32x4 (&s)[2][3], Attn& st) {
  float mx[3];
  bool need = false;
#pragma unroll
  for (int t = 0; t < 3; t++) {
    float a = fmaxf(fmaxf(s[0][t][0], s[0][t][1]), fmaxf(s[0][t][2], s[0][t][3]));
    float b = fmaxf(fmaxf(s[1][t][0], s[1][t][1]), fmaxf(s[1][t][2], s[1][t][3]));
    mx[t] = fmaxf(a, b) * CSC;
    need |= (mx[t] - st.m[t] > THR);
  }
  if (__any(need)) {
#pragma unroll
    for (int t = 0; t < 3; t++) {
      float mn = fmaxf(st.m[t], xmax(mx[t]));
      float al = __builtin_amdgcn_exp2f(st.m[t] - mn);
      st.m[t] = mn;
      st.l[t] *= al;
#pragma unroll
      for (int d = 0; d < 8; d++) st.o[d][t] *= al;
    }
  }
#pragma unroll
  for (int t = 0; t < 3; t++) {
    float e[8];
#pragma unroll
    for (int i = 0; i < 8; i++) e[i] = __builtin_amdgcn_exp2f(fmaf(s[i >> 2][t][i & 3], CSC, -st.m[t]));
    st.l[t] += ((e[0] + e[1]) + (e[2] + e[3])) + ((e[4] + e[5]) + (e[6] + e[7]));
    p[t] = u32x4{cvtpk(e[0], e[1]), cvtpk(e[2], e[3]), cvtpk(e[4], e[5]), cvtpk(e[6], e[7])};
  }
}
DI void tile_pv(Attn& st, const u32x4 (&p)[3], const u32 (&va)[8], u32 base) {
#pragma unroll
  for (int d = 0; d < 8; d++) {
    u32x2 lo = lds_rdtr(va[d] + base), hi = lds_rdtr(va[d] + base + 4096);
    u32x4 vf = {lo[0], lo[1], hi[0], hi[1]};
#pragma unroll
    for (int t = 0; t < 3; t++) st.o[d][t] = mfma(vf, p[t], st.o[d][t]);
  }
}

#define STAMP_AT(i) do { tst[i] = __builtin_amdgcn_s_memrealtime(); asm volatile("s_waitcnt lgkmcnt(0)" ::: "memory"); } while (0)

template <int BR>
DI void body(const u16* x, const u16* kc, const u16* vc, const u16* wq, const u16* wo, u16* part, u32* sync, u16* out, u64* stamps) {
  const u32 wg = blockIdx.x, j = wg & 7, b = wg >> 4;
  const u32 lane = threadIdx.x & 63, w = rfl(threadIdx.x >> 6);
  const u32 r = lane & 15, g = lane >> 4;
  u64 tst[11];
  STAMP_AT(0);
  u64 c0_ = __builtin_amdgcn_s_memtime();
  typedef __attribute__((address_space(1))) u32 gu32;
  gu32* const Rw = (gu32*)sync + (BR * 16 + b) * 512;
  gu32* const Fw = Rw + 256;
  u32 nonce = 0, oldf = 0;
  if (w == 3) asm volatile("global_load_dword %0, %1, off sc1" : "+v"(oldf) : "v"(Fw + j * 32 + (lane & 7)) : "memory");
  constexpr u32 KVB = 512 * 128 * 2;
  const u32 kvi = BR == 0 ? b * 8 + j : (b * 32 + j * 4 + w);
  const srd_t sK = mksrd((const char*)kc + (size_t)kvi * KVB, KVB), sV = mksrd((const char*)vc + (size_t)kvi * KVB, KVB);
  const srd_t sX = mksrd((const char*)x + (size_t)b * 49152, 49152);
  const srd_t sWq = mksrd(wq, 512 * 4096 * 2), sWo = mksrd(wo, 4096 * 512 * 2);
  constexpr int KVNT = 1;
  u32 kv_[4], vv_[2];
#pragma unroll
  for (int n = 0; n < 4; n++) kv_[n] = g * 256 + ((r ^ kswz(4 * n + g)) << 4);
#pragma unroll
  for (int n = 0; n < 2; n++) vv_[n] = g * 256 + ((r ^ ((4 * n + g) << 1)) << 4);
  auto dmaK = [&](u32 lds, u32 tile) { dma4<KVNT>(lds, sK, tile * 8192, kv_[0], kv_[1], kv_[2], kv_[3]); dma4<KVNT>(lds + 4096, sK, tile * 8192 + 4096, kv_[0], kv_[1], kv_[2], kv_[3]); };
  auto dmaV = [&](u32 lds, u32 tile) { dma4<KVNT>(lds, sV, tile * 8192, vv_[0], vv_[1], vv_[0], vv_[1]); dma4<KVNT>(lds + 4096, sV, tile * 8192 + 4096, vv_[0], vv_[1], vv_[0], vv_[1]); };
  const u32 ka0 = (w & 1) ? kv_[2] : kv_[0], ka1 = (w & 1) ? kv_[3] : kv_[1];
  auto dmaA = [&](u32 slot, u32 tile) {
    dma2<KVNT>(slot * 16384 + w * 2048, sK, tile * 8192 + w * 2048, ka0, ka1);
    dma2<KVNT>(slot * 16384 + 8192 + w * 2048, sV, tile * 8192 + w * 2048, vv_[0], vv_[1]);
  };
  if constexpr (BR == 0) {
#pragma unroll
    for (int t = 0; t < 7; t++) dmaA(t, t);
  }
  const u32 n0 = 8 * r;
  const u32 rot = b;
  f32x4 acc[8][3];
  Attn st;
  {
    u32 wv[8];
#pragma unroll
    for (int i = 0; i < 8; i++) wv[i] = (8 * g + i) * 8192 + (j * 512 + w * 128 + n0) * 2;
    WReg w0, w1, w2;
    zero_w(w0); zero_w(w1); zero_w(w2);
    ldw_q(w0, sWq, ((rot + 0) & 15) * 262144, wv);
  const u32 xlds = BR == 0 ? 114688 + w * 12288 : w * 40960 + 28672;
#pragma unroll
  for (int i4 = 0; i4 < 3; i4++) {
    u32 vo[4];
#pragma unroll
    for (int n = 0; n < 4; n++) { u32 row = 12 * w + 4 * i4 + n; vo[n] = ((lane & 48) | (r ^ (row & 15))) << 4; }
    dma4<0>(xlds + i4 * 4096, sX, (12 * w + 4 * i4) * 1024, vo[0], vo[1], vo[2], vo[3]);
  }
    ldw_q(w1, sWq, ((rot + 1) & 15) * 262144, wv);
    ldw_q(w2, sWq, ((rot + 2) & 15) * 262144, wv);
    u32 xb[3];
#pragma unroll
    for (int t = 0; t < 3; t++) {
      u32 q = 16 * t + r;
      u32 rowb = BR == 0 ? 114688 + q * 1024 : (q / 12) * 40960 + 28672 + (q % 12) * 1024;
      xb[t] = rowb + ((g ^ r) << 4);
    }
#pragma unroll
    for (int c = 0; c < 8; c++)
#pragma unroll
      for (int t = 0; t < 3; t++) acc[c][t] = f32x4{0, 0, 0, 0};
    asm volatile("s_waitcnt vmcnt(16)" : "+v"(oldf) :: "memory");
    barrier();
    STAMP_AT(1);
    auto xfr = [&](u32x4 (&bf)[3], u32 it) {
      u32 s6 = ((rot + it) & 15) << 6;
#pragma unroll
      for (int t = 0; t < 3; t++) bf[t] = lds_rd128(xb[t] ^ s6);
    };
    u32x4 bf[3];
    for (u32 it = 0; it < 12; it += 3) {
      xfr(bf, it); wait_w<16>(w0); proj_step(acc, w0, bf); ldw_q(w0, sWq, ((rot + it + 3) & 15) * 262144, wv);
      xfr(bf, it + 1); wait_w<16>(w1); proj_step(acc, w1, bf); ldw_q(w1, sWq, ((rot + it + 4) & 15) * 262144, wv);
      xfr(bf, it + 2); wait_w<16>(w2); proj_step(acc, w2, bf); ldw_q(w2, sWq, ((rot + it + 5) & 15) * 262144, wv);
      if (it == 0) STAMP_AT(2);
    }
    STAMP_AT(3);
    xfr(bf, 12); wait_w<16>(w0); proj_step(acc, w0, bf);
    if (w == 3) {
      u32 used = 0;
#pragma unroll
      for (int c = 0; c < 9; c++) used |= __any(oldf == (u32)c) ? (1u << c) : 0u;
      nonce = rfl(__builtin_ctz(~used));
      if (lane == 0) __hip_atomic_store(Rw + j * 32, nonce, __ATOMIC_RELAXED, __HIP_MEMORY_SCOPE_AGENT);
    }
    ldw_q(w0, sWq, ((rot + 15) & 15) * 262144, wv);
    if constexpr (BR == 1) {
      dmaK(w * 40960, 0);
      xfr(bf, 13); wait_w<24>(w1); proj_step(acc, w1, bf); dmaV(w * 40960 + 8192, 0);
      xfr(bf, 14); wait_w<24>(w2); proj_step(acc, w2, bf); dmaK(w * 40960 + 16384, 1);
      xfr(bf, 15); wait_w<24>(w0); proj_step(acc, w0, bf);
    } else {
      xfr(bf, 13); wait_w<16>(w1); proj_step(acc, w1, bf);
      xfr(bf, 14); wait_w<8>(w2); proj_step(acc, w2, bf);
      xfr(bf, 15); wait_w<0>(w0); proj_step(acc, w0, bf);
    }
    barrier();
    if constexpr (BR == 1) dmaV(w * 40960 + 24576, 1);
    else { dmaA(7, 7); dmaA(8, 8); dmaA(9, 9); }
#pragma unroll
    for (int ks = 0; ks < 4; ks++)
#pragma unroll
      for (int t = 0; t < 3; t++)
        st.q[ks][t] = u32x4{cvtpk(acc[0][t][ks], acc[1][t][ks]), cvtpk(acc[2][t][ks], acc[3][t][ks]), cvtpk(acc[4][t][ks], acc[5][t][ks]), cvtpk(acc[6][t][ks], acc[7][t][ks])};
  }
  STAMP_AT(4);
#pragma unroll
  for (int t = 0; t < 3; t++) {
    st.m[t] = -__builtin_inff(); st.l[t] = 0.f;
#pragma unroll
    for (int d = 0; d < 8; d++) st.o[d][t] = f32x4{0, 0, 0, 0};
  }
  u32 ka[4], va[8];
  {
    const u32 wb = BR == 0 ? 0 : w * 40960;
#pragma unroll
    for (int ks = 0; ks < 4; ks++) ka[ks] = wb + r * 256 + (((4 * g + ks) ^ kswz(r)) << 4);
    const u32 row = 4 * g + (r >> 2), p = r & 3;
#pragma unroll
    for (int d = 0; d < 8; d++) va[d] = wb + 8192 + row * 256 + ((d ^ (row & 7)) << 5) + p * 8;
  }
  f32x4 s[2][3];
  u32x4 p[3];
  if constexpr (BR == 1) {
    for (u32 t = 0; t < 14; t++) {
      const u32 sb = (t & 1) * 16384;
      waitv<24>();
      tile_qk(s, st, ka, sb);
      asm volatile("s_waitcnt lgkmcnt(0)" ::: "memory");
      dmaK(w * 40960 + sb, t + 2);
      tile_sm(p, s, st);
      waitv<24>();
      tile_pv(st, p, va, sb);
      asm volatile("s_waitcnt lgkmcnt(0)" ::: "memory");
      dmaV(w * 40960 + sb + 8192, t + 2);
    }
    waitv<24>(); tile_qk(s, st, ka, 0); tile_sm(p, s, st); waitv<16>(); tile_pv(st, p, va, 0);
    waitv<8>(); tile_qk(s, st, ka, 16384); tile_sm(p, s, st); waitv<0>(); tile_pv(st, p, va, 16384);
  } else {
    u32 slot = 0;
    for (u32 t = 0; t < 16; t++) {
      if (t == 7) waitv<24>();
      if (t == 10) waitv<0>();
      if (t > 0) barrier();
      if (t >= 1 && t <= 6) dmaA(t - 1, t + 9);
      const u32 sb = slot * 16384;
      tile_qk(s, st, ka, sb);
      tile_sm(p, s, st);
      tile_pv(st, p, va, sb);
      slot = slot == 9 ? 0 : slot + 1;
    }
    barrier();
  }
  STAMP_AT(5);
  {
    const u32 wv0 = 4 * g * 1024 + (w * 128 + n0) * 2, wv1 = wv0 + 16384;
    const u32 so = j * 524288;
    WReg w0, w1, w2;
    zero_w(w0); zero_w(w1); zero_w(w2);
    ldw_o(w0, sWo, so + ((rot + 0) & 15) * 32768, wv0, wv1);
    ldw_o(w1, sWo, so + ((rot + 1) & 15) * 32768, wv0, wv1);
    ldw_o(w2, sWo, so + ((rot + 2) & 15) * 32768, wv0, wv1);
#pragma unroll
    for (int t = 0; t < 3; t++) {
      const float inv = 1.0f / xsum(st.l[t]);
#pragma unroll
      for (int ks = 0; ks < 4; ks++) {
        f32x4 a = st.o[2 * ks][t] * inv, c = st.o[2 * ks + 1][t] * inv;
        lds_wr128(w * 40960 + (ks * 3 + t) * 1024 + lane * 16, u32x4{cvtpk(a[0], a[1]), cvtpk(a[2], a[3]), cvtpk(c[0], c[1]), cvtpk(c[2], c[3])});
      }
    }
#pragma unroll
    for (int c = 0; c < 8; c++)
#pragma unroll
      for (int t = 0; t < 3; t++) acc[c][t] = f32x4{0, 0, 0, 0};
    barrier();
    STAMP_AT(6);
    auto ofr = [&](u32x4 (&bf)[3], u32 it) {
      u32 ks = (rot + it) & 15;
      u32 a = (ks >> 2) * 40960 + (ks & 3) * 3072 + lane * 16;
#pragma unroll
      for (int t = 0; t < 3; t++) bf[t] = lds_rd128(a + t * 1024);
    };
    u32x4 bf[3];
    for (u32 it = 0; it < 12; it += 3) {
      ofr(bf, it); wait_w<16>(w0); proj_step(acc, w0, bf); ldw_o(w0, sWo, so + ((rot + it + 3) & 15) * 32768, wv0, wv1);
      ofr(bf, it + 1); wait_w<16>(w1); proj_step(acc, w1, bf); ldw_o(w1, sWo, so + ((rot + it + 4) & 15) * 32768, wv0, wv1);
      ofr(bf, it + 2); wait_w<16>(w2); proj_step(acc, w2, bf); ldw_o(w2, sWo, so + ((rot + it + 5) & 15) * 32768, wv0, wv1);
    }
    ofr(bf, 12); wait_w<16>(w0); proj_step(acc, w0, bf); ldw_o(w0, sWo, so + ((rot + 15) & 15) * 32768, wv0, wv1);
    ofr(bf, 13); wait_w<16>(w1); proj_step(acc, w1, bf);
    ofr(bf, 14); wait_w<8>(w2); proj_step(acc, w2, bf);
    ofr(bf, 15); wait_w<0>(w0); proj_step(acc, w0, bf);
  }
  STAMP_AT(7);
  {
    const srd_t sP = mksrd((const char*)part + (size_t)(BR * 16 + b) * 8 * 49152, 8 * 49152);
#pragma unroll
    for (int t = 0; t < 3; t++)
#pragma unroll
      for (int i = 0; i < 4; i++)
        __builtin_amdgcn_raw_buffer_store_b128(u32x4{cvtpk(acc[0][t][i], acc[1][t][i]), cvtpk(acc[2][t][i], acc[3][t][i]), cvtpk(acc[4][t][i], acc[5][t][i]), cvtpk(acc[6][t][i], acc[7][t][i])},
                                               sP, lane * 16 + (t * 4 + i) * 1024, j * 49152 + w * 12288, 16);
    u32 echoed = 0;
    if (w == 3) echoed = __hip_atomic_load(Rw + (lane & 7) * 32, __ATOMIC_RELAXED, __HIP_MEMORY_SCOPE_AGENT);
    waitv<0>();
    barrier();
    STAMP_AT(8);
    if (w == 3) {
      if (lane < 8) __hip_atomic_store(Fw + lane * 32 + j, echoed, __ATOMIC_RELAXED, __HIP_MEMORY_SCOPE_AGENT);
      for (u32 spin = 0; spin < (1u << 20); spin++) {
        const u32 f = __hip_atomic_load(Fw + j * 32 + (lane & 7), __ATOMIC_RELAXED, __HIP_MEMORY_SCOPE_AGENT);
        if (__all(f == nonce)) break;
        if ((spin & 15) == 15) {
          const u32 rq = __hip_atomic_load(Rw + (lane & 7) * 32, __ATOMIC_RELAXED, __HIP_MEMORY_SCOPE_AGENT);
          if (rq != echoed && lane < 8) __hip_atomic_store(Fw + lane * 32 + j, rq, __ATOMIC_RELAXED, __HIP_MEMORY_SCOPE_AGENT);
          echoed = rq;
        }
      }
    }
    barrier();
    STAMP_AT(9);
    if (w == 3) {
      const u32 rq = __hip_atomic_load(Rw + (lane & 7) * 32, __ATOMIC_RELAXED, __HIP_MEMORY_SCOPE_AGENT);
      if (rq != echoed && lane < 8) __hip_atomic_store(Fw + lane * 32 + j, rq, __ATOMIC_RELAXED, __HIP_MEMORY_SCOPE_AGENT);
    } else {
      const u32 blk = 6 * j + 2 * w;
      u32x4 v[2][8];
#pragma unroll
      for (int h = 0; h < 2; h++)
#pragma unroll
        for (int s = 0; s < 8; s++) v[h][s] = __builtin_amdgcn_raw_buffer_load_b128(sP, lane * 16 + h * 1024, s * 49152 + blk * 1024, 16);
      const u32 ww = blk / 12, t = (blk % 12) >> 2, i = blk & 3;
      u16* o = out + (size_t)(b * 48 + 16 * t + r) * 1024 + BR * 512 + ww * 128 + 8 * i + 32 * g;
#pragma unroll
      for (int h = 0; h < 2; h++) {
        float a[8] = {0, 0, 0, 0, 0, 0, 0, 0};
#pragma unroll
        for (int s = 0; s < 8; s++)
#pragma unroll
          for (int c = 0; c < 4; c++) { a[2 * c] += __uint_as_float(v[h][s][c] << 16); a[2 * c + 1] += __uint_as_float(v[h][s][c] & 0xffff0000u); }
        *(u32x4*)(o + 8 * h) = u32x4{cvtpk(a[0], a[1]), cvtpk(a[2], a[3]), cvtpk(a[4], a[5]), cvtpk(a[6], a[7])};
      }
    }
  }
  waitv<0>();
  STAMP_AT(10);
  if (true) {
    u64 end_ = tst[0] + 6000;
    while (__builtin_amdgcn_s_memrealtime() < end_) __builtin_amdgcn_s_sleep(4);
  }
}

__global__ __launch_bounds__(256, 2) void attn_main(const u16* xA, const u16* kA, const u16* vA, const u16* wqA, const u16* woA,
                                                    const u16* xB, const u16* kB, const u16* vB, const u16* wqB, const u16* woB, u16* part, u32* sync, u16* out
) {
  u64* stamps = nullptr;
  if ((blockIdx.x >> 3) & 1) body<1>(xB, kB, vB, wqB, woB, part, sync, out, stamps);
  else body<0>(xA, kA, vA, wqA, woA, part, sync, out, stamps);
}

extern "C" void kernel_launch(void* const* d_in, const int* in_sizes, int n_in, void* d_out, int out_size, void* d_ws, size_t ws_size, hipStream_t stream) {
  (void)hipFuncSetAttribute((const void*)attn_main, hipFuncAttributeMaxDynamicSharedMemorySize, LDS_BYTES);
  u16* part = (u16*)d_ws;
  attn_main<<<256, 256, LDS_BYTES, stream>>>((const u16*)d_in[0], (const u16*)d_in[1], (const u16*)d_in[2], (const u16*)d_in[3], (const u16*)d_in[4],
                                             (const u16*)d_in[5], (const u16*)d_in[6], (const u16*)d_in[7], (const u16*)d_in[8], (const u16*)d_in[9], part, (u32*)((char*)d_ws + (32 << 20)), (u16*)d_out
  );
}
```

```cpp
#include <hip/hip_runtime.h>
#include <hip/hip_bf16.h>
#include <stdint.h>

typedef unsigned short u16;
typedef unsigned int u32;
typedef unsigned long long u64;
typedef float f32x4 __attribute__((ext_vector_type(4)));
typedef float f32x2 __attribute__((ext_vector_type(2)));
typedef u32 u32x4 __attribute__((ext_vector_type(4)));
typedef u32 u32x2 __attribute__((ext_vector_type(2)));
typedef short s16x4 __attribute__((ext_vector_type(4)));
typedef __bf16 bf16x8 __attribute__((ext_vector_type(8)));
typedef __bf16 bf16x2 __attribute__((ext_vector_type(2)));
#define LAS __attribute__((address_space(3)))
#define DI __device__ __forceinline__
typedef __amdgpu_buffer_rsrc_t srd_t;

constexpr int LDS_BYTES = 160 * 1024;
constexpr float CSC = 0.08838834764831845f * 1.4426950408889634f;
constexpr float THR = 8.0f;

DI u32 rfl(u32 x) { return __builtin_amdgcn_readfirstlane(x); }
DI u32 cvtpk(float lo, float hi) { f32x2 v = {lo, hi}; bf16x2 b = __builtin_convertvector(v, bf16x2); return __builtin_bit_cast(u32, b); }
DI f32x4 mfma(u32x4 a, u32x4 b, f32x4 c) { return __builtin_amdgcn_mfma_f32_16x16x32_bf16(__builtin_bit_cast(bf16x8, a), __builtin_bit_cast(bf16x8, b), c, 0, 0, 0); }
DI u32x4 lds_rd128(u32 a) { return *(LAS u32x4*)(a); }
DI void lds_wr128(u32 a, u32x4 v) { *(LAS u32x4*)(a) = v; }
DI u32x2 lds_rdtr(u32 a) { return __builtin_bit_cast(u32x2, __builtin_amdgcn_ds_read_tr16_b64_v4i16((LAS s16x4*)(a))); }
DI srd_t mksrd(const void* p, u32 bytes) { return __builtin_amdgcn_make_buffer_rsrc((void*)p, (short)0, (int)bytes, 0x00020000); }
template <int N> DI void waitv() { asm volatile("s_waitcnt vmcnt(%0)" ::"i"(N) : "memory"); }
DI void barrier() { asm volatile("s_waitcnt lgkmcnt(0)\n\ts_barrier" ::: "memory"); }

template <int NT> DI void dma4(u32 m0, srd_t s, u32 soff, u32 v0, u32 v1, u32 v2, u32 v3) {
  if constexpr (NT)
    asm volatile("s_mov_b32 m0, %0\n\ts_nop 0\n\t"
               "buffer_load_dwordx4 %3, %1, %2 offen nt lds\n\t"
               "buffer_load_dwordx4 %4, %1, %2 offen offset:1024 nt lds\n\t"
               "buffer_load_dwordx4 %5, %1, %2 offen offset:2048 nt lds\n\t"
               "buffer_load_dwordx4 %6, %1, %2 offen offset:3072 nt lds"
               ::"s"(m0), "s"(s), "s"(soff), "v"(v0), "v"(v1), "v"(v2), "v"(v3) : "memory");
  else
    asm volatile("s_mov_b32 m0, %0\n\ts_nop 0\n\t"
               "buffer_load_dwordx4 %3, %1, %2 offen lds\n\t"
               "buffer_load_dwordx4 %4, %1, %2 offen offset:1024 lds\n\t"
               "buffer_load_dwordx4 %5, %1, %2 offen offset:2048 lds\n\t"
               "buffer_load_dwordx4 %6, %1, %2 offen offset:3072 lds"
               ::"s"(m0), "s"(s), "s"(soff), "v"(v0), "v"(v1), "v"(v2), "v"(v3) : "memory");
}
template <int NT> DI void dma2(u32 m0, srd_t s, u32 soff, u32 v0, u32 v1) {
  if constexpr (NT)
    asm volatile("s_mov_b32 m0, %0\n\ts_nop 0\n\t"
               "buffer_load_dwordx4 %3, %1, %2 offen nt lds\n\t"
               "buffer_load_dwordx4 %4, %1, %2 offen offset:1024 nt lds"
               ::"s"(m0), "s"(s), "s"(soff), "v"(v0), "v"(v1) : "memory");
  else
    asm volatile("s_mov_b32 m0, %0\n\ts_nop 0\n\t"
               "buffer_load_dwordx4 %3, %1, %2 offen lds\n\t"
               "buffer_load_dwordx4 %4, %1, %2 offen offset:1024 lds"
               ::"s"(m0), "s"(s), "s"(soff), "v"(v0), "v"(v1) : "memory");
}
struct WReg { u32x4 r[8]; };
DI void ldw_q(WReg& w, srd_t s, u32 soff, const u32 (&v)[8]) {
  asm volatile("buffer_load_dwordx4 %0, %8, %16, %17 offen\n\t"
               "buffer_load_dwordx4 %1, %9, %16, %17 offen\n\t"
               "buffer_load_dwordx4 %2, %10, %16, %17 offen\n\t"
               "buffer_load_dwordx4 %3, %11, %16, %17 offen\n\t"
               "buffer_load_dwordx4 %4, %12, %16, %17 offen\n\t"
               "buffer_load_dwordx4 %5, %13, %16, %17 offen\n\t"
               "buffer_load_dwordx4 %6, %14, %16, %17 offen\n\t"
               "buffer_load_dwordx4 %7, %15, %16, %17 offen"
               : "+v"(w.r[0]), "+v"(w.r[1]), "+v"(w.r[2]), "+v"(w.r[3]), "+v"(w.r[4]), "+v"(w.r[5]), "+v"(w.r[6]), "+v"(w.r[7])
               : "v"(v[0]), "v"(v[1]), "v"(v[2]), "v"(v[3]), "v"(v[4]), "v"(v[5]), "v"(v[6]), "v"(v[7]), "s"(s), "s"(soff) : "memory");
}
DI void ldw_o(WReg& w, srd_t s, u32 soff, u32 v0, u32 v1) {
  asm volatile("buffer_load_dwordx4 %0, %8, %10, %11 offen\n\t"
               "buffer_load_dwordx4 %1, %8, %10, %11 offen offset:1024\n\t"
               "buffer_load_dwordx4 %2, %8, %10, %11 offen offset:2048\n\t"
               "buffer_load_dwordx4 %3, %8, %10, %11 offen offset:3072\n\t"
               "buffer_load_dwordx4 %4, %9, %10, %11 offen\n\t"
               "buffer_load_dwordx4 %5, %9, %10, %11 offen offset:1024\n\t"
               "buffer_load_dwordx4 %6, %9, %10, %11 offen offset:2048\n\t"
               "buffer_load_dwordx4 %7, %9, %10, %11 offen offset:3072"
               : "+v"(w.r[0]), "+v"(w.r[1]), "+v"(w.r[2]), "+v"(w.r[3]), "+v"(w.r[4]), "+v"(w.r[5]), "+v"(w.r[6]), "+v"(w.r[7])
               : "v"(v0), "v"(v1), "s"(s), "s"(soff) : "memory");
}
template <int N> DI void wait_w(WReg& w) {
  asm volatile("s_waitcnt vmcnt(%8)"
               : "+v"(w.r[0]), "+v"(w.r[1]), "+v"(w.r[2]), "+v"(w.r[3]), "+v"(w.r[4]), "+v"(w.r[5]), "+v"(w.r[6]), "+v"(w.r[7])
               : "i"(N) : "memory");
}
DI void zero_w(WReg& w) {
#pragma unroll
  for (int j = 0; j < 8; j++) w.r[j] = u32x4{0, 0, 0, 0};
}
DI void proj_step(f32x4 (&acc)[8][3], const WReg& w, const u32x4 (&bf)[3]) {
#pragma unroll
  for (int c = 0; c < 8; c++) {
    const u32 sel = (c & 1) ? 0x07060302u : 0x05040100u;
    u32x4 a;
#pragma unroll
    for (int jj = 0; jj < 4; jj++) a[jj] = __builtin_amdgcn_perm(w.r[2 * jj + 1][c >> 1], w.r[2 * jj][c >> 1], sel);
#pragma unroll
    for (int t = 0; t < 3; t++) acc[c][t] = mfma(a, bf[t], acc[c][t]);
  }
}
DI float xmax(float v) {
  auto a = __builtin_amdgcn_permlane32_swap(__float_as_uint(v), __float_as_uint(v), false, false);
  v = fmaxf(__uint_as_float(a[0]), __uint_as_float(a[1]));
  auto b = __builtin_amdgcn_permlane16_swap(__float_as_uint(v), __float_as_uint(v), false, false);
  return fmaxf(__uint_as_float(b[0]), __uint_as_float(b[1]));
}
DI float xsum(float v) {
  auto a = __builtin_amdgcn_permlane32_swap(__float_as_uint(v), __float_as_uint(v), false, false);
  v = __uint_as_float(a[0]) + __uint_as_float(a[1]);
  auto b = __builtin_amdgcn_permlane16_swap(__float_as_uint(v), __float_as_uint(v), false, false);
  return __uint_as_float(b[0]) + __uint_as_float(b[1]);
}

DI u32 kswz(u32 r) { return (r & 10) | ((r & 1) << 2) | ((r >> 2) & 1); }
struct Attn {
  u32x4 q[4][3];
  f32x4 o[8][3];
  float m[3], l[3];
};
DI void tile_qk(f32x4 (&s)[2][3], const Attn& st, const u32 (&ka)[4], u32 base) {
#pragma unroll
  for (int u = 0; u < 2; u++)
#pragma unroll
    for (int t = 0; t < 3; t++) s[u][t] = f32x4{0, 0, 0, 0};
#pragma unroll
  for (int ks = 0; ks < 4; ks++)
#pragma unroll
    for (int u = 0; u < 2; u++) {
      u32x4 kf = lds_rd128(ka[ks] + base + u * 4096);
#pragma unroll
      for (int t = 0; t < 3; t++) s[u][t] = mfma(kf, st.q[ks][t], s[u][t]);
    }
}
DI void tile_sm(u32x4 (&p)[3], f32x4 (&s)[2][3], Attn& st) {
  float mx[3];
  bool need = false;
#pragma unroll
  for (int t = 0; t < 3; t++) {
    float a = fmaxf(fmaxf(s[0][t][0], s[0][t][1]), fmaxf(s[0][t][2], s[0][t][3]));
    float b = fmaxf(fmaxf(s[1][t][0], s[1][t][1]), fmaxf(s[1][t][2], s[1][t][3]));
    mx[t] = fmaxf(a, b) * CSC;
    need |= (mx[t] - st.m[t] > THR);
  }
  if (__any(need)) {
#pragma unroll
    for (int t = 0; t < 3; t++) {
      float mn = fmaxf(st.m[t], xmax(mx[t]));
      float al = __builtin_amdgcn_exp2f(st.m[t] - mn);
      st.m[t] = mn;
      st.l[t] *= al;
#pragma unroll
      for (int d = 0; d < 8; d++) st.o[d][t] *= al;
    }
  }
#pragma unroll
  for (int t = 0; t < 3; t++) {
    float e[8];
#pragma unroll
    for (int i = 0; i < 8; i++) e[i] = __builtin_amdgcn_exp2f(fmaf(s[i >> 2][t][i & 3], CSC, -st.m[t]));
    st.l[t] += ((e[0] + e[1]) + (e[2] + e[3])) + ((e[4] + e[5]) + (e[6] + e[7]));
    p[t] = u32x4{cvtpk(e[0], e[1]), cvtpk(e[2], e[3]), cvtpk(e[4], e[5]), cvtpk(e[6], e[7])};
  }
}
DI void tile_pv(Attn& st, const u32x4 (&p)[3], const u32 (&va)[8], u32 base) {
#pragma unroll
  for (int d = 0; d < 8; d++) {
    u32x2 lo = lds_rdtr(va[d] + base), hi = lds_rdtr(va[d] + base + 4096);
    u32x4 vf = {lo[0], lo[1], hi[0], hi[1]};
#pragma unroll
    for (int t = 0; t < 3; t++) st.o[d][t] = mfma(vf, p[t], st.o[d][t]);
  }
}

#define STAMP_AT(i)

template <int BR>
DI void body(const u16* x, const u16* kc, const u16* vc, const u16* wq, const u16* wo, u16* part, u32* sync, u16* out, u64* stamps) {
  const u32 wg = blockIdx.x, j = wg & 7, b = wg >> 4;
  const u32 lane = threadIdx.x & 63, w = rfl(threadIdx.x >> 6);
  const u32 r = lane & 15, g = lane >> 4;
  STAMP_AT(0);
  typedef __attribute__((address_space(1))) u32 gu32;
  gu32* const Rw = (gu32*)sync + (BR * 16 + b) * 512;
  gu32* const Fw = Rw + 256;
  u32 nonce = 0, oldf = 0;
  if (w == 3) asm volatile("global_load_dword %0, %1, off sc1" : "+v"(oldf) : "v"(Fw + j * 32 + (lane & 7)) : "memory");
  constexpr u32 KVB = 512 * 128 * 2;
  const u32 kvi = BR == 0 ? b * 8 + j : (b * 32 + j * 4 + w);
  const srd_t sK = mksrd((const char*)kc + (size_t)kvi * KVB, KVB), sV = mksrd((const char*)vc + (size_t)kvi * KVB, KVB);
  const srd_t sX = mksrd((const char*)x + (size_t)b * 49152, 49152);
  const srd_t sWq = mksrd(wq, 512 * 4096 * 2), sWo = mksrd(wo, 4096 * 512 * 2);
  constexpr int KVNT = 1;
  u32 kv_[4], vv_[2];
#pragma unroll
  for (int n = 0; n < 4; n++) kv_[n] = g * 256 + ((r ^ kswz(4 * n + g)) << 4);
#pragma unroll
  for (int n = 0; n < 2; n++) vv_[n] = g * 256 + ((r ^ ((4 * n + g) << 1)) << 4);
  auto dmaK = [&](u32 lds, u32 tile) { dma4<KVNT>(lds, sK, tile * 8192, kv_[0], kv_[1], kv_[2], kv_[3]); dma4<KVNT>(lds + 4096, sK, tile * 8192 + 4096, kv_[0], kv_[1], kv_[2], kv_[3]); };
  auto dmaV = [&](u32 lds, u32 tile) { dma4<KVNT>(lds, sV, tile * 8192, vv_[0], vv_[1], vv_[0], vv_[1]); dma4<KVNT>(lds + 4096, sV, tile * 8192 + 4096, vv_[0], vv_[1], vv_[0], vv_[1]); };
  const u32 ka0 = (w & 1) ? kv_[2] : kv_[0], ka1 = (w & 1) ? kv_[3] : kv_[1];
  auto dmaA = [&](u32 slot, u32 tile) {
    dma2<KVNT>(slot * 16384 + w * 2048, sK, tile * 8192 + w * 2048, ka0, ka1);
    dma2<KVNT>(slot * 16384 + 8192 + w * 2048, sV, tile * 8192 + w * 2048, vv_[0], vv_[1]);
  };
  if constexpr (BR == 0) {
#pragma unroll
    for (int t = 0; t < 7; t++) dmaA(t, t);
  }
  const u32 n0 = 8 * r;
  const u32 rot = b;
  f32x4 acc[8][3];
  Attn st;
  {
    u32 wv[8];
#pragma unroll
    for (int i = 0; i < 8; i++) wv[i] = (8 * g + i) * 8192 + (j * 512 + w * 128 + n0) * 2;
    WReg w0, w1, w2;
    zero_w(w0); zero_w(w1); zero_w(w2);
    ldw_q(w0, sWq, ((rot + 0) & 15) * 262144, wv);
  const u32 xlds = BR == 0 ? 114688 + w * 12288 : w * 40960 + 28672;
#pragma unroll
  for (int i4 = 0; i4 < 3; i4++) {
    u32 vo[4];
#pragma unroll
    for (int n = 0; n < 4; n++) { u32 row = 12 * w + 4 * i4 + n; vo[n] = ((lane & 48) | (r ^ (row & 15))) << 4; }
    dma4<0>(xlds + i4 * 4096, sX, (12 * w + 4 * i4) * 1024, vo[0], vo[1], vo[2], vo[3]);
  }
    ldw_q(w1, sWq, ((rot + 1) & 15) * 262144, wv);
    ldw_q(w2, sWq, ((rot + 2) & 15) * 262144, wv);
    u32 xb[3];
#pragma unroll
    for (int t = 0; t < 3; t++) {
      u32 q = 16 * t + r;
      u32 rowb = BR == 0 ? 114688 + q * 1024 : (q / 12) * 40960 + 28672 + (q % 12) * 1024;
      xb[t] = rowb + ((g ^ r) << 4);
    }
#pragma unroll
    for (int c = 0; c < 8; c++)
#pragma unroll
      for (int t = 0; t < 3; t++) acc[c][t] = f32x4{0, 0, 0, 0};
    asm volatile("s_waitcnt vmcnt(16)" : "+v"(oldf) :: "memory");
    barrier();
    STAMP_AT(1);
    auto xfr = [&](u32x4 (&bf)[3], u32 it) {
      u32 s6 = ((rot + it) & 15) << 6;
#pragma unroll
      for (int t = 0; t < 3; t++) bf[t] = lds_rd128(xb[t] ^ s6);
    };
    u32x4 bf[3];
    for (u32 it = 0; it < 12; it += 3) {
      xfr(bf, it); wait_w<16>(w0); proj_step(acc, w0, bf); ldw_q(w0, sWq, ((rot + it + 3) & 15) * 262144, wv);
      xfr(bf, it + 1); wait_w<16>(w1); proj_step(acc, w1, bf); ldw_q(w1, sWq, ((rot + it + 4) & 15) * 262144, wv);
      xfr(bf, it + 2); wait_w<16>(w2); proj_step(acc, w2, bf); ldw_q(w2, sWq, ((rot + it + 5) & 15) * 262144, wv);
      if (it == 0) STAMP_AT(2);
    }
    STAMP_AT(3);
    xfr(bf, 12); wait_w<16>(w0); proj_step(acc, w0, bf);
    if (w == 3) {
      u32 used = 0;
#pragma unroll
      for (int c = 0; c < 9; c++) used |= __any(oldf == (u32)c) ? (1u << c) : 0u;
      nonce = rfl(__builtin_ctz(~used));
      if (lane == 0) __hip_atomic_store(Rw + j * 32, nonce, __ATOMIC_RELAXED, __HIP_MEMORY_SCOPE_AGENT);
    }
    ldw_q(w0, sWq, ((rot + 15) & 15) * 262144, wv);
    if constexpr (BR == 1) {
      dmaK(w * 40960, 0);
      xfr(bf, 13); wait_w<24>(w1); proj_step(acc, w1, bf); dmaV(w * 40960 + 8192, 0);
      xfr(bf, 14); wait_w<24>(w2); proj_step(acc, w2, bf); dmaK(w * 40960 + 16384, 1);
      xfr(bf, 15); wait_w<24>(w0); proj_step(acc, w0, bf);
    } else {
      xfr(bf, 13); wait_w<16>(w1); proj_step(acc, w1, bf);
      xfr(bf, 14); wait_w<8>(w2); proj_step(acc, w2, bf);
      xfr(bf, 15); wait_w<0>(w0); proj_step(acc, w0, bf);
    }
    barrier();
    if constexpr (BR == 1) dmaV(w * 40960 + 24576, 1);
    else { dmaA(7, 7); dmaA(8, 8); dmaA(9, 9); }
#pragma unroll
    for (int ks = 0; ks < 4; ks++)
#pragma unroll
      for (int t = 0; t < 3; t++)
        st.q[ks][t] = u32x4{cvtpk(acc[0][t][ks], acc[1][t][ks]), cvtpk(acc[2][t][ks], acc[3][t][ks]), cvtpk(acc[4][t][ks], acc[5][t][ks]), cvtpk(acc[6][t][ks], acc[7][t][ks])};
  }
  STAMP_AT(4);
#pragma unroll
  for (int t = 0; t < 3; t++) {
    st.m[t] = -__builtin_inff(); st.l[t] = 0.f;
#pragma unroll
    for (int d = 0; d < 8; d++) st.o[d][t] = f32x4{0, 0, 0, 0};
  }
  u32 ka[4], va[8];
  {
    const u32 wb = BR == 0 ? 0 : w * 40960;
#pragma unroll
    for (int ks = 0; ks < 4; ks++) ka[ks] = wb + r * 256 + (((4 * g + ks) ^ kswz(r)) << 4);
    const u32 row = 4 * g + (r >> 2), p = r & 3;
#pragma unroll
    for (int d = 0; d < 8; d++) va[d] = wb + 8192 + row * 256 + ((d ^ (row & 7)) << 5) + p * 8;
  }
  f32x4 s[2][3];
  u32x4 p[3];
  if constexpr (BR == 1) {
    for (u32 t = 0; t < 14; t++) {
      const u32 sb = (t & 1) * 16384;
      waitv<24>();
      tile_qk(s, st, ka, sb);
      asm volatile("s_waitcnt lgkmcnt(0)" ::: "memory");
      dmaK(w * 40960 + sb, t + 2);
      tile_sm(p, s, st);
      waitv<24>();
      tile_pv(st, p, va, sb);
      asm volatile("s_waitcnt lgkmcnt(0)" ::: "memory");
      dmaV(w * 40960 + sb + 8192, t + 2);
    }
    waitv<24>(); tile_qk(s, st, ka, 0); tile_sm(p, s, st); waitv<16>(); tile_pv(st, p, va, 0);
    waitv<8>(); tile_qk(s, st, ka, 16384); tile_sm(p, s, st); waitv<0>(); tile_pv(st, p, va, 16384);
  } else {
    u32 slot = 0;
    for (u32 t = 0; t < 16; t++) {
      if (t == 7) waitv<24>();
      if (t == 10) waitv<0>();
      if (t > 0) barrier();
      if (t >= 1 && t <= 6) dmaA(t - 1, t + 9);
      const u32 sb = slot * 16384;
      tile_qk(s, st, ka, sb);
      tile_sm(p, s, st);
      tile_pv(st, p, va, sb);
      slot = slot == 9 ? 0 : slot + 1;
    }
    barrier();
  }
  STAMP_AT(5);
  {
    const u32 wv0 = 4 * g * 1024 + (w * 128 + n0) * 2, wv1 = wv0 + 16384;
    const u32 so = j * 524288;
    WReg w0, w1, w2;
    zero_w(w0); zero_w(w1); zero_w(w2);
    ldw_o(w0, sWo, so + ((rot + 0) & 15) * 32768, wv0, wv1);
    ldw_o(w1, sWo, so + ((rot + 1) & 15) * 32768, wv0, wv1);
    ldw_o(w2, sWo, so + ((rot + 2) & 15) * 32768, wv0, wv1);
#pragma unroll
    for (int t = 0; t < 3; t++) {
      const float inv = 1.0f / xsum(st.l[t]);
#pragma unroll
      for (int ks = 0; ks < 4; ks++) {
        f32x4 a = st.o[2 * ks][t] * inv, c = st.o[2 * ks + 1][t] * inv;
        lds_wr128(w * 40960 + (ks * 3 + t) * 1024 + lane * 16, u32x4{cvtpk(a[0], a[1]), cvtpk(a[2], a[3]), cvtpk(c[0], c[1]), cvtpk(c[2], c[3])});
      }
    }
#pragma unroll
    for (int c = 0; c < 8; c++)
#pragma unroll
      for (int t = 0; t < 3; t++) acc[c][t] = f32x4{0, 0, 0, 0};
    barrier();
    STAMP_AT(6);
    auto ofr = [&](u32x4 (&bf)[3], u32 it) {
      u32 ks = (rot + it) & 15;
      u32 a = (ks >> 2) * 40960 + (ks & 3) * 3072 + lane * 16;
#pragma unroll
      for (int t = 0; t < 3; t++) bf[t] = lds_rd128(a + t * 1024);
    };
    u32x4 bf[3];
    for (u32 it = 0; it < 12; it += 3) {
      ofr(bf, it); wait_w<16>(w0); proj_step(acc, w0, bf); ldw_o(w0, sWo, so + ((rot + it + 3) & 15) * 32768, wv0, wv1);
      ofr(bf, it + 1); wait_w<16>(w1); proj_step(acc, w1, bf); ldw_o(w1, sWo, so + ((rot + it + 4) & 15) * 32768, wv0, wv1);
      ofr(bf, it + 2); wait_w<16>(w2); proj_step(acc, w2, bf); ldw_o(w2, sWo, so + ((rot + it + 5) & 15) * 32768, wv0, wv1);
    }
    ofr(bf, 12); wait_w<16>(w0); proj_step(acc, w0, bf); ldw_o(w0, sWo, so + ((rot + 15) & 15) * 32768, wv0, wv1);
    ofr(bf, 13); wait_w<16>(w1); proj_step(acc, w1, bf);
    ofr(bf, 14); wait_w<8>(w2); proj_step(acc, w2, bf);
    ofr(bf, 15); wait_w<0>(w0); proj_step(acc, w0, bf);
  }
  STAMP_AT(7);
  {
    const srd_t sP = mksrd((const char*)part + (size_t)(BR * 16 + b) * 8 * 49152, 8 * 49152);
#pragma unroll
    for (int t = 0; t < 3; t++)
#pragma unroll
      for (int i = 0; i < 4; i++)
        __builtin_amdgcn_raw_buffer_store_b128(u32x4{cvtpk(acc[0][t][i], acc[1][t][i]), cvtpk(acc[2][t][i], acc[3][t][i]), cvtpk(acc[4][t][i], acc[5][t][i]), cvtpk(acc[6][t][i], acc[7][t][i])},
                                               sP, lane * 16 + (t * 4 + i) * 1024, j * 49152 + w * 12288, 16);
    u32 echoed = 0;
    if (w == 3) echoed = __hip_atomic_load(Rw + (lane & 7) * 32, __ATOMIC_RELAXED, __HIP_MEMORY_SCOPE_AGENT);
    waitv<0>();
    barrier();
    STAMP_AT(8);
    if (w == 3) {
      if (lane < 8) __hip_atomic_store(Fw + lane * 32 + j, echoed, __ATOMIC_RELAXED, __HIP_MEMORY_SCOPE_AGENT);
      for (u32 spin = 0; spin < (1u << 20); spin++) {
        const u32 f = __hip_atomic_load(Fw + j * 32 + (lane & 7), __ATOMIC_RELAXED, __HIP_MEMORY_SCOPE_AGENT);
        if (__all(f == nonce)) break;
        if ((spin & 15) == 15) {
          const u32 rq = __hip_atomic_load(Rw + (lane & 7) * 32, __ATOMIC_RELAXED, __HIP_MEMORY_SCOPE_AGENT);
          if (rq != echoed && lane < 8) __hip_atomic_store(Fw + lane * 32 + j, rq, __ATOMIC_RELAXED, __HIP_MEMORY_SCOPE_AGENT);
          echoed = rq;
        }
      }
    }
    barrier();
    STAMP_AT(9);
    if (w == 3) {
      const u32 rq = __hip_atomic_load(Rw + (lane & 7) * 32, __ATOMIC_RELAXED, __HIP_MEMORY_SCOPE_AGENT);
      if (rq != echoed && lane < 8) __hip_atomic_store(Fw + lane * 32 + j, rq, __ATOMIC_RELAXED, __HIP_MEMORY_SCOPE_AGENT);
    } else {
      const u32 blk = 6 * j + 2 * w;
      u32x4 v[2][8];
#pragma unroll
      for (int h = 0; h < 2; h++)
#pragma unroll
        for (int s = 0; s < 8; s++) v[h][s] = __builtin_amdgcn_raw_buffer_load_b128(sP, lane * 16 + h * 1024, s * 49152 + blk * 1024, 16);
      const u32 ww = blk / 12, t = (blk % 12) >> 2, i = blk & 3;
      u16* o = out + (size_t)(b * 48 + 16 * t + r) * 1024 + BR * 512 + ww * 128 + 8 * i + 32 * g;
#pragma unroll
      for (int h = 0; h < 2; h++) {
        float a[8] = {0, 0, 0, 0, 0, 0, 0, 0};
#pragma unroll
        for (int s = 0; s < 8; s++)
#pragma unroll
          for (int c = 0; c < 4; c++) { a[2 * c] += __uint_as_float(v[h][s][c] << 16); a[2 * c + 1] += __uint_as_float(v[h][s][c] & 0xffff0000u); }
        *(u32x4*)(o + 8 * h) = u32x4{cvtpk(a[0], a[1]), cvtpk(a[2], a[3]), cvtpk(a[4], a[5]), cvtpk(a[6], a[7])};
      }
    }
  }
  STAMP_AT(10);
}

__global__ __launch_bounds__(256, 2) void attn_main(const u16* xA, const u16* kA, const u16* vA, const u16* wqA, const u16* woA,
                                                    const u16* xB, const u16* kB, const u16* vB, const u16* wqB, const u16* woB, u16* part, u32* sync, u16* out
) {
  u64* stamps = nullptr;
  if ((blockIdx.x >> 3) & 1) body<1>(xB, kB, vB, wqB, woB, part, sync, out, stamps);
  else body<0>(xA, kA, vA, wqA, woA, part, sync, out, stamps);
}

__global__ void probe_k(u32* p) { if (p == nullptr) __builtin_trap(); }
extern "C" void kernel_launch(void* const* d_in, const int* in_sizes, int n_in, void* d_out, int out_size, void* d_ws, size_t ws_size, hipStream_t stream) {
  (void)hipFuncSetAttribute((const void*)attn_main, hipFuncAttributeMaxDynamicSharedMemorySize, LDS_BYTES);
  u16* part = (u16*)d_ws;
  attn_main<<<256, 256, LDS_BYTES, stream>>>((const u16*)d_in[0], (const u16*)d_in[1], (const u16*)d_in[2], (const u16*)d_in[3], (const u16*)d_in[4],
                                             (const u16*)d_in[5], (const u16*)d_in[6], (const u16*)d_in[7], (const u16*)d_in[8], (const u16*)d_in[9], part, (u32*)((char*)d_ws + (32 << 20)), (u16*)d_out
  );
  (void)hipFuncSetAttribute((const void*)probe_k, hipFuncAttributeMaxDynamicSharedMemorySize, LDS_BYTES);
  probe_k<<<256, 256, LDS_BYTES, stream>>>((u32*)d_ws);
}
```

```cpp
#include <hip/hip_runtime.h>

__global__ __launch_bounds__(256, 1) void concat_heads_fused(const void* xA, const void* kA, const void* vA, const void* wqA, const void* woA,
                                                             const void* xB, const void* kB, const void* vB, const void* wqB, const void* woB,
                                                             void* ws, void* out) {
  asm volatile(
"s_mov_b64 s[4:5],%0\n"
"s_mov_b32 s6,%1\n"
"v_mov_b32 v242,%2\n"
"s_mov_b32 s93,s6\n"
"s_bfe_u32 s60,s6,0x10003\n"
"s_mul_i32 s60,s60,40\n"
"s_add_u32 s61,s60,32\n"
"s_load_dwordx8 s[8:15],s[4:5],s60\n"
"s_load_dwordx2 s[16:17],s[4:5],s61\n"
"s_load_dwordx4 s[20:23],s[4:5],0x50\n"
"v_lshrrev_b32 v246,6,v242\n"
"s_nop 0\n"
"v_readfirstlane_b32 s7,v246\n"
"v_and_b32 v243,15,v242\n"
"v_bfe_u32 v244,v242,4,2\n"
"v_and_b32 v242,63,v242\n"
"v_lshlrev_b32 v242,4,v242\n"
"s_and_b32 s56,s6,7\n"
"s_lshr_b32 s57,s6,4\n"
"s_mov_b32 s64,0x41000000\n"
"s_mov_b32 s18,0x5040100\n"
"s_mov_b32 s19,0x7060302\n"
"v_mov_b32 v246,0x3f803f80\n"
"v_accvgpr_write_b32 a48,v246\n"
"v_accvgpr_write_b32 a49,v246\n"
"v_accvgpr_write_b32 a50,v246\n"
"v_accvgpr_write_b32 a51,v246\n"
"v_lshlrev_b32 v246,1,v244\n"
"v_xor_b32 v246,v246,v243\n"
"v_xor_b32 v246,0,v246\n"
"v_lshlrev_b32 v246,4,v246\n"
"v_lshl_or_b32 v236,v244,8,v246\n"
"v_lshlrev_b32 v246,1,v244\n"
"v_xor_b32 v246,v246,v243\n"
"v_xor_b32 v246,8,v246\n"
"v_lshlrev_b32 v246,4,v246\n"
"v_lshl_or_b32 v237,v244,8,v246\n"
"s_getpc_b64 s[48:49]\n"
"Lcode0:\n"
"s_and_b32 s49,s49,0xffff\n"
"s_mov_b32 s50,Lcode1-Lcode0\n"
"s_mov_b32 s51,0x20000\n"
"s_lshr_b32 s60,s6,3\n"
"s_lshl_b32 s60,s60,2\n"
"s_add_u32 s60,s60,s7\n"
"s_and_b32 s60,s60,15\n"
"s_lshl_b32 s60,s60,13\n"
"v_lshlrev_b32 v246,3,v242\n"
"buffer_load_dword v0,v246,s[48:51],s60 offen\n"
"s_bitcmp1_b32 s6,3\n"
"s_cbranch_scc1 LpathB\n"
"s_load_dwordx2 s[80:81],s[4:5],0x28\n"
"s_mul_i32 s60,s57,0xc000\n"
"s_mul_i32 s61,s7,0x3000\n"
"s_add_u32 s60,s60,s61\n"
"s_waitcnt lgkmcnt(0)\n"
"s_add_u32 s32,s80,s60\n"
"s_addc_u32 s33,s81,0\n"
"s_and_b32 s33,s33,0xffff\n"
"s_mov_b32 s34,0x3000\n"
"s_mov_b32 s35,0x20000\n"
"s_mov_b32 s67,0\n"
"buffer_load_dwordx4 a[252:255],v242,s[32:35],s67 offen\n"
"buffer_load_dwordx4 a[252:255],v242,s[32:35],s67 offen offset:1024\n"
"buffer_load_dwordx4 a[252:255],v242,s[32:35],s67 offen offset:2048\n"
"buffer_load_dwordx4 a[252:255],v242,s[32:35],s67 offen offset:3072\n"
"s_mov_b32 s67,0x1000\n"
"buffer_load_dwordx4 a[252:255],v242,s[32:35],s67 offen\n"
"buffer_load_dwordx4 a[252:255],v242,s[32:35],s67 offen offset:1024\n"
"buffer_load_dwordx4 a[252:255],v242,s[32:35],s67 offen offset:2048\n"
"buffer_load_dwordx4 a[252:255],v242,s[32:35],s67 offen offset:3072\n"
"s_mov_b32 s67,0x2000\n"
"buffer_load_dwordx4 a[252:255],v242,s[32:35],s67 offen\n"
"buffer_load_dwordx4 a[252:255],v242,s[32:35],s67 offen offset:1024\n"
"buffer_load_dwordx4 a[252:255],v242,s[32:35],s67 offen offset:2048\n"
"buffer_load_dwordx4 a[252:255],v242,s[32:35],s67 offen offset:3072\n"
"s_sleep 16\n"
"s_sleep 16\n"
"s_lshl_b32 s60,s57,3\n"
"s_add_u32 s60,s60,s56\n"
"s_lshl_b32 s61,s60,17\n"
"s_lshl_b32 s66,s7,11\n"
"s_lshl_b32 s63,s7,15\n"
"s_add_u32 s61,s61,s63\n"
"s_sub_u32 s62,0x20000,s63\n"
"s_add_u32 s24,s10,s61\n"
"s_addc_u32 s25,s11,0\n"
"s_and_b32 s25,s25,0xffff\n"
"s_mov_b32 s26,s62\n"
"s_mov_b32 s27,0x20000\n"
"s_add_u32 s28,s12,s61\n"
"s_addc_u32 s29,s13,0\n"
"s_and_b32 s29,s29,0xffff\n"
"s_mov_b32 s30,s62\n"
"s_mov_b32 s31,0x20000\n"
"s_lshl_b32 s1,s57,10\n"
"s_and_b32 s60,s7,1\n"
"s_lshl_b32 s60,s60,3\n"
"s_add_u32 s60,s60,0\n"
"v_or_b32 v246,s60,v244\n"
"v_and_b32 v247,10,v246\n"
"v_and_b32 v248,1,v246\n"
"v_lshl_or_b32 v247,v248,2,v247\n"
"v_bfe_u32 v248,v246,2,1\n"
"v_or_b32 v247,v247,v248\n"
"v_xor_b32 v247,v247,v243\n"
"v_lshlrev_b32 v247,4,v247\n"
"v_lshl_or_b32 v232,v244,8,v247\n"
"s_and_b32 s60,s7,1\n"
"s_lshl_b32 s60,s60,3\n"
"s_add_u32 s60,s60,4\n"
"v_or_b32 v246,s60,v244\n"
"v_and_b32 v247,10,v246\n"
"v_and_b32 v248,1,v246\n"
"v_lshl_or_b32 v247,v248,2,v247\n"
"v_bfe_u32 v248,v246,2,1\n"
"v_or_b32 v247,v247,v248\n"
"v_xor_b32 v247,v247,v243\n"
"v_lshlrev_b32 v247,4,v247\n"
"v_lshl_or_b32 v233,v244,8,v247\n"
"v_add_u32 v233,0x3c00,v233\n"
"v_add_u32 v237,0x3c00,v237\n"
"s_mul_i32 s60,s57,0xc000\n"
"s_add_u32 s32,s8,s60\n"
"s_addc_u32 s33,s9,0\n"
"s_and_b32 s33,s33,0xffff\n"
"s_mov_b32 s34,0xc000\n"
"s_mov_b32 s35,0x20000\n"
"s_add_u32 s36,s14,0\n"
"s_addc_u32 s37,s15,0\n"
"s_and_b32 s37,s37,0xffff\n"
"s_mov_b32 s38,0x400000\n"
"s_mov_b32 s39,0x20000\n"
"s_lshl_b32 s60,s56,19\n"
"s_add_u32 s40,s16,s60\n"
"s_addc_u32 s41,s17,0\n"
"s_and_b32 s41,s41,0xffff\n"
"s_mov_b32 s42,0x80000\n"
"s_mov_b32 s43,0x20000\n"
"s_mul_i32 s60,s57,0x62400\n"
"s_add_u32 s60,s60,0\n"
"s_add_u32 s44,s20,s60\n"
"s_addc_u32 s45,s21,0\n"
"s_and_b32 s45,s45,0xffff\n"
"s_mov_b32 s46,0x62000\n"
"s_mov_b32 s47,0x20000\n"
"s_lshl_b32 s60,s57,13\n"
"s_add_u32 s60,s60,0x2000000\n"
"s_add_u32 s52,s20,s60\n"
"s_addc_u32 s53,s21,0\n"
"s_and_b32 s53,s53,0xffff\n"
"s_mov_b32 s54,0x2000\n"
"s_mov_b32 s55,0x20000\n"
"s_add_u32 s48,s22,0\n"
"s_addc_u32 s49,s23,0\n"
"s_and_b32 s49,s49,0xffff\n"
"s_mov_b32 s50,0x180000\n"
"s_mov_b32 s51,0x20000\n"
"s_mov_b32 s68,s56\n"
"s_mov_b32 s59,s57\n"
"v_min_u32 v246,8,v243\n"
"v_lshlrev_b32 v246,2,v246\n"
"s_lshl_b32 s60,s68,7\n"
"s_add_u32 s60,s60,0x1000\n"
"buffer_load_dword v245,v246,s[52:55],s60 offen sc1\n"
"s_lshl_b32 s60,s56,10\n"
"s_lshl_b32 s61,s7,8\n"
"s_add_u32 s60,s60,s61\n"
"v_lshlrev_b32 v246,4,v243\n"
"v_lshl_or_b32 v246,v244,16,v246\n"
"v_add_u32 v220,s60,v246\n"
"v_add_u32 v221,0x2000,v220\n"
"v_add_u32 v222,0x4000,v220\n"
"v_add_u32 v223,0x6000,v220\n"
"v_add_u32 v224,0x8000,v220\n"
"v_add_u32 v225,0xa000,v220\n"
"v_add_u32 v226,0xc000,v220\n"
"v_add_u32 v227,0xe000,v220\n"
"s_add_u32 s67,s59,0\n"
"s_and_b32 s67,s67,15\n"
"s_lshl_b32 s67,s67,18\n"
"buffer_load_dwordx4 v[96:99],v220,s[36:39],s67 offen\n"
"buffer_load_dwordx4 v[100:103],v221,s[36:39],s67 offen\n"
"buffer_load_dwordx4 v[104:107],v222,s[36:39],s67 offen\n"
"buffer_load_dwordx4 v[108:111],v223,s[36:39],s67 offen\n"
"buffer_load_dwordx4 v[112:115],v224,s[36:39],s67 offen\n"
"buffer_load_dwordx4 v[116:119],v225,s[36:39],s67 offen\n"
"buffer_load_dwordx4 v[120:123],v226,s[36:39],s67 offen\n"
"buffer_load_dwordx4 v[124:127],v227,s[36:39],s67 offen\n"
"s_mul_i32 s61,s7,12\n"
"v_and_b32 v250,0x300,v242\n"
"s_add_u32 s60,s61,0\n"
"s_and_b32 s60,s60,15\n"
"v_xor_b32 v246,s60,v243\n"
"v_lshl_or_b32 v246,v246,4,v250\n"
"s_add_u32 s60,s61,1\n"
"s_and_b32 s60,s60,15\n"
"v_xor_b32 v247,s60,v243\n"
"v_lshl_or_b32 v247,v247,4,v250\n"
"s_add_u32 s60,s61,2\n"
"s_and_b32 s60,s60,15\n"
"v_xor_b32 v248,s60,v243\n"
"v_lshl_or_b32 v248,v248,4,v250\n"
"s_add_u32 s60,s61,3\n"
"s_and_b32 s60,s60,15\n"
"v_xor_b32 v249,s60,v243\n"
"v_lshl_or_b32 v249,v249,4,v250\n"
"s_add_u32 s67,s61,0\n"
"s_lshl_b32 s67,s67,10\n"
"s_add_u32 m0,s67,0x1c000\n"
"s_nop 0\n"
"buffer_load_dwordx4 v246,s[32:35],s67 offen lds\n"
"buffer_load_dwordx4 v247,s[32:35],s67 offen offset:1024 lds\n"
"buffer_load_dwordx4 v248,s[32:35],s67 offen offset:2048 lds\n"
"buffer_load_dwordx4 v249,s[32:35],s67 offen offset:3072 lds\n"
"s_add_u32 s60,s61,4\n"
"s_and_b32 s60,s60,15\n"
"v_xor_b32 v246,s60,v243\n"
"v_lshl_or_b32 v246,v246,4,v250\n"
"s_add_u32 s60,s61,5\n"
"s_and_b32 s60,s60,15\n"
"v_xor_b32 v247,s60,v243\n"
"v_lshl_or_b32 v247,v247,4,v250\n"
"s_add_u32 s60,s61,6\n"
"s_and_b32 s60,s60,15\n"
"v_xor_b32 v248,s60,v243\n"
"v_lshl_or_b32 v248,v248,4,v250\n"
"s_add_u32 s60,s61,7\n"
"s_and_b32 s60,s60,15\n"
"v_xor_b32 v249,s60,v243\n"
"v_lshl_or_b32 v249,v249,4,v250\n"
"s_add_u32 s67,s61,4\n"
"s_lshl_b32 s67,s67,10\n"
"s_add_u32 m0,s67,0x1c000\n"
"s_nop 0\n"
"buffer_load_dwordx4 v246,s[32:35],s67 offen lds\n"
"buffer_load_dwordx4 v247,s[32:35],s67 offen offset:1024 lds\n"
"buffer_load_dwordx4 v248,s[32:35],s67 offen offset:2048 lds\n"
"buffer_load_dwordx4 v249,s[32:35],s67 offen offset:3072 lds\n"
"s_add_u32 s60,s61,8\n"
"s_and_b32 s60,s60,15\n"
"v_xor_b32 v246,s60,v243\n"
"v_lshl_or_b32 v246,v246,4,v250\n"
"s_add_u32 s60,s61,9\n"
"s_and_b32 s60,s60,15\n"
"v_xor_b32 v247,s60,v243\n"
"v_lshl_or_b32 v247,v247,4,v250\n"
"s_add_u32 s60,s61,10\n"
"s_and_b32 s60,s60,15\n"
"v_xor_b32 v248,s60,v243\n"
"v_lshl_or_b32 v248,v248,4,v250\n"
"s_add_u32 s60,s61,11\n"
"s_and_b32 s60,s60,15\n"
"v_xor_b32 v249,s60,v243\n"
"v_lshl_or_b32 v249,v249,4,v250\n"
"s_add_u32 s67,s61,8\n"
"s_lshl_b32 s67,s67,10\n"
"s_add_u32 m0,s67,0x1c000\n"
"s_nop 0\n"
"buffer_load_dwordx4 v246,s[32:35],s67 offen lds\n"
"buffer_load_dwordx4 v247,s[32:35],s67 offen offset:1024 lds\n"
"buffer_load_dwordx4 v248,s[32:35],s67 offen offset:2048 lds\n"
"buffer_load_dwordx4 v249,s[32:35],s67 offen offset:3072 lds\n"
"s_add_u32 s67,s1,0\n"
"s_and_b32 s67,s67,0x3fff\n"
"s_add_u32 m0,s66,0\n"
"s_nop 0\n"
"buffer_load_dwordx4 v232,s[24:27],s67 offen nt lds\n"
"buffer_load_dwordx4 v233,s[24:27],s67 offen offset:1024 nt lds\n"
"s_add_u32 m0,s66,0x2000\n"
"s_nop 0\n"
"buffer_load_dwordx4 v236,s[28:31],s67 offen nt lds\n"
"buffer_load_dwordx4 v237,s[28:31],s67 offen offset:1024 nt lds\n"
"s_add_u32 s67,s1,0x400\n"
"s_and_b32 s67,s67,0x3fff\n"
"s_add_u32 m0,s66,0x4000\n"
"s_nop 0\n"
"buffer_load_dwordx4 v232,s[24:27],s67 offen nt lds\n"
"buffer_load_dwordx4 v233,s[24:27],s67 offen offset:1024 nt lds\n"
"s_add_u32 m0,s66,0x6000\n"
"s_nop 0\n"
"buffer_load_dwordx4 v236,s[28:31],s67 offen nt lds\n"
"buffer_load_dwordx4 v237,s[28:31],s67 offen offset:1024 nt lds\n"
"s_add_u32 s67,s1,0x800\n"
"s_and_b32 s67,s67,0x3fff\n"
"s_add_u32 m0,s66,0x8000\n"
"s_nop 0\n"
"buffer_load_dwordx4 v232,s[24:27],s67 offen nt lds\n"
"buffer_load_dwordx4 v233,s[24:27],s67 offen offset:1024 nt lds\n"
"s_add_u32 m0,s66,0xa000\n"
"s_nop 0\n"
"buffer_load_dwordx4 v236,s[28:31],s67 offen nt lds\n"
"buffer_load_dwordx4 v237,s[28:31],s67 offen offset:1024 nt lds\n"
"s_add_u32 s67,s1,0xc00\n"
"s_and_b32 s67,s67,0x3fff\n"
"s_add_u32 m0,s66,0xc000\n"
"s_nop 0\n"
"buffer_load_dwordx4 v232,s[24:27],s67 offen nt lds\n"
"buffer_load_dwordx4 v233,s[24:27],s67 offen offset:1024 nt lds\n"
"s_add_u32 m0,s66,0xe000\n"
"s_nop 0\n"
"buffer_load_dwordx4 v236,s[28:31],s67 offen nt lds\n"
"buffer_load_dwordx4 v237,s[28:31],s67 offen offset:1024 nt lds\n"
"s_add_u32 s67,s1,0x1000\n"
"s_and_b32 s67,s67,0x3fff\n"
"s_add_u32 m0,s66,0x10000\n"
"s_nop 0\n"
"buffer_load_dwordx4 v232,s[24:27],s67 offen nt lds\n"
"buffer_load_dwordx4 v233,s[24:27],s67 offen offset:1024 nt lds\n"
"s_add_u32 m0,s66,0x12000\n"
"s_nop 0\n"
"buffer_load_dwordx4 v236,s[28:31],s67 offen nt lds\n"
"buffer_load_dwordx4 v237,s[28:31],s67 offen offset:1024 nt lds\n"
"s_add_u32 s67,s1,0x1400\n"
"s_and_b32 s67,s67,0x3fff\n"
"s_add_u32 m0,s66,0x14000\n"
"s_nop 0\n"
"buffer_load_dwordx4 v232,s[24:27],s67 offen nt lds\n"
"buffer_load_dwordx4 v233,s[24:27],s67 offen offset:1024 nt lds\n"
"s_add_u32 m0,s66,0x16000\n"
"s_nop 0\n"
"buffer_load_dwordx4 v236,s[28:31],s67 offen nt lds\n"
"buffer_load_dwordx4 v237,s[28:31],s67 offen offset:1024 nt lds\n"
"s_add_u32 s67,s1,0x1800\n"
"s_and_b32 s67,s67,0x3fff\n"
"s_add_u32 m0,s66,0x18000\n"
"s_nop 0\n"
"buffer_load_dwordx4 v232,s[24:27],s67 offen nt lds\n"
"buffer_load_dwordx4 v233,s[24:27],s67 offen offset:1024 nt lds\n"
"s_add_u32 m0,s66,0x1a000\n"
"s_nop 0\n"
"buffer_load_dwordx4 v236,s[28:31],s67 offen nt lds\n"
"s_waitcnt vmcnt(59)\n"
"buffer_load_dwordx4 v237,s[28:31],s67 offen offset:1024 nt lds\n"
"s_add_u32 s67,s1,0x1c00\n"
"s_and_b32 s67,s67,0x3fff\n"
"s_waitcnt vmcnt(59)\n"
"buffer_load_dwordx4 a[64:67],v232,s[24:27],s67 offen nt\n"
"s_waitcnt vmcnt(59)\n"
"buffer_load_dwordx4 a[68:71],v233,s[24:27],s67 offen offset:1024 nt\n"
"s_waitcnt vmcnt(59)\n"
"buffer_load_dwordx4 a[72:75],v236,s[28:31],s67 offen nt\n"
"s_waitcnt vmcnt(59)\n"
"buffer_load_dwordx4 a[76:79],v237,s[28:31],s67 offen offset:1024 nt\n"
"s_add_u32 s67,s1,0x2000\n"
"s_and_b32 s67,s67,0x3fff\n"
"s_waitcnt vmcnt(59)\n"
"buffer_load_dwordx4 a[80:83],v232,s[24:27],s67 offen nt\n"
"s_waitcnt vmcnt(59)\n"
"buffer_load_dwordx4 a[84:87],v233,s[24:27],s67 offen offset:1024 nt\n"
"s_waitcnt vmcnt(59)\n"
"buffer_load_dwordx4 a[88:91],v236,s[28:31],s67 offen nt\n"
"s_waitcnt vmcnt(59)\n"
"buffer_load_dwordx4 a[92:95],v237,s[28:31],s67 offen offset:1024 nt\n"
"s_add_u32 s67,s1,0x2400\n"
"s_and_b32 s67,s67,0x3fff\n"
"s_waitcnt vmcnt(59)\n"
"buffer_load_dwordx4 a[96:99],v232,s[24:27],s67 offen nt\n"
"s_waitcnt vmcnt(59)\n"
"buffer_load_dwordx4 a[100:103],v233,s[24:27],s67 offen offset:1024 nt\n"
"s_waitcnt vmcnt(59)\n"
"buffer_load_dwordx4 a[104:107],v236,s[28:31],s67 offen nt\n"
"s_waitcnt vmcnt(59)\n"
"buffer_load_dwordx4 a[108:111],v237,s[28:31],s67 offen offset:1024 nt\n"
"s_add_u32 s67,s1,0x2800\n"
"s_and_b32 s67,s67,0x3fff\n"
"s_waitcnt vmcnt(59)\n"
"buffer_load_dwordx4 a[112:115],v232,s[24:27],s67 offen nt\n"
"s_waitcnt vmcnt(59)\n"
"buffer_load_dwordx4 a[116:119],v233,s[24:27],s67 offen offset:1024 nt\n"
"s_waitcnt vmcnt(59)\n"
"buffer_load_dwordx4 a[120:123],v236,s[28:31],s67 offen nt\n"
"s_waitcnt vmcnt(59)\n"
"buffer_load_dwordx4 a[124:127],v237,s[28:31],s67 offen offset:1024 nt\n"
"s_add_u32 s67,s1,0x2c00\n"
"s_and_b32 s67,s67,0x3fff\n"
"s_waitcnt vmcnt(59)\n"
"buffer_load_dwordx4 a[128:131],v232,s[24:27],s67 offen nt\n"
"s_waitcnt vmcnt(59)\n"
"buffer_load_dwordx4 a[132:135],v233,s[24:27],s67 offen offset:1024 nt\n"
"s_waitcnt vmcnt(59)\n"
"buffer_load_dwordx4 a[136:139],v236,s[28:31],s67 offen nt\n"
"s_waitcnt vmcnt(59)\n"
"buffer_load_dwordx4 a[140:143],v237,s[28:31],s67 offen offset:1024 nt\n"
"s_add_u32 s67,s59,1\n"
"s_and_b32 s67,s67,15\n"
"s_lshl_b32 s67,s67,18\n"
"s_waitcnt vmcnt(59)\n"
"buffer_load_dwordx4 v[128:131],v220,s[36:39],s67 offen\n"
"s_waitcnt vmcnt(59)\n"
"buffer_load_dwordx4 v[132:135],v221,s[36:39],s67 offen\n"
"s_waitcnt vmcnt(59)\n"
"buffer_load_dwordx4 v[136:139],v222,s[36:39],s67 offen\n"
"s_waitcnt vmcnt(59)\n"
"buffer_load_dwordx4 v[140:143],v223,s[36:39],s67 offen\n"
"s_waitcnt vmcnt(59)\n"
"buffer_load_dwordx4 v[144:147],v224,s[36:39],s67 offen\n"
"s_waitcnt vmcnt(59)\n"
"buffer_load_dwordx4 v[148:151],v225,s[36:39],s67 offen\n"
"s_waitcnt vmcnt(59)\n"
"buffer_load_dwordx4 v[152:155],v226,s[36:39],s67 offen\n"
"s_waitcnt vmcnt(59)\n"
"buffer_load_dwordx4 v[156:159],v227,s[36:39],s67 offen\n"
"s_add_u32 s67,s59,2\n"
"s_and_b32 s67,s67,15\n"
"s_lshl_b32 s67,s67,18\n"
"s_waitcnt vmcnt(59)\n"
"buffer_load_dwordx4 v[160:163],v220,s[36:39],s67 offen\n"
"s_waitcnt vmcnt(59)\n"
"buffer_load_dwordx4 v[164:167],v221,s[36:39],s67 offen\n"
"s_waitcnt vmcnt(59)\n"
"buffer_load_dwordx4 v[168:171],v222,s[36:39],s67 offen\n"
"s_waitcnt vmcnt(59)\n"
"buffer_load_dwordx4 v[172:175],v223,s[36:39],s67 offen\n"
"s_waitcnt vmcnt(59)\n"
"buffer_load_dwordx4 v[176:179],v224,s[36:39],s67 offen\n"
"s_waitcnt vmcnt(59)\n"
"buffer_load_dwordx4 v[180:183],v225,s[36:39],s67 offen\n"
"s_waitcnt vmcnt(59)\n"
"buffer_load_dwordx4 v[184:187],v226,s[36:39],s67 offen\n"
"s_waitcnt vmcnt(59)\n"
"buffer_load_dwordx4 v[188:191],v227,s[36:39],s67 offen\n"
"v_xor_b32 v246,v244,v243\n"
"v_lshlrev_b32 v246,4,v246\n"
"v_lshl_or_b32 v246,v243,10,v246\n"
"v_add_u32 v228,0x1c000,v246\n"
"s_getpc_b64 s[70:71]\n"
"Lpc_1:\n"
"s_add_u32 s72,s70,LsubA0-Lpc_1\n"
"s_addc_u32 s73,s71,0\n"
"s_add_u32 s74,s70,LsubA1-Lpc_1\n"
"s_addc_u32 s75,s71,0\n"
"s_add_u32 s76,s70,LsubA2-Lpc_1\n"
"s_addc_u32 s77,s71,0\n"
"s_cmp_lg_u32 s7,3\n"
"s_cbranch_scc1 Ln_2\n"
"v_readfirstlane_b32 s65,v245\n"
"s_nop 1\n"
"Ln_3:\n"
"s_add_u32 s65,s65,1\n"
"v_cmp_eq_u32 vcc,s65,v245\n"
"s_cbranch_vccnz Ln_3\n"
"v_mov_b32 v246,s65\n"
"v_mov_b32 v247,0\n"
"s_lshl_b32 s60,s68,7\n"
"s_mov_b64 exec,1\n"
"s_waitcnt vmcnt(59)\n"
"buffer_store_dword v246,v247,s[52:55],s60 offen sc1\n"
"s_mov_b64 exec,-1\n"
"Ln_2:\n"
"s_barrier\n"
"s_add_u32 s60,s59,0\n"
"s_and_b32 s60,s60,15\n"
"s_lshl_b32 s60,s60,6\n"
"v_xor_b32 v229,s60,v228\n"
"ds_read_b128 v[200:203],v229\n"
"ds_read_b128 v[204:207],v229 offset:16384\n"
"ds_read_b128 v[208:211],v229 offset:32768\n"
"v_perm_b32 v192,v100,v96,s18\n"
"v_perm_b32 v193,v108,v104,s18\n"
"v_perm_b32 v194,v116,v112,s18\n"
"v_perm_b32 v195,v124,v120,s18\n"
"s_waitcnt lgkmcnt(2)\n"
"s_nop 0\n"
"v_mfma_f32_16x16x32_bf16 v[0:3],v[192:195],v[200:203],0\n"
"v_perm_b32 v196,v100,v96,s19\n"
"v_perm_b32 v197,v108,v104,s19\n"
"v_perm_b32 v198,v116,v112,s19\n"
"s_waitcnt lgkmcnt(1)\n"
"v_mfma_f32_16x16x32_bf16 v[4:7],v[192:195],v[204:207],0\n"
"v_perm_b32 v199,v124,v120,s19\n"
"s_waitcnt lgkmcnt(0)\n"
"v_mfma_f32_16x16x32_bf16 v[8:11],v[192:195],v[208:211],0\n"
"s_add_u32 s60,s59,1\n"
"s_and_b32 s60,s60,15\n"
"s_lshl_b32 s60,s60,6\n"
"v_xor_b32 v229,s60,v228\n"
"ds_read_b128 v[212:215],v229\n"
"v_mfma_f32_16x16x32_bf16 v[12:15],v[196:199],v[200:203],0\n"
"v_perm_b32 v192,v101,v97,s18\n"
"v_perm_b32 v193,v109,v105,s18\n"
"v_mfma_f32_16x16x32_bf16 v[16:19],v[196:199],v[204:207],0\n"
"v_perm_b32 v194,v117,v113,s18\n"
"v_mfma_f32_16x16x32_bf16 v[20:23],v[196:199],v[208:211],0\n"
"v_perm_b32 v195,v125,v121,s18\n"
"ds_read_b128 v[216:219],v229 offset:16384\n"
"s_nop 0\n"
"v_mfma_f32_16x16x32_bf16 v[24:27],v[192:195],v[200:203],0\n"
"v_perm_b32 v196,v101,v97,s19\n"
"v_perm_b32 v197,v109,v105,s19\n"
"v_mfma_f32_16x16x32_bf16 v[28:31],v[192:195],v[204:207],0\n"
"v_perm_b32 v198,v117,v113,s19\n"
"v_mfma_f32_16x16x32_bf16 v[32:35],v[192:195],v[208:211],0\n"
"v_perm_b32 v199,v125,v121,s19\n"
"ds_read_b128 v[238:241],v229 offset:32768\n"
"s_nop 0\n"
"v_mfma_f32_16x16x32_bf16 v[36:39],v[196:199],v[200:203],0\n"
"v_perm_b32 v192,v102,v98,s18\n"
"v_mfma_f32_16x16x32_bf16 v[40:43],v[196:199],v[204:207],0\n"
"v_perm_b32 v193,v110,v106,s18\n"
"v_perm_b32 v194,v118,v114,s18\n"
"v_mfma_f32_16x16x32_bf16 v[44:47],v[196:199],v[208:211],0\n"
"v_perm_b32 v195,v126,v122,s18\n"
"s_nop 1\n"
"v_mfma_f32_16x16x32_bf16 v[48:51],v[192:195],v[200:203],0\n"
"v_perm_b32 v196,v102,v98,s19\n"
"v_mfma_f32_16x16x32_bf16 v[52:55],v[192:195],v[204:207],0\n"
"v_perm_b32 v197,v110,v106,s19\n"
"v_perm_b32 v198,v118,v114,s19\n"
"v_mfma_f32_16x16x32_bf16 v[56:59],v[192:195],v[208:211],0\n"
"v_perm_b32 v199,v126,v122,s19\n"
"s_nop 1\n"
"v_mfma_f32_16x16x32_bf16 v[60:63],v[196:199],v[200:203],0\n"
"v_perm_b32 v192,v103,v99,s18\n"
"v_mfma_f32_16x16x32_bf16 v[64:67],v[196:199],v[204:207],0\n"
"v_perm_b32 v193,v111,v107,s18\n"
"v_perm_b32 v194,v119,v115,s18\n"
"v_mfma_f32_16x16x32_bf16 v[68:71],v[196:199],v[208:211],0\n"
"v_perm_b32 v195,v127,v123,s18\n"
"s_nop 1\n"
"v_mfma_f32_16x16x32_bf16 v[72:75],v[192:195],v[200:203],0\n"
"v_perm_b32 v196,v103,v99,s19\n"
"v_mfma_f32_16x16x32_bf16 v[76:79],v[192:195],v[204:207],0\n"
"v_perm_b32 v197,v111,v107,s19\n"
"v_perm_b32 v198,v119,v115,s19\n"
"v_mfma_f32_16x16x32_bf16 v[80:83],v[192:195],v[208:211],0\n"
"v_perm_b32 v199,v127,v123,s19\n"
"s_nop 1\n"
"v_mfma_f32_16x16x32_bf16 v[84:87],v[196:199],v[200:203],0\n"
"v_mfma_f32_16x16x32_bf16 v[88:91],v[196:199],v[204:207],0\n"
"v_mfma_f32_16x16x32_bf16 v[92:95],v[196:199],v[208:211],0\n"
"s_waitcnt vmcnt(14)\n"
"v_perm_b32 v192,v132,v128,s18\n"
"s_waitcnt vmcnt(12)\n"
"v_perm_b32 v193,v140,v136,s18\n"
"s_waitcnt vmcnt(10)\n"
"v_perm_b32 v194,v148,v144,s18\n"
"s_waitcnt vmcnt(8)\n"
"v_perm_b32 v195,v156,v152,s18\n"
"s_waitcnt lgkmcnt(0)\n"
"s_nop 0\n"
"v_mfma_f32_16x16x32_bf16 v[0:3],v[192:195],v[212:215],v[0:3]\n"
"v_perm_b32 v196,v132,v128,s19\n"
"v_perm_b32 v197,v140,v136,s19\n"
"v_perm_b32 v198,v148,v144,s19\n"
"v_mfma_f32_16x16x32_bf16 v[4:7],v[192:195],v[216:219],v[4:7]\n"
"v_perm_b32 v199,v156,v152,s19\n"
"s_add_u32 s60,s59,2\n"
"s_and_b32 s60,s60,15\n"
"s_lshl_b32 s60,s60,6\n"
"v_xor_b32 v229,s60,v228\n"
"ds_read_b128 v[200:203],v229\n"
"v_mfma_f32_16x16x32_bf16 v[8:11],v[192:195],v[238:241],v[8:11]\n"
"ds_read_b128 v[204:207],v229 offset:16384\n"
"v_mfma_f32_16x16x32_bf16 v[12:15],v[196:199],v[212:215],v[12:15]\n"
"v_perm_b32 v192,v133,v129,s18\n"
"v_perm_b32 v193,v141,v137,s18\n"
"v_perm_b32 v194,v149,v145,s18\n"
"v_mfma_f32_16x16x32_bf16 v[16:19],v[196:199],v[216:219],v[16:19]\n"
"v_perm_b32 v195,v157,v153,s18\n"
"ds_read_b128 v[208:211],v229 offset:32768\n"
"v_mfma_f32_16x16x32_bf16 v[20:23],v[196:199],v[238:241],v[20:23]\n"
"s_add_u32 s67,s59,3\n"
"s_and_b32 s67,s67,15\n"
"s_lshl_b32 s67,s67,18\n"
"buffer_load_dwordx4 v[96:99],v220,s[36:39],s67 offen\n"
"v_mfma_f32_16x16x32_bf16 v[24:27],v[192:195],v[212:215],v[24:27]\n"
"v_perm_b32 v196,v133,v129,s19\n"
"v_perm_b32 v197,v141,v137,s19\n"
"v_mfma_f32_16x16x32_bf16 v[28:31],v[192:195],v[216:219],v[28:31]\n"
"v_perm_b32 v198,v149,v145,s19\n"
"v_perm_b32 v199,v157,v153,s19\n"
"v_mfma_f32_16x16x32_bf16 v[32:35],v[192:195],v[238:241],v[32:35]\n"
"buffer_load_dwordx4 v[100:103],v221,s[36:39],s67 offen\n"
"buffer_load_dwordx4 v[104:107],v222,s[36:39],s67 offen\n"
"v_mfma_f32_16x16x32_bf16 v[36:39],v[196:199],v[212:215],v[36:39]\n"
"v_perm_b32 v192,v134,v130,s18\n"
"v_perm_b32 v193,v142,v138,s18\n"
"v_mfma_f32_16x16x32_bf16 v[40:43],v[196:199],v[216:219],v[40:43]\n"
"v_perm_b32 v194,v150,v146,s18\n"
"v_perm_b32 v195,v158,v154,s18\n"
"v_mfma_f32_16x16x32_bf16 v[44:47],v[196:199],v[238:241],v[44:47]\n"
"buffer_load_dwordx4 v[108:111],v223,s[36:39],s67 offen\n"
"buffer_load_dwordx4 v[112:115],v224,s[36:39],s67 offen\n"
"v_mfma_f32_16x16x32_bf16 v[48:51],v[192:195],v[212:215],v[48:51]\n"
"v_perm_b32 v196,v134,v130,s19\n"
"v_perm_b32 v197,v142,v138,s19\n"
"v_mfma_f32_16x16x32_bf16 v[52:55],v[192:195],v[216:219],v[52:55]\n"
"v_perm_b32 v198,v150,v146,s19\n"
"v_perm_b32 v199,v158,v154,s19\n"
"v_mfma_f32_16x16x32_bf16 v[56:59],v[192:195],v[238:241],v[56:59]\n"
"buffer_load_dwordx4 v[116:119],v225,s[36:39],s67 offen\n"
"buffer_load_dwordx4 v[120:123],v226,s[36:39],s67 offen\n"
"v_mfma_f32_16x16x32_bf16 v[60:63],v[196:199],v[212:215],v[60:63]\n"
"v_perm_b32 v192,v135,v131,s18\n"
"v_perm_b32 v193,v143,v139,s18\n"
"v_mfma_f32_16x16x32_bf16 v[64:67],v[196:199],v[216:219],v[64:67]\n"
"v_perm_b32 v194,v151,v147,s18\n"
"v_mfma_f32_16x16x32_bf16 v[68:71],v[196:199],v[238:241],v[68:71]\n"
"v_perm_b32 v195,v159,v155,s18\n"
"buffer_load_dwordx4 v[124:127],v227,s[36:39],s67 offen\n"
"s_nop 0\n"
"v_mfma_f32_16x16x32_bf16 v[72:75],v[192:195],v[212:215],v[72:75]\n"
"v_perm_b32 v196,v135,v131,s19\n"
"v_mfma_f32_16x16x32_bf16 v[76:79],v[192:195],v[216:219],v[76:79]\n"
"v_perm_b32 v197,v143,v139,s19\n"
"v_perm_b32 v198,v151,v147,s19\n"
"v_mfma_f32_16x16x32_bf16 v[80:83],v[192:195],v[238:241],v[80:83]\n"
"v_perm_b32 v199,v159,v155,s19\n"
"s_nop 1\n"
"v_mfma_f32_16x16x32_bf16 v[84:87],v[196:199],v[212:215],v[84:87]\n"
"v_mfma_f32_16x16x32_bf16 v[88:91],v[196:199],v[216:219],v[88:91]\n"
"v_mfma_f32_16x16x32_bf16 v[92:95],v[196:199],v[238:241],v[92:95]\n"
"s_waitcnt vmcnt(14)\n"
"v_perm_b32 v192,v164,v160,s18\n"
"s_waitcnt vmcnt(12)\n"
"v_perm_b32 v193,v172,v168,s18\n"
"s_waitcnt vmcnt(10)\n"
"v_perm_b32 v194,v180,v176,s18\n"
"s_waitcnt vmcnt(8)\n"
"v_perm_b32 v195,v188,v184,s18\n"
"s_waitcnt lgkmcnt(0)\n"
"s_nop 0\n"
"v_mfma_f32_16x16x32_bf16 v[0:3],v[192:195],v[200:203],v[0:3]\n"
"v_perm_b32 v196,v164,v160,s19\n"
"v_perm_b32 v197,v172,v168,s19\n"
"v_perm_b32 v198,v180,v176,s19\n"
"v_mfma_f32_16x16x32_bf16 v[4:7],v[192:195],v[204:207],v[4:7]\n"
"v_perm_b32 v199,v188,v184,s19\n"
"s_add_u32 s60,s59,3\n"
"s_and_b32 s60,s60,15\n"
"s_lshl_b32 s60,s60,6\n"
"v_xor_b32 v229,s60,v228\n"
"ds_read_b128 v[212:215],v229\n"
"v_mfma_f32_16x16x32_bf16 v[8:11],v[192:195],v[208:211],v[8:11]\n"
"ds_read_b128 v[216:219],v229 offset:16384\n"
"v_mfma_f32_16x16x32_bf16 v[12:15],v[196:199],v[200:203],v[12:15]\n"
"v_perm_b32 v192,v165,v161,s18\n"
"v_perm_b32 v193,v173,v169,s18\n"
"v_perm_b32 v194,v181,v177,s18\n"
"v_mfma_f32_16x16x32_bf16 v[16:19],v[196:199],v[204:207],v[16:19]\n"
"v_perm_b32 v195,v189,v185,s18\n"
"ds_read_b128 v[238:241],v229 offset:32768\n"
"v_mfma_f32_16x16x32_bf16 v[20:23],v[196:199],v[208:211],v[20:23]\n"
"s_add_u32 s67,s59,4\n"
"s_and_b32 s67,s67,15\n"
"s_lshl_b32 s67,s67,18\n"
"buffer_load_dwordx4 v[128:131],v220,s[36:39],s67 offen\n"
"v_mfma_f32_16x16x32_bf16 v[24:27],v[192:195],v[200:203],v[24:27]\n"
"v_perm_b32 v196,v165,v161,s19\n"
"v_perm_b32 v197,v173,v169,s19\n"
"v_mfma_f32_16x16x32_bf16 v[28:31],v[192:195],v[204:207],v[28:31]\n"
"v_perm_b32 v198,v181,v177,s19\n"
"v_perm_b32 v199,v189,v185,s19\n"
"v_mfma_f32_16x16x32_bf16 v[32:35],v[192:195],v[208:211],v[32:35]\n"
"buffer_load_dwordx4 v[132:135],v221,s[36:39],s67 offen\n"
"buffer_load_dwordx4 v[136:139],v222,s[36:39],s67 offen\n"
"v_mfma_f32_16x16x32_bf16 v[36:39],v[196:199],v[200:203],v[36:39]\n"
"v_perm_b32 v192,v166,v162,s18\n"
"v_perm_b32 v193,v174,v170,s18\n"
"v_mfma_f32_16x16x32_bf16 v[40:43],v[196:199],v[204:207],v[40:43]\n"
"v_perm_b32 v194,v182,v178,s18\n"
"v_perm_b32 v195,v190,v186,s18\n"
"v_mfma_f32_16x16x32_bf16 v[44:47],v[196:199],v[208:211],v[44:47]\n"
"buffer_load_dwordx4 v[140:143],v223,s[36:39],s67 offen\n"
"buffer_load_dwordx4 v[144:147],v224,s[36:39],s67 offen\n"
"v_mfma_f32_16x16x32_bf16 v[48:51],v[192:195],v[200:203],v[48:51]\n"
"v_perm_b32 v196,v166,v162,s19\n"
"v_perm_b32 v197,v174,v170,s19\n"
"v_mfma_f32_16x16x32_bf16 v[52:55],v[192:195],v[204:207],v[52:55]\n"
"v_perm_b32 v198,v182,v178,s19\n"
"v_perm_b32 v199,v190,v186,s19\n"
"v_mfma_f32_16x16x32_bf16 v[56:59],v[192:195],v[208:211],v[56:59]\n"
"buffer_load_dwordx4 v[148:151],v225,s[36:39],s67 offen\n"
"buffer_load_dwordx4 v[152:155],v226,s[36:39],s67 offen\n"
"v_mfma_f32_16x16x32_bf16 v[60:63],v[196:199],v[200:203],v[60:63]\n"
"v_perm_b32 v192,v167,v163,s18\n"
"v_perm_b32 v193,v175,v171,s18\n"
"v_mfma_f32_16x16x32_bf16 v[64:67],v[196:199],v[204:207],v[64:67]\n"
"v_perm_b32 v194,v183,v179,s18\n"
"v_mfma_f32_16x16x32_bf16 v[68:71],v[196:199],v[208:211],v[68:71]\n"
"v_perm_b32 v195,v191,v187,s18\n"
"buffer_load_dwordx4 v[156:159],v227,s[36:39],s67 offen\n"
"s_nop 0\n"
"v_mfma_f32_16x16x32_bf16 v[72:75],v[192:195],v[200:203],v[72:75]\n"
"v_perm_b32 v196,v167,v163,s19\n"
"v_mfma_f32_16x16x32_bf16 v[76:79],v[192:195],v[204:207],v[76:79]\n"
"v_perm_b32 v197,v175,v171,s19\n"
"v_perm_b32 v198,v183,v179,s19\n"
"v_mfma_f32_16x16x32_bf16 v[80:83],v[192:195],v[208:211],v[80:83]\n"
"v_perm_b32 v199,v191,v187,s19\n"
"s_nop 1\n"
"v_mfma_f32_16x16x32_bf16 v[84:87],v[196:199],v[200:203],v[84:87]\n"
"v_mfma_f32_16x16x32_bf16 v[88:91],v[196:199],v[204:207],v[88:91]\n"
"v_mfma_f32_16x16x32_bf16 v[92:95],v[196:199],v[208:211],v[92:95]\n"
"s_waitcnt vmcnt(14)\n"
"v_perm_b32 v192,v100,v96,s18\n"
"s_waitcnt vmcnt(12)\n"
"v_perm_b32 v193,v108,v104,s18\n"
"s_waitcnt vmcnt(10)\n"
"v_perm_b32 v194,v116,v112,s18\n"
"s_waitcnt vmcnt(8)\n"
"v_perm_b32 v195,v124,v120,s18\n"
"s_waitcnt lgkmcnt(0)\n"
"s_nop 0\n"
"v_mfma_f32_16x16x32_bf16 v[0:3],v[192:195],v[212:215],v[0:3]\n"
"v_perm_b32 v196,v100,v96,s19\n"
"v_perm_b32 v197,v108,v104,s19\n"
"v_perm_b32 v198,v116,v112,s19\n"
"v_mfma_f32_16x16x32_bf16 v[4:7],v[192:195],v[216:219],v[4:7]\n"
"v_perm_b32 v199,v124,v120,s19\n"
"s_add_u32 s60,s59,4\n"
"s_and_b32 s60,s60,15\n"
"s_lshl_b32 s60,s60,6\n"
"v_xor_b32 v229,s60,v228\n"
"ds_read_b128 v[200:203],v229\n"
"v_mfma_f32_16x16x32_bf16 v[8:11],v[192:195],v[238:241],v[8:11]\n"
"ds_read_b128 v[204:207],v229 offset:16384\n"
"v_mfma_f32_16x16x32_bf16 v[12:15],v[196:199],v[212:215],v[12:15]\n"
"v_perm_b32 v192,v101,v97,s18\n"
"v_perm_b32 v193,v109,v105,s18\n"
"v_perm_b32 v194,v117,v113,s18\n"
"v_mfma_f32_16x16x32_bf16 v[16:19],v[196:199],v[216:219],v[16:19]\n"
"v_perm_b32 v195,v125,v121,s18\n"
"ds_read_b128 v[208:211],v229 offset:32768\n"
"v_mfma_f32_16x16x32_bf16 v[20:23],v[196:199],v[238:241],v[20:23]\n"
"s_add_u32 s67,s59,5\n"
"s_and_b32 s67,s67,15\n"
"s_lshl_b32 s67,s67,18\n"
"buffer_load_dwordx4 v[160:163],v220,s[36:39],s67 offen\n"
"v_mfma_f32_16x16x32_bf16 v[24:27],v[192:195],v[212:215],v[24:27]\n"
"v_perm_b32 v196,v101,v97,s19\n"
"v_perm_b32 v197,v109,v105,s19\n"
"v_mfma_f32_16x16x32_bf16 v[28:31],v[192:195],v[216:219],v[28:31]\n"
"v_perm_b32 v198,v117,v113,s19\n"
"v_perm_b32 v199,v125,v121,s19\n"
"v_mfma_f32_16x16x32_bf16 v[32:35],v[192:195],v[238:241],v[32:35]\n"
"buffer_load_dwordx4 v[164:167],v221,s[36:39],s67 offen\n"
"buffer_load_dwordx4 v[168:171],v222,s[36:39],s67 offen\n"
"v_mfma_f32_16x16x32_bf16 v[36:39],v[196:199],v[212:215],v[36:39]\n"
"v_perm_b32 v192,v102,v98,s18\n"
"v_perm_b32 v193,v110,v106,s18\n"
"v_mfma_f32_16x16x32_bf16 v[40:43],v[196:199],v[216:219],v[40:43]\n"
"v_perm_b32 v194,v118,v114,s18\n"
"v_perm_b32 v195,v126,v122,s18\n"
"v_mfma_f32_16x16x32_bf16 v[44:47],v[196:199],v[238:241],v[44:47]\n"
"buffer_load_dwordx4 v[172:175],v223,s[36:39],s67 offen\n"
"buffer_load_dwordx4 v[176:179],v224,s[36:39],s67 offen\n"
"v_mfma_f32_16x16x32_bf16 v[48:51],v[192:195],v[212:215],v[48:51]\n"
"v_perm_b32 v196,v102,v98,s19\n"
"v_perm_b32 v197,v110,v106,s19\n"
"v_mfma_f32_16x16x32_bf16 v[52:55],v[192:195],v[216:219],v[52:55]\n"
"v_perm_b32 v198,v118,v114,s19\n"
"v_perm_b32 v199,v126,v122,s19\n"
"v_mfma_f32_16x16x32_bf16 v[56:59],v[192:195],v[238:241],v[56:59]\n"
"buffer_load_dwordx4 v[180:183],v225,s[36:39],s67 offen\n"
"buffer_load_dwordx4 v[184:187],v226,s[36:39],s67 offen\n"
"v_mfma_f32_16x16x32_bf16 v[60:63],v[196:199],v[212:215],v[60:63]\n"
"v_perm_b32 v192,v103,v99,s18\n"
"v_perm_b32 v193,v111,v107,s18\n"
"v_mfma_f32_16x16x32_bf16 v[64:67],v[196:199],v[216:219],v[64:67]\n"
"v_perm_b32 v194,v119,v115,s18\n"
"v_mfma_f32_16x16x32_bf16 v[68:71],v[196:199],v[238:241],v[68:71]\n"
"v_perm_b32 v195,v127,v123,s18\n"
"buffer_load_dwordx4 v[188:191],v227,s[36:39],s67 offen\n"
"s_nop 0\n"
"v_mfma_f32_16x16x32_bf16 v[72:75],v[192:195],v[212:215],v[72:75]\n"
"v_perm_b32 v196,v103,v99,s19\n"
"v_mfma_f32_16x16x32_bf16 v[76:79],v[192:195],v[216:219],v[76:79]\n"
"v_perm_b32 v197,v111,v107,s19\n"
"v_perm_b32 v198,v119,v115,s19\n"
"v_mfma_f32_16x16x32_bf16 v[80:83],v[192:195],v[238:241],v[80:83]\n"
"v_perm_b32 v199,v127,v123,s19\n"
"s_nop 1\n"
"v_mfma_f32_16x16x32_bf16 v[84:87],v[196:199],v[212:215],v[84:87]\n"
"v_mfma_f32_16x16x32_bf16 v[88:91],v[196:199],v[216:219],v[88:91]\n"
"v_mfma_f32_16x16x32_bf16 v[92:95],v[196:199],v[238:241],v[92:95]\n"
"s_waitcnt vmcnt(14)\n"
"v_perm_b32 v192,v132,v128,s18\n"
"s_waitcnt vmcnt(12)\n"
"v_perm_b32 v193,v140,v136,s18\n"
"s_waitcnt vmcnt(10)\n"
"v_perm_b32 v194,v148,v144,s18\n"
"s_waitcnt vmcnt(8)\n"
"v_perm_b32 v195,v156,v152,s18\n"
"s_waitcnt lgkmcnt(0)\n"
"s_nop 0\n"
"v_mfma_f32_16x16x32_bf16 v[0:3],v[192:195],v[200:203],v[0:3]\n"
"v_perm_b32 v196,v132,v128,s19\n"
"v_perm_b32 v197,v140,v136,s19\n"
"v_perm_b32 v198,v148,v144,s19\n"
"v_mfma_f32_16x16x32_bf16 v[4:7],v[192:195],v[204:207],v[4:7]\n"
"v_perm_b32 v199,v156,v152,s19\n"
"s_add_u32 s60,s59,5\n"
"s_and_b32 s60,s60,15\n"
"s_lshl_b32 s60,s60,6\n"
"v_xor_b32 v229,s60,v228\n"
"ds_read_b128 v[212:215],v229\n"
"v_mfma_f32_16x16x32_bf16 v[8:11],v[192:195],v[208:211],v[8:11]\n"
"ds_read_b128 v[216:219],v229 offset:16384\n"
"v_mfma_f32_16x16x32_bf16 v[12:15],v[196:199],v[200:203],v[12:15]\n"
"v_perm_b32 v192,v133,v129,s18\n"
"v_perm_b32 v193,v141,v137,s18\n"
"v_perm_b32 v194,v149,v145,s18\n"
"v_mfma_f32_16x16x32_bf16 v[16:19],v[196:199],v[204:207],v[16:19]\n"
"v_perm_b32 v195,v157,v153,s18\n"
"ds_read_b128 v[238:241],v229 offset:32768\n"
"v_mfma_f32_16x16x32_bf16 v[20:23],v[196:199],v[208:211],v[20:23]\n"
"s_add_u32 s67,s59,6\n"
"s_and_b32 s67,s67,15\n"
"s_lshl_b32 s67,s67,18\n"
"buffer_load_dwordx4 v[96:99],v220,s[36:39],s67 offen\n"
"v_mfma_f32_16x16x32_bf16 v[24:27],v[192:195],v[200:203],v[24:27]\n"
"v_perm_b32 v196,v133,v129,s19\n"
"v_perm_b32 v197,v141,v137,s19\n"
"v_mfma_f32_16x16x32_bf16 v[28:31],v[192:195],v[204:207],v[28:31]\n"
"v_perm_b32 v198,v149,v145,s19\n"
"v_perm_b32 v199,v157,v153,s19\n"
"v_mfma_f32_16x16x32_bf16 v[32:35],v[192:195],v[208:211],v[32:35]\n"
"buffer_load_dwordx4 v[100:103],v221,s[36:39],s67 offen\n"
"buffer_load_dwordx4 v[104:107],v222,s[36:39],s67 offen\n"
"v_mfma_f32_16x16x32_bf16 v[36:39],v[196:199],v[200:203],v[36:39]\n"
"v_perm_b32 v192,v134,v130,s18\n"
"v_perm_b32 v193,v142,v138,s18\n"
"v_mfma_f32_16x16x32_bf16 v[40:43],v[196:199],v[204:207],v[40:43]\n"
"v_perm_b32 v194,v150,v146,s18\n"
"v_perm_b32 v195,v158,v154,s18\n"
"v_mfma_f32_16x16x32_bf16 v[44:47],v[196:199],v[208:211],v[44:47]\n"
"buffer_load_dwordx4 v[108:111],v223,s[36:39],s67 offen\n"
"buffer_load_dwordx4 v[112:115],v224,s[36:39],s67 offen\n"
"v_mfma_f32_16x16x32_bf16 v[48:51],v[192:195],v[200:203],v[48:51]\n"
"v_perm_b32 v196,v134,v130,s19\n"
"v_perm_b32 v197,v142,v138,s19\n"
"v_mfma_f32_16x16x32_bf16 v[52:55],v[192:195],v[204:207],v[52:55]\n"
"v_perm_b32 v198,v150,v146,s19\n"
"v_perm_b32 v199,v158,v154,s19\n"
"v_mfma_f32_16x16x32_bf16 v[56:59],v[192:195],v[208:211],v[56:59]\n"
"buffer_load_dwordx4 v[116:119],v225,s[36:39],s67 offen\n"
"buffer_load_dwordx4 v[120:123],v226,s[36:39],s67 offen\n"
"v_mfma_f32_16x16x32_bf16 v[60:63],v[196:199],v[200:203],v[60:63]\n"
"v_perm_b32 v192,v135,v131,s18\n"
"v_perm_b32 v193,v143,v139,s18\n"
"v_mfma_f32_16x16x32_bf16 v[64:67],v[196:199],v[204:207],v[64:67]\n"
"v_perm_b32 v194,v151,v147,s18\n"
"v_mfma_f32_16x16x32_bf16 v[68:71],v[196:199],v[208:211],v[68:71]\n"
"v_perm_b32 v195,v159,v155,s18\n"
"buffer_load_dwordx4 v[124:127],v227,s[36:39],s67 offen\n"
"s_nop 0\n"
"v_mfma_f32_16x16x32_bf16 v[72:75],v[192:195],v[200:203],v[72:75]\n"
"v_perm_b32 v196,v135,v131,s19\n"
"v_mfma_f32_16x16x32_bf16 v[76:79],v[192:195],v[204:207],v[76:79]\n"
"v_perm_b32 v197,v143,v139,s19\n"
"v_perm_b32 v198,v151,v147,s19\n"
"v_mfma_f32_16x16x32_bf16 v[80:83],v[192:195],v[208:211],v[80:83]\n"
"v_perm_b32 v199,v159,v155,s19\n"
"s_nop 1\n"
"v_mfma_f32_16x16x32_bf16 v[84:87],v[196:199],v[200:203],v[84:87]\n"
"v_mfma_f32_16x16x32_bf16 v[88:91],v[196:199],v[204:207],v[88:91]\n"
"v_mfma_f32_16x16x32_bf16 v[92:95],v[196:199],v[208:211],v[92:95]\n"
"s_waitcnt vmcnt(14)\n"
"v_perm_b32 v192,v164,v160,s18\n"
"s_waitcnt vmcnt(12)\n"
"v_perm_b32 v193,v172,v168,s18\n"
"s_waitcnt vmcnt(10)\n"
"v_perm_b32 v194,v180,v176,s18\n"
"s_waitcnt vmcnt(8)\n"
"v_perm_b32 v195,v188,v184,s18\n"
"s_waitcnt lgkmcnt(0)\n"
"s_nop 0\n"
"v_mfma_f32_16x16x32_bf16 v[0:3],v[192:195],v[212:215],v[0:3]\n"
"v_perm_b32 v196,v164,v160,s19\n"
"v_perm_b32 v197,v172,v168,s19\n"
"v_perm_b32 v198,v180,v176,s19\n"
"v_mfma_f32_16x16x32_bf16 v[4:7],v[192:195],v[216:219],v[4:7]\n"
"v_perm_b32 v199,v188,v184,s19\n"
"s_add_u32 s60,s59,6\n"
"s_and_b32 s60,s60,15\n"
"s_lshl_b32 s60,s60,6\n"
"v_xor_b32 v229,s60,v228\n"
"ds_read_b128 v[200:203],v229\n"
"v_mfma_f32_16x16x32_bf16 v[8:11],v[192:195],v[238:241],v[8:11]\n"
"ds_read_b128 v[204:207],v229 offset:16384\n"
"v_mfma_f32_16x16x32_bf16 v[12:15],v[196:199],v[212:215],v[12:15]\n"
"v_perm_b32 v192,v165,v161,s18\n"
"v_perm_b32 v193,v173,v169,s18\n"
"v_perm_b32 v194,v181,v177,s18\n"
"v_mfma_f32_16x16x32_bf16 v[16:19],v[196:199],v[216:219],v[16:19]\n"
"v_perm_b32 v195,v189,v185,s18\n"
"ds_read_b128 v[208:211],v229 offset:32768\n"
"v_mfma_f32_16x16x32_bf16 v[20:23],v[196:199],v[238:241],v[20:23]\n"
"s_add_u32 s67,s59,7\n"
"s_and_b32 s67,s67,15\n"
"s_lshl_b32 s67,s67,18\n"
"buffer_load_dwordx4 v[128:131],v220,s[36:39],s67 offen\n"
"v_mfma_f32_16x16x32_bf16 v[24:27],v[192:195],v[212:215],v[24:27]\n"
"v_perm_b32 v196,v165,v161,s19\n"
"v_perm_b32 v197,v173,v169,s19\n"
"v_mfma_f32_16x16x32_bf16 v[28:31],v[192:195],v[216:219],v[28:31]\n"
"v_perm_b32 v198,v181,v177,s19\n"
"v_perm_b32 v199,v189,v185,s19\n"
"v_mfma_f32_16x16x32_bf16 v[32:35],v[192:195],v[238:241],v[32:35]\n"
"buffer_load_dwordx4 v[132:135],v221,s[36:39],s67 offen\n"
"buffer_load_dwordx4 v[136:139],v222,s[36:39],s67 offen\n"
"v_mfma_f32_16x16x32_bf16 v[36:39],v[196:199],v[212:215],v[36:39]\n"
"v_perm_b32 v192,v166,v162,s18\n"
"v_perm_b32 v193,v174,v170,s18\n"
"v_mfma_f32_16x16x32_bf16 v[40:43],v[196:199],v[216:219],v[40:43]\n"
"v_perm_b32 v194,v182,v178,s18\n"
"v_perm_b32 v195,v190,v186,s18\n"
"v_mfma_f32_16x16x32_bf16 v[44:47],v[196:199],v[238:241],v[44:47]\n"
"buffer_load_dwordx4 v[140:143],v223,s[36:39],s67 offen\n"
"buffer_load_dwordx4 v[144:147],v224,s[36:39],s67 offen\n"
"v_mfma_f32_16x16x32_bf16 v[48:51],v[192:195],v[212:215],v[48:51]\n"
"v_perm_b32 v196,v166,v162,s19\n"
"v_perm_b32 v197,v174,v170,s19\n"
"v_mfma_f32_16x16x32_bf16 v[52:55],v[192:195],v[216:219],v[52:55]\n"
"v_perm_b32 v198,v182,v178,s19\n"
"v_perm_b32 v199,v190,v186,s19\n"
"v_mfma_f32_16x16x32_bf16 v[56:59],v[192:195],v[238:241],v[56:59]\n"
"buffer_load_dwordx4 v[148:151],v225,s[36:39],s67 offen\n"
"buffer_load_dwordx4 v[152:155],v226,s[36:39],s67 offen\n"
"v_mfma_f32_16x16x32_bf16 v[60:63],v[196:199],v[212:215],v[60:63]\n"
"v_perm_b32 v192,v167,v163,s18\n"
"v_perm_b32 v193,v175,v171,s18\n"
"v_mfma_f32_16x16x32_bf16 v[64:67],v[196:199],v[216:219],v[64:67]\n"
"v_perm_b32 v194,v183,v179,s18\n"
"v_mfma_f32_16x16x32_bf16 v[68:71],v[196:199],v[238:241],v[68:71]\n"
"v_perm_b32 v195,v191,v187,s18\n"
"buffer_load_dwordx4 v[156:159],v227,s[36:39],s67 offen\n"
"s_nop 0\n"
"v_mfma_f32_16x16x32_bf16 v[72:75],v[192:195],v[212:215],v[72:75]\n"
"v_perm_b32 v196,v167,v163,s19\n"
"v_mfma_f32_16x16x32_bf16 v[76:79],v[192:195],v[216:219],v[76:79]\n"
"v_perm_b32 v197,v175,v171,s19\n"
"v_perm_b32 v198,v183,v179,s19\n"
"v_mfma_f32_16x16x32_bf16 v[80:83],v[192:195],v[238:241],v[80:83]\n"
"v_perm_b32 v199,v191,v187,s19\n"
"s_nop 1\n"
"v_mfma_f32_16x16x32_bf16 v[84:87],v[196:199],v[212:215],v[84:87]\n"
"v_mfma_f32_16x16x32_bf16 v[88:91],v[196:199],v[216:219],v[88:91]\n"
"v_mfma_f32_16x16x32_bf16 v[92:95],v[196:199],v[238:241],v[92:95]\n"
"s_waitcnt vmcnt(14)\n"
"v_perm_b32 v192,v100,v96,s18\n"
"s_waitcnt vmcnt(12)\n"
"v_perm_b32 v193,v108,v104,s18\n"
"s_waitcnt vmcnt(10)\n"
"v_perm_b32 v194,v116,v112,s18\n"
"s_waitcnt vmcnt(8)\n"
"v_perm_b32 v195,v124,v120,s18\n"
"s_waitcnt lgkmcnt(0)\n"
"s_nop 0\n"
"v_mfma_f32_16x16x32_bf16 v[0:3],v[192:195],v[200:203],v[0:3]\n"
"v_perm_b32 v196,v100,v96,s19\n"
"v_perm_b32 v197,v108,v104,s19\n"
"v_perm_b32 v198,v116,v112,s19\n"
"v_mfma_f32_16x16x32_bf16 v[4:7],v[192:195],v[204:207],v[4:7]\n"
"v_perm_b32 v199,v124,v120,s19\n"
"s_add_u32 s60,s59,7\n"
"s_and_b32 s60,s60,15\n"
"s_lshl_b32 s60,s60,6\n"
"v_xor_b32 v229,s60,v228\n"
"ds_read_b128 v[212:215],v229\n"
"v_mfma_f32_16x16x32_bf16 v[8:11],v[192:195],v[208:211],v[8:11]\n"
"ds_read_b128 v[216:219],v229 offset:16384\n"
"v_mfma_f32_16x16x32_bf16 v[12:15],v[196:199],v[200:203],v[12:15]\n"
"v_perm_b32 v192,v101,v97,s18\n"
"v_perm_b32 v193,v109,v105,s18\n"
"v_perm_b32 v194,v117,v113,s18\n"
"v_mfma_f32_16x16x32_bf16 v[16:19],v[196:199],v[204:207],v[16:19]\n"
"v_perm_b32 v195,v125,v121,s18\n"
"ds_read_b128 v[238:241],v229 offset:32768\n"
"v_mfma_f32_16x16x32_bf16 v[20:23],v[196:199],v[208:211],v[20:23]\n"
"s_add_u32 s67,s59,8\n"
"s_and_b32 s67,s67,15\n"
"s_lshl_b32 s67,s67,18\n"
"buffer_load_dwordx4 v[160:163],v220,s[36:39],s67 offen\n"
"v_mfma_f32_16x16x32_bf16 v[24:27],v[192:195],v[200:203],v[24:27]\n"
"v_perm_b32 v196,v101,v97,s19\n"
"v_perm_b32 v197,v109,v105,s19\n"
"v_mfma_f32_16x16x32_bf16 v[28:31],v[192:195],v[204:207],v[28:31]\n"
"v_perm_b32 v198,v117,v113,s19\n"
"v_perm_b32 v199,v125,v121,s19\n"
"v_mfma_f32_16x16x32_bf16 v[32:35],v[192:195],v[208:211],v[32:35]\n"
"buffer_load_dwordx4 v[164:167],v221,s[36:39],s67 offen\n"
"buffer_load_dwordx4 v[168:171],v222,s[36:39],s67 offen\n"
"v_mfma_f32_16x16x32_bf16 v[36:39],v[196:199],v[200:203],v[36:39]\n"
"v_perm_b32 v192,v102,v98,s18\n"
"v_perm_b32 v193,v110,v106,s18\n"
"v_mfma_f32_16x16x32_bf16 v[40:43],v[196:199],v[204:207],v[40:43]\n"
"v_perm_b32 v194,v118,v114,s18\n"
"v_perm_b32 v195,v126,v122,s18\n"
"v_mfma_f32_16x16x32_bf16 v[44:47],v[196:199],v[208:211],v[44:47]\n"
"buffer_load_dwordx4 v[172:175],v223,s[36:39],s67 offen\n"
"buffer_load_dwordx4 v[176:179],v224,s[36:39],s67 offen\n"
"v_mfma_f32_16x16x32_bf16 v[48:51],v[192:195],v[200:203],v[48:51]\n"
"v_perm_b32 v196,v102,v98,s19\n"
"v_perm_b32 v197,v110,v106,s19\n"
"v_mfma_f32_16x16x32_bf16 v[52:55],v[192:195],v[204:207],v[52:55]\n"
"v_perm_b32 v198,v118,v114,s19\n"
"v_perm_b32 v199,v126,v122,s19\n"
"v_mfma_f32_16x16x32_bf16 v[56:59],v[192:195],v[208:211],v[56:59]\n"
"buffer_load_dwordx4 v[180:183],v225,s[36:39],s67 offen\n"
"buffer_load_dwordx4 v[184:187],v226,s[36:39],s67 offen\n"
"v_mfma_f32_16x16x32_bf16 v[60:63],v[196:199],v[200:203],v[60:63]\n"
"v_perm_b32 v192,v103,v99,s18\n"
"v_perm_b32 v193,v111,v107,s18\n"
"v_mfma_f32_16x16x32_bf16 v[64:67],v[196:199],v[204:207],v[64:67]\n"
"v_perm_b32 v194,v119,v115,s18\n"
"v_mfma_f32_16x16x32_bf16 v[68:71],v[196:199],v[208:211],v[68:71]\n"
"v_perm_b32 v195,v127,v123,s18\n"
"buffer_load_dwordx4 v[188:191],v227,s[36:39],s67 offen\n"
"s_nop 0\n"
"v_mfma_f32_16x16x32_bf16 v[72:75],v[192:195],v[200:203],v[72:75]\n"
"v_perm_b32 v196,v103,v99,s19\n"
"v_mfma_f32_16x16x32_bf16 v[76:79],v[192:195],v[204:207],v[76:79]\n"
"v_perm_b32 v197,v111,v107,s19\n"
"v_perm_b32 v198,v119,v115,s19\n"
"v_mfma_f32_16x16x32_bf16 v[80:83],v[192:195],v[208:211],v[80:83]\n"
"v_perm_b32 v199,v127,v123,s19\n"
"s_nop 1\n"
"v_mfma_f32_16x16x32_bf16 v[84:87],v[196:199],v[200:203],v[84:87]\n"
"v_mfma_f32_16x16x32_bf16 v[88:91],v[196:199],v[204:207],v[88:91]\n"
"v_mfma_f32_16x16x32_bf16 v[92:95],v[196:199],v[208:211],v[92:95]\n"
"s_waitcnt vmcnt(14)\n"
"v_perm_b32 v192,v132,v128,s18\n"
"s_waitcnt vmcnt(12)\n"
"v_perm_b32 v193,v140,v136,s18\n"
"s_waitcnt vmcnt(10)\n"
"v_perm_b32 v194,v148,v144,s18\n"
"s_waitcnt vmcnt(8)\n"
"v_perm_b32 v195,v156,v152,s18\n"
"s_waitcnt lgkmcnt(0)\n"
"s_nop 0\n"
"v_mfma_f32_16x16x32_bf16 v[0:3],v[192:195],v[212:215],v[0:3]\n"
"v_perm_b32 v196,v132,v128,s19\n"
"v_perm_b32 v197,v140,v136,s19\n"
"v_perm_b32 v198,v148,v144,s19\n"
"v_mfma_f32_16x16x32_bf16 v[4:7],v[192:195],v[216:219],v[4:7]\n"
"v_perm_b32 v199,v156,v152,s19\n"
"s_add_u32 s60,s59,8\n"
"s_and_b32 s60,s60,15\n"
"s_lshl_b32 s60,s60,6\n"
"v_xor_b32 v229,s60,v228\n"
"ds_read_b128 v[200:203],v229\n"
"v_mfma_f32_16x16x32_bf16 v[8:11],v[192:195],v[238:241],v[8:11]\n"
"ds_read_b128 v[204:207],v229 offset:16384\n"
"v_mfma_f32_16x16x32_bf16 v[12:15],v[196:199],v[212:215],v[12:15]\n"
"v_perm_b32 v192,v133,v129,s18\n"
"v_perm_b32 v193,v141,v137,s18\n"
"v_perm_b32 v194,v149,v145,s18\n"
"v_mfma_f32_16x16x32_bf16 v[16:19],v[196:199],v[216:219],v[16:19]\n"
"v_perm_b32 v195,v157,v153,s18\n"
"ds_read_b128 v[208:211],v229 offset:32768\n"
"v_mfma_f32_16x16x32_bf16 v[20:23],v[196:199],v[238:241],v[20:23]\n"
"s_add_u32 s67,s59,9\n"
"s_and_b32 s67,s67,15\n"
"s_lshl_b32 s67,s67,18\n"
"buffer_load_dwordx4 v[96:99],v220,s[36:39],s67 offen\n"
"v_mfma_f32_16x16x32_bf16 v[24:27],v[192:195],v[212:215],v[24:27]\n"
"v_perm_b32 v196,v133,v129,s19\n"
"v_perm_b32 v197,v141,v137,s19\n"
"v_mfma_f32_16x16x32_bf16 v[28:31],v[192:195],v[216:219],v[28:31]\n"
"v_perm_b32 v198,v149,v145,s19\n"
"v_perm_b32 v199,v157,v153,s19\n"
"v_mfma_f32_16x16x32_bf16 v[32:35],v[192:195],v[238:241],v[32:35]\n"
"buffer_load_dwordx4 v[100:103],v221,s[36:39],s67 offen\n"
"buffer_load_dwordx4 v[104:107],v222,s[36:39],s67 offen\n"
"v_mfma_f32_16x16x32_bf16 v[36:39],v[196:199],v[212:215],v[36:39]\n"
"v_perm_b32 v192,v134,v130,s18\n"
"v_perm_b32 v193,v142,v138,s18\n"
"v_mfma_f32_16x16x32_bf16 v[40:43],v[196:199],v[216:219],v[40:43]\n"
"v_perm_b32 v194,v150,v146,s18\n"
"v_perm_b32 v195,v158,v154,s18\n"
"v_mfma_f32_16x16x32_bf16 v[44:47],v[196:199],v[238:241],v[44:47]\n"
"buffer_load_dwordx4 v[108:111],v223,s[36:39],s67 offen\n"
"buffer_load_dwordx4 v[112:115],v224,s[36:39],s67 offen\n"
"v_mfma_f32_16x16x32_bf16 v[48:51],v[192:195],v[212:215],v[48:51]\n"
"v_perm_b32 v196,v134,v130,s19\n"
"v_perm_b32 v197,v142,v138,s19\n"
"v_mfma_f32_16x16x32_bf16 v[52:55],v[192:195],v[216:219],v[52:55]\n"
"v_perm_b32 v198,v150,v146,s19\n"
"v_perm_b32 v199,v158,v154,s19\n"
"v_mfma_f32_16x16x32_bf16 v[56:59],v[192:195],v[238:241],v[56:59]\n"
"buffer_load_dwordx4 v[116:119],v225,s[36:39],s67 offen\n"
"buffer_load_dwordx4 v[120:123],v226,s[36:39],s67 offen\n"
"v_mfma_f32_16x16x32_bf16 v[60:63],v[196:199],v[212:215],v[60:63]\n"
"v_perm_b32 v192,v135,v131,s18\n"
"v_perm_b32 v193,v143,v139,s18\n"
"v_mfma_f32_16x16x32_bf16 v[64:67],v[196:199],v[216:219],v[64:67]\n"
"v_perm_b32 v194,v151,v147,s18\n"
"v_mfma_f32_16x16x32_bf16 v[68:71],v[196:199],v[238:241],v[68:71]\n"
"v_perm_b32 v195,v159,v155,s18\n"
"buffer_load_dwordx4 v[124:127],v227,s[36:39],s67 offen\n"
"s_nop 0\n"
"v_mfma_f32_16x16x32_bf16 v[72:75],v[192:195],v[212:215],v[72:75]\n"
"v_perm_b32 v196,v135,v131,s19\n"
"v_mfma_f32_16x16x32_bf16 v[76:79],v[192:195],v[216:219],v[76:79]\n"
"v_perm_b32 v197,v143,v139,s19\n"
"v_perm_b32 v198,v151,v147,s19\n"
"v_mfma_f32_16x16x32_bf16 v[80:83],v[192:195],v[238:241],v[80:83]\n"
"v_perm_b32 v199,v159,v155,s19\n"
"s_nop 1\n"
"v_mfma_f32_16x16x32_bf16 v[84:87],v[196:199],v[212:215],v[84:87]\n"
"v_mfma_f32_16x16x32_bf16 v[88:91],v[196:199],v[216:219],v[88:91]\n"
"v_mfma_f32_16x16x32_bf16 v[92:95],v[196:199],v[238:241],v[92:95]\n"
"s_waitcnt vmcnt(14)\n"
"v_perm_b32 v192,v164,v160,s18\n"
"s_waitcnt vmcnt(12)\n"
"v_perm_b32 v193,v172,v168,s18\n"
"s_waitcnt vmcnt(10)\n"
"v_perm_b32 v194,v180,v176,s18\n"
"s_waitcnt vmcnt(8)\n"
"v_perm_b32 v195,v188,v184,s18\n"
"s_waitcnt lgkmcnt(0)\n"
"s_nop 0\n"
"v_mfma_f32_16x16x32_bf16 v[0:3],v[192:195],v[200:203],v[0:3]\n"
"v_perm_b32 v196,v164,v160,s19\n"
"v_perm_b32 v197,v172,v168,s19\n"
"v_perm_b32 v198,v180,v176,s19\n"
"v_mfma_f32_16x16x32_bf16 v[4:7],v[192:195],v[204:207],v[4:7]\n"
"v_perm_b32 v199,v188,v184,s19\n"
"s_add_u32 s60,s59,9\n"
"s_and_b32 s60,s60,15\n"
"s_lshl_b32 s60,s60,6\n"
"v_xor_b32 v229,s60,v228\n"
"ds_read_b128 v[212:215],v229\n"
"v_mfma_f32_16x16x32_bf16 v[8:11],v[192:195],v[208:211],v[8:11]\n"
"ds_read_b128 v[216:219],v229 offset:16384\n"
"v_mfma_f32_16x16x32_bf16 v[12:15],v[196:199],v[200:203],v[12:15]\n"
"v_perm_b32 v192,v165,v161,s18\n"
"v_perm_b32 v193,v173,v169,s18\n"
"v_perm_b32 v194,v181,v177,s18\n"
"v_mfma_f32_16x16x32_bf16 v[16:19],v[196:199],v[204:207],v[16:19]\n"
"v_perm_b32 v195,v189,v185,s18\n"
"ds_read_b128 v[238:241],v229 offset:32768\n"
"v_mfma_f32_16x16x32_bf16 v[20:23],v[196:199],v[208:211],v[20:23]\n"
"s_add_u32 s67,s59,10\n"
"s_and_b32 s67,s67,15\n"
"s_lshl_b32 s67,s67,18\n"
"buffer_load_dwordx4 v[128:131],v220,s[36:39],s67 offen\n"
"v_mfma_f32_16x16x32_bf16 v[24:27],v[192:195],v[200:203],v[24:27]\n"
"v_perm_b32 v196,v165,v161,s19\n"
"v_perm_b32 v197,v173,v169,s19\n"
"v_mfma_f32_16x16x32_bf16 v[28:31],v[192:195],v[204:207],v[28:31]\n"
"v_perm_b32 v198,v181,v177,s19\n"
"v_perm_b32 v199,v189,v185,s19\n"
"v_mfma_f32_16x16x32_bf16 v[32:35],v[192:195],v[208:211],v[32:35]\n"
"buffer_load_dwordx4 v[132:135],v221,s[36:39],s67 offen\n"
"buffer_load_dwordx4 v[136:139],v222,s[36:39],s67 offen\n"
"v_mfma_f32_16x16x32_bf16 v[36:39],v[196:199],v[200:203],v[36:39]\n"
"v_perm_b32 v192,v166,v162,s18\n"
"v_perm_b32 v193,v174,v170,s18\n"
"v_mfma_f32_16x16x32_bf16 v[40:43],v[196:199],v[204:207],v[40:43]\n"
"v_perm_b32 v194,v182,v178,s18\n"
"v_perm_b32 v195,v190,v186,s18\n"
"v_mfma_f32_16x16x32_bf16 v[44:47],v[196:199],v[208:211],v[44:47]\n"
"buffer_load_dwordx4 v[140:143],v223,s[36:39],s67 offen\n"
"buffer_load_dwordx4 v[144:147],v224,s[36:39],s67 offen\n"
"v_mfma_f32_16x16x32_bf16 v[48:51],v[192:195],v[200:203],v[48:51]\n"
"v_perm_b32 v196,v166,v162,s19\n"
"v_perm_b32 v197,v174,v170,s19\n"
"v_mfma_f32_16x16x32_bf16 v[52:55],v[192:195],v[204:207],v[52:55]\n"
"v_perm_b32 v198,v182,v178,s19\n"
"v_perm_b32 v199,v190,v186,s19\n"
"v_mfma_f32_16x16x32_bf16 v[56:59],v[192:195],v[208:211],v[56:59]\n"
"buffer_load_dwordx4 v[148:151],v225,s[36:39],s67 offen\n"
"buffer_load_dwordx4 v[152:155],v226,s[36:39],s67 offen\n"
"v_mfma_f32_16x16x32_bf16 v[60:63],v[196:199],v[200:203],v[60:63]\n"
"v_perm_b32 v192,v167,v163,s18\n"
"v_perm_b32 v193,v175,v171,s18\n"
"v_mfma_f32_16x16x32_bf16 v[64:67],v[196:199],v[204:207],v[64:67]\n"
"v_perm_b32 v194,v183,v179,s18\n"
"v_mfma_f32_16x16x32_bf16 v[68:71],v[196:199],v[208:211],v[68:71]\n"
"v_perm_b32 v195,v191,v187,s18\n"
"buffer_load_dwordx4 v[156:159],v227,s[36:39],s67 offen\n"
"s_nop 0\n"
"v_mfma_f32_16x16x32_bf16 v[72:75],v[192:195],v[200:203],v[72:75]\n"
"v_perm_b32 v196,v167,v163,s19\n"
"v_mfma_f32_16x16x32_bf16 v[76:79],v[192:195],v[204:207],v[76:79]\n"
"v_perm_b32 v197,v175,v171,s19\n"
"v_perm_b32 v198,v183,v179,s19\n"
"v_mfma_f32_16x16x32_bf16 v[80:83],v[192:195],v[208:211],v[80:83]\n"
"v_perm_b32 v199,v191,v187,s19\n"
"s_nop 1\n"
"v_mfma_f32_16x16x32_bf16 v[84:87],v[196:199],v[200:203],v[84:87]\n"
"v_mfma_f32_16x16x32_bf16 v[88:91],v[196:199],v[204:207],v[88:91]\n"
"v_mfma_f32_16x16x32_bf16 v[92:95],v[196:199],v[208:211],v[92:95]\n"
"s_waitcnt vmcnt(14)\n"
"v_perm_b32 v192,v100,v96,s18\n"
"s_waitcnt vmcnt(12)\n"
"v_perm_b32 v193,v108,v104,s18\n"
"s_waitcnt vmcnt(10)\n"
"v_perm_b32 v194,v116,v112,s18\n"
"s_waitcnt vmcnt(8)\n"
"v_perm_b32 v195,v124,v120,s18\n"
"s_waitcnt lgkmcnt(0)\n"
"s_nop 0\n"
"v_mfma_f32_16x16x32_bf16 v[0:3],v[192:195],v[212:215],v[0:3]\n"
"v_perm_b32 v196,v100,v96,s19\n"
"v_perm_b32 v197,v108,v104,s19\n"
"v_perm_b32 v198,v116,v112,s19\n"
"v_mfma_f32_16x16x32_bf16 v[4:7],v[192:195],v[216:219],v[4:7]\n"
"v_perm_b32 v199,v124,v120,s19\n"
"s_add_u32 s60,s59,10\n"
"s_and_b32 s60,s60,15\n"
"s_lshl_b32 s60,s60,6\n"
"v_xor_b32 v229,s60,v228\n"
"ds_read_b128 v[200:203],v229\n"
"v_mfma_f32_16x16x32_bf16 v[8:11],v[192:195],v[238:241],v[8:11]\n"
"ds_read_b128 v[204:207],v229 offset:16384\n"
"v_mfma_f32_16x16x32_bf16 v[12:15],v[196:199],v[212:215],v[12:15]\n"
"v_perm_b32 v192,v101,v97,s18\n"
"v_perm_b32 v193,v109,v105,s18\n"
"v_perm_b32 v194,v117,v113,s18\n"
"v_mfma_f32_16x16x32_bf16 v[16:19],v[196:199],v[216:219],v[16:19]\n"
"v_perm_b32 v195,v125,v121,s18\n"
"ds_read_b128 v[208:211],v229 offset:32768\n"
"v_mfma_f32_16x16x32_bf16 v[20:23],v[196:199],v[238:241],v[20:23]\n"
"s_add_u32 s67,s59,11\n"
"s_and_b32 s67,s67,15\n"
"s_lshl_b32 s67,s67,18\n"
"buffer_load_dwordx4 v[160:163],v220,s[36:39],s67 offen\n"
"v_mfma_f32_16x16x32_bf16 v[24:27],v[192:195],v[212:215],v[24:27]\n"
"v_perm_b32 v196,v101,v97,s19\n"
"v_perm_b32 v197,v109,v105,s19\n"
"v_mfma_f32_16x16x32_bf16 v[28:31],v[192:195],v[216:219],v[28:31]\n"
"v_perm_b32 v198,v117,v113,s19\n"
"v_perm_b32 v199,v125,v121,s19\n"
"v_mfma_f32_16x16x32_bf16 v[32:35],v[192:195],v[238:241],v[32:35]\n"
"buffer_load_dwordx4 v[164:167],v221,s[36:39],s67 offen\n"
"buffer_load_dwordx4 v[168:171],v222,s[36:39],s67 offen\n"
"v_mfma_f32_16x16x32_bf16 v[36:39],v[196:199],v[212:215],v[36:39]\n"
"v_perm_b32 v192,v102,v98,s18\n"
"v_perm_b32 v193,v110,v106,s18\n"
"v_mfma_f32_16x16x32_bf16 v[40:43],v[196:199],v[216:219],v[40:43]\n"
"v_perm_b32 v194,v118,v114,s18\n"
"v_perm_b32 v195,v126,v122,s18\n"
"v_mfma_f32_16x16x32_bf16 v[44:47],v[196:199],v[238:241],v[44:47]\n"
"buffer_load_dwordx4 v[172:175],v223,s[36:39],s67 offen\n"
"buffer_load_dwordx4 v[176:179],v224,s[36:39],s67 offen\n"
"v_mfma_f32_16x16x32_bf16 v[48:51],v[192:195],v[212:215],v[48:51]\n"
"v_perm_b32 v196,v102,v98,s19\n"
"v_perm_b32 v197,v110,v106,s19\n"
"v_mfma_f32_16x16x32_bf16 v[52:55],v[192:195],v[216:219],v[52:55]\n"
"v_perm_b32 v198,v118,v114,s19\n"
"v_perm_b32 v199,v126,v122,s19\n"
"v_mfma_f32_16x16x32_bf16 v[56:59],v[192:195],v[238:241],v[56:59]\n"
"buffer_load_dwordx4 v[180:183],v225,s[36:39],s67 offen\n"
"buffer_load_dwordx4 v[184:187],v226,s[36:39],s67 offen\n"
"v_mfma_f32_16x16x32_bf16 v[60:63],v[196:199],v[212:215],v[60:63]\n"
"v_perm_b32 v192,v103,v99,s18\n"
"v_perm_b32 v193,v111,v107,s18\n"
"v_mfma_f32_16x16x32_bf16 v[64:67],v[196:199],v[216:219],v[64:67]\n"
"v_perm_b32 v194,v119,v115,s18\n"
"v_mfma_f32_16x16x32_bf16 v[68:71],v[196:199],v[238:241],v[68:71]\n"
"v_perm_b32 v195,v127,v123,s18\n"
"buffer_load_dwordx4 v[188:191],v227,s[36:39],s67 offen\n"
"s_nop 0\n"
"v_mfma_f32_16x16x32_bf16 v[72:75],v[192:195],v[212:215],v[72:75]\n"
"v_perm_b32 v196,v103,v99,s19\n"
"v_mfma_f32_16x16x32_bf16 v[76:79],v[192:195],v[216:219],v[76:79]\n"
"v_perm_b32 v197,v111,v107,s19\n"
"v_perm_b32 v198,v119,v115,s19\n"
"v_mfma_f32_16x16x32_bf16 v[80:83],v[192:195],v[238:241],v[80:83]\n"
"v_perm_b32 v199,v127,v123,s19\n"
"s_nop 1\n"
"v_mfma_f32_16x16x32_bf16 v[84:87],v[196:199],v[212:215],v[84:87]\n"
"v_mfma_f32_16x16x32_bf16 v[88:91],v[196:199],v[216:219],v[88:91]\n"
"v_mfma_f32_16x16x32_bf16 v[92:95],v[196:199],v[238:241],v[92:95]\n"
"s_waitcnt vmcnt(14)\n"
"v_perm_b32 v192,v132,v128,s18\n"
"s_waitcnt vmcnt(12)\n"
"v_perm_b32 v193,v140,v136,s18\n"
"s_waitcnt vmcnt(10)\n"
"v_perm_b32 v194,v148,v144,s18\n"
"s_waitcnt vmcnt(8)\n"
"v_perm_b32 v195,v156,v152,s18\n"
"s_waitcnt lgkmcnt(0)\n"
"s_nop 0\n"
"v_mfma_f32_16x16x32_bf16 v[0:3],v[192:195],v[200:203],v[0:3]\n"
"v_perm_b32 v196,v132,v128,s19\n"
"v_perm_b32 v197,v140,v136,s19\n"
"v_perm_b32 v198,v148,v144,s19\n"
"v_mfma_f32_16x16x32_bf16 v[4:7],v[192:195],v[204:207],v[4:7]\n"
"v_perm_b32 v199,v156,v152,s19\n"
"s_add_u32 s60,s59,11\n"
"s_and_b32 s60,s60,15\n"
"s_lshl_b32 s60,s60,6\n"
"v_xor_b32 v229,s60,v228\n"
"ds_read_b128 v[212:215],v229\n"
"v_mfma_f32_16x16x32_bf16 v[8:11],v[192:195],v[208:211],v[8:11]\n"
"ds_read_b128 v[216:219],v229 offset:16384\n"
"v_mfma_f32_16x16x32_bf16 v[12:15],v[196:199],v[200:203],v[12:15]\n"
"v_perm_b32 v192,v133,v129,s18\n"
"v_perm_b32 v193,v141,v137,s18\n"
"v_perm_b32 v194,v149,v145,s18\n"
"v_mfma_f32_16x16x32_bf16 v[16:19],v[196:199],v[204:207],v[16:19]\n"
"v_perm_b32 v195,v157,v153,s18\n"
"ds_read_b128 v[238:241],v229 offset:32768\n"
"v_mfma_f32_16x16x32_bf16 v[20:23],v[196:199],v[208:211],v[20:23]\n"
"s_add_u32 s67,s59,12\n"
"s_and_b32 s67,s67,15\n"
"s_lshl_b32 s67,s67,18\n"
"buffer_load_dwordx4 v[96:99],v220,s[36:39],s67 offen\n"
"v_mfma_f32_16x16x32_bf16 v[24:27],v[192:195],v[200:203],v[24:27]\n"
"v_perm_b32 v196,v133,v129,s19\n"
"v_perm_b32 v197,v141,v137,s19\n"
"v_mfma_f32_16x16x32_bf16 v[28:31],v[192:195],v[204:207],v[28:31]\n"
"v_perm_b32 v198,v149,v145,s19\n"
"v_perm_b32 v199,v157,v153,s19\n"
"v_mfma_f32_16x16x32_bf16 v[32:35],v[192:195],v[208:211],v[32:35]\n"
"buffer_load_dwordx4 v[100:103],v221,s[36:39],s67 offen\n"
"buffer_load_dwordx4 v[104:107],v222,s[36:39],s67 offen\n"
"v_mfma_f32_16x16x32_bf16 v[36:39],v[196:199],v[200:203],v[36:39]\n"
"v_perm_b32 v192,v134,v130,s18\n"
"v_perm_b32 v193,v142,v138,s18\n"
"v_mfma_f32_16x16x32_bf16 v[40:43],v[196:199],v[204:207],v[40:43]\n"
"v_perm_b32 v194,v150,v146,s18\n"
"v_perm_b32 v195,v158,v154,s18\n"
"v_mfma_f32_16x16x32_bf16 v[44:47],v[196:199],v[208:211],v[44:47]\n"
"buffer_load_dwordx4 v[108:111],v223,s[36:39],s67 offen\n"
"buffer_load_dwordx4 v[112:115],v224,s[36:39],s67 offen\n"
"v_mfma_f32_16x16x32_bf16 v[48:51],v[192:195],v[200:203],v[48:51]\n"
"v_perm_b32 v196,v134,v130,s19\n"
"v_perm_b32 v197,v142,v138,s19\n"
"v_mfma_f32_16x16x32_bf16 v[52:55],v[192:195],v[204:207],v[52:55]\n"
"v_perm_b32 v198,v150,v146,s19\n"
"v_perm_b32 v199,v158,v154,s19\n"
"v_mfma_f32_16x16x32_bf16 v[56:59],v[192:195],v[208:211],v[56:59]\n"
"buffer_load_dwordx4 v[116:119],v225,s[36:39],s67 offen\n"
"buffer_load_dwordx4 v[120:123],v226,s[36:39],s67 offen\n"
"v_mfma_f32_16x16x32_bf16 v[60:63],v[196:199],v[200:203],v[60:63]\n"
"v_perm_b32 v192,v135,v131,s18\n"
"v_perm_b32 v193,v143,v139,s18\n"
"v_mfma_f32_16x16x32_bf16 v[64:67],v[196:199],v[204:207],v[64:67]\n"
"v_perm_b32 v194,v151,v147,s18\n"
"v_mfma_f32_16x16x32_bf16 v[68:71],v[196:199],v[208:211],v[68:71]\n"
"v_perm_b32 v195,v159,v155,s18\n"
"buffer_load_dwordx4 v[124:127],v227,s[36:39],s67 offen\n"
"s_nop 0\n"
"v_mfma_f32_16x16x32_bf16 v[72:75],v[192:195],v[200:203],v[72:75]\n"
"v_perm_b32 v196,v135,v131,s19\n"
"v_mfma_f32_16x16x32_bf16 v[76:79],v[192:195],v[204:207],v[76:79]\n"
"v_perm_b32 v197,v143,v139,s19\n"
"v_perm_b32 v198,v151,v147,s19\n"
"v_mfma_f32_16x16x32_bf16 v[80:83],v[192:195],v[208:211],v[80:83]\n"
"v_perm_b32 v199,v159,v155,s19\n"
"s_nop 1\n"
"v_mfma_f32_16x16x32_bf16 v[84:87],v[196:199],v[200:203],v[84:87]\n"
"v_mfma_f32_16x16x32_bf16 v[88:91],v[196:199],v[204:207],v[88:91]\n"
"v_mfma_f32_16x16x32_bf16 v[92:95],v[196:199],v[208:211],v[92:95]\n"
"s_waitcnt vmcnt(14)\n"
"v_perm_b32 v192,v164,v160,s18\n"
"s_waitcnt vmcnt(12)\n"
"v_perm_b32 v193,v172,v168,s18\n"
"s_waitcnt vmcnt(10)\n"
"v_perm_b32 v194,v180,v176,s18\n"
"s_waitcnt vmcnt(8)\n"
"v_perm_b32 v195,v188,v184,s18\n"
"s_waitcnt lgkmcnt(0)\n"
"s_nop 0\n"
"v_mfma_f32_16x16x32_bf16 v[0:3],v[192:195],v[212:215],v[0:3]\n"
"v_perm_b32 v196,v164,v160,s19\n"
"v_perm_b32 v197,v172,v168,s19\n"
"v_perm_b32 v198,v180,v176,s19\n"
"v_mfma_f32_16x16x32_bf16 v[4:7],v[192:195],v[216:219],v[4:7]\n"
"v_perm_b32 v199,v188,v184,s19\n"
"s_add_u32 s60,s59,12\n"
"s_and_b32 s60,s60,15\n"
"s_lshl_b32 s60,s60,6\n"
"v_xor_b32 v229,s60,v228\n"
"ds_read_b128 v[200:203],v229\n"
"v_mfma_f32_16x16x32_bf16 v[8:11],v[192:195],v[238:241],v[8:11]\n"
"ds_read_b128 v[204:207],v229 offset:16384\n"
"v_mfma_f32_16x16x32_bf16 v[12:15],v[196:199],v[212:215],v[12:15]\n"
"v_perm_b32 v192,v165,v161,s18\n"
"v_perm_b32 v193,v173,v169,s18\n"
"v_perm_b32 v194,v181,v177,s18\n"
"v_mfma_f32_16x16x32_bf16 v[16:19],v[196:199],v[216:219],v[16:19]\n"
"v_perm_b32 v195,v189,v185,s18\n"
"ds_read_b128 v[208:211],v229 offset:32768\n"
"v_mfma_f32_16x16x32_bf16 v[20:23],v[196:199],v[238:241],v[20:23]\n"
"s_add_u32 s67,s59,13\n"
"s_and_b32 s67,s67,15\n"
"s_lshl_b32 s67,s67,18\n"
"buffer_load_dwordx4 v[128:131],v220,s[36:39],s67 offen\n"
"v_mfma_f32_16x16x32_bf16 v[24:27],v[192:195],v[212:215],v[24:27]\n"
"v_perm_b32 v196,v165,v161,s19\n"
"v_perm_b32 v197,v173,v169,s19\n"
"v_mfma_f32_16x16x32_bf16 v[28:31],v[192:195],v[216:219],v[28:31]\n"
"v_perm_b32 v198,v181,v177,s19\n"
"v_perm_b32 v199,v189,v185,s19\n"
"v_mfma_f32_16x16x32_bf16 v[32:35],v[192:195],v[238:241],v[32:35]\n"
"buffer_load_dwordx4 v[132:135],v221,s[36:39],s67 offen\n"
"buffer_load_dwordx4 v[136:139],v222,s[36:39],s67 offen\n"
"v_mfma_f32_16x16x32_bf16 v[36:39],v[196:199],v[212:215],v[36:39]\n"
"v_perm_b32 v192,v166,v162,s18\n"
"v_perm_b32 v193,v174,v170,s18\n"
"v_mfma_f32_16x16x32_bf16 v[40:43],v[196:199],v[216:219],v[40:43]\n"
"v_perm_b32 v194,v182,v178,s18\n"
"v_perm_b32 v195,v190,v186,s18\n"
"v_mfma_f32_16x16x32_bf16 v[44:47],v[196:199],v[238:241],v[44:47]\n"
"buffer_load_dwordx4 v[140:143],v223,s[36:39],s67 offen\n"
"buffer_load_dwordx4 v[144:147],v224,s[36:39],s67 offen\n"
"v_mfma_f32_16x16x32_bf16 v[48:51],v[192:195],v[212:215],v[48:51]\n"
"v_perm_b32 v196,v166,v162,s19\n"
"v_perm_b32 v197,v174,v170,s19\n"
"v_mfma_f32_16x16x32_bf16 v[52:55],v[192:195],v[216:219],v[52:55]\n"
"v_perm_b32 v198,v182,v178,s19\n"
"v_perm_b32 v199,v190,v186,s19\n"
"v_mfma_f32_16x16x32_bf16 v[56:59],v[192:195],v[238:241],v[56:59]\n"
"buffer_load_dwordx4 v[148:151],v225,s[36:39],s67 offen\n"
"buffer_load_dwordx4 v[152:155],v226,s[36:39],s67 offen\n"
"v_mfma_f32_16x16x32_bf16 v[60:63],v[196:199],v[212:215],v[60:63]\n"
"v_perm_b32 v192,v167,v163,s18\n"
"v_perm_b32 v193,v175,v171,s18\n"
"v_mfma_f32_16x16x32_bf16 v[64:67],v[196:199],v[216:219],v[64:67]\n"
"v_perm_b32 v194,v183,v179,s18\n"
"v_mfma_f32_16x16x32_bf16 v[68:71],v[196:199],v[238:241],v[68:71]\n"
"v_perm_b32 v195,v191,v187,s18\n"
"buffer_load_dwordx4 v[156:159],v227,s[36:39],s67 offen\n"
"s_nop 0\n"
"v_mfma_f32_16x16x32_bf16 v[72:75],v[192:195],v[212:215],v[72:75]\n"
"v_perm_b32 v196,v167,v163,s19\n"
"v_mfma_f32_16x16x32_bf16 v[76:79],v[192:195],v[216:219],v[76:79]\n"
"v_perm_b32 v197,v175,v171,s19\n"
"v_perm_b32 v198,v183,v179,s19\n"
"v_mfma_f32_16x16x32_bf16 v[80:83],v[192:195],v[238:241],v[80:83]\n"
"v_perm_b32 v199,v191,v187,s19\n"
"s_nop 1\n"
"v_mfma_f32_16x16x32_bf16 v[84:87],v[196:199],v[212:215],v[84:87]\n"
"v_mfma_f32_16x16x32_bf16 v[88:91],v[196:199],v[216:219],v[88:91]\n"
"v_mfma_f32_16x16x32_bf16 v[92:95],v[196:199],v[238:241],v[92:95]\n"
"s_waitcnt vmcnt(14)\n"
"v_perm_b32 v192,v100,v96,s18\n"
"s_waitcnt vmcnt(12)\n"
"v_perm_b32 v193,v108,v104,s18\n"
"s_waitcnt vmcnt(10)\n"
"v_perm_b32 v194,v116,v112,s18\n"
"s_waitcnt vmcnt(8)\n"
"v_perm_b32 v195,v124,v120,s18\n"
"s_waitcnt lgkmcnt(0)\n"
"s_nop 0\n"
"v_mfma_f32_16x16x32_bf16 v[0:3],v[192:195],v[200:203],v[0:3]\n"
"v_perm_b32 v196,v100,v96,s19\n"
"v_perm_b32 v197,v108,v104,s19\n"
"v_perm_b32 v198,v116,v112,s19\n"
"v_mfma_f32_16x16x32_bf16 v[4:7],v[192:195],v[204:207],v[4:7]\n"
"v_perm_b32 v199,v124,v120,s19\n"
"s_add_u32 s60,s59,13\n"
"s_and_b32 s60,s60,15\n"
"s_lshl_b32 s60,s60,6\n"
"v_xor_b32 v229,s60,v228\n"
"ds_read_b128 v[212:215],v229\n"
"v_mfma_f32_16x16x32_bf16 v[8:11],v[192:195],v[208:211],v[8:11]\n"
"ds_read_b128 v[216:219],v229 offset:16384\n"
"v_mfma_f32_16x16x32_bf16 v[12:15],v[196:199],v[200:203],v[12:15]\n"
"v_perm_b32 v192,v101,v97,s18\n"
"v_perm_b32 v193,v109,v105,s18\n"
"v_perm_b32 v194,v117,v113,s18\n"
"v_mfma_f32_16x16x32_bf16 v[16:19],v[196:199],v[204:207],v[16:19]\n"
"v_perm_b32 v195,v125,v121,s18\n"
"ds_read_b128 v[238:241],v229 offset:32768\n"
"v_mfma_f32_16x16x32_bf16 v[20:23],v[196:199],v[208:211],v[20:23]\n"
"s_add_u32 s67,s59,14\n"
"s_and_b32 s67,s67,15\n"
"s_lshl_b32 s67,s67,18\n"
"buffer_load_dwordx4 v[160:163],v220,s[36:39],s67 offen\n"
"v_mfma_f32_16x16x32_bf16 v[24:27],v[192:195],v[200:203],v[24:27]\n"
"v_perm_b32 v196,v101,v97,s19\n"
"v_perm_b32 v197,v109,v105,s19\n"
"v_mfma_f32_16x16x32_bf16 v[28:31],v[192:195],v[204:207],v[28:31]\n"
"v_perm_b32 v198,v117,v113,s19\n"
"v_perm_b32 v199,v125,v121,s19\n"
"v_mfma_f32_16x16x32_bf16 v[32:35],v[192:195],v[208:211],v[32:35]\n"
"buffer_load_dwordx4 v[164:167],v221,s[36:39],s67 offen\n"
"buffer_load_dwordx4 v[168:171],v222,s[36:39],s67 offen\n"
"v_mfma_f32_16x16x32_bf16 v[36:39],v[196:199],v[200:203],v[36:39]\n"
"v_perm_b32 v192,v102,v98,s18\n"
"v_perm_b32 v193,v110,v106,s18\n"
"v_mfma_f32_16x16x32_bf16 v[40:43],v[196:199],v[204:207],v[40:43]\n"
"v_perm_b32 v194,v118,v114,s18\n"
"v_perm_b32 v195,v126,v122,s18\n"
"v_mfma_f32_16x16x32_bf16 v[44:47],v[196:199],v[208:211],v[44:47]\n"
"buffer_load_dwordx4 v[172:175],v223,s[36:39],s67 offen\n"
"buffer_load_dwordx4 v[176:179],v224,s[36:39],s67 offen\n"
"v_mfma_f32_16x16x32_bf16 v[48:51],v[192:195],v[200:203],v[48:51]\n"
"v_perm_b32 v196,v102,v98,s19\n"
"v_perm_b32 v197,v110,v106,s19\n"
"v_mfma_f32_16x16x32_bf16 v[52:55],v[192:195],v[204:207],v[52:55]\n"
"v_perm_b32 v198,v118,v114,s19\n"
"v_perm_b32 v199,v126,v122,s19\n"
"v_mfma_f32_16x16x32_bf16 v[56:59],v[192:195],v[208:211],v[56:59]\n"
"buffer_load_dwordx4 v[180:183],v225,s[36:39],s67 offen\n"
"buffer_load_dwordx4 v[184:187],v226,s[36:39],s67 offen\n"
"v_mfma_f32_16x16x32_bf16 v[60:63],v[196:199],v[200:203],v[60:63]\n"
"v_perm_b32 v192,v103,v99,s18\n"
"v_perm_b32 v193,v111,v107,s18\n"
"v_mfma_f32_16x16x32_bf16 v[64:67],v[196:199],v[204:207],v[64:67]\n"
"v_perm_b32 v194,v119,v115,s18\n"
"v_mfma_f32_16x16x32_bf16 v[68:71],v[196:199],v[208:211],v[68:71]\n"
"v_perm_b32 v195,v127,v123,s18\n"
"buffer_load_dwordx4 v[188:191],v227,s[36:39],s67 offen\n"
"s_nop 0\n"
"v_mfma_f32_16x16x32_bf16 v[72:75],v[192:195],v[200:203],v[72:75]\n"
"v_perm_b32 v196,v103,v99,s19\n"
"v_mfma_f32_16x16x32_bf16 v[76:79],v[192:195],v[204:207],v[76:79]\n"
"v_perm_b32 v197,v111,v107,s19\n"
"v_perm_b32 v198,v119,v115,s19\n"
"v_mfma_f32_16x16x32_bf16 v[80:83],v[192:195],v[208:211],v[80:83]\n"
"v_perm_b32 v199,v127,v123,s19\n"
"s_nop 1\n"
"v_mfma_f32_16x16x32_bf16 v[84:87],v[196:199],v[200:203],v[84:87]\n"
"v_mfma_f32_16x16x32_bf16 v[88:91],v[196:199],v[204:207],v[88:91]\n"
"v_mfma_f32_16x16x32_bf16 v[92:95],v[196:199],v[208:211],v[92:95]\n"
"s_waitcnt vmcnt(14)\n"
"v_perm_b32 v192,v132,v128,s18\n"
"s_waitcnt vmcnt(12)\n"
"v_perm_b32 v193,v140,v136,s18\n"
"s_waitcnt vmcnt(10)\n"
"v_perm_b32 v194,v148,v144,s18\n"
"s_waitcnt vmcnt(8)\n"
"v_perm_b32 v195,v156,v152,s18\n"
"s_waitcnt lgkmcnt(0)\n"
"s_nop 0\n"
"v_mfma_f32_16x16x32_bf16 v[0:3],v[192:195],v[212:215],v[0:3]\n"
"v_perm_b32 v196,v132,v128,s19\n"
"v_perm_b32 v197,v140,v136,s19\n"
"v_perm_b32 v198,v148,v144,s19\n"
"v_mfma_f32_16x16x32_bf16 v[4:7],v[192:195],v[216:219],v[4:7]\n"
"v_perm_b32 v199,v156,v152,s19\n"
"s_add_u32 s60,s59,14\n"
"s_and_b32 s60,s60,15\n"
"s_lshl_b32 s60,s60,6\n"
"v_xor_b32 v229,s60,v228\n"
"ds_read_b128 v[200:203],v229\n"
"v_mfma_f32_16x16x32_bf16 v[8:11],v[192:195],v[238:241],v[8:11]\n"
"ds_read_b128 v[204:207],v229 offset:16384\n"
"v_mfma_f32_16x16x32_bf16 v[12:15],v[196:199],v[212:215],v[12:15]\n"
"v_perm_b32 v192,v133,v129,s18\n"
"v_perm_b32 v193,v141,v137,s18\n"
"v_perm_b32 v194,v149,v145,s18\n"
"v_mfma_f32_16x16x32_bf16 v[16:19],v[196:199],v[216:219],v[16:19]\n"
"v_perm_b32 v195,v157,v153,s18\n"
"ds_read_b128 v[208:211],v229 offset:32768\n"
"v_mfma_f32_16x16x32_bf16 v[20:23],v[196:199],v[238:241],v[20:23]\n"
"s_add_u32 s67,s59,15\n"
"s_and_b32 s67,s67,15\n"
"s_lshl_b32 s67,s67,18\n"
"buffer_load_dwordx4 v[96:99],v220,s[36:39],s67 offen\n"
"v_mfma_f32_16x16x32_bf16 v[24:27],v[192:195],v[212:215],v[24:27]\n"
"v_perm_b32 v196,v133,v129,s19\n"
"v_perm_b32 v197,v141,v137,s19\n"
"v_mfma_f32_16x16x32_bf16 v[28:31],v[192:195],v[216:219],v[28:31]\n"
"v_perm_b32 v198,v149,v145,s19\n"
"v_perm_b32 v199,v157,v153,s19\n"
"v_mfma_f32_16x16x32_bf16 v[32:35],v[192:195],v[238:241],v[32:35]\n"
"buffer_load_dwordx4 v[100:103],v221,s[36:39],s67 offen\n"
"buffer_load_dwordx4 v[104:107],v222,s[36:39],s67 offen\n"
"v_mfma_f32_16x16x32_bf16 v[36:39],v[196:199],v[212:215],v[36:39]\n"
"v_perm_b32 v192,v134,v130,s18\n"
"v_perm_b32 v193,v142,v138,s18\n"
"v_mfma_f32_16x16x32_bf16 v[40:43],v[196:199],v[216:219],v[40:43]\n"
"v_perm_b32 v194,v150,v146,s18\n"
"v_perm_b32 v195,v158,v154,s18\n"
"v_mfma_f32_16x16x32_bf16 v[44:47],v[196:199],v[238:241],v[44:47]\n"
"buffer_load_dwordx4 v[108:111],v223,s[36:39],s67 offen\n"
"buffer_load_dwordx4 v[112:115],v224,s[36:39],s67 offen\n"
"v_mfma_f32_16x16x32_bf16 v[48:51],v[192:195],v[212:215],v[48:51]\n"
"v_perm_b32 v196,v134,v130,s19\n"
"v_perm_b32 v197,v142,v138,s19\n"
"v_mfma_f32_16x16x32_bf16 v[52:55],v[192:195],v[216:219],v[52:55]\n"
"v_perm_b32 v198,v150,v146,s19\n"
"v_perm_b32 v199,v158,v154,s19\n"
"v_mfma_f32_16x16x32_bf16 v[56:59],v[192:195],v[238:241],v[56:59]\n"
"buffer_load_dwordx4 v[116:119],v225,s[36:39],s67 offen\n"
"buffer_load_dwordx4 v[120:123],v226,s[36:39],s67 offen\n"
"v_mfma_f32_16x16x32_bf16 v[60:63],v[196:199],v[212:215],v[60:63]\n"
"v_perm_b32 v192,v135,v131,s18\n"
"v_perm_b32 v193,v143,v139,s18\n"
"v_mfma_f32_16x16x32_bf16 v[64:67],v[196:199],v[216:219],v[64:67]\n"
"v_perm_b32 v194,v151,v147,s18\n"
"v_mfma_f32_16x16x32_bf16 v[68:71],v[196:199],v[238:241],v[68:71]\n"
"v_perm_b32 v195,v159,v155,s18\n"
"buffer_load_dwordx4 v[124:127],v227,s[36:39],s67 offen\n"
"s_nop 0\n"
"v_mfma_f32_16x16x32_bf16 v[72:75],v[192:195],v[212:215],v[72:75]\n"
"v_perm_b32 v196,v135,v131,s19\n"
"v_mfma_f32_16x16x32_bf16 v[76:79],v[192:195],v[216:219],v[76:79]\n"
"v_perm_b32 v197,v143,v139,s19\n"
"v_perm_b32 v198,v151,v147,s19\n"
"v_mfma_f32_16x16x32_bf16 v[80:83],v[192:195],v[238:241],v[80:83]\n"
"v_perm_b32 v199,v159,v155,s19\n"
"s_nop 1\n"
"v_mfma_f32_16x16x32_bf16 v[84:87],v[196:199],v[212:215],v[84:87]\n"
"v_mfma_f32_16x16x32_bf16 v[88:91],v[196:199],v[216:219],v[88:91]\n"
"v_mfma_f32_16x16x32_bf16 v[92:95],v[196:199],v[238:241],v[92:95]\n"
"s_waitcnt vmcnt(14)\n"
"v_perm_b32 v192,v164,v160,s18\n"
"s_waitcnt vmcnt(12)\n"
"v_perm_b32 v193,v172,v168,s18\n"
"s_waitcnt vmcnt(10)\n"
"v_perm_b32 v194,v180,v176,s18\n"
"s_waitcnt vmcnt(8)\n"
"v_perm_b32 v195,v188,v184,s18\n"
"s_waitcnt lgkmcnt(0)\n"
"s_nop 0\n"
"v_mfma_f32_16x16x32_bf16 v[0:3],v[192:195],v[200:203],v[0:3]\n"
"v_perm_b32 v196,v164,v160,s19\n"
"v_perm_b32 v197,v172,v168,s19\n"
"v_perm_b32 v198,v180,v176,s19\n"
"v_mfma_f32_16x16x32_bf16 v[4:7],v[192:195],v[204:207],v[4:7]\n"
"v_perm_b32 v199,v188,v184,s19\n"
"v_mfma_f32_16x16x32_bf16 v[8:11],v[192:195],v[208:211],v[8:11]\n"
"s_add_u32 s60,s59,15\n"
"s_and_b32 s60,s60,15\n"
"s_lshl_b32 s60,s60,6\n"
"v_xor_b32 v229,s60,v228\n"
"ds_read_b128 v[212:215],v229\n"
"v_mfma_f32_16x16x32_bf16 v[12:15],v[196:199],v[200:203],v[12:15]\n"
"v_perm_b32 v192,v165,v161,s18\n"
"v_perm_b32 v193,v173,v169,s18\n"
"v_mfma_f32_16x16x32_bf16 v[16:19],v[196:199],v[204:207],v[16:19]\n"
"v_perm_b32 v194,v181,v177,s18\n"
"v_mfma_f32_16x16x32_bf16 v[20:23],v[196:199],v[208:211],v[20:23]\n"
"v_perm_b32 v195,v189,v185,s18\n"
"ds_read_b128 v[216:219],v229 offset:16384\n"
"s_nop 0\n"
"v_mfma_f32_16x16x32_bf16 v[24:27],v[192:195],v[200:203],v[24:27]\n"
"v_perm_b32 v196,v165,v161,s19\n"
"v_perm_b32 v197,v173,v169,s19\n"
"v_mfma_f32_16x16x32_bf16 v[28:31],v[192:195],v[204:207],v[28:31]\n"
"v_perm_b32 v198,v181,v177,s19\n"
"v_mfma_f32_16x16x32_bf16 v[32:35],v[192:195],v[208:211],v[32:35]\n"
"v_perm_b32 v199,v189,v185,s19\n"
"ds_read_b128 v[238:241],v229 offset:32768\n"
"s_nop 0\n"
"v_mfma_f32_16x16x32_bf16 v[36:39],v[196:199],v[200:203],v[36:39]\n"
"v_perm_b32 v192,v166,v162,s18\n"
"v_mfma_f32_16x16x32_bf16 v[40:43],v[196:199],v[204:207],v[40:43]\n"
"v_perm_b32 v193,v174,v170,s18\n"
"v_perm_b32 v194,v182,v178,s18\n"
"v_mfma_f32_16x16x32_bf16 v[44:47],v[196:199],v[208:211],v[44:47]\n"
"v_perm_b32 v195,v190,v186,s18\n"
"s_nop 1\n"
"v_mfma_f32_16x16x32_bf16 v[48:51],v[192:195],v[200:203],v[48:51]\n"
"v_perm_b32 v196,v166,v162,s19\n"
"v_mfma_f32_16x16x32_bf16 v[52:55],v[192:195],v[204:207],v[52:55]\n"
"v_perm_b32 v197,v174,v170,s19\n"
"v_perm_b32 v198,v182,v178,s19\n"
"v_mfma_f32_16x16x32_bf16 v[56:59],v[192:195],v[208:211],v[56:59]\n"
"v_perm_b32 v199,v190,v186,s19\n"
"s_nop 1\n"
"v_mfma_f32_16x16x32_bf16 v[60:63],v[196:199],v[200:203],v[60:63]\n"
"v_perm_b32 v192,v167,v163,s18\n"
"v_mfma_f32_16x16x32_bf16 v[64:67],v[196:199],v[204:207],v[64:67]\n"
"v_perm_b32 v193,v175,v171,s18\n"
"v_perm_b32 v194,v183,v179,s18\n"
"v_mfma_f32_16x16x32_bf16 v[68:71],v[196:199],v[208:211],v[68:71]\n"
"v_perm_b32 v195,v191,v187,s18\n"
"s_nop 1\n"
"v_mfma_f32_16x16x32_bf16 v[72:75],v[192:195],v[200:203],v[72:75]\n"
"v_perm_b32 v196,v167,v163,s19\n"
"v_mfma_f32_16x16x32_bf16 v[76:79],v[192:195],v[204:207],v[76:79]\n"
"v_perm_b32 v197,v175,v171,s19\n"
"v_perm_b32 v198,v183,v179,s19\n"
"v_mfma_f32_16x16x32_bf16 v[80:83],v[192:195],v[208:211],v[80:83]\n"
"v_perm_b32 v199,v191,v187,s19\n"
"s_nop 1\n"
"v_mfma_f32_16x16x32_bf16 v[84:87],v[196:199],v[200:203],v[84:87]\n"
"v_mfma_f32_16x16x32_bf16 v[88:91],v[196:199],v[204:207],v[88:91]\n"
"v_mfma_f32_16x16x32_bf16 v[92:95],v[196:199],v[208:211],v[92:95]\n"
"s_waitcnt vmcnt(6)\n"
"v_perm_b32 v192,v100,v96,s18\n"
"s_waitcnt vmcnt(4)\n"
"v_perm_b32 v193,v108,v104,s18\n"
"s_waitcnt vmcnt(2)\n"
"v_perm_b32 v194,v116,v112,s18\n"
"s_waitcnt vmcnt(0)\n"
"v_perm_b32 v195,v124,v120,s18\n"
"s_waitcnt lgkmcnt(0)\n"
"s_nop 0\n"
"v_mfma_f32_16x16x32_bf16 v[0:3],v[192:195],v[212:215],v[0:3]\n"
"v_perm_b32 v196,v100,v96,s19\n"
"v_mfma_f32_16x16x32_bf16 v[4:7],v[192:195],v[216:219],v[4:7]\n"
"v_perm_b32 v197,v108,v104,s19\n"
"v_perm_b32 v198,v116,v112,s19\n"
"v_mfma_f32_16x16x32_bf16 v[8:11],v[192:195],v[238:241],v[8:11]\n"
"v_perm_b32 v199,v124,v120,s19\n"
"s_nop 1\n"
"v_mfma_f32_16x16x32_bf16 v[12:15],v[196:199],v[212:215],v[12:15]\n"
"v_perm_b32 v192,v101,v97,s18\n"
"v_mfma_f32_16x16x32_bf16 v[16:19],v[196:199],v[216:219],v[16:19]\n"
"v_perm_b32 v193,v109,v105,s18\n"
"v_perm_b32 v194,v117,v113,s18\n"
"v_mfma_f32_16x16x32_bf16 v[20:23],v[196:199],v[238:241],v[20:23]\n"
"v_perm_b32 v195,v125,v121,s18\n"
"s_nop 1\n"
"v_mfma_f32_16x16x32_bf16 v[24:27],v[192:195],v[212:215],v[24:27]\n"
"v_perm_b32 v196,v101,v97,s19\n"
"v_mfma_f32_16x16x32_bf16 v[28:31],v[192:195],v[216:219],v[28:31]\n"
"v_perm_b32 v197,v109,v105,s19\n"
"v_perm_b32 v198,v117,v113,s19\n"
"v_mfma_f32_16x16x32_bf16 v[32:35],v[192:195],v[238:241],v[32:35]\n"
"v_perm_b32 v199,v125,v121,s19\n"
"s_nop 1\n"
"v_mfma_f32_16x16x32_bf16 v[36:39],v[196:199],v[212:215],v[36:39]\n"
"v_perm_b32 v192,v102,v98,s18\n"
"v_mfma_f32_16x16x32_bf16 v[40:43],v[196:199],v[216:219],v[40:43]\n"
"v_perm_b32 v193,v110,v106,s18\n"
"v_perm_b32 v194,v118,v114,s18\n"
"v_mfma_f32_16x16x32_bf16 v[44:47],v[196:199],v[238:241],v[44:47]\n"
"v_perm_b32 v195,v126,v122,s18\n"
"s_nop 1\n"
"v_mfma_f32_16x16x32_bf16 v[48:51],v[192:195],v[212:215],v[48:51]\n"
"v_perm_b32 v196,v102,v98,s19\n"
"v_mfma_f32_16x16x32_bf16 v[52:55],v[192:195],v[216:219],v[52:55]\n"
"v_perm_b32 v197,v110,v106,s19\n"
"v_perm_b32 v198,v118,v114,s19\n"
"v_mfma_f32_16x16x32_bf16 v[56:59],v[192:195],v[238:241],v[56:59]\n"
"v_perm_b32 v199,v126,v122,s19\n"
"s_nop 1\n"
"v_mfma_f32_16x16x32_bf16 v[60:63],v[196:199],v[212:215],v[60:63]\n"
"v_perm_b32 v192,v103,v99,s18\n"
"v_mfma_f32_16x16x32_bf16 v[64:67],v[196:199],v[216:219],v[64:67]\n"
"v_perm_b32 v193,v111,v107,s18\n"
"v_perm_b32 v194,v119,v115,s18\n"
"v_mfma_f32_16x16x32_bf16 v[68:71],v[196:199],v[238:241],v[68:71]\n"
"v_perm_b32 v195,v127,v123,s18\n"
"s_nop 1\n"
"v_mfma_f32_16x16x32_bf16 v[72:75],v[192:195],v[212:215],v[72:75]\n"
"v_perm_b32 v196,v103,v99,s19\n"
"v_mfma_f32_16x16x32_bf16 v[76:79],v[192:195],v[216:219],v[76:79]\n"
"v_perm_b32 v197,v111,v107,s19\n"
"v_perm_b32 v198,v119,v115,s19\n"
"v_mfma_f32_16x16x32_bf16 v[80:83],v[192:195],v[238:241],v[80:83]\n"
"v_perm_b32 v199,v127,v123,s19\n"
"s_nop 1\n"
"v_mfma_f32_16x16x32_bf16 v[84:87],v[196:199],v[212:215],v[84:87]\n"
"v_mfma_f32_16x16x32_bf16 v[88:91],v[196:199],v[216:219],v[88:91]\n"
"v_mfma_f32_16x16x32_bf16 v[92:95],v[196:199],v[238:241],v[92:95]\n"
"v_add_u32 v253,s66,v242\n"
"v_add_u32 v254,0x10000,v253\n"
"v_add_u32 v255,0x20000,v253\n"
"s_barrier\n"
"ds_write_b128 v254,a[64:67] offset:49152\n"
"ds_write_b128 v254,a[68:71] offset:50176\n"
"ds_write_b128 v254,a[72:75] offset:57344\n"
"ds_write_b128 v254,a[76:79] offset:58368\n"
"ds_write_b128 v255,a[80:83]\n"
"ds_write_b128 v255,a[84:87] offset:1024\n"
"ds_write_b128 v255,a[88:91] offset:8192\n"
"ds_write_b128 v255,a[92:95] offset:9216\n"
"ds_write_b128 v255,a[96:99] offset:16384\n"
"ds_write_b128 v255,a[100:103] offset:17408\n"
"ds_write_b128 v255,a[104:107] offset:24576\n"
"ds_write_b128 v255,a[108:111] offset:25600\n"
"v_mov_b32 v250,0x3e0293ee\n"
"v_pk_mul_f32 v[0:1],v[0:1],v[250:251] op_sel_hi:[1,0]\n"
"v_pk_mul_f32 v[2:3],v[2:3],v[250:251] op_sel_hi:[1,0]\n"
"v_pk_mul_f32 v[12:13],v[12:13],v[250:251] op_sel_hi:[1,0]\n"
"v_pk_mul_f32 v[14:15],v[14:15],v[250:251] op_sel_hi:[1,0]\n"
"v_pk_mul_f32 v[24:25],v[24:25],v[250:251] op_sel_hi:[1,0]\n"
"v_pk_mul_f32 v[26:27],v[26:27],v[250:251] op_sel_hi:[1,0]\n"
"v_pk_mul_f32 v[36:37],v[36:37],v[250:251] op_sel_hi:[1,0]\n"
"v_pk_mul_f32 v[38:39],v[38:39],v[250:251] op_sel_hi:[1,0]\n"
"v_pk_mul_f32 v[48:49],v[48:49],v[250:251] op_sel_hi:[1,0]\n"
"v_pk_mul_f32 v[50:51],v[50:51],v[250:251] op_sel_hi:[1,0]\n"
"v_pk_mul_f32 v[60:61],v[60:61],v[250:251] op_sel_hi:[1,0]\n"
"v_pk_mul_f32 v[62:63],v[62:63],v[250:251] op_sel_hi:[1,0]\n"
"v_pk_mul_f32 v[72:73],v[72:73],v[250:251] op_sel_hi:[1,0]\n"
"v_pk_mul_f32 v[74:75],v[74:75],v[250:251] op_sel_hi:[1,0]\n"
"v_pk_mul_f32 v[84:85],v[84:85],v[250:251] op_sel_hi:[1,0]\n"
"v_pk_mul_f32 v[86:87],v[86:87],v[250:251] op_sel_hi:[1,0]\n"
"v_cvt_pk_bf16_f32 v246,v0,v12\n"
"v_cvt_pk_bf16_f32 v247,v24,v36\n"
"v_cvt_pk_bf16_f32 v248,v48,v60\n"
"v_cvt_pk_bf16_f32 v249,v72,v84\n"
"v_accvgpr_write_b32 a0,v246\n"
"v_accvgpr_write_b32 a1,v247\n"
"v_accvgpr_write_b32 a2,v248\n"
"v_accvgpr_write_b32 a3,v249\n"
"v_cvt_pk_bf16_f32 v246,v1,v13\n"
"v_cvt_pk_bf16_f32 v247,v25,v37\n"
"v_cvt_pk_bf16_f32 v248,v49,v61\n"
"v_cvt_pk_bf16_f32 v249,v73,v85\n"
"v_accvgpr_write_b32 a12,v246\n"
"v_accvgpr_write_b32 a13,v247\n"
"v_accvgpr_write_b32 a14,v248\n"
"v_accvgpr_write_b32 a15,v249\n"
"v_cvt_pk_bf16_f32 v246,v2,v14\n"
"v_cvt_pk_bf16_f32 v247,v26,v38\n"
"v_cvt_pk_bf16_f32 v248,v50,v62\n"
"v_cvt_pk_bf16_f32 v249,v74,v86\n"
"v_accvgpr_write_b32 a24,v246\n"
"v_accvgpr_write_b32 a25,v247\n"
"v_accvgpr_write_b32 a26,v248\n"
"v_accvgpr_write_b32 a27,v249\n"
"v_cvt_pk_bf16_f32 v246,v3,v15\n"
"v_cvt_pk_bf16_f32 v247,v27,v39\n"
"v_cvt_pk_bf16_f32 v248,v51,v63\n"
"v_cvt_pk_bf16_f32 v249,v75,v87\n"
"v_accvgpr_write_b32 a36,v246\n"
"v_accvgpr_write_b32 a37,v247\n"
"v_accvgpr_write_b32 a38,v248\n"
"v_accvgpr_write_b32 a39,v249\n"
"v_pk_mul_f32 v[4:5],v[4:5],v[250:251] op_sel_hi:[1,0]\n"
"v_pk_mul_f32 v[6:7],v[6:7],v[250:251] op_sel_hi:[1,0]\n"
"v_pk_mul_f32 v[16:17],v[16:17],v[250:251] op_sel_hi:[1,0]\n"
"v_pk_mul_f32 v[18:19],v[18:19],v[250:251] op_sel_hi:[1,0]\n"
"v_pk_mul_f32 v[28:29],v[28:29],v[250:251] op_sel_hi:[1,0]\n"
"v_pk_mul_f32 v[30:31],v[30:31],v[250:251] op_sel_hi:[1,0]\n"
"v_pk_mul_f32 v[40:41],v[40:41],v[250:251] op_sel_hi:[1,0]\n"
"v_pk_mul_f32 v[42:43],v[42:43],v[250:251] op_sel_hi:[1,0]\n"
"v_pk_mul_f32 v[52:53],v[52:53],v[250:251] op_sel_hi:[1,0]\n"
"v_pk_mul_f32 v[54:55],v[54:55],v[250:251] op_sel_hi:[1,0]\n"
"v_pk_mul_f32 v[64:65],v[64:65],v[250:251] op_sel_hi:[1,0]\n"
"v_pk_mul_f32 v[66:67],v[66:67],v[250:251] op_sel_hi:[1,0]\n"
"v_pk_mul_f32 v[76:77],v[76:77],v[250:251] op_sel_hi:[1,0]\n"
"v_pk_mul_f32 v[78:79],v[78:79],v[250:251] op_sel_hi:[1,0]\n"
"v_pk_mul_f32 v[88:89],v[88:89],v[250:251] op_sel_hi:[1,0]\n"
"v_pk_mul_f32 v[90:91],v[90:91],v[250:251] op_sel_hi:[1,0]\n"
"v_cvt_pk_bf16_f32 v246,v4,v16\n"
"v_cvt_pk_bf16_f32 v247,v28,v40\n"
"v_cvt_pk_bf16_f32 v248,v52,v64\n"
"v_cvt_pk_bf16_f32 v249,v76,v88\n"
"v_accvgpr_write_b32 a4,v246\n"
"v_accvgpr_write_b32 a5,v247\n"
"v_accvgpr_write_b32 a6,v248\n"
"v_accvgpr_write_b32 a7,v249\n"
"v_cvt_pk_bf16_f32 v246,v5,v17\n"
"v_cvt_pk_bf16_f32 v247,v29,v41\n"
"v_cvt_pk_bf16_f32 v248,v53,v65\n"
"v_cvt_pk_bf16_f32 v249,v77,v89\n"
"v_accvgpr_write_b32 a16,v246\n"
"v_accvgpr_write_b32 a17,v247\n"
"v_accvgpr_write_b32 a18,v248\n"
"v_accvgpr_write_b32 a19,v249\n"
"v_cvt_pk_bf16_f32 v246,v6,v18\n"
"v_cvt_pk_bf16_f32 v247,v30,v42\n"
"v_cvt_pk_bf16_f32 v248,v54,v66\n"
"v_cvt_pk_bf16_f32 v249,v78,v90\n"
"v_accvgpr_write_b32 a28,v246\n"
"v_accvgpr_write_b32 a29,v247\n"
"v_accvgpr_write_b32 a30,v248\n"
"v_accvgpr_write_b32 a31,v249\n"
"v_cvt_pk_bf16_f32 v246,v7,v19\n"
"v_cvt_pk_bf16_f32 v247,v31,v43\n"
"v_cvt_pk_bf16_f32 v248,v55,v67\n"
"v_cvt_pk_bf16_f32 v249,v79,v91\n"
"v_accvgpr_write_b32 a40,v246\n"
"v_accvgpr_write_b32 a41,v247\n"
"v_accvgpr_write_b32 a42,v248\n"
"v_accvgpr_write_b32 a43,v249\n"
"v_pk_mul_f32 v[8:9],v[8:9],v[250:251] op_sel_hi:[1,0]\n"
"v_pk_mul_f32 v[10:11],v[10:11],v[250:251] op_sel_hi:[1,0]\n"
"v_pk_mul_f32 v[20:21],v[20:21],v[250:251] op_sel_hi:[1,0]\n"
"v_pk_mul_f32 v[22:23],v[22:23],v[250:251] op_sel_hi:[1,0]\n"
"v_pk_mul_f32 v[32:33],v[32:33],v[250:251] op_sel_hi:[1,0]\n"
"v_pk_mul_f32 v[34:35],v[34:35],v[250:251] op_sel_hi:[1,0]\n"
"v_pk_mul_f32 v[44:45],v[44:45],v[250:251] op_sel_hi:[1,0]\n"
"v_pk_mul_f32 v[46:47],v[46:47],v[250:251] op_sel_hi:[1,0]\n"
"v_pk_mul_f32 v[56:57],v[56:57],v[250:251] op_sel_hi:[1,0]\n"
"v_pk_mul_f32 v[58:59],v[58:59],v[250:251] op_sel_hi:[1,0]\n"
"v_pk_mul_f32 v[68:69],v[68:69],v[250:251] op_sel_hi:[1,0]\n"
"v_pk_mul_f32 v[70:71],v[70:71],v[250:251] op_sel_hi:[1,0]\n"
"v_pk_mul_f32 v[80:81],v[80:81],v[250:251] op_sel_hi:[1,0]\n"
"v_pk_mul_f32 v[82:83],v[82:83],v[250:251] op_sel_hi:[1,0]\n"
"v_pk_mul_f32 v[92:93],v[92:93],v[250:251] op_sel_hi:[1,0]\n"
"v_pk_mul_f32 v[94:95],v[94:95],v[250:251] op_sel_hi:[1,0]\n"
"v_cvt_pk_bf16_f32 v246,v8,v20\n"
"v_cvt_pk_bf16_f32 v247,v32,v44\n"
"v_cvt_pk_bf16_f32 v248,v56,v68\n"
"v_cvt_pk_bf16_f32 v249,v80,v92\n"
"v_accvgpr_write_b32 a8,v246\n"
"v_accvgpr_write_b32 a9,v247\n"
"v_accvgpr_write_b32 a10,v248\n"
"v_accvgpr_write_b32 a11,v249\n"
"v_cvt_pk_bf16_f32 v246,v9,v21\n"
"v_cvt_pk_bf16_f32 v247,v33,v45\n"
"v_cvt_pk_bf16_f32 v248,v57,v69\n"
"v_cvt_pk_bf16_f32 v249,v81,v93\n"
"v_accvgpr_write_b32 a20,v246\n"
"v_accvgpr_write_b32 a21,v247\n"
"v_accvgpr_write_b32 a22,v248\n"
"v_accvgpr_write_b32 a23,v249\n"
"v_cvt_pk_bf16_f32 v246,v10,v22\n"
"v_cvt_pk_bf16_f32 v247,v34,v46\n"
"v_cvt_pk_bf16_f32 v248,v58,v70\n"
"v_cvt_pk_bf16_f32 v249,v82,v94\n"
"v_accvgpr_write_b32 a32,v246\n"
"v_accvgpr_write_b32 a33,v247\n"
"v_accvgpr_write_b32 a34,v248\n"
"v_accvgpr_write_b32 a35,v249\n"
"v_cvt_pk_bf16_f32 v246,v11,v23\n"
"v_cvt_pk_bf16_f32 v247,v35,v47\n"
"v_cvt_pk_bf16_f32 v248,v59,v71\n"
"v_cvt_pk_bf16_f32 v249,v83,v95\n"
"v_accvgpr_write_b32 a44,v246\n"
"v_accvgpr_write_b32 a45,v247\n"
"v_accvgpr_write_b32 a46,v248\n"
"v_accvgpr_write_b32 a47,v249\n"
"v_and_b32 v246,10,v243\n"
"v_and_b32 v247,1,v243\n"
"v_lshl_or_b32 v246,v247,2,v246\n"
"v_bfe_u32 v247,v243,2,1\n"
"v_or_b32 v246,v246,v247\n"
"v_lshlrev_b32 v247,2,v244\n"
"v_xor_b32 v246,v246,v247\n"
"v_lshlrev_b32 v246,4,v246\n"
"v_lshl_or_b32 v246,v243,8,v246\n"
"v_xor_b32 v220,0,v246\n"
"v_xor_b32 v221,16,v246\n"
"v_xor_b32 v222,32,v246\n"
"v_xor_b32 v223,48,v246\n"
"v_lshrrev_b32 v246,2,v243\n"
"v_lshl_or_b32 v246,v244,2,v246\n"
"v_and_b32 v247,7,v246\n"
"v_lshlrev_b32 v247,5,v247\n"
"v_lshl_or_b32 v247,v246,8,v247\n"
"v_and_b32 v248,3,v243\n"
"v_lshl_or_b32 v247,v248,3,v247\n"
"v_add_u32 v247,0x2000,v247\n"
"v_xor_b32 v224,0,v247\n"
"v_xor_b32 v225,32,v247\n"
"v_xor_b32 v226,64,v247\n"
"v_xor_b32 v227,0x60,v247\n"
"v_xor_b32 v228,0x80,v247\n"
"v_xor_b32 v229,0xa0,v247\n"
"v_xor_b32 v230,0xc0,v247\n"
"v_xor_b32 v231,0xe0,v247\n"
"v_lshlrev_b32 v246,1,v244\n"
"v_xor_b32 v246,v246,v243\n"
"v_xor_b32 v246,0,v246\n"
"v_lshlrev_b32 v246,4,v246\n"
"v_lshl_or_b32 v238,v244,10,v246\n"
"s_lshl_b32 s60,s7,8\n"
"s_add_u32 s60,s60,0\n"
"v_add_u32 v238,s60,v238\n"
"v_lshlrev_b32 v246,1,v244\n"
"v_xor_b32 v246,v246,v243\n"
"v_xor_b32 v246,8,v246\n"
"v_lshlrev_b32 v246,4,v246\n"
"v_lshl_or_b32 v239,v244,10,v246\n"
"s_lshl_b32 s60,s7,8\n"
"s_add_u32 s60,s60,0xc00\n"
"v_add_u32 v239,s60,v239\n"
"v_lshlrev_b32 v246,1,v244\n"
"v_xor_b32 v246,v246,v243\n"
"v_xor_b32 v246,0,v246\n"
"v_lshlrev_b32 v246,4,v246\n"
"v_lshl_or_b32 v240,v244,10,v246\n"
"s_lshl_b32 s60,s7,8\n"
"s_add_u32 s60,s60,0x1800\n"
"v_add_u32 v240,s60,v240\n"
"v_lshlrev_b32 v246,1,v244\n"
"v_xor_b32 v246,v246,v243\n"
"v_xor_b32 v246,8,v246\n"
"v_lshlrev_b32 v246,4,v246\n"
"v_lshl_or_b32 v241,v244,10,v246\n"
"s_lshl_b32 s60,s7,8\n"
"s_add_u32 s60,s60,0x2400\n"
"v_add_u32 v241,s60,v241\n"
"s_lshl_b32 s3,s7,14\n"
"s_lshl_b32 s2,s7,13\n"
"ds_read_b128 v[156:159],v220\n"
"ds_read_b128 v[172:175],v220 offset:4096\n"
"ds_read_b128 v[160:163],v221\n"
"s_waitcnt lgkmcnt(3)\n"
"ds_read_b128 v[176:179],v221 offset:4096\n"
"ds_read_b128 v[164:167],v222\n"
"ds_read_b128 v[180:183],v222 offset:4096\n"
"ds_read_b128 v[168:171],v223\n"
"ds_read_b128 v[184:187],v223 offset:4096\n"
"s_waitcnt lgkmcnt(7)\n"
"v_mfma_f32_16x16x32_bf16 v[108:111],v[156:159],a[0:3],0\n"
"ds_read_b64_tr_b16 v[188:189],v224\n"
"s_waitcnt lgkmcnt(7)\n"
"v_mfma_f32_16x16x32_bf16 v[112:115],v[172:175],a[0:3],0\n"
"ds_read_b64_tr_b16 v[190:191],v224 offset:4096\n"
"ds_read_b64_tr_b16 v[192:193],v225\n"
"s_waitcnt lgkmcnt(8)\n"
"v_mfma_f32_16x16x32_bf16 v[108:111],v[160:163],a[12:15],v[108:111]\n"
"ds_read_b64_tr_b16 v[194:195],v225 offset:4096\n"
"s_waitcnt lgkmcnt(8)\n"
"v_mfma_f32_16x16x32_bf16 v[112:115],v[176:179],a[12:15],v[112:115]\n"
"ds_read_b64_tr_b16 v[196:197],v226\n"
"s_waitcnt lgkmcnt(8)\n"
"v_mfma_f32_16x16x32_bf16 v[108:111],v[164:167],a[24:27],v[108:111]\n"
"ds_read_b64_tr_b16 v[198:199],v226 offset:4096\n"
"s_waitcnt lgkmcnt(8)\n"
"v_mfma_f32_16x16x32_bf16 v[112:115],v[180:183],a[24:27],v[112:115]\n"
"ds_read_b64_tr_b16 v[200:201],v227\n"
"ds_read_b64_tr_b16 v[202:203],v227 offset:4096\n"
"s_waitcnt lgkmcnt(9)\n"
"v_mfma_f32_16x16x32_bf16 v[108:111],v[168:171],a[36:39],v[108:111]\n"
"ds_read_b64_tr_b16 v[204:205],v228\n"
"s_waitcnt lgkmcnt(9)\n"
"v_mfma_f32_16x16x32_bf16 v[112:115],v[184:187],a[36:39],v[112:115]\n"
"ds_read_b64_tr_b16 v[206:207],v228 offset:4096\n"
"v_mfma_f32_16x16x32_bf16 v[116:119],v[156:159],a[4:7],0\n"
"ds_read_b64_tr_b16 v[208:209],v229\n"
"ds_read_b64_tr_b16 v[210:211],v229 offset:4096\n"
"ds_read_b64_tr_b16 v[212:213],v230\n"
"ds_read_b64_tr_b16 v[214:215],v230 offset:4096\n"
"ds_read_b64_tr_b16 v[216:217],v231\n"
"v_mfma_f32_16x16x32_bf16 v[120:123],v[172:175],a[4:7],0\n"
"s_waitcnt lgkmcnt(12)\n"
"ds_read_b64_tr_b16 v[218:219],v231 offset:4096\n"
"v_max3_f32 v246,v108,v109,v110\n"
"v_max3_f32 v246,v246,v111,v112\n"
"v_max3_f32 v246,v246,v113,v114\n"
"v_mfma_f32_16x16x32_bf16 v[116:119],v[160:163],a[16:19],v[116:119]\n"
"v_max_f32 v246,v246,v115\n"
"v_mov_b32 v249,v246\n"
"s_nop 1\n"
"v_permlane32_swap_b32 v246,v249\n"
"v_max_f32 v246,v246,v249\n"
"v_mov_b32 v249,v246\n"
"v_mfma_f32_16x16x32_bf16 v[120:123],v[176:179],a[16:19],v[120:123]\n"
"s_nop 0\n"
"v_permlane16_swap_b32 v246,v249\n"
"v_max_f32 v246,v246,v249\n"
"v_sub_f32 v108,v108,v246\n"
"v_sub_f32 v109,v109,v246\n"
"v_mfma_f32_16x16x32_bf16 v[116:119],v[164:167],a[28:31],v[116:119]\n"
"v_sub_f32 v110,v110,v246\n"
"v_sub_f32 v111,v111,v246\n"
"v_sub_f32 v112,v112,v246\n"
"v_sub_f32 v113,v113,v246\n"
"v_sub_f32 v114,v114,v246\n"
"v_mfma_f32_16x16x32_bf16 v[120:123],v[180:183],a[28:31],v[120:123]\n"
"v_sub_f32 v115,v115,v246\n"
"v_sub_f32 v132,0,v246\n"
"v_sub_f32 v133,0,v246\n"
"v_sub_f32 v134,0,v246\n"
"v_mfma_f32_16x16x32_bf16 v[116:119],v[168:171],a[40:43],v[116:119]\n"
"v_sub_f32 v135,0,v246\n"
"v_exp_f32 v108,v108\n"
"v_exp_f32 v109,v109\n"
"v_mfma_f32_16x16x32_bf16 v[120:123],v[184:187],a[40:43],v[120:123]\n"
"v_exp_f32 v110,v110\n"
"v_exp_f32 v111,v111\n"
"v_mfma_f32_16x16x32_bf16 v[124:127],v[156:159],a[8:11],0\n"
"v_cvt_pk_bf16_f32 v144,v108,v109\n"
"v_exp_f32 v112,v112\n"
"v_exp_f32 v113,v113\n"
"v_mfma_f32_16x16x32_bf16 v[128:131],v[172:175],a[8:11],0\n"
"v_cvt_pk_bf16_f32 v145,v110,v111\n"
"v_exp_f32 v114,v114\n"
"v_mfma_f32_16x16x32_bf16 v[124:127],v[160:163],a[20:23],v[124:127]\n"
"v_exp_f32 v115,v115\n"
"v_cvt_pk_bf16_f32 v146,v112,v113\n"
"v_cvt_pk_bf16_f32 v147,v114,v115\n"
"v_max3_f32 v247,v116,v117,v118\n"
"v_mfma_f32_16x16x32_bf16 v[128:131],v[176:179],a[20:23],v[128:131]\n"
"v_max3_f32 v247,v247,v119,v120\n"
"v_max3_f32 v247,v247,v121,v122\n"
"v_max_f32 v247,v247,v123\n"
"v_mov_b32 v249,v247\n"
"v_mfma_f32_16x16x32_bf16 v[124:127],v[164:167],a[32:35],v[124:127]\n"
"s_nop 0\n"
"v_permlane32_swap_b32 v247,v249\n"
"v_max_f32 v247,v247,v249\n"
"v_mov_b32 v249,v247\n"
"s_nop 1\n"
"v_permlane16_swap_b32 v247,v249\n"
"v_mfma_f32_16x16x32_bf16 v[128:131],v[180:183],a[32:35],v[128:131]\n"
"v_max_f32 v247,v247,v249\n"
"v_sub_f32 v116,v116,v247\n"
"v_sub_f32 v117,v117,v247\n"
"v_sub_f32 v118,v118,v247\n"
"v_sub_f32 v119,v119,v247\n"
"v_mfma_f32_16x16x32_bf16 v[124:127],v[168:171],a[44:47],v[124:127]\n"
"v_sub_f32 v120,v120,v247\n"
"v_sub_f32 v121,v121,v247\n"
"v_sub_f32 v122,v122,v247\n"
"v_sub_f32 v123,v123,v247\n"
"v_mfma_f32_16x16x32_bf16 v[128:131],v[184:187],a[44:47],v[128:131]\n"
"v_sub_f32 v136,0,v247\n"
"v_sub_f32 v137,0,v247\n"
"v_sub_f32 v138,0,v247\n"
"v_sub_f32 v139,0,v247\n"
"v_mfma_f32_16x16x32_bf16 v[0:3],v[188:191],v[144:147],0\n"
"v_exp_f32 v116,v116\n"
"v_exp_f32 v117,v117\n"
"s_waitcnt lgkmcnt(0)\n"
"v_mfma_f32_16x16x32_bf16 v[4:7],v[192:195],v[144:147],0\n"
"v_exp_f32 v118,v118\n"
"v_exp_f32 v119,v119\n"
"v_cvt_pk_bf16_f32 v148,v116,v117\n"
"v_mfma_f32_16x16x32_bf16 v[8:11],v[196:199],v[144:147],0\n"
"v_exp_f32 v120,v120\n"
"v_exp_f32 v121,v121\n"
"v_cvt_pk_bf16_f32 v149,v118,v119\n"
"v_mfma_f32_16x16x32_bf16 v[12:15],v[200:203],v[144:147],0\n"
"v_exp_f32 v122,v122\n"
"v_exp_f32 v123,v123\n"
"v_cvt_pk_bf16_f32 v150,v120,v121\n"
"v_mfma_f32_16x16x32_bf16 v[16:19],v[204:207],v[144:147],0\n"
"v_cvt_pk_bf16_f32 v151,v122,v123\n"
"v_max3_f32 v248,v124,v125,v126\n"
"v_max3_f32 v248,v248,v127,v128\n"
"v_max3_f32 v248,v248,v129,v130\n"
"v_mfma_f32_16x16x32_bf16 v[20:23],v[208:211],v[144:147],0\n"
"v_max_f32 v248,v248,v131\n"
"v_mov_b32 v249,v248\n"
"s_nop 1\n"
"v_permlane32_swap_b32 v248,v249\n"
"v_max_f32 v248,v248,v249\n"
"v_mov_b32 v249,v248\n"
"v_mfma_f32_16x16x32_bf16 v[24:27],v[212:215],v[144:147],0\n"
"s_nop 0\n"
"v_permlane16_swap_b32 v248,v249\n"
"v_max_f32 v248,v248,v249\n"
"v_sub_f32 v124,v124,v248\n"
"v_sub_f32 v125,v125,v248\n"
"v_sub_f32 v126,v126,v248\n"
"v_mfma_f32_16x16x32_bf16 v[28:31],v[216:219],v[144:147],0\n"
"v_sub_f32 v127,v127,v248\n"
"v_sub_f32 v128,v128,v248\n"
"v_sub_f32 v129,v129,v248\n"
"v_sub_f32 v130,v130,v248\n"
"v_mfma_f32_16x16x32_bf16 v[96:99],a[48:51],v[144:147],0\n"
"v_sub_f32 v131,v131,v248\n"
"v_sub_f32 v140,0,v248\n"
"v_sub_f32 v141,0,v248\n"
"v_sub_f32 v142,0,v248\n"
"v_sub_f32 v143,0,v248\n"
"v_mfma_f32_16x16x32_bf16 v[32:35],v[188:191],v[148:151],0\n"
"v_exp_f32 v124,v124\n"
"v_mfma_f32_16x16x32_bf16 v[36:39],v[192:195],v[148:151],0\n"
"v_exp_f32 v125,v125\n"
"v_mfma_f32_16x16x32_bf16 v[40:43],v[196:199],v[148:151],0\n"
"v_exp_f32 v126,v126\n"
"v_mfma_f32_16x16x32_bf16 v[44:47],v[200:203],v[148:151],0\n"
"v_exp_f32 v127,v127\n"
"v_cvt_pk_bf16_f32 v152,v124,v125\n"
"v_mfma_f32_16x16x32_bf16 v[48:51],v[204:207],v[148:151],0\n"
"v_exp_f32 v128,v128\n"
"v_mfma_f32_16x16x32_bf16 v[52:55],v[208:211],v[148:151],0\n"
"v_exp_f32 v129,v129\n"
"v_mfma_f32_16x16x32_bf16 v[56:59],v[212:215],v[148:151],0\n"
"v_cvt_pk_bf16_f32 v153,v126,v127\n"
"v_exp_f32 v130,v130\n"
"v_mfma_f32_16x16x32_bf16 v[60:63],v[216:219],v[148:151],0\n"
"v_exp_f32 v131,v131\n"
"v_mfma_f32_16x16x32_bf16 v[100:103],a[48:51],v[148:151],0\n"
"v_cvt_pk_bf16_f32 v154,v128,v129\n"
"v_cvt_pk_bf16_f32 v155,v130,v131\n"
"s_nop 1\n"
"v_mfma_f32_16x16x32_bf16 v[64:67],v[188:191],v[152:155],0\n"
"ds_read_b128 v[156:159],v220 offset:16384\n"
"v_mfma_f32_16x16x32_bf16 v[68:71],v[192:195],v[152:155],0\n"
"ds_read_b128 v[172:175],v220 offset:20480\n"
"v_mfma_f32_16x16x32_bf16 v[72:75],v[196:199],v[152:155],0\n"
"ds_read_b128 v[160:163],v221 offset:16384\n"
"v_mfma_f32_16x16x32_bf16 v[76:79],v[200:203],v[152:155],0\n"
"ds_read_b128 v[176:179],v221 offset:20480\n"
"v_mfma_f32_16x16x32_bf16 v[80:83],v[204:207],v[152:155],0\n"
"v_mfma_f32_16x16x32_bf16 v[84:87],v[208:211],v[152:155],0\n"
"ds_read_b128 v[164:167],v222 offset:16384\n"
"v_mfma_f32_16x16x32_bf16 v[88:91],v[212:215],v[152:155],0\n"
"ds_read_b128 v[180:183],v222 offset:20480\n"
"v_mfma_f32_16x16x32_bf16 v[92:95],v[216:219],v[152:155],0\n"
"ds_read_b128 v[168:171],v223 offset:16384\n"
"v_mfma_f32_16x16x32_bf16 v[104:107],a[48:51],v[152:155],0\n"
"ds_read_b128 v[184:187],v223 offset:20480\n"
"s_waitcnt lgkmcnt(7)\n"
"v_mfma_f32_16x16x32_bf16 v[108:111],v[156:159],a[0:3],v[132:135]\n"
"ds_read_b64_tr_b16 v[188:189],v224 offset:16384\n"
"s_waitcnt lgkmcnt(7)\n"
"v_mfma_f32_16x16x32_bf16 v[112:115],v[172:175],a[0:3],v[132:135]\n"
"ds_read_b64_tr_b16 v[190:191],v224 offset:20480\n"
"ds_read_b64_tr_b16 v[192:193],v225 offset:16384\n"
"s_waitcnt lgkmcnt(8)\n"
"v_mfma_f32_16x16x32_bf16 v[108:111],v[160:163],a[12:15],v[108:111]\n"
"ds_read_b64_tr_b16 v[194:195],v225 offset:20480\n"
"s_waitcnt lgkmcnt(8)\n"
"v_mfma_f32_16x16x32_bf16 v[112:115],v[176:179],a[12:15],v[112:115]\n"
"ds_read_b64_tr_b16 v[196:197],v226 offset:16384\n"
"s_waitcnt lgkmcnt(8)\n"
"v_mfma_f32_16x16x32_bf16 v[108:111],v[164:167],a[24:27],v[108:111]\n"
"ds_read_b64_tr_b16 v[198:199],v226 offset:20480\n"
"s_waitcnt lgkmcnt(8)\n"
"v_mfma_f32_16x16x32_bf16 v[112:115],v[180:183],a[24:27],v[112:115]\n"
"ds_read_b64_tr_b16 v[200:201],v227 offset:16384\n"
"ds_read_b64_tr_b16 v[202:203],v227 offset:20480\n"
"s_waitcnt lgkmcnt(9)\n"
"v_mfma_f32_16x16x32_bf16 v[108:111],v[168:171],a[36:39],v[108:111]\n"
"ds_read_b64_tr_b16 v[204:205],v228 offset:16384\n"
"s_waitcnt lgkmcnt(9)\n"
"v_mfma_f32_16x16x32_bf16 v[112:115],v[184:187],a[36:39],v[112:115]\n"
"ds_read_b64_tr_b16 v[206:207],v228 offset:20480\n"
"v_mfma_f32_16x16x32_bf16 v[116:119],v[156:159],a[4:7],v[136:139]\n"
"s_barrier\n"
"ds_read_b64_tr_b16 v[208:209],v229 offset:16384\n"
"ds_read_b64_tr_b16 v[210:211],v229 offset:20480\n"
"v_mfma_f32_16x16x32_bf16 v[120:123],v[172:175],a[4:7],v[136:139]\n"
"ds_read_b64_tr_b16 v[212:213],v230 offset:16384\n"
"ds_read_b64_tr_b16 v[214:215],v230 offset:20480\n"
"v_mfma_f32_16x16x32_bf16 v[116:119],v[160:163],a[16:19],v[116:119]\n"
"ds_read_b64_tr_b16 v[216:217],v231 offset:16384\n"
"s_waitcnt lgkmcnt(11)\n"
"ds_read_b64_tr_b16 v[218:219],v231 offset:20480\n"
"v_max3_f32 v246,v108,v109,v110\n"
"v_mfma_f32_16x16x32_bf16 v[120:123],v[176:179],a[16:19],v[120:123]\n"
"v_max3_f32 v246,v246,v111,v112\n"
"v_max3_f32 v246,v246,v113,v114\n"
"v_mfma_f32_16x16x32_bf16 v[116:119],v[164:167],a[28:31],v[116:119]\n"
"v_max_f32 v246,v246,v115\n"
"v_cmp_lt_f32 s[80:81],s64,v246\n"
"v_mfma_f32_16x16x32_bf16 v[120:123],v[180:183],a[28:31],v[120:123]\n"
"v_exp_f32 v108,v108\n"
"v_exp_f32 v109,v109\n"
"v_mfma_f32_16x16x32_bf16 v[116:119],v[168:171],a[40:43],v[116:119]\n"
"v_exp_f32 v110,v110\n"
"v_mfma_f32_16x16x32_bf16 v[120:123],v[184:187],a[40:43],v[120:123]\n"
"v_exp_f32 v111,v111\n"
"v_mfma_f32_16x16x32_bf16 v[124:127],v[156:159],a[8:11],v[140:143]\n"
"v_cvt_pk_bf16_f32 v144,v108,v109\n"
"v_exp_f32 v112,v112\n"
"v_mfma_f32_16x16x32_bf16 v[128:131],v[172:175],a[8:11],v[140:143]\n"
"v_exp_f32 v113,v113\n"
"v_mfma_f32_16x16x32_bf16 v[124:127],v[160:163],a[20:23],v[124:127]\n"
"v_cvt_pk_bf16_f32 v145,v110,v111\n"
"v_mfma_f32_16x16x32_bf16 v[128:131],v[176:179],a[20:23],v[128:131]\n"
"v_exp_f32 v114,v114\n"
"v_mfma_f32_16x16x32_bf16 v[124:127],v[164:167],a[32:35],v[124:127]\n"
"v_exp_f32 v115,v115\n"
"v_cvt_pk_bf16_f32 v146,v112,v113\n"
"v_mfma_f32_16x16x32_bf16 v[128:131],v[180:183],a[32:35],v[128:131]\n"
"v_cvt_pk_bf16_f32 v147,v114,v115\n"
"v_max3_f32 v247,v116,v117,v118\n"
"v_mfma_f32_16x16x32_bf16 v[124:127],v[168:171],a[44:47],v[124:127]\n"
"v_max3_f32 v247,v247,v119,v120\n"
"v_max3_f32 v247,v247,v121,v122\n"
"v_mfma_f32_16x16x32_bf16 v[128:131],v[184:187],a[44:47],v[128:131]\n"
"v_max_f32 v247,v247,v123\n"
"v_cmp_lt_f32 vcc,s64,v247\n"
"s_cmp_lg_u64 s[80:81],0\n"
"s_cbranch_scc1 Lk_4x\n"
"Lk_4:\n"
"v_mfma_f32_16x16x32_bf16 v[0:3],v[188:191],v[144:147],v[0:3]\n"
"v_exp_f32 v116,v116\n"
"v_mfma_f32_16x16x32_bf16 v[4:7],v[192:195],v[144:147],v[4:7]\n"
"v_exp_f32 v117,v117\n"
"v_exp_f32 v118,v118\n"
"s_waitcnt lgkmcnt(0)\n"
"v_mfma_f32_16x16x32_bf16 v[8:11],v[196:199],v[144:147],v[8:11]\n"
"v_exp_f32 v119,v119\n"
"v_mfma_f32_16x16x32_bf16 v[12:15],v[200:203],v[144:147],v[12:15]\n"
"v_cvt_pk_bf16_f32 v148,v116,v117\n"
"v_exp_f32 v120,v120\n"
"v_mfma_f32_16x16x32_bf16 v[16:19],v[204:207],v[144:147],v[16:19]\n"
"v_exp_f32 v121,v121\n"
"v_cvt_pk_bf16_f32 v149,v118,v119\n"
"v_mfma_f32_16x16x32_bf16 v[20:23],v[208:211],v[144:147],v[20:23]\n"
"v_exp_f32 v122,v122\n"
"v_mfma_f32_16x16x32_bf16 v[24:27],v[212:215],v[144:147],v[24:27]\n"
"v_exp_f32 v123,v123\n"
"v_cvt_pk_bf16_f32 v150,v120,v121\n"
"v_mfma_f32_16x16x32_bf16 v[28:31],v[216:219],v[144:147],v[28:31]\n"
"v_cvt_pk_bf16_f32 v151,v122,v123\n"
"v_max3_f32 v248,v124,v125,v126\n"
"v_max3_f32 v248,v248,v127,v128\n"
"v_mfma_f32_16x16x32_bf16 v[96:99],a[48:51],v[144:147],v[96:99]\n"
"v_max3_f32 v248,v248,v129,v130\n"
"v_max_f32 v248,v248,v131\n"
"v_cmp_lt_f32 s[82:83],s64,v248\n"
"s_cbranch_vccnz Lk_5x\n"
"Lk_5:\n"
"v_mfma_f32_16x16x32_bf16 v[32:35],v[188:191],v[148:151],v[32:35]\n"
"v_exp_f32 v124,v124\n"
"v_mfma_f32_16x16x32_bf16 v[36:39],v[192:195],v[148:151],v[36:39]\n"
"v_exp_f32 v125,v125\n"
"v_exp_f32 v126,v126\n"
"v_mfma_f32_16x16x32_bf16 v[40:43],v[196:199],v[148:151],v[40:43]\n"
"v_exp_f32 v127,v127\n"
"v_mfma_f32_16x16x32_bf16 v[44:47],v[200:203],v[148:151],v[44:47]\n"
"v_cvt_pk_bf16_f32 v152,v124,v125\n"
"v_exp_f32 v128,v128\n"
"v_mfma_f32_16x16x32_bf16 v[48:51],v[204:207],v[148:151],v[48:51]\n"
"v_exp_f32 v129,v129\n"
"v_mfma_f32_16x16x32_bf16 v[52:55],v[208:211],v[148:151],v[52:55]\n"
"v_cvt_pk_bf16_f32 v153,v126,v127\n"
"v_exp_f32 v130,v130\n"
"v_mfma_f32_16x16x32_bf16 v[56:59],v[212:215],v[148:151],v[56:59]\n"
"v_exp_f32 v131,v131\n"
"v_cvt_pk_bf16_f32 v154,v128,v129\n"
"v_mfma_f32_16x16x32_bf16 v[60:63],v[216:219],v[148:151],v[60:63]\n"
"v_cvt_pk_bf16_f32 v155,v130,v131\n"
"ds_write_b128 v253,a[112:115]\n"
"v_mfma_f32_16x16x32_bf16 v[100:103],a[48:51],v[148:151],v[100:103]\n"
"ds_write_b128 v253,a[116:119] offset:1024\n"
"ds_write_b128 v253,a[120:123] offset:8192\n"
"ds_write_b128 v253,a[124:127] offset:9216\n"
"s_cmp_lg_u64 s[82:83],0\n"
"s_cbranch_scc1 Lk_6x\n"
"Lk_6:\n"
"v_mfma_f32_16x16x32_bf16 v[64:67],v[188:191],v[152:155],v[64:67]\n"
"ds_read_b128 v[156:159],v220 offset:32768\n"
"v_mfma_f32_16x16x32_bf16 v[68:71],v[192:195],v[152:155],v[68:71]\n"
"ds_read_b128 v[172:175],v220 offset:36864\n"
"v_mfma_f32_16x16x32_bf16 v[72:75],v[196:199],v[152:155],v[72:75]\n"
"ds_read_b128 v[160:163],v221 offset:32768\n"
"v_mfma_f32_16x16x32_bf16 v[76:79],v[200:203],v[152:155],v[76:79]\n"
"ds_read_b128 v[176:179],v221 offset:36864\n"
"v_mfma_f32_16x16x32_bf16 v[80:83],v[204:207],v[152:155],v[80:83]\n"
"v_mfma_f32_16x16x32_bf16 v[84:87],v[208:211],v[152:155],v[84:87]\n"
"ds_read_b128 v[164:167],v222 offset:32768\n"
"v_mfma_f32_16x16x32_bf16 v[88:91],v[212:215],v[152:155],v[88:91]\n"
"ds_read_b128 v[180:183],v222 offset:36864\n"
"v_mfma_f32_16x16x32_bf16 v[92:95],v[216:219],v[152:155],v[92:95]\n"
"ds_read_b128 v[168:171],v223 offset:32768\n"
"v_mfma_f32_16x16x32_bf16 v[104:107],a[48:51],v[152:155],v[104:107]\n"
"ds_read_b128 v[184:187],v223 offset:36864\n"
"s_waitcnt lgkmcnt(7)\n"
"v_mfma_f32_16x16x32_bf16 v[108:111],v[156:159],a[0:3],v[132:135]\n"
"ds_read_b64_tr_b16 v[188:189],v224 offset:32768\n"
"s_waitcnt lgkmcnt(7)\n"
"v_mfma_f32_16x16x32_bf16 v[112:115],v[172:175],a[0:3],v[132:135]\n"
"ds_read_b64_tr_b16 v[190:191],v224 offset:36864\n"
"ds_read_b64_tr_b16 v[192:193],v225 offset:32768\n"
"s_waitcnt lgkmcnt(8)\n"
"v_mfma_f32_16x16x32_bf16 v[108:111],v[160:163],a[12:15],v[108:111]\n"
"ds_read_b64_tr_b16 v[194:195],v225 offset:36864\n"
"s_waitcnt lgkmcnt(8)\n"
"v_mfma_f32_16x16x32_bf16 v[112:115],v[176:179],a[12:15],v[112:115]\n"
"ds_read_b64_tr_b16 v[196:197],v226 offset:32768\n"
"s_waitcnt lgkmcnt(8)\n"
"v_mfma_f32_16x16x32_bf16 v[108:111],v[164:167],a[24:27],v[108:111]\n"
"ds_read_b64_tr_b16 v[198:199],v226 offset:36864\n"
"s_waitcnt lgkmcnt(8)\n"
"v_mfma_f32_16x16x32_bf16 v[112:115],v[180:183],a[24:27],v[112:115]\n"
"ds_read_b64_tr_b16 v[200:201],v227 offset:32768\n"
"ds_read_b64_tr_b16 v[202:203],v227 offset:36864\n"
"s_waitcnt lgkmcnt(9)\n"
"v_mfma_f32_16x16x32_bf16 v[108:111],v[168:171],a[36:39],v[108:111]\n"
"ds_read_b64_tr_b16 v[204:205],v228 offset:32768\n"
"s_waitcnt lgkmcnt(9)\n"
"v_mfma_f32_16x16x32_bf16 v[112:115],v[184:187],a[36:39],v[112:115]\n"
"ds_read_b64_tr_b16 v[206:207],v228 offset:36864\n"
"v_mfma_f32_16x16x32_bf16 v[116:119],v[156:159],a[4:7],v[136:139]\n"
"s_barrier\n"
"ds_read_b64_tr_b16 v[208:209],v229 offset:32768\n"
"ds_read_b64_tr_b16 v[210:211],v229 offset:36864\n"
"v_mfma_f32_16x16x32_bf16 v[120:123],v[172:175],a[4:7],v[136:139]\n"
"ds_read_b64_tr_b16 v[212:213],v230 offset:32768\n"
"ds_read_b64_tr_b16 v[214:215],v230 offset:36864\n"
"v_mfma_f32_16x16x32_bf16 v[116:119],v[160:163],a[16:19],v[116:119]\n"
"ds_read_b64_tr_b16 v[216:217],v231 offset:32768\n"
"s_waitcnt lgkmcnt(11)\n"
"ds_read_b64_tr_b16 v[218:219],v231 offset:36864\n"
"v_max3_f32 v246,v108,v109,v110\n"
"v_mfma_f32_16x16x32_bf16 v[120:123],v[176:179],a[16:19],v[120:123]\n"
"v_max3_f32 v246,v246,v111,v112\n"
"v_max3_f32 v246,v246,v113,v114\n"
"v_mfma_f32_16x16x32_bf16 v[116:119],v[164:167],a[28:31],v[116:119]\n"
"v_max_f32 v246,v246,v115\n"
"v_cmp_lt_f32 s[80:81],s64,v246\n"
"v_mfma_f32_16x16x32_bf16 v[120:123],v[180:183],a[28:31],v[120:123]\n"
"v_exp_f32 v108,v108\n"
"v_exp_f32 v109,v109\n"
"v_mfma_f32_16x16x32_bf16 v[116:119],v[168:171],a[40:43],v[116:119]\n"
"v_exp_f32 v110,v110\n"
"v_mfma_f32_16x16x32_bf16 v[120:123],v[184:187],a[40:43],v[120:123]\n"
"v_exp_f32 v111,v111\n"
"v_mfma_f32_16x16x32_bf16 v[124:127],v[156:159],a[8:11],v[140:143]\n"
"v_cvt_pk_bf16_f32 v144,v108,v109\n"
"v_exp_f32 v112,v112\n"
"v_mfma_f32_16x16x32_bf16 v[128:131],v[172:175],a[8:11],v[140:143]\n"
"v_exp_f32 v113,v113\n"
"v_mfma_f32_16x16x32_bf16 v[124:127],v[160:163],a[20:23],v[124:127]\n"
"v_cvt_pk_bf16_f32 v145,v110,v111\n"
"v_mfma_f32_16x16x32_bf16 v[128:131],v[176:179],a[20:23],v[128:131]\n"
"v_exp_f32 v114,v114\n"
"v_mfma_f32_16x16x32_bf16 v[124:127],v[164:167],a[32:35],v[124:127]\n"
"v_exp_f32 v115,v115\n"
"v_cvt_pk_bf16_f32 v146,v112,v113\n"
"v_mfma_f32_16x16x32_bf16 v[128:131],v[180:183],a[32:35],v[128:131]\n"
"v_cvt_pk_bf16_f32 v147,v114,v115\n"
"v_max3_f32 v247,v116,v117,v118\n"
"v_mfma_f32_16x16x32_bf16 v[124:127],v[168:171],a[44:47],v[124:127]\n"
"v_max3_f32 v247,v247,v119,v120\n"
"v_max3_f32 v247,v247,v121,v122\n"
"v_mfma_f32_16x16x32_bf16 v[128:131],v[184:187],a[44:47],v[128:131]\n"
"v_max_f32 v247,v247,v123\n"
"v_cmp_lt_f32 vcc,s64,v247\n"
"s_cmp_lg_u64 s[80:81],0\n"
"s_cbranch_scc1 Lk_7x\n"
"Lk_7:\n"
"v_mfma_f32_16x16x32_bf16 v[0:3],v[188:191],v[144:147],v[0:3]\n"
"v_exp_f32 v116,v116\n"
"v_mfma_f32_16x16x32_bf16 v[4:7],v[192:195],v[144:147],v[4:7]\n"
"v_exp_f32 v117,v117\n"
"v_exp_f32 v118,v118\n"
"s_waitcnt lgkmcnt(0)\n"
"v_mfma_f32_16x16x32_bf16 v[8:11],v[196:199],v[144:147],v[8:11]\n"
"v_exp_f32 v119,v119\n"
"v_mfma_f32_16x16x32_bf16 v[12:15],v[200:203],v[144:147],v[12:15]\n"
"v_cvt_pk_bf16_f32 v148,v116,v117\n"
"v_exp_f32 v120,v120\n"
"v_mfma_f32_16x16x32_bf16 v[16:19],v[204:207],v[144:147],v[16:19]\n"
"v_exp_f32 v121,v121\n"
"v_cvt_pk_bf16_f32 v149,v118,v119\n"
"v_mfma_f32_16x16x32_bf16 v[20:23],v[208:211],v[144:147],v[20:23]\n"
"v_exp_f32 v122,v122\n"
"v_mfma_f32_16x16x32_bf16 v[24:27],v[212:215],v[144:147],v[24:27]\n"
"v_exp_f32 v123,v123\n"
"v_cvt_pk_bf16_f32 v150,v120,v121\n"
"v_mfma_f32_16x16x32_bf16 v[28:31],v[216:219],v[144:147],v[28:31]\n"
"v_cvt_pk_bf16_f32 v151,v122,v123\n"
"v_max3_f32 v248,v124,v125,v126\n"
"v_max3_f32 v248,v248,v127,v128\n"
"v_mfma_f32_16x16x32_bf16 v[96:99],a[48:51],v[144:147],v[96:99]\n"
"v_max3_f32 v248,v248,v129,v130\n"
"v_max_f32 v248,v248,v131\n"
"v_cmp_lt_f32 s[82:83],s64,v248\n"
"s_cbranch_vccnz Lk_8x\n"
"Lk_8:\n"
"v_mfma_f32_16x16x32_bf16 v[32:35],v[188:191],v[148:151],v[32:35]\n"
"v_exp_f32 v124,v124\n"
"v_mfma_f32_16x16x32_bf16 v[36:39],v[192:195],v[148:151],v[36:39]\n"
"v_exp_f32 v125,v125\n"
"v_exp_f32 v126,v126\n"
"v_mfma_f32_16x16x32_bf16 v[40:43],v[196:199],v[148:151],v[40:43]\n"
"v_exp_f32 v127,v127\n"
"v_mfma_f32_16x16x32_bf16 v[44:47],v[200:203],v[148:151],v[44:47]\n"
"v_cvt_pk_bf16_f32 v152,v124,v125\n"
"v_exp_f32 v128,v128\n"
"v_mfma_f32_16x16x32_bf16 v[48:51],v[204:207],v[148:151],v[48:51]\n"
"v_exp_f32 v129,v129\n"
"v_mfma_f32_16x16x32_bf16 v[52:55],v[208:211],v[148:151],v[52:55]\n"
"v_cvt_pk_bf16_f32 v153,v126,v127\n"
"v_exp_f32 v130,v130\n"
"v_mfma_f32_16x16x32_bf16 v[56:59],v[212:215],v[148:151],v[56:59]\n"
"v_exp_f32 v131,v131\n"
"v_cvt_pk_bf16_f32 v154,v128,v129\n"
"v_mfma_f32_16x16x32_bf16 v[60:63],v[216:219],v[148:151],v[60:63]\n"
"v_cvt_pk_bf16_f32 v155,v130,v131\n"
"ds_write_b128 v253,a[128:131] offset:16384\n"
"v_mfma_f32_16x16x32_bf16 v[100:103],a[48:51],v[148:151],v[100:103]\n"
"ds_write_b128 v253,a[132:135] offset:17408\n"
"ds_write_b128 v253,a[136:139] offset:24576\n"
"ds_write_b128 v253,a[140:143] offset:25600\n"
"s_cmp_lg_u64 s[82:83],0\n"
"s_cbranch_scc1 Lk_9x\n"
"Lk_9:\n"
"v_mfma_f32_16x16x32_bf16 v[64:67],v[188:191],v[152:155],v[64:67]\n"
"ds_read_b128 v[156:159],v220 offset:49152\n"
"v_mfma_f32_16x16x32_bf16 v[68:71],v[192:195],v[152:155],v[68:71]\n"
"ds_read_b128 v[172:175],v220 offset:53248\n"
"v_mfma_f32_16x16x32_bf16 v[72:75],v[196:199],v[152:155],v[72:75]\n"
"ds_read_b128 v[160:163],v221 offset:49152\n"
"v_mfma_f32_16x16x32_bf16 v[76:79],v[200:203],v[152:155],v[76:79]\n"
"ds_read_b128 v[176:179],v221 offset:53248\n"
"v_mfma_f32_16x16x32_bf16 v[80:83],v[204:207],v[152:155],v[80:83]\n"
"v_mfma_f32_16x16x32_bf16 v[84:87],v[208:211],v[152:155],v[84:87]\n"
"ds_read_b128 v[164:167],v222 offset:49152\n"
"v_mfma_f32_16x16x32_bf16 v[88:91],v[212:215],v[152:155],v[88:91]\n"
"ds_read_b128 v[180:183],v222 offset:53248\n"
"v_mfma_f32_16x16x32_bf16 v[92:95],v[216:219],v[152:155],v[92:95]\n"
"ds_read_b128 v[168:171],v223 offset:49152\n"
"v_mfma_f32_16x16x32_bf16 v[104:107],a[48:51],v[152:155],v[104:107]\n"
"ds_read_b128 v[184:187],v223 offset:53248\n"
"s_waitcnt lgkmcnt(7)\n"
"v_mfma_f32_16x16x32_bf16 v[108:111],v[156:159],a[0:3],v[132:135]\n"
"ds_read_b64_tr_b16 v[188:189],v224 offset:49152\n"
"s_waitcnt lgkmcnt(7)\n"
"v_mfma_f32_16x16x32_bf16 v[112:115],v[172:175],a[0:3],v[132:135]\n"
"ds_read_b64_tr_b16 v[190:191],v224 offset:53248\n"
"ds_read_b64_tr_b16 v[192:193],v225 offset:49152\n"
"s_waitcnt lgkmcnt(8)\n"
"v_mfma_f32_16x16x32_bf16 v[108:111],v[160:163],a[12:15],v[108:111]\n"
"ds_read_b64_tr_b16 v[194:195],v225 offset:53248\n"
"s_waitcnt lgkmcnt(8)\n"
"v_mfma_f32_16x16x32_bf16 v[112:115],v[176:179],a[12:15],v[112:115]\n"
"ds_read_b64_tr_b16 v[196:197],v226 offset:49152\n"
"s_waitcnt lgkmcnt(8)\n"
"v_mfma_f32_16x16x32_bf16 v[108:111],v[164:167],a[24:27],v[108:111]\n"
"ds_read_b64_tr_b16 v[198:199],v226 offset:53248\n"
"s_waitcnt lgkmcnt(8)\n"
"v_mfma_f32_16x16x32_bf16 v[112:115],v[180:183],a[24:27],v[112:115]\n"
"ds_read_b64_tr_b16 v[200:201],v227 offset:49152\n"
"ds_read_b64_tr_b16 v[202:203],v227 offset:53248\n"
"s_waitcnt lgkmcnt(9)\n"
"v_mfma_f32_16x16x32_bf16 v[108:111],v[168:171],a[36:39],v[108:111]\n"
"ds_read_b64_tr_b16 v[204:205],v228 offset:49152\n"
"s_waitcnt lgkmcnt(9)\n"
"v_mfma_f32_16x16x32_bf16 v[112:115],v[184:187],a[36:39],v[112:115]\n"
"ds_read_b64_tr_b16 v[206:207],v228 offset:53248\n"
"v_mfma_f32_16x16x32_bf16 v[116:119],v[156:159],a[4:7],v[136:139]\n"
"s_barrier\n"
"ds_read_b64_tr_b16 v[208:209],v229 offset:49152\n"
"ds_read_b64_tr_b16 v[210:211],v229 offset:53248\n"
"v_mfma_f32_16x16x32_bf16 v[120:123],v[172:175],a[4:7],v[136:139]\n"
"ds_read_b64_tr_b16 v[212:213],v230 offset:49152\n"
"ds_read_b64_tr_b16 v[214:215],v230 offset:53248\n"
"v_mfma_f32_16x16x32_bf16 v[116:119],v[160:163],a[16:19],v[116:119]\n"
"ds_read_b64_tr_b16 v[216:217],v231 offset:49152\n"
"s_waitcnt lgkmcnt(11)\n"
"ds_read_b64_tr_b16 v[218:219],v231 offset:53248\n"
"v_max3_f32 v246,v108,v109,v110\n"
"v_mfma_f32_16x16x32_bf16 v[120:123],v[176:179],a[16:19],v[120:123]\n"
"v_max3_f32 v246,v246,v111,v112\n"
"v_max3_f32 v246,v246,v113,v114\n"
"v_mfma_f32_16x16x32_bf16 v[116:119],v[164:167],a[28:31],v[116:119]\n"
"v_max_f32 v246,v246,v115\n"
"v_cmp_lt_f32 s[80:81],s64,v246\n"
"v_mfma_f32_16x16x32_bf16 v[120:123],v[180:183],a[28:31],v[120:123]\n"
"v_exp_f32 v108,v108\n"
"v_exp_f32 v109,v109\n"
"v_mfma_f32_16x16x32_bf16 v[116:119],v[168:171],a[40:43],v[116:119]\n"
"v_exp_f32 v110,v110\n"
"v_mfma_f32_16x16x32_bf16 v[120:123],v[184:187],a[40:43],v[120:123]\n"
"v_exp_f32 v111,v111\n"
"v_mfma_f32_16x16x32_bf16 v[124:127],v[156:159],a[8:11],v[140:143]\n"
"v_cvt_pk_bf16_f32 v144,v108,v109\n"
"v_exp_f32 v112,v112\n"
"v_mfma_f32_16x16x32_bf16 v[128:131],v[172:175],a[8:11],v[140:143]\n"
"v_exp_f32 v113,v113\n"
"v_mfma_f32_16x16x32_bf16 v[124:127],v[160:163],a[20:23],v[124:127]\n"
"v_cvt_pk_bf16_f32 v145,v110,v111\n"
"v_mfma_f32_16x16x32_bf16 v[128:131],v[176:179],a[20:23],v[128:131]\n"
"v_exp_f32 v114,v114\n"
"v_mfma_f32_16x16x32_bf16 v[124:127],v[164:167],a[32:35],v[124:127]\n"
"v_exp_f32 v115,v115\n"
"v_cvt_pk_bf16_f32 v146,v112,v113\n"
"v_mfma_f32_16x16x32_bf16 v[128:131],v[180:183],a[32:35],v[128:131]\n"
"v_cvt_pk_bf16_f32 v147,v114,v115\n"
"v_max3_f32 v247,v116,v117,v118\n"
"v_mfma_f32_16x16x32_bf16 v[124:127],v[168:171],a[44:47],v[124:127]\n"
"v_max3_f32 v247,v247,v119,v120\n"
"v_max3_f32 v247,v247,v121,v122\n"
"v_mfma_f32_16x16x32_bf16 v[128:131],v[184:187],a[44:47],v[128:131]\n"
"v_max_f32 v247,v247,v123\n"
"v_cmp_lt_f32 vcc,s64,v247\n"
"s_cmp_lg_u64 s[80:81],0\n"
"s_cbranch_scc1 Lk_10x\n"
"Lk_10:\n"
"v_mfma_f32_16x16x32_bf16 v[0:3],v[188:191],v[144:147],v[0:3]\n"
"v_exp_f32 v116,v116\n"
"v_mfma_f32_16x16x32_bf16 v[4:7],v[192:195],v[144:147],v[4:7]\n"
"v_exp_f32 v117,v117\n"
"v_exp_f32 v118,v118\n"
"s_waitcnt lgkmcnt(0)\n"
"v_mfma_f32_16x16x32_bf16 v[8:11],v[196:199],v[144:147],v[8:11]\n"
"v_exp_f32 v119,v119\n"
"v_mfma_f32_16x16x32_bf16 v[12:15],v[200:203],v[144:147],v[12:15]\n"
"v_cvt_pk_bf16_f32 v148,v116,v117\n"
"v_exp_f32 v120,v120\n"
"v_mfma_f32_16x16x32_bf16 v[16:19],v[204:207],v[144:147],v[16:19]\n"
"v_exp_f32 v121,v121\n"
"v_cvt_pk_bf16_f32 v149,v118,v119\n"
"v_mfma_f32_16x16x32_bf16 v[20:23],v[208:211],v[144:147],v[20:23]\n"
"v_exp_f32 v122,v122\n"
"v_mfma_f32_16x16x32_bf16 v[24:27],v[212:215],v[144:147],v[24:27]\n"
"v_exp_f32 v123,v123\n"
"v_cvt_pk_bf16_f32 v150,v120,v121\n"
"v_mfma_f32_16x16x32_bf16 v[28:31],v[216:219],v[144:147],v[28:31]\n"
"v_cvt_pk_bf16_f32 v151,v122,v123\n"
"v_max3_f32 v248,v124,v125,v126\n"
"v_max3_f32 v248,v248,v127,v128\n"
"v_mfma_f32_16x16x32_bf16 v[96:99],a[48:51],v[144:147],v[96:99]\n"
"v_max3_f32 v248,v248,v129,v130\n"
"v_max_f32 v248,v248,v131\n"
"v_cmp_lt_f32 s[82:83],s64,v248\n"
"s_cbranch_vccnz Lk_11x\n"
"Lk_11:\n"
"v_mfma_f32_16x16x32_bf16 v[32:35],v[188:191],v[148:151],v[32:35]\n"
"v_exp_f32 v124,v124\n"
"v_mfma_f32_16x16x32_bf16 v[36:39],v[192:195],v[148:151],v[36:39]\n"
"v_exp_f32 v125,v125\n"
"v_mfma_f32_16x16x32_bf16 v[40:43],v[196:199],v[148:151],v[40:43]\n"
"v_exp_f32 v126,v126\n"
"v_mfma_f32_16x16x32_bf16 v[44:47],v[200:203],v[148:151],v[44:47]\n"
"v_exp_f32 v127,v127\n"
"v_cvt_pk_bf16_f32 v152,v124,v125\n"
"v_mfma_f32_16x16x32_bf16 v[48:51],v[204:207],v[148:151],v[48:51]\n"
"v_exp_f32 v128,v128\n"
"v_mfma_f32_16x16x32_bf16 v[52:55],v[208:211],v[148:151],v[52:55]\n"
"v_exp_f32 v129,v129\n"
"v_mfma_f32_16x16x32_bf16 v[56:59],v[212:215],v[148:151],v[56:59]\n"
"v_cvt_pk_bf16_f32 v153,v126,v127\n"
"v_exp_f32 v130,v130\n"
"v_mfma_f32_16x16x32_bf16 v[60:63],v[216:219],v[148:151],v[60:63]\n"
"v_exp_f32 v131,v131\n"
"v_mfma_f32_16x16x32_bf16 v[100:103],a[48:51],v[148:151],v[100:103]\n"
"v_cvt_pk_bf16_f32 v154,v128,v129\n"
"v_cvt_pk_bf16_f32 v155,v130,v131\n"
"s_cmp_lg_u64 s[82:83],0\n"
"s_cbranch_scc1 Lk_12x\n"
"Lk_12:\n"
"v_mfma_f32_16x16x32_bf16 v[64:67],v[188:191],v[152:155],v[64:67]\n"
"s_mov_b32 s60,0x10000\n"
"v_add_u32 v220,s60,v220\n"
"v_mfma_f32_16x16x32_bf16 v[68:71],v[192:195],v[152:155],v[68:71]\n"
"v_add_u32 v221,s60,v221\n"
"v_mfma_f32_16x16x32_bf16 v[72:75],v[196:199],v[152:155],v[72:75]\n"
"v_add_u32 v222,s60,v222\n"
"v_add_u32 v223,s60,v223\n"
"v_mfma_f32_16x16x32_bf16 v[76:79],v[200:203],v[152:155],v[76:79]\n"
"ds_read_b128 v[156:159],v220\n"
"v_mfma_f32_16x16x32_bf16 v[80:83],v[204:207],v[152:155],v[80:83]\n"
"ds_read_b128 v[172:175],v220 offset:4096\n"
"v_mfma_f32_16x16x32_bf16 v[84:87],v[208:211],v[152:155],v[84:87]\n"
"ds_read_b128 v[160:163],v221\n"
"ds_read_b128 v[176:179],v221 offset:4096\n"
"v_mfma_f32_16x16x32_bf16 v[88:91],v[212:215],v[152:155],v[88:91]\n"
"ds_read_b128 v[164:167],v222\n"
"v_mfma_f32_16x16x32_bf16 v[92:95],v[216:219],v[152:155],v[92:95]\n"
"ds_read_b128 v[180:183],v222 offset:4096\n"
"ds_read_b128 v[168:171],v223\n"
"v_mfma_f32_16x16x32_bf16 v[104:107],a[48:51],v[152:155],v[104:107]\n"
"ds_read_b128 v[184:187],v223 offset:4096\n"
"s_mov_b32 s60,0x10000\n"
"v_add_u32 v224,s60,v224\n"
"v_add_u32 v225,s60,v225\n"
"v_add_u32 v226,s60,v226\n"
"v_add_u32 v227,s60,v227\n"
"v_add_u32 v228,s60,v228\n"
"v_add_u32 v229,s60,v229\n"
"v_add_u32 v230,s60,v230\n"
"v_add_u32 v231,s60,v231\n"
"s_waitcnt lgkmcnt(7)\n"
"v_mfma_f32_16x16x32_bf16 v[108:111],v[156:159],a[0:3],v[132:135]\n"
"ds_read_b64_tr_b16 v[188:189],v224\n"
"s_waitcnt lgkmcnt(7)\n"
"v_mfma_f32_16x16x32_bf16 v[112:115],v[172:175],a[0:3],v[132:135]\n"
"ds_read_b64_tr_b16 v[190:191],v224 offset:4096\n"
"ds_read_b64_tr_b16 v[192:193],v225\n"
"s_waitcnt lgkmcnt(8)\n"
"v_mfma_f32_16x16x32_bf16 v[108:111],v[160:163],a[12:15],v[108:111]\n"
"ds_read_b64_tr_b16 v[194:195],v225 offset:4096\n"
"s_waitcnt lgkmcnt(8)\n"
"v_mfma_f32_16x16x32_bf16 v[112:115],v[176:179],a[12:15],v[112:115]\n"
"ds_read_b64_tr_b16 v[196:197],v226\n"
"s_waitcnt lgkmcnt(8)\n"
"v_mfma_f32_16x16x32_bf16 v[108:111],v[164:167],a[24:27],v[108:111]\n"
"ds_read_b64_tr_b16 v[198:199],v226 offset:4096\n"
"s_waitcnt lgkmcnt(8)\n"
"v_mfma_f32_16x16x32_bf16 v[112:115],v[180:183],a[24:27],v[112:115]\n"
"ds_read_b64_tr_b16 v[200:201],v227\n"
"ds_read_b64_tr_b16 v[202:203],v227 offset:4096\n"
"s_waitcnt lgkmcnt(8)\n"
"v_mfma_f32_16x16x32_bf16 v[108:111],v[168:171],a[36:39],v[108:111]\n"
"ds_read_b64_tr_b16 v[204:205],v228\n"
"v_mfma_f32_16x16x32_bf16 v[112:115],v[184:187],a[36:39],v[112:115]\n"
"ds_read_b64_tr_b16 v[206:207],v228 offset:4096\n"
"v_mfma_f32_16x16x32_bf16 v[116:119],v[156:159],a[4:7],v[136:139]\n"
"s_barrier\n"
"ds_read_b64_tr_b16 v[208:209],v229\n"
"ds_read_b64_tr_b16 v[210:211],v229 offset:4096\n"
"v_mfma_f32_16x16x32_bf16 v[120:123],v[172:175],a[4:7],v[136:139]\n"
"ds_read_b64_tr_b16 v[212:213],v230\n"
"ds_read_b64_tr_b16 v[214:215],v230 offset:4096\n"
"v_mfma_f32_16x16x32_bf16 v[116:119],v[160:163],a[16:19],v[116:119]\n"
"ds_read_b64_tr_b16 v[216:217],v231\n"
"s_waitcnt lgkmcnt(12)\n"
"ds_read_b64_tr_b16 v[218:219],v231 offset:4096\n"
"v_max3_f32 v246,v108,v109,v110\n"
"v_mfma_f32_16x16x32_bf16 v[120:123],v[176:179],a[16:19],v[120:123]\n"
"v_max3_f32 v246,v246,v111,v112\n"
"v_max3_f32 v246,v246,v113,v114\n"
"v_mfma_f32_16x16x32_bf16 v[116:119],v[164:167],a[28:31],v[116:119]\n"
"v_max_f32 v246,v246,v115\n"
"v_cmp_lt_f32 s[80:81],s64,v246\n"
"v_mfma_f32_16x16x32_bf16 v[120:123],v[180:183],a[28:31],v[120:123]\n"
"v_exp_f32 v108,v108\n"
"v_exp_f32 v109,v109\n"
"v_mfma_f32_16x16x32_bf16 v[116:119],v[168:171],a[40:43],v[116:119]\n"
"v_exp_f32 v110,v110\n"
"v_mfma_f32_16x16x32_bf16 v[120:123],v[184:187],a[40:43],v[120:123]\n"
"v_exp_f32 v111,v111\n"
"v_mfma_f32_16x16x32_bf16 v[124:127],v[156:159],a[8:11],v[140:143]\n"
"v_cvt_pk_bf16_f32 v144,v108,v109\n"
"v_exp_f32 v112,v112\n"
"v_mfma_f32_16x16x32_bf16 v[128:131],v[172:175],a[8:11],v[140:143]\n"
"v_exp_f32 v113,v113\n"
"v_mfma_f32_16x16x32_bf16 v[124:127],v[160:163],a[20:23],v[124:127]\n"
"v_cvt_pk_bf16_f32 v145,v110,v111\n"
"v_mfma_f32_16x16x32_bf16 v[128:131],v[176:179],a[20:23],v[128:131]\n"
"v_exp_f32 v114,v114\n"
"v_mfma_f32_16x16x32_bf16 v[124:127],v[164:167],a[32:35],v[124:127]\n"
"v_exp_f32 v115,v115\n"
"v_cvt_pk_bf16_f32 v146,v112,v113\n"
"v_mfma_f32_16x16x32_bf16 v[128:131],v[180:183],a[32:35],v[128:131]\n"
"v_cvt_pk_bf16_f32 v147,v114,v115\n"
"v_max3_f32 v247,v116,v117,v118\n"
"v_mfma_f32_16x16x32_bf16 v[124:127],v[168:171],a[44:47],v[124:127]\n"
"v_max3_f32 v247,v247,v119,v120\n"
"v_max3_f32 v247,v247,v121,v122\n"
"v_mfma_f32_16x16x32_bf16 v[128:131],v[184:187],a[44:47],v[128:131]\n"
"v_max_f32 v247,v247,v123\n"
"v_cmp_lt_f32 vcc,s64,v247\n"
"s_cmp_lg_u64 s[80:81],0\n"
"s_cbranch_scc1 Lk_13x\n"
"Lk_13:\n"
"v_mfma_f32_16x16x32_bf16 v[0:3],v[188:191],v[144:147],v[0:3]\n"
"v_exp_f32 v116,v116\n"
"s_waitcnt lgkmcnt(0)\n"
"v_mfma_f32_16x16x32_bf16 v[4:7],v[192:195],v[144:147],v[4:7]\n"
"v_exp_f32 v117,v117\n"
"v_exp_f32 v118,v118\n"
"v_mfma_f32_16x16x32_bf16 v[8:11],v[196:199],v[144:147],v[8:11]\n"
"v_exp_f32 v119,v119\n"
"v_mfma_f32_16x16x32_bf16 v[12:15],v[200:203],v[144:147],v[12:15]\n"
"v_cvt_pk_bf16_f32 v148,v116,v117\n"
"v_exp_f32 v120,v120\n"
"v_mfma_f32_16x16x32_bf16 v[16:19],v[204:207],v[144:147],v[16:19]\n"
"v_exp_f32 v121,v121\n"
"v_cvt_pk_bf16_f32 v149,v118,v119\n"
"v_mfma_f32_16x16x32_bf16 v[20:23],v[208:211],v[144:147],v[20:23]\n"
"v_exp_f32 v122,v122\n"
"v_mfma_f32_16x16x32_bf16 v[24:27],v[212:215],v[144:147],v[24:27]\n"
"v_exp_f32 v123,v123\n"
"v_cvt_pk_bf16_f32 v150,v120,v121\n"
"v_mfma_f32_16x16x32_bf16 v[28:31],v[216:219],v[144:147],v[28:31]\n"
"v_cvt_pk_bf16_f32 v151,v122,v123\n"
"v_max3_f32 v248,v124,v125,v126\n"
"v_max3_f32 v248,v248,v127,v128\n"
"v_mfma_f32_16x16x32_bf16 v[96:99],a[48:51],v[144:147],v[96:99]\n"
"v_max3_f32 v248,v248,v129,v130\n"
"v_max_f32 v248,v248,v131\n"
"v_cmp_lt_f32 s[82:83],s64,v248\n"
"s_cbranch_vccnz Lk_14x\n"
"Lk_14:\n"
"v_mfma_f32_16x16x32_bf16 v[32:35],v[188:191],v[148:151],v[32:35]\n"
"v_exp_f32 v124,v124\n"
"v_mfma_f32_16x16x32_bf16 v[36:39],v[192:195],v[148:151],v[36:39]\n"
"v_exp_f32 v125,v125\n"
"v_mfma_f32_16x16x32_bf16 v[40:43],v[196:199],v[148:151],v[40:43]\n"
"v_exp_f32 v126,v126\n"
"v_mfma_f32_16x16x32_bf16 v[44:47],v[200:203],v[148:151],v[44:47]\n"
"v_exp_f32 v127,v127\n"
"v_cvt_pk_bf16_f32 v152,v124,v125\n"
"v_mfma_f32_16x16x32_bf16 v[48:51],v[204:207],v[148:151],v[48:51]\n"
"v_exp_f32 v128,v128\n"
"v_mfma_f32_16x16x32_bf16 v[52:55],v[208:211],v[148:151],v[52:55]\n"
"v_exp_f32 v129,v129\n"
"v_mfma_f32_16x16x32_bf16 v[56:59],v[212:215],v[148:151],v[56:59]\n"
"v_cvt_pk_bf16_f32 v153,v126,v127\n"
"v_exp_f32 v130,v130\n"
"v_mfma_f32_16x16x32_bf16 v[60:63],v[216:219],v[148:151],v[60:63]\n"
"v_exp_f32 v131,v131\n"
"v_mfma_f32_16x16x32_bf16 v[100:103],a[48:51],v[148:151],v[100:103]\n"
"v_cvt_pk_bf16_f32 v154,v128,v129\n"
"v_cvt_pk_bf16_f32 v155,v130,v131\n"
"s_cmp_lg_u64 s[82:83],0\n"
"s_cbranch_scc1 Lk_15x\n"
"Lk_15:\n"
"v_mfma_f32_16x16x32_bf16 v[64:67],v[188:191],v[152:155],v[64:67]\n"
"ds_read_b128 v[156:159],v220 offset:16384\n"
"v_mfma_f32_16x16x32_bf16 v[68:71],v[192:195],v[152:155],v[68:71]\n"
"ds_read_b128 v[172:175],v220 offset:20480\n"
"v_mfma_f32_16x16x32_bf16 v[72:75],v[196:199],v[152:155],v[72:75]\n"
"ds_read_b128 v[160:163],v221 offset:16384\n"
"v_mfma_f32_16x16x32_bf16 v[76:79],v[200:203],v[152:155],v[76:79]\n"
"ds_read_b128 v[176:179],v221 offset:20480\n"
"v_mfma_f32_16x16x32_bf16 v[80:83],v[204:207],v[152:155],v[80:83]\n"
"v_mfma_f32_16x16x32_bf16 v[84:87],v[208:211],v[152:155],v[84:87]\n"
"ds_read_b128 v[164:167],v222 offset:16384\n"
"v_mfma_f32_16x16x32_bf16 v[88:91],v[212:215],v[152:155],v[88:91]\n"
"ds_read_b128 v[180:183],v222 offset:20480\n"
"v_mfma_f32_16x16x32_bf16 v[92:95],v[216:219],v[152:155],v[92:95]\n"
"ds_read_b128 v[168:171],v223 offset:16384\n"
"v_mfma_f32_16x16x32_bf16 v[104:107],a[48:51],v[152:155],v[104:107]\n"
"ds_read_b128 v[184:187],v223 offset:20480\n"
"s_waitcnt lgkmcnt(7)\n"
"v_mfma_f32_16x16x32_bf16 v[108:111],v[156:159],a[0:3],v[132:135]\n"
"ds_read_b64_tr_b16 v[188:189],v224 offset:16384\n"
"s_waitcnt lgkmcnt(7)\n"
"v_mfma_f32_16x16x32_bf16 v[112:115],v[172:175],a[0:3],v[132:135]\n"
"ds_read_b64_tr_b16 v[190:191],v224 offset:20480\n"
"ds_read_b64_tr_b16 v[192:193],v225 offset:16384\n"
"s_waitcnt lgkmcnt(8)\n"
"v_mfma_f32_16x16x32_bf16 v[108:111],v[160:163],a[12:15],v[108:111]\n"
"ds_read_b64_tr_b16 v[194:195],v225 offset:20480\n"
"s_waitcnt lgkmcnt(8)\n"
"v_mfma_f32_16x16x32_bf16 v[112:115],v[176:179],a[12:15],v[112:115]\n"
"ds_read_b64_tr_b16 v[196:197],v226 offset:16384\n"
"s_waitcnt lgkmcnt(8)\n"
"v_mfma_f32_16x16x32_bf16 v[108:111],v[164:167],a[24:27],v[108:111]\n"
"ds_read_b64_tr_b16 v[198:199],v226 offset:20480\n"
"s_waitcnt lgkmcnt(8)\n"
"v_mfma_f32_16x16x32_bf16 v[112:115],v[180:183],a[24:27],v[112:115]\n"
"ds_read_b64_tr_b16 v[200:201],v227 offset:16384\n"
"ds_read_b64_tr_b16 v[202:203],v227 offset:20480\n"
"s_waitcnt lgkmcnt(9)\n"
"v_mfma_f32_16x16x32_bf16 v[108:111],v[168:171],a[36:39],v[108:111]\n"
"ds_read_b64_tr_b16 v[204:205],v228 offset:16384\n"
"s_waitcnt lgkmcnt(9)\n"
"v_mfma_f32_16x16x32_bf16 v[112:115],v[184:187],a[36:39],v[112:115]\n"
"ds_read_b64_tr_b16 v[206:207],v228 offset:20480\n"
"v_mfma_f32_16x16x32_bf16 v[116:119],v[156:159],a[4:7],v[136:139]\n"
"s_barrier\n"
"ds_read_b64_tr_b16 v[208:209],v229 offset:16384\n"
"ds_read_b64_tr_b16 v[210:211],v229 offset:20480\n"
"v_mfma_f32_16x16x32_bf16 v[120:123],v[172:175],a[4:7],v[136:139]\n"
"ds_read_b64_tr_b16 v[212:213],v230 offset:16384\n"
"ds_read_b64_tr_b16 v[214:215],v230 offset:20480\n"
"v_mfma_f32_16x16x32_bf16 v[116:119],v[160:163],a[16:19],v[116:119]\n"
"ds_read_b64_tr_b16 v[216:217],v231 offset:16384\n"
"s_waitcnt lgkmcnt(11)\n"
"ds_read_b64_tr_b16 v[218:219],v231 offset:20480\n"
"v_max3_f32 v246,v108,v109,v110\n"
"v_mfma_f32_16x16x32_bf16 v[120:123],v[176:179],a[16:19],v[120:123]\n"
"v_max3_f32 v246,v246,v111,v112\n"
"v_max3_f32 v246,v246,v113,v114\n"
"v_mfma_f32_16x16x32_bf16 v[116:119],v[164:167],a[28:31],v[116:119]\n"
"v_max_f32 v246,v246,v115\n"
"v_cmp_lt_f32 s[80:81],s64,v246\n"
"v_mfma_f32_16x16x32_bf16 v[120:123],v[180:183],a[28:31],v[120:123]\n"
"v_exp_f32 v108,v108\n"
"v_exp_f32 v109,v109\n"
"v_mfma_f32_16x16x32_bf16 v[116:119],v[168:171],a[40:43],v[116:119]\n"
"v_exp_f32 v110,v110\n"
"v_mfma_f32_16x16x32_bf16 v[120:123],v[184:187],a[40:43],v[120:123]\n"
"v_exp_f32 v111,v111\n"
"v_mfma_f32_16x16x32_bf16 v[124:127],v[156:159],a[8:11],v[140:143]\n"
"v_cvt_pk_bf16_f32 v144,v108,v109\n"
"v_exp_f32 v112,v112\n"
"v_mfma_f32_16x16x32_bf16 v[128:131],v[172:175],a[8:11],v[140:143]\n"
"v_exp_f32 v113,v113\n"
"v_mfma_f32_16x16x32_bf16 v[124:127],v[160:163],a[20:23],v[124:127]\n"
"v_cvt_pk_bf16_f32 v145,v110,v111\n"
"v_mfma_f32_16x16x32_bf16 v[128:131],v[176:179],a[20:23],v[128:131]\n"
"v_exp_f32 v114,v114\n"
"v_mfma_f32_16x16x32_bf16 v[124:127],v[164:167],a[32:35],v[124:127]\n"
"v_exp_f32 v115,v115\n"
"v_cvt_pk_bf16_f32 v146,v112,v113\n"
"v_mfma_f32_16x16x32_bf16 v[128:131],v[180:183],a[32:35],v[128:131]\n"
"v_cvt_pk_bf16_f32 v147,v114,v115\n"
"v_max3_f32 v247,v116,v117,v118\n"
"v_mfma_f32_16x16x32_bf16 v[124:127],v[168:171],a[44:47],v[124:127]\n"
"v_max3_f32 v247,v247,v119,v120\n"
"v_max3_f32 v247,v247,v121,v122\n"
"v_mfma_f32_16x16x32_bf16 v[128:131],v[184:187],a[44:47],v[128:131]\n"
"v_max_f32 v247,v247,v123\n"
"v_cmp_lt_f32 vcc,s64,v247\n"
"s_cmp_lg_u64 s[80:81],0\n"
"s_cbranch_scc1 Lk_16x\n"
"Lk_16:\n"
"v_mfma_f32_16x16x32_bf16 v[0:3],v[188:191],v[144:147],v[0:3]\n"
"v_exp_f32 v116,v116\n"
"v_mfma_f32_16x16x32_bf16 v[4:7],v[192:195],v[144:147],v[4:7]\n"
"v_exp_f32 v117,v117\n"
"v_exp_f32 v118,v118\n"
"s_waitcnt lgkmcnt(0)\n"
"v_mfma_f32_16x16x32_bf16 v[8:11],v[196:199],v[144:147],v[8:11]\n"
"v_exp_f32 v119,v119\n"
"v_mfma_f32_16x16x32_bf16 v[12:15],v[200:203],v[144:147],v[12:15]\n"
"v_cvt_pk_bf16_f32 v148,v116,v117\n"
"v_exp_f32 v120,v120\n"
"v_mfma_f32_16x16x32_bf16 v[16:19],v[204:207],v[144:147],v[16:19]\n"
"v_exp_f32 v121,v121\n"
"v_cvt_pk_bf16_f32 v149,v118,v119\n"
"v_mfma_f32_16x16x32_bf16 v[20:23],v[208:211],v[144:147],v[20:23]\n"
"v_exp_f32 v122,v122\n"
"v_mfma_f32_16x16x32_bf16 v[24:27],v[212:215],v[144:147],v[24:27]\n"
"v_exp_f32 v123,v123\n"
"v_cvt_pk_bf16_f32 v150,v120,v121\n"
"v_mfma_f32_16x16x32_bf16 v[28:31],v[216:219],v[144:147],v[28:31]\n"
"v_cvt_pk_bf16_f32 v151,v122,v123\n"
"v_max3_f32 v248,v124,v125,v126\n"
"v_max3_f32 v248,v248,v127,v128\n"
"v_mfma_f32_16x16x32_bf16 v[96:99],a[48:51],v[144:147],v[96:99]\n"
"v_max3_f32 v248,v248,v129,v130\n"
"v_max_f32 v248,v248,v131\n"
"v_cmp_lt_f32 s[82:83],s64,v248\n"
"s_cbranch_vccnz Lk_17x\n"
"Lk_17:\n"
"v_mfma_f32_16x16x32_bf16 v[32:35],v[188:191],v[148:151],v[32:35]\n"
"v_exp_f32 v124,v124\n"
"v_mfma_f32_16x16x32_bf16 v[36:39],v[192:195],v[148:151],v[36:39]\n"
"v_exp_f32 v125,v125\n"
"v_mfma_f32_16x16x32_bf16 v[40:43],v[196:199],v[148:151],v[40:43]\n"
"v_exp_f32 v126,v126\n"
"v_mfma_f32_16x16x32_bf16 v[44:47],v[200:203],v[148:151],v[44:47]\n"
"v_exp_f32 v127,v127\n"
"v_cvt_pk_bf16_f32 v152,v124,v125\n"
"v_mfma_f32_16x16x32_bf16 v[48:51],v[204:207],v[148:151],v[48:51]\n"
"v_exp_f32 v128,v128\n"
"v_mfma_f32_16x16x32_bf16 v[52:55],v[208:211],v[148:151],v[52:55]\n"
"v_exp_f32 v129,v129\n"
"v_mfma_f32_16x16x32_bf16 v[56:59],v[212:215],v[148:151],v[56:59]\n"
"v_cvt_pk_bf16_f32 v153,v126,v127\n"
"v_exp_f32 v130,v130\n"
"v_mfma_f32_16x16x32_bf16 v[60:63],v[216:219],v[148:151],v[60:63]\n"
"v_exp_f32 v131,v131\n"
"v_mfma_f32_16x16x32_bf16 v[100:103],a[48:51],v[148:151],v[100:103]\n"
"v_cvt_pk_bf16_f32 v154,v128,v129\n"
"v_cvt_pk_bf16_f32 v155,v130,v131\n"
"s_cmp_lg_u64 s[82:83],0\n"
"s_cbranch_scc1 Lk_18x\n"
"Lk_18:\n"
"v_mfma_f32_16x16x32_bf16 v[64:67],v[188:191],v[152:155],v[64:67]\n"
"ds_read_b128 v[156:159],v220 offset:32768\n"
"v_mfma_f32_16x16x32_bf16 v[68:71],v[192:195],v[152:155],v[68:71]\n"
"ds_read_b128 v[172:175],v220 offset:36864\n"
"v_mfma_f32_16x16x32_bf16 v[72:75],v[196:199],v[152:155],v[72:75]\n"
"ds_read_b128 v[160:163],v221 offset:32768\n"
"v_mfma_f32_16x16x32_bf16 v[76:79],v[200:203],v[152:155],v[76:79]\n"
"ds_read_b128 v[176:179],v221 offset:36864\n"
"v_mfma_f32_16x16x32_bf16 v[80:83],v[204:207],v[152:155],v[80:83]\n"
"v_mfma_f32_16x16x32_bf16 v[84:87],v[208:211],v[152:155],v[84:87]\n"
"ds_read_b128 v[164:167],v222 offset:32768\n"
"v_mfma_f32_16x16x32_bf16 v[88:91],v[212:215],v[152:155],v[88:91]\n"
"ds_read_b128 v[180:183],v222 offset:36864\n"
"v_mfma_f32_16x16x32_bf16 v[92:95],v[216:219],v[152:155],v[92:95]\n"
"ds_read_b128 v[168:171],v223 offset:32768\n"
"v_mfma_f32_16x16x32_bf16 v[104:107],a[48:51],v[152:155],v[104:107]\n"
"ds_read_b128 v[184:187],v223 offset:36864\n"
"s_waitcnt lgkmcnt(7)\n"
"v_mfma_f32_16x16x32_bf16 v[108:111],v[156:159],a[0:3],v[132:135]\n"
"ds_read_b64_tr_b16 v[188:189],v224 offset:32768\n"
"s_waitcnt lgkmcnt(7)\n"
"v_mfma_f32_16x16x32_bf16 v[112:115],v[172:175],a[0:3],v[132:135]\n"
"ds_read_b64_tr_b16 v[190:191],v224 offset:36864\n"
"ds_read_b64_tr_b16 v[192:193],v225 offset:32768\n"
"s_waitcnt lgkmcnt(8)\n"
"v_mfma_f32_16x16x32_bf16 v[108:111],v[160:163],a[12:15],v[108:111]\n"
"ds_read_b64_tr_b16 v[194:195],v225 offset:36864\n"
"s_waitcnt lgkmcnt(8)\n"
"v_mfma_f32_16x16x32_bf16 v[112:115],v[176:179],a[12:15],v[112:115]\n"
"ds_read_b64_tr_b16 v[196:197],v226 offset:32768\n"
"s_waitcnt lgkmcnt(8)\n"
"v_mfma_f32_16x16x32_bf16 v[108:111],v[164:167],a[24:27],v[108:111]\n"
"ds_read_b64_tr_b16 v[198:199],v226 offset:36864\n"
"s_waitcnt lgkmcnt(8)\n"
"v_mfma_f32_16x16x32_bf16 v[112:115],v[180:183],a[24:27],v[112:115]\n"
"ds_read_b64_tr_b16 v[200:201],v227 offset:32768\n"
"ds_read_b64_tr_b16 v[202:203],v227 offset:36864\n"
"s_waitcnt lgkmcnt(9)\n"
"v_mfma_f32_16x16x32_bf16 v[108:111],v[168:171],a[36:39],v[108:111]\n"
"ds_read_b64_tr_b16 v[204:205],v228 offset:32768\n"
"s_waitcnt lgkmcnt(9)\n"
"v_mfma_f32_16x16x32_bf16 v[112:115],v[184:187],a[36:39],v[112:115]\n"
"ds_read_b64_tr_b16 v[206:207],v228 offset:36864\n"
"v_mfma_f32_16x16x32_bf16 v[116:119],v[156:159],a[4:7],v[136:139]\n"
"s_barrier\n"
"ds_read_b64_tr_b16 v[208:209],v229 offset:32768\n"
"ds_read_b64_tr_b16 v[210:211],v229 offset:36864\n"
"v_mfma_f32_16x16x32_bf16 v[120:123],v[172:175],a[4:7],v[136:139]\n"
"ds_read_b64_tr_b16 v[212:213],v230 offset:32768\n"
"ds_read_b64_tr_b16 v[214:215],v230 offset:36864\n"
"v_mfma_f32_16x16x32_bf16 v[116:119],v[160:163],a[16:19],v[116:119]\n"
"ds_read_b64_tr_b16 v[216:217],v231 offset:32768\n"
"s_waitcnt lgkmcnt(11)\n"
"ds_read_b64_tr_b16 v[218:219],v231 offset:36864\n"
"v_max3_f32 v246,v108,v109,v110\n"
"v_mfma_f32_16x16x32_bf16 v[120:123],v[176:179],a[16:19],v[120:123]\n"
"v_max3_f32 v246,v246,v111,v112\n"
"v_max3_f32 v246,v246,v113,v114\n"
"v_mfma_f32_16x16x32_bf16 v[116:119],v[164:167],a[28:31],v[116:119]\n"
"v_max_f32 v246,v246,v115\n"
"v_cmp_lt_f32 s[80:81],s64,v246\n"
"v_mfma_f32_16x16x32_bf16 v[120:123],v[180:183],a[28:31],v[120:123]\n"
"v_exp_f32 v108,v108\n"
"v_exp_f32 v109,v109\n"
"v_mfma_f32_16x16x32_bf16 v[116:119],v[168:171],a[40:43],v[116:119]\n"
"v_exp_f32 v110,v110\n"
"v_mfma_f32_16x16x32_bf16 v[120:123],v[184:187],a[40:43],v[120:123]\n"
"v_exp_f32 v111,v111\n"
"v_mfma_f32_16x16x32_bf16 v[124:127],v[156:159],a[8:11],v[140:143]\n"
"v_cvt_pk_bf16_f32 v144,v108,v109\n"
"v_exp_f32 v112,v112\n"
"v_mfma_f32_16x16x32_bf16 v[128:131],v[172:175],a[8:11],v[140:143]\n"
"v_exp_f32 v113,v113\n"
"v_mfma_f32_16x16x32_bf16 v[124:127],v[160:163],a[20:23],v[124:127]\n"
"v_cvt_pk_bf16_f32 v145,v110,v111\n"
"v_mfma_f32_16x16x32_bf16 v[128:131],v[176:179],a[20:23],v[128:131]\n"
"v_exp_f32 v114,v114\n"
"v_mfma_f32_16x16x32_bf16 v[124:127],v[164:167],a[32:35],v[124:127]\n"
"v_exp_f32 v115,v115\n"
"v_cvt_pk_bf16_f32 v146,v112,v113\n"
"v_mfma_f32_16x16x32_bf16 v[128:131],v[180:183],a[32:35],v[128:131]\n"
"v_cvt_pk_bf16_f32 v147,v114,v115\n"
"v_max3_f32 v247,v116,v117,v118\n"
"v_mfma_f32_16x16x32_bf16 v[124:127],v[168:171],a[44:47],v[124:127]\n"
"v_max3_f32 v247,v247,v119,v120\n"
"v_max3_f32 v247,v247,v121,v122\n"
"v_mfma_f32_16x16x32_bf16 v[128:131],v[184:187],a[44:47],v[128:131]\n"
"v_max_f32 v247,v247,v123\n"
"v_cmp_lt_f32 vcc,s64,v247\n"
"s_cmp_lg_u64 s[80:81],0\n"
"s_cbranch_scc1 Lk_19x\n"
"Lk_19:\n"
"v_mfma_f32_16x16x32_bf16 v[0:3],v[188:191],v[144:147],v[0:3]\n"
"v_exp_f32 v116,v116\n"
"v_mfma_f32_16x16x32_bf16 v[4:7],v[192:195],v[144:147],v[4:7]\n"
"v_exp_f32 v117,v117\n"
"v_exp_f32 v118,v118\n"
"s_waitcnt lgkmcnt(0)\n"
"v_mfma_f32_16x16x32_bf16 v[8:11],v[196:199],v[144:147],v[8:11]\n"
"v_exp_f32 v119,v119\n"
"v_mfma_f32_16x16x32_bf16 v[12:15],v[200:203],v[144:147],v[12:15]\n"
"v_cvt_pk_bf16_f32 v148,v116,v117\n"
"v_exp_f32 v120,v120\n"
"v_mfma_f32_16x16x32_bf16 v[16:19],v[204:207],v[144:147],v[16:19]\n"
"v_exp_f32 v121,v121\n"
"v_cvt_pk_bf16_f32 v149,v118,v119\n"
"v_mfma_f32_16x16x32_bf16 v[20:23],v[208:211],v[144:147],v[20:23]\n"
"v_exp_f32 v122,v122\n"
"v_mfma_f32_16x16x32_bf16 v[24:27],v[212:215],v[144:147],v[24:27]\n"
"v_exp_f32 v123,v123\n"
"v_cvt_pk_bf16_f32 v150,v120,v121\n"
"v_mfma_f32_16x16x32_bf16 v[28:31],v[216:219],v[144:147],v[28:31]\n"
"v_cvt_pk_bf16_f32 v151,v122,v123\n"
"v_max3_f32 v248,v124,v125,v126\n"
"v_max3_f32 v248,v248,v127,v128\n"
"v_mfma_f32_16x16x32_bf16 v[96:99],a[48:51],v[144:147],v[96:99]\n"
"v_max3_f32 v248,v248,v129,v130\n"
"v_max_f32 v248,v248,v131\n"
"v_cmp_lt_f32 s[82:83],s64,v248\n"
"s_cbranch_vccnz Lk_20x\n"
"Lk_20:\n"
"v_mfma_f32_16x16x32_bf16 v[32:35],v[188:191],v[148:151],v[32:35]\n"
"v_exp_f32 v124,v124\n"
"v_mfma_f32_16x16x32_bf16 v[36:39],v[192:195],v[148:151],v[36:39]\n"
"v_exp_f32 v125,v125\n"
"v_mfma_f32_16x16x32_bf16 v[40:43],v[196:199],v[148:151],v[40:43]\n"
"v_exp_f32 v126,v126\n"
"v_mfma_f32_16x16x32_bf16 v[44:47],v[200:203],v[148:151],v[44:47]\n"
"v_exp_f32 v127,v127\n"
"v_cvt_pk_bf16_f32 v152,v124,v125\n"
"v_mfma_f32_16x16x32_bf16 v[48:51],v[204:207],v[148:151],v[48:51]\n"
"v_exp_f32 v128,v128\n"
"v_mfma_f32_16x16x32_bf16 v[52:55],v[208:211],v[148:151],v[52:55]\n"
"v_exp_f32 v129,v129\n"
"v_mfma_f32_16x16x32_bf16 v[56:59],v[212:215],v[148:151],v[56:59]\n"
"v_cvt_pk_bf16_f32 v153,v126,v127\n"
"v_exp_f32 v130,v130\n"
"v_mfma_f32_16x16x32_bf16 v[60:63],v[216:219],v[148:151],v[60:63]\n"
"v_exp_f32 v131,v131\n"
"v_mfma_f32_16x16x32_bf16 v[100:103],a[48:51],v[148:151],v[100:103]\n"
"v_cvt_pk_bf16_f32 v154,v128,v129\n"
"v_cvt_pk_bf16_f32 v155,v130,v131\n"
"s_cmp_lg_u64 s[82:83],0\n"
"s_cbranch_scc1 Lk_21x\n"
"Lk_21:\n"
"v_mfma_f32_16x16x32_bf16 v[64:67],v[188:191],v[152:155],v[64:67]\n"
"ds_read_b128 v[156:159],v220 offset:49152\n"
"v_mfma_f32_16x16x32_bf16 v[68:71],v[192:195],v[152:155],v[68:71]\n"
"ds_read_b128 v[172:175],v220 offset:53248\n"
"v_mfma_f32_16x16x32_bf16 v[72:75],v[196:199],v[152:155],v[72:75]\n"
"ds_read_b128 v[160:163],v221 offset:49152\n"
"v_mfma_f32_16x16x32_bf16 v[76:79],v[200:203],v[152:155],v[76:79]\n"
"ds_read_b128 v[176:179],v221 offset:53248\n"
"v_mfma_f32_16x16x32_bf16 v[80:83],v[204:207],v[152:155],v[80:83]\n"
"v_mfma_f32_16x16x32_bf16 v[84:87],v[208:211],v[152:155],v[84:87]\n"
"ds_read_b128 v[164:167],v222 offset:49152\n"
"v_mfma_f32_16x16x32_bf16 v[88:91],v[212:215],v[152:155],v[88:91]\n"
"ds_read_b128 v[180:183],v222 offset:53248\n"
"v_mfma_f32_16x16x32_bf16 v[92:95],v[216:219],v[152:155],v[92:95]\n"
"ds_read_b128 v[168:171],v223 offset:49152\n"
"v_mfma_f32_16x16x32_bf16 v[104:107],a[48:51],v[152:155],v[104:107]\n"
"ds_read_b128 v[184:187],v223 offset:53248\n"
"s_waitcnt lgkmcnt(7)\n"
"v_mfma_f32_16x16x32_bf16 v[108:111],v[156:159],a[0:3],v[132:135]\n"
"ds_read_b64_tr_b16 v[188:189],v224 offset:49152\n"
"s_waitcnt lgkmcnt(7)\n"
"v_mfma_f32_16x16x32_bf16 v[112:115],v[172:175],a[0:3],v[132:135]\n"
"ds_read_b64_tr_b16 v[190:191],v224 offset:53248\n"
"ds_read_b64_tr_b16 v[192:193],v225 offset:49152\n"
"s_waitcnt lgkmcnt(8)\n"
"v_mfma_f32_16x16x32_bf16 v[108:111],v[160:163],a[12:15],v[108:111]\n"
"ds_read_b64_tr_b16 v[194:195],v225 offset:53248\n"
"s_waitcnt lgkmcnt(8)\n"
"v_mfma_f32_16x16x32_bf16 v[112:115],v[176:179],a[12:15],v[112:115]\n"
"ds_read_b64_tr_b16 v[196:197],v226 offset:49152\n"
"s_waitcnt lgkmcnt(8)\n"
"v_mfma_f32_16x16x32_bf16 v[108:111],v[164:167],a[24:27],v[108:111]\n"
"ds_read_b64_tr_b16 v[198:199],v226 offset:53248\n"
"s_waitcnt lgkmcnt(8)\n"
"v_mfma_f32_16x16x32_bf16 v[112:115],v[180:183],a[24:27],v[112:115]\n"
"ds_read_b64_tr_b16 v[200:201],v227 offset:49152\n"
"ds_read_b64_tr_b16 v[202:203],v227 offset:53248\n"
"s_waitcnt lgkmcnt(9)\n"
"v_mfma_f32_16x16x32_bf16 v[108:111],v[168:171],a[36:39],v[108:111]\n"
"ds_read_b64_tr_b16 v[204:205],v228 offset:49152\n"
"s_waitcnt lgkmcnt(9)\n"
"v_mfma_f32_16x16x32_bf16 v[112:115],v[184:187],a[36:39],v[112:115]\n"
"ds_read_b64_tr_b16 v[206:207],v228 offset:53248\n"
"v_mfma_f32_16x16x32_bf16 v[116:119],v[156:159],a[4:7],v[136:139]\n"
"s_barrier\n"
"ds_read_b64_tr_b16 v[208:209],v229 offset:49152\n"
"ds_read_b64_tr_b16 v[210:211],v229 offset:53248\n"
"v_mfma_f32_16x16x32_bf16 v[120:123],v[172:175],a[4:7],v[136:139]\n"
"ds_read_b64_tr_b16 v[212:213],v230 offset:49152\n"
"ds_read_b64_tr_b16 v[214:215],v230 offset:53248\n"
"v_mfma_f32_16x16x32_bf16 v[116:119],v[160:163],a[16:19],v[116:119]\n"
"ds_read_b64_tr_b16 v[216:217],v231 offset:49152\n"
"s_waitcnt lgkmcnt(11)\n"
"ds_read_b64_tr_b16 v[218:219],v231 offset:53248\n"
"v_max3_f32 v246,v108,v109,v110\n"
"v_mfma_f32_16x16x32_bf16 v[120:123],v[176:179],a[16:19],v[120:123]\n"
"v_max3_f32 v246,v246,v111,v112\n"
"v_max3_f32 v246,v246,v113,v114\n"
"v_mfma_f32_16x16x32_bf16 v[116:119],v[164:167],a[28:31],v[116:119]\n"
"v_max_f32 v246,v246,v115\n"
"v_cmp_lt_f32 s[80:81],s64,v246\n"
"v_mfma_f32_16x16x32_bf16 v[120:123],v[180:183],a[28:31],v[120:123]\n"
"v_exp_f32 v108,v108\n"
"v_exp_f32 v109,v109\n"
"v_mfma_f32_16x16x32_bf16 v[116:119],v[168:171],a[40:43],v[116:119]\n"
"v_exp_f32 v110,v110\n"
"v_mfma_f32_16x16x32_bf16 v[120:123],v[184:187],a[40:43],v[120:123]\n"
"v_exp_f32 v111,v111\n"
"v_mfma_f32_16x16x32_bf16 v[124:127],v[156:159],a[8:11],v[140:143]\n"
"v_cvt_pk_bf16_f32 v144,v108,v109\n"
"v_exp_f32 v112,v112\n"
"v_mfma_f32_16x16x32_bf16 v[128:131],v[172:175],a[8:11],v[140:143]\n"
"v_exp_f32 v113,v113\n"
"v_mfma_f32_16x16x32_bf16 v[124:127],v[160:163],a[20:23],v[124:127]\n"
"v_cvt_pk_bf16_f32 v145,v110,v111\n"
"v_mfma_f32_16x16x32_bf16 v[128:131],v[176:179],a[20:23],v[128:131]\n"
"v_exp_f32 v114,v114\n"
"v_mfma_f32_16x16x32_bf16 v[124:127],v[164:167],a[32:35],v[124:127]\n"
"v_exp_f32 v115,v115\n"
"v_cvt_pk_bf16_f32 v146,v112,v113\n"
"v_mfma_f32_16x16x32_bf16 v[128:131],v[180:183],a[32:35],v[128:131]\n"
"v_cvt_pk_bf16_f32 v147,v114,v115\n"
"v_max3_f32 v247,v116,v117,v118\n"
"v_mfma_f32_16x16x32_bf16 v[124:127],v[168:171],a[44:47],v[124:127]\n"
"v_max3_f32 v247,v247,v119,v120\n"
"v_max3_f32 v247,v247,v121,v122\n"
"v_mfma_f32_16x16x32_bf16 v[128:131],v[184:187],a[44:47],v[128:131]\n"
"v_max_f32 v247,v247,v123\n"
"v_cmp_lt_f32 vcc,s64,v247\n"
"s_cmp_lg_u64 s[80:81],0\n"
"s_cbranch_scc1 Lk_22x\n"
"Lk_22:\n"
"v_mfma_f32_16x16x32_bf16 v[0:3],v[188:191],v[144:147],v[0:3]\n"
"v_exp_f32 v116,v116\n"
"v_mfma_f32_16x16x32_bf16 v[4:7],v[192:195],v[144:147],v[4:7]\n"
"v_exp_f32 v117,v117\n"
"v_exp_f32 v118,v118\n"
"s_waitcnt lgkmcnt(0)\n"
"v_mfma_f32_16x16x32_bf16 v[8:11],v[196:199],v[144:147],v[8:11]\n"
"v_exp_f32 v119,v119\n"
"v_mfma_f32_16x16x32_bf16 v[12:15],v[200:203],v[144:147],v[12:15]\n"
"v_cvt_pk_bf16_f32 v148,v116,v117\n"
"v_exp_f32 v120,v120\n"
"v_mfma_f32_16x16x32_bf16 v[16:19],v[204:207],v[144:147],v[16:19]\n"
"v_exp_f32 v121,v121\n"
"v_cvt_pk_bf16_f32 v149,v118,v119\n"
"v_mfma_f32_16x16x32_bf16 v[20:23],v[208:211],v[144:147],v[20:23]\n"
"v_exp_f32 v122,v122\n"
"v_mfma_f32_16x16x32_bf16 v[24:27],v[212:215],v[144:147],v[24:27]\n"
"v_exp_f32 v123,v123\n"
"v_cvt_pk_bf16_f32 v150,v120,v121\n"
"v_mfma_f32_16x16x32_bf16 v[28:31],v[216:219],v[144:147],v[28:31]\n"
"v_cvt_pk_bf16_f32 v151,v122,v123\n"
"v_max3_f32 v248,v124,v125,v126\n"
"v_max3_f32 v248,v248,v127,v128\n"
"v_mfma_f32_16x16x32_bf16 v[96:99],a[48:51],v[144:147],v[96:99]\n"
"v_max3_f32 v248,v248,v129,v130\n"
"v_max_f32 v248,v248,v131\n"
"v_cmp_lt_f32 s[82:83],s64,v248\n"
"s_cbranch_vccnz Lk_23x\n"
"Lk_23:\n"
"v_mfma_f32_16x16x32_bf16 v[32:35],v[188:191],v[148:151],v[32:35]\n"
"v_exp_f32 v124,v124\n"
"v_mfma_f32_16x16x32_bf16 v[36:39],v[192:195],v[148:151],v[36:39]\n"
"v_exp_f32 v125,v125\n"
"v_mfma_f32_16x16x32_bf16 v[40:43],v[196:199],v[148:151],v[40:43]\n"
"v_exp_f32 v126,v126\n"
"v_mfma_f32_16x16x32_bf16 v[44:47],v[200:203],v[148:151],v[44:47]\n"
"v_exp_f32 v127,v127\n"
"v_cvt_pk_bf16_f32 v152,v124,v125\n"
"v_mfma_f32_16x16x32_bf16 v[48:51],v[204:207],v[148:151],v[48:51]\n"
"v_exp_f32 v128,v128\n"
"v_mfma_f32_16x16x32_bf16 v[52:55],v[208:211],v[148:151],v[52:55]\n"
"v_exp_f32 v129,v129\n"
"v_mfma_f32_16x16x32_bf16 v[56:59],v[212:215],v[148:151],v[56:59]\n"
"v_cvt_pk_bf16_f32 v153,v126,v127\n"
"v_exp_f32 v130,v130\n"
"v_mfma_f32_16x16x32_bf16 v[60:63],v[216:219],v[148:151],v[60:63]\n"
"v_exp_f32 v131,v131\n"
"v_mfma_f32_16x16x32_bf16 v[100:103],a[48:51],v[148:151],v[100:103]\n"
"v_cvt_pk_bf16_f32 v154,v128,v129\n"
"v_cvt_pk_bf16_f32 v155,v130,v131\n"
"s_cmp_lg_u64 s[82:83],0\n"
"s_cbranch_scc1 Lk_24x\n"
"Lk_24:\n"
"v_mfma_f32_16x16x32_bf16 v[64:67],v[188:191],v[152:155],v[64:67]\n"
"s_mov_b32 s60,0x10000\n"
"v_add_u32 v220,s60,v220\n"
"v_mfma_f32_16x16x32_bf16 v[68:71],v[192:195],v[152:155],v[68:71]\n"
"v_add_u32 v221,s60,v221\n"
"v_mfma_f32_16x16x32_bf16 v[72:75],v[196:199],v[152:155],v[72:75]\n"
"v_add_u32 v222,s60,v222\n"
"v_add_u32 v223,s60,v223\n"
"v_mfma_f32_16x16x32_bf16 v[76:79],v[200:203],v[152:155],v[76:79]\n"
"ds_read_b128 v[156:159],v220\n"
"v_mfma_f32_16x16x32_bf16 v[80:83],v[204:207],v[152:155],v[80:83]\n"
"ds_read_b128 v[172:175],v220 offset:4096\n"
"v_mfma_f32_16x16x32_bf16 v[84:87],v[208:211],v[152:155],v[84:87]\n"
"ds_read_b128 v[160:163],v221\n"
"ds_read_b128 v[176:179],v221 offset:4096\n"
"v_mfma_f32_16x16x32_bf16 v[88:91],v[212:215],v[152:155],v[88:91]\n"
"ds_read_b128 v[164:167],v222\n"
"v_mfma_f32_16x16x32_bf16 v[92:95],v[216:219],v[152:155],v[92:95]\n"
"ds_read_b128 v[180:183],v222 offset:4096\n"
"ds_read_b128 v[168:171],v223\n"
"v_mfma_f32_16x16x32_bf16 v[104:107],a[48:51],v[152:155],v[104:107]\n"
"ds_read_b128 v[184:187],v223 offset:4096\n"
"s_mov_b32 s60,0x10000\n"
"v_add_u32 v224,s60,v224\n"
"v_add_u32 v225,s60,v225\n"
"v_add_u32 v226,s60,v226\n"
"v_add_u32 v227,s60,v227\n"
"v_add_u32 v228,s60,v228\n"
"v_add_u32 v229,s60,v229\n"
"v_add_u32 v230,s60,v230\n"
"v_add_u32 v231,s60,v231\n"
"s_waitcnt lgkmcnt(7)\n"
"v_mfma_f32_16x16x32_bf16 v[108:111],v[156:159],a[0:3],v[132:135]\n"
"ds_read_b64_tr_b16 v[188:189],v224\n"
"s_waitcnt lgkmcnt(7)\n"
"v_mfma_f32_16x16x32_bf16 v[112:115],v[172:175],a[0:3],v[132:135]\n"
"ds_read_b64_tr_b16 v[190:191],v224 offset:4096\n"
"ds_read_b64_tr_b16 v[192:193],v225\n"
"s_waitcnt lgkmcnt(8)\n"
"v_mfma_f32_16x16x32_bf16 v[108:111],v[160:163],a[12:15],v[108:111]\n"
"ds_read_b64_tr_b16 v[194:195],v225 offset:4096\n"
"s_waitcnt lgkmcnt(8)\n"
"v_mfma_f32_16x16x32_bf16 v[112:115],v[176:179],a[12:15],v[112:115]\n"
"ds_read_b64_tr_b16 v[196:197],v226\n"
"s_waitcnt lgkmcnt(8)\n"
"v_mfma_f32_16x16x32_bf16 v[108:111],v[164:167],a[24:27],v[108:111]\n"
"ds_read_b64_tr_b16 v[198:199],v226 offset:4096\n"
"s_waitcnt lgkmcnt(8)\n"
"v_mfma_f32_16x16x32_bf16 v[112:115],v[180:183],a[24:27],v[112:115]\n"
"ds_read_b64_tr_b16 v[200:201],v227\n"
"ds_read_b64_tr_b16 v[202:203],v227 offset:4096\n"
"s_waitcnt lgkmcnt(8)\n"
"v_mfma_f32_16x16x32_bf16 v[108:111],v[168:171],a[36:39],v[108:111]\n"
"ds_read_b64_tr_b16 v[204:205],v228\n"
"v_mfma_f32_16x16x32_bf16 v[112:115],v[184:187],a[36:39],v[112:115]\n"
"ds_read_b64_tr_b16 v[206:207],v228 offset:4096\n"
"v_mfma_f32_16x16x32_bf16 v[116:119],v[156:159],a[4:7],v[136:139]\n"
"s_barrier\n"
"s_add_u32 s67,s1,0x3000\n"
"s_and_b32 s67,s67,0x3fff\n"
"s_add_u32 m0,s66,0x8000\n"
"s_nop 0\n"
"buffer_load_dwordx4 v232,s[24:27],s67 offen nt lds\n"
"v_mfma_f32_16x16x32_bf16 v[120:123],v[172:175],a[4:7],v[136:139]\n"
"buffer_load_dwordx4 v233,s[24:27],s67 offen offset:1024 nt lds\n"
"s_add_u32 m0,s66,0xa000\n"
"s_nop 0\n"
"buffer_load_dwordx4 v236,s[28:31],s67 offen nt lds\n"
"v_mfma_f32_16x16x32_bf16 v[116:119],v[160:163],a[16:19],v[116:119]\n"
"buffer_load_dwordx4 v237,s[28:31],s67 offen offset:1024 nt lds\n"
"ds_read_b64_tr_b16 v[208:209],v229\n"
"ds_read_b64_tr_b16 v[210:211],v229 offset:4096\n"
"v_mfma_f32_16x16x32_bf16 v[120:123],v[176:179],a[16:19],v[120:123]\n"
"ds_read_b64_tr_b16 v[212:213],v230\n"
"ds_read_b64_tr_b16 v[214:215],v230 offset:4096\n"
"ds_read_b64_tr_b16 v[216:217],v231\n"
"s_waitcnt lgkmcnt(9)\n"
"ds_read_b64_tr_b16 v[218:219],v231 offset:4096\n"
"v_mfma_f32_16x16x32_bf16 v[116:119],v[164:167],a[28:31],v[116:119]\n"
"v_max3_f32 v246,v108,v109,v110\n"
"v_max3_f32 v246,v246,v111,v112\n"
"v_max3_f32 v246,v246,v113,v114\n"
"v_mfma_f32_16x16x32_bf16 v[120:123],v[180:183],a[28:31],v[120:123]\n"
"v_max_f32 v246,v246,v115\n"
"v_cmp_lt_f32 s[80:81],s64,v246\n"
"v_exp_f32 v108,v108\n"
"v_mfma_f32_16x16x32_bf16 v[116:119],v[168:171],a[40:43],v[116:119]\n"
"v_exp_f32 v109,v109\n"
"v_mfma_f32_16x16x32_bf16 v[120:123],v[184:187],a[40:43],v[120:123]\n"
"v_exp_f32 v110,v110\n"
"v_exp_f32 v111,v111\n"
"v_mfma_f32_16x16x32_bf16 v[124:127],v[156:159],a[8:11],v[140:143]\n"
"v_cvt_pk_bf16_f32 v144,v108,v109\n"
"v_exp_f32 v112,v112\n"
"v_mfma_f32_16x16x32_bf16 v[128:131],v[172:175],a[8:11],v[140:143]\n"
"v_exp_f32 v113,v113\n"
"v_mfma_f32_16x16x32_bf16 v[124:127],v[160:163],a[20:23],v[124:127]\n"
"v_cvt_pk_bf16_f32 v145,v110,v111\n"
"v_mfma_f32_16x16x32_bf16 v[128:131],v[176:179],a[20:23],v[128:131]\n"
"v_exp_f32 v114,v114\n"
"v_mfma_f32_16x16x32_bf16 v[124:127],v[164:167],a[32:35],v[124:127]\n"
"v_exp_f32 v115,v115\n"
"v_cvt_pk_bf16_f32 v146,v112,v113\n"
"v_mfma_f32_16x16x32_bf16 v[128:131],v[180:183],a[32:35],v[128:131]\n"
"v_cvt_pk_bf16_f32 v147,v114,v115\n"
"v_max3_f32 v247,v116,v117,v118\n"
"v_mfma_f32_16x16x32_bf16 v[124:127],v[168:171],a[44:47],v[124:127]\n"
"v_max3_f32 v247,v247,v119,v120\n"
"v_max3_f32 v247,v247,v121,v122\n"
"v_mfma_f32_16x16x32_bf16 v[128:131],v[184:187],a[44:47],v[128:131]\n"
"v_max_f32 v247,v247,v123\n"
"v_cmp_lt_f32 vcc,s64,v247\n"
"s_cmp_lg_u64 s[80:81],0\n"
"s_cbranch_scc1 Lk_25x\n"
"Lk_25:\n"
"v_mfma_f32_16x16x32_bf16 v[0:3],v[188:191],v[144:147],v[0:3]\n"
"v_exp_f32 v116,v116\n"
"v_mfma_f32_16x16x32_bf16 v[4:7],v[192:195],v[144:147],v[4:7]\n"
"v_exp_f32 v117,v117\n"
"v_exp_f32 v118,v118\n"
"v_mfma_f32_16x16x32_bf16 v[8:11],v[196:199],v[144:147],v[8:11]\n"
"v_exp_f32 v119,v119\n"
"s_waitcnt lgkmcnt(0)\n"
"v_mfma_f32_16x16x32_bf16 v[12:15],v[200:203],v[144:147],v[12:15]\n"
"v_cvt_pk_bf16_f32 v148,v116,v117\n"
"v_exp_f32 v120,v120\n"
"v_mfma_f32_16x16x32_bf16 v[16:19],v[204:207],v[144:147],v[16:19]\n"
"v_exp_f32 v121,v121\n"
"v_cvt_pk_bf16_f32 v149,v118,v119\n"
"v_mfma_f32_16x16x32_bf16 v[20:23],v[208:211],v[144:147],v[20:23]\n"
"v_exp_f32 v122,v122\n"
"v_mfma_f32_16x16x32_bf16 v[24:27],v[212:215],v[144:147],v[24:27]\n"
"v_exp_f32 v123,v123\n"
"v_cvt_pk_bf16_f32 v150,v120,v121\n"
"v_mfma_f32_16x16x32_bf16 v[28:31],v[216:219],v[144:147],v[28:31]\n"
"v_cvt_pk_bf16_f32 v151,v122,v123\n"
"v_max3_f32 v248,v124,v125,v126\n"
"v_max3_f32 v248,v248,v127,v128\n"
"v_mfma_f32_16x16x32_bf16 v[96:99],a[48:51],v[144:147],v[96:99]\n"
"v_max3_f32 v248,v248,v129,v130\n"
"v_max_f32 v248,v248,v131\n"
"v_cmp_lt_f32 s[82:83],s64,v248\n"
"s_cbranch_vccnz Lk_26x\n"
"Lk_26:\n"
"v_mfma_f32_16x16x32_bf16 v[32:35],v[188:191],v[148:151],v[32:35]\n"
"v_exp_f32 v124,v124\n"
"v_mfma_f32_16x16x32_bf16 v[36:39],v[192:195],v[148:151],v[36:39]\n"
"v_exp_f32 v125,v125\n"
"v_mfma_f32_16x16x32_bf16 v[40:43],v[196:199],v[148:151],v[40:43]\n"
"v_exp_f32 v126,v126\n"
"v_mfma_f32_16x16x32_bf16 v[44:47],v[200:203],v[148:151],v[44:47]\n"
"v_exp_f32 v127,v127\n"
"v_cvt_pk_bf16_f32 v152,v124,v125\n"
"v_mfma_f32_16x16x32_bf16 v[48:51],v[204:207],v[148:151],v[48:51]\n"
"v_exp_f32 v128,v128\n"
"v_mfma_f32_16x16x32_bf16 v[52:55],v[208:211],v[148:151],v[52:55]\n"
"v_exp_f32 v129,v129\n"
"v_mfma_f32_16x16x32_bf16 v[56:59],v[212:215],v[148:151],v[56:59]\n"
"v_cvt_pk_bf16_f32 v153,v126,v127\n"
"v_exp_f32 v130,v130\n"
"v_mfma_f32_16x16x32_bf16 v[60:63],v[216:219],v[148:151],v[60:63]\n"
"v_exp_f32 v131,v131\n"
"v_mfma_f32_16x16x32_bf16 v[100:103],a[48:51],v[148:151],v[100:103]\n"
"v_cvt_pk_bf16_f32 v154,v128,v129\n"
"v_cvt_pk_bf16_f32 v155,v130,v131\n"
"s_cmp_lg_u64 s[82:83],0\n"
"s_cbranch_scc1 Lk_27x\n"
"Lk_27:\n"
"v_mfma_f32_16x16x32_bf16 v[64:67],v[188:191],v[152:155],v[64:67]\n"
"ds_read_b128 v[156:159],v220 offset:16384\n"
"v_mfma_f32_16x16x32_bf16 v[68:71],v[192:195],v[152:155],v[68:71]\n"
"ds_read_b128 v[172:175],v220 offset:20480\n"
"v_mfma_f32_16x16x32_bf16 v[72:75],v[196:199],v[152:155],v[72:75]\n"
"ds_read_b128 v[160:163],v221 offset:16384\n"
"v_mfma_f32_16x16x32_bf16 v[76:79],v[200:203],v[152:155],v[76:79]\n"
"ds_read_b128 v[176:179],v221 offset:20480\n"
"v_mfma_f32_16x16x32_bf16 v[80:83],v[204:207],v[152:155],v[80:83]\n"
"v_mfma_f32_16x16x32_bf16 v[84:87],v[208:211],v[152:155],v[84:87]\n"
"ds_read_b128 v[164:167],v222 offset:16384\n"
"v_mfma_f32_16x16x32_bf16 v[88:91],v[212:215],v[152:155],v[88:91]\n"
"ds_read_b128 v[180:183],v222 offset:20480\n"
"v_mfma_f32_16x16x32_bf16 v[92:95],v[216:219],v[152:155],v[92:95]\n"
"ds_read_b128 v[168:171],v223 offset:16384\n"
"v_mfma_f32_16x16x32_bf16 v[104:107],a[48:51],v[152:155],v[104:107]\n"
"ds_read_b128 v[184:187],v223 offset:20480\n"
"s_sleep 12\n"
"s_waitcnt lgkmcnt(7)\n"
"v_mfma_f32_16x16x32_bf16 v[108:111],v[156:159],a[0:3],v[132:135]\n"
"ds_read_b64_tr_b16 v[188:189],v224 offset:16384\n"
"s_waitcnt lgkmcnt(7)\n"
"v_mfma_f32_16x16x32_bf16 v[112:115],v[172:175],a[0:3],v[132:135]\n"
"ds_read_b64_tr_b16 v[190:191],v224 offset:20480\n"
"ds_read_b64_tr_b16 v[192:193],v225 offset:16384\n"
"s_waitcnt lgkmcnt(8)\n"
"v_mfma_f32_16x16x32_bf16 v[108:111],v[160:163],a[12:15],v[108:111]\n"
"ds_read_b64_tr_b16 v[194:195],v225 offset:20480\n"
"s_waitcnt lgkmcnt(8)\n"
"v_mfma_f32_16x16x32_bf16 v[112:115],v[176:179],a[12:15],v[112:115]\n"
"ds_read_b64_tr_b16 v[196:197],v226 offset:16384\n"
"s_waitcnt lgkmcnt(8)\n"
"v_mfma_f32_16x16x32_bf16 v[108:111],v[164:167],a[24:27],v[108:111]\n"
"ds_read_b64_tr_b16 v[198:199],v226 offset:20480\n"
"s_waitcnt lgkmcnt(8)\n"
"v_mfma_f32_16x16x32_bf16 v[112:115],v[180:183],a[24:27],v[112:115]\n"
"ds_read_b64_tr_b16 v[200:201],v227 offset:16384\n"
"ds_read_b64_tr_b16 v[202:203],v227 offset:20480\n"
"s_waitcnt lgkmcnt(9)\n"
"v_mfma_f32_16x16x32_bf16 v[108:111],v[168:171],a[36:39],v[108:111]\n"
"ds_read_b64_tr_b16 v[204:205],v228 offset:16384\n"
"s_waitcnt lgkmcnt(9)\n"
"v_mfma_f32_16x16x32_bf16 v[112:115],v[184:187],a[36:39],v[112:115]\n"
"ds_read_b64_tr_b16 v[206:207],v228 offset:20480\n"
"v_mfma_f32_16x16x32_bf16 v[116:119],v[156:159],a[4:7],v[136:139]\n"
"s_barrier\n"
"s_add_u32 s67,s1,0x3400\n"
"s_and_b32 s67,s67,0x3fff\n"
"s_add_u32 m0,s66,0xc000\n"
"s_nop 0\n"
"buffer_load_dwordx4 v232,s[24:27],s67 offen nt lds\n"
"v_mfma_f32_16x16x32_bf16 v[120:123],v[172:175],a[4:7],v[136:139]\n"
"buffer_load_dwordx4 v233,s[24:27],s67 offen offset:1024 nt lds\n"
"s_add_u32 m0,s66,0xe000\n"
"s_nop 0\n"
"buffer_load_dwordx4 v236,s[28:31],s67 offen nt lds\n"
"v_mfma_f32_16x16x32_bf16 v[116:119],v[160:163],a[16:19],v[116:119]\n"
"buffer_load_dwordx4 v237,s[28:31],s67 offen offset:1024 nt lds\n"
"ds_read_b64_tr_b16 v[208:209],v229 offset:16384\n"
"ds_read_b64_tr_b16 v[210:211],v229 offset:20480\n"
"v_mfma_f32_16x16x32_bf16 v[120:123],v[176:179],a[16:19],v[120:123]\n"
"ds_read_b64_tr_b16 v[212:213],v230 offset:16384\n"
"ds_read_b64_tr_b16 v[214:215],v230 offset:20480\n"
"ds_read_b64_tr_b16 v[216:217],v231 offset:16384\n"
"s_waitcnt lgkmcnt(9)\n"
"ds_read_b64_tr_b16 v[218:219],v231 offset:20480\n"
"v_mfma_f32_16x16x32_bf16 v[116:119],v[164:167],a[28:31],v[116:119]\n"
"v_max3_f32 v246,v108,v109,v110\n"
"v_max3_f32 v246,v246,v111,v112\n"
"v_max3_f32 v246,v246,v113,v114\n"
"v_mfma_f32_16x16x32_bf16 v[120:123],v[180:183],a[28:31],v[120:123]\n"
"v_max_f32 v246,v246,v115\n"
"v_cmp_lt_f32 s[80:81],s64,v246\n"
"v_exp_f32 v108,v108\n"
"v_mfma_f32_16x16x32_bf16 v[116:119],v[168:171],a[40:43],v[116:119]\n"
"v_exp_f32 v109,v109\n"
"v_mfma_f32_16x16x32_bf16 v[120:123],v[184:187],a[40:43],v[120:123]\n"
"v_exp_f32 v110,v110\n"
"v_exp_f32 v111,v111\n"
"v_mfma_f32_16x16x32_bf16 v[124:127],v[156:159],a[8:11],v[140:143]\n"
"v_cvt_pk_bf16_f32 v144,v108,v109\n"
"v_exp_f32 v112,v112\n"
"v_mfma_f32_16x16x32_bf16 v[128:131],v[172:175],a[8:11],v[140:143]\n"
"v_exp_f32 v113,v113\n"
"v_mfma_f32_16x16x32_bf16 v[124:127],v[160:163],a[20:23],v[124:127]\n"
"v_cvt_pk_bf16_f32 v145,v110,v111\n"
"v_mfma_f32_16x16x32_bf16 v[128:131],v[176:179],a[20:23],v[128:131]\n"
"v_exp_f32 v114,v114\n"
"v_mfma_f32_16x16x32_bf16 v[124:127],v[164:167],a[32:35],v[124:127]\n"
"v_exp_f32 v115,v115\n"
"v_cvt_pk_bf16_f32 v146,v112,v113\n"
"v_mfma_f32_16x16x32_bf16 v[128:131],v[180:183],a[32:35],v[128:131]\n"
"v_cvt_pk_bf16_f32 v147,v114,v115\n"
"v_max3_f32 v247,v116,v117,v118\n"
"v_mfma_f32_16x16x32_bf16 v[124:127],v[168:171],a[44:47],v[124:127]\n"
"v_max3_f32 v247,v247,v119,v120\n"
"v_max3_f32 v247,v247,v121,v122\n"
"v_mfma_f32_16x16x32_bf16 v[128:131],v[184:187],a[44:47],v[128:131]\n"
"v_max_f32 v247,v247,v123\n"
"v_cmp_lt_f32 vcc,s64,v247\n"
"s_cmp_lg_u64 s[80:81],0\n"
"s_cbranch_scc1 Lk_28x\n"
"Lk_28:\n"
"v_mfma_f32_16x16x32_bf16 v[0:3],v[188:191],v[144:147],v[0:3]\n"
"v_exp_f32 v116,v116\n"
"v_mfma_f32_16x16x32_bf16 v[4:7],v[192:195],v[144:147],v[4:7]\n"
"v_exp_f32 v117,v117\n"
"v_exp_f32 v118,v118\n"
"v_mfma_f32_16x16x32_bf16 v[8:11],v[196:199],v[144:147],v[8:11]\n"
"v_exp_f32 v119,v119\n"
"s_waitcnt lgkmcnt(0)\n"
"v_mfma_f32_16x16x32_bf16 v[12:15],v[200:203],v[144:147],v[12:15]\n"
"v_cvt_pk_bf16_f32 v148,v116,v117\n"
"v_exp_f32 v120,v120\n"
"v_mfma_f32_16x16x32_bf16 v[16:19],v[204:207],v[144:147],v[16:19]\n"
"v_exp_f32 v121,v121\n"
"v_cvt_pk_bf16_f32 v149,v118,v119\n"
"v_mfma_f32_16x16x32_bf16 v[20:23],v[208:211],v[144:147],v[20:23]\n"
"v_exp_f32 v122,v122\n"
"v_mfma_f32_16x16x32_bf16 v[24:27],v[212:215],v[144:147],v[24:27]\n"
"v_exp_f32 v123,v123\n"
"v_cvt_pk_bf16_f32 v150,v120,v121\n"
"v_mfma_f32_16x16x32_bf16 v[28:31],v[216:219],v[144:147],v[28:31]\n"
"v_cvt_pk_bf16_f32 v151,v122,v123\n"
"v_max3_f32 v248,v124,v125,v126\n"
"v_max3_f32 v248,v248,v127,v128\n"
"v_mfma_f32_16x16x32_bf16 v[96:99],a[48:51],v[144:147],v[96:99]\n"
"v_max3_f32 v248,v248,v129,v130\n"
"v_max_f32 v248,v248,v131\n"
"v_cmp_lt_f32 s[82:83],s64,v248\n"
"s_cbranch_vccnz Lk_29x\n"
"Lk_29:\n"
"v_mfma_f32_16x16x32_bf16 v[32:35],v[188:191],v[148:151],v[32:35]\n"
"v_exp_f32 v124,v124\n"
"v_mfma_f32_16x16x32_bf16 v[36:39],v[192:195],v[148:151],v[36:39]\n"
"v_exp_f32 v125,v125\n"
"v_mfma_f32_16x16x32_bf16 v[40:43],v[196:199],v[148:151],v[40:43]\n"
"v_exp_f32 v126,v126\n"
"v_mfma_f32_16x16x32_bf16 v[44:47],v[200:203],v[148:151],v[44:47]\n"
"v_exp_f32 v127,v127\n"
"v_cvt_pk_bf16_f32 v152,v124,v125\n"
"v_mfma_f32_16x16x32_bf16 v[48:51],v[204:207],v[148:151],v[48:51]\n"
"v_exp_f32 v128,v128\n"
"v_mfma_f32_16x16x32_bf16 v[52:55],v[208:211],v[148:151],v[52:55]\n"
"v_exp_f32 v129,v129\n"
"v_mfma_f32_16x16x32_bf16 v[56:59],v[212:215],v[148:151],v[56:59]\n"
"v_cvt_pk_bf16_f32 v153,v126,v127\n"
"v_exp_f32 v130,v130\n"
"v_mfma_f32_16x16x32_bf16 v[60:63],v[216:219],v[148:151],v[60:63]\n"
"v_exp_f32 v131,v131\n"
"v_mfma_f32_16x16x32_bf16 v[100:103],a[48:51],v[148:151],v[100:103]\n"
"v_cvt_pk_bf16_f32 v154,v128,v129\n"
"v_cvt_pk_bf16_f32 v155,v130,v131\n"
"s_cmp_lg_u64 s[82:83],0\n"
"s_cbranch_scc1 Lk_30x\n"
"Lk_30:\n"
"v_mfma_f32_16x16x32_bf16 v[64:67],v[188:191],v[152:155],v[64:67]\n"
"s_mov_b32 s60,0xfffe0000\n"
"v_add_u32 v220,s60,v220\n"
"v_mfma_f32_16x16x32_bf16 v[68:71],v[192:195],v[152:155],v[68:71]\n"
"v_add_u32 v221,s60,v221\n"
"v_mfma_f32_16x16x32_bf16 v[72:75],v[196:199],v[152:155],v[72:75]\n"
"v_add_u32 v222,s60,v222\n"
"v_add_u32 v223,s60,v223\n"
"v_mfma_f32_16x16x32_bf16 v[76:79],v[200:203],v[152:155],v[76:79]\n"
"ds_read_b128 v[156:159],v220\n"
"v_mfma_f32_16x16x32_bf16 v[80:83],v[204:207],v[152:155],v[80:83]\n"
"ds_read_b128 v[172:175],v220 offset:4096\n"
"v_mfma_f32_16x16x32_bf16 v[84:87],v[208:211],v[152:155],v[84:87]\n"
"ds_read_b128 v[160:163],v221\n"
"ds_read_b128 v[176:179],v221 offset:4096\n"
"v_mfma_f32_16x16x32_bf16 v[88:91],v[212:215],v[152:155],v[88:91]\n"
"ds_read_b128 v[164:167],v222\n"
"v_mfma_f32_16x16x32_bf16 v[92:95],v[216:219],v[152:155],v[92:95]\n"
"ds_read_b128 v[180:183],v222 offset:4096\n"
"ds_read_b128 v[168:171],v223\n"
"v_mfma_f32_16x16x32_bf16 v[104:107],a[48:51],v[152:155],v[104:107]\n"
"ds_read_b128 v[184:187],v223 offset:4096\n"
"s_mov_b32 s60,0xfffe0000\n"
"v_add_u32 v224,s60,v224\n"
"v_add_u32 v225,s60,v225\n"
"v_add_u32 v226,s60,v226\n"
"v_add_u32 v227,s60,v227\n"
"v_add_u32 v228,s60,v228\n"
"v_add_u32 v229,s60,v229\n"
"v_add_u32 v230,s60,v230\n"
"v_add_u32 v231,s60,v231\n"
"s_sleep 12\n"
"s_waitcnt lgkmcnt(7)\n"
"v_mfma_f32_16x16x32_bf16 v[108:111],v[156:159],a[0:3],v[132:135]\n"
"ds_read_b64_tr_b16 v[188:189],v224\n"
"s_waitcnt lgkmcnt(7)\n"
"v_mfma_f32_16x16x32_bf16 v[112:115],v[172:175],a[0:3],v[132:135]\n"
"ds_read_b64_tr_b16 v[190:191],v224 offset:4096\n"
"ds_read_b64_tr_b16 v[192:193],v225\n"
"s_waitcnt lgkmcnt(8)\n"
"v_mfma_f32_16x16x32_bf16 v[108:111],v[160:163],a[12:15],v[108:111]\n"
"ds_read_b64_tr_b16 v[194:195],v225 offset:4096\n"
"s_waitcnt lgkmcnt(8)\n"
"v_mfma_f32_16x16x32_bf16 v[112:115],v[176:179],a[12:15],v[112:115]\n"
"ds_read_b64_tr_b16 v[196:197],v226\n"
"s_waitcnt lgkmcnt(8)\n"
"v_mfma_f32_16x16x32_bf16 v[108:111],v[164:167],a[24:27],v[108:111]\n"
"ds_read_b64_tr_b16 v[198:199],v226 offset:4096\n"
"s_waitcnt lgkmcnt(8)\n"
"v_mfma_f32_16x16x32_bf16 v[112:115],v[180:183],a[24:27],v[112:115]\n"
"ds_read_b64_tr_b16 v[200:201],v227\n"
"ds_read_b64_tr_b16 v[202:203],v227 offset:4096\n"
"s_waitcnt lgkmcnt(8)\n"
"v_mfma_f32_16x16x32_bf16 v[108:111],v[168:171],a[36:39],v[108:111]\n"
"ds_read_b64_tr_b16 v[204:205],v228\n"
"v_mfma_f32_16x16x32_bf16 v[112:115],v[184:187],a[36:39],v[112:115]\n"
"ds_read_b64_tr_b16 v[206:207],v228 offset:4096\n"
"v_mfma_f32_16x16x32_bf16 v[116:119],v[156:159],a[4:7],v[136:139]\n"
"s_barrier\n"
"s_add_u32 s67,s1,0x3800\n"
"s_and_b32 s67,s67,0x3fff\n"
"s_add_u32 m0,s66,0x10000\n"
"s_nop 0\n"
"buffer_load_dwordx4 v232,s[24:27],s67 offen nt lds\n"
"v_mfma_f32_16x16x32_bf16 v[120:123],v[172:175],a[4:7],v[136:139]\n"
"buffer_load_dwordx4 v233,s[24:27],s67 offen offset:1024 nt lds\n"
"s_add_u32 m0,s66,0x12000\n"
"s_nop 0\n"
"buffer_load_dwordx4 v236,s[28:31],s67 offen nt lds\n"
"v_mfma_f32_16x16x32_bf16 v[116:119],v[160:163],a[16:19],v[116:119]\n"
"buffer_load_dwordx4 v237,s[28:31],s67 offen offset:1024 nt lds\n"
"ds_read_b64_tr_b16 v[208:209],v229\n"
"ds_read_b64_tr_b16 v[210:211],v229 offset:4096\n"
"v_mfma_f32_16x16x32_bf16 v[120:123],v[176:179],a[16:19],v[120:123]\n"
"ds_read_b64_tr_b16 v[212:213],v230\n"
"ds_read_b64_tr_b16 v[214:215],v230 offset:4096\n"
"ds_read_b64_tr_b16 v[216:217],v231\n"
"s_waitcnt lgkmcnt(9)\n"
"ds_read_b64_tr_b16 v[218:219],v231 offset:4096\n"
"v_mfma_f32_16x16x32_bf16 v[116:119],v[164:167],a[28:31],v[116:119]\n"
"v_max3_f32 v246,v108,v109,v110\n"
"v_max3_f32 v246,v246,v111,v112\n"
"v_max3_f32 v246,v246,v113,v114\n"
"v_mfma_f32_16x16x32_bf16 v[120:123],v[180:183],a[28:31],v[120:123]\n"
"v_max_f32 v246,v246,v115\n"
"v_cmp_lt_f32 s[80:81],s64,v246\n"
"v_exp_f32 v108,v108\n"
"v_mfma_f32_16x16x32_bf16 v[116:119],v[168:171],a[40:43],v[116:119]\n"
"v_exp_f32 v109,v109\n"
"v_mfma_f32_16x16x32_bf16 v[120:123],v[184:187],a[40:43],v[120:123]\n"
"v_exp_f32 v110,v110\n"
"v_exp_f32 v111,v111\n"
"v_mfma_f32_16x16x32_bf16 v[124:127],v[156:159],a[8:11],v[140:143]\n"
"v_cvt_pk_bf16_f32 v144,v108,v109\n"
"v_exp_f32 v112,v112\n"
"v_mfma_f32_16x16x32_bf16 v[128:131],v[172:175],a[8:11],v[140:143]\n"
"v_exp_f32 v113,v113\n"
"v_mfma_f32_16x16x32_bf16 v[124:127],v[160:163],a[20:23],v[124:127]\n"
"v_cvt_pk_bf16_f32 v145,v110,v111\n"
"v_mfma_f32_16x16x32_bf16 v[128:131],v[176:179],a[20:23],v[128:131]\n"
"v_exp_f32 v114,v114\n"
"v_mfma_f32_16x16x32_bf16 v[124:127],v[164:167],a[32:35],v[124:127]\n"
"v_exp_f32 v115,v115\n"
"v_cvt_pk_bf16_f32 v146,v112,v113\n"
"v_mfma_f32_16x16x32_bf16 v[128:131],v[180:183],a[32:35],v[128:131]\n"
"v_cvt_pk_bf16_f32 v147,v114,v115\n"
"v_max3_f32 v247,v116,v117,v118\n"
"v_mfma_f32_16x16x32_bf16 v[124:127],v[168:171],a[44:47],v[124:127]\n"
"v_max3_f32 v247,v247,v119,v120\n"
"v_max3_f32 v247,v247,v121,v122\n"
"v_mfma_f32_16x16x32_bf16 v[128:131],v[184:187],a[44:47],v[128:131]\n"
"v_max_f32 v247,v247,v123\n"
"v_cmp_lt_f32 vcc,s64,v247\n"
"s_cmp_lg_u64 s[80:81],0\n"
"s_cbranch_scc1 Lk_31x\n"
"Lk_31:\n"
"v_mfma_f32_16x16x32_bf16 v[0:3],v[188:191],v[144:147],v[0:3]\n"
"v_exp_f32 v116,v116\n"
"v_mfma_f32_16x16x32_bf16 v[4:7],v[192:195],v[144:147],v[4:7]\n"
"v_exp_f32 v117,v117\n"
"v_exp_f32 v118,v118\n"
"v_mfma_f32_16x16x32_bf16 v[8:11],v[196:199],v[144:147],v[8:11]\n"
"v_exp_f32 v119,v119\n"
"s_waitcnt lgkmcnt(0)\n"
"v_mfma_f32_16x16x32_bf16 v[12:15],v[200:203],v[144:147],v[12:15]\n"
"v_cvt_pk_bf16_f32 v148,v116,v117\n"
"v_exp_f32 v120,v120\n"
"v_mfma_f32_16x16x32_bf16 v[16:19],v[204:207],v[144:147],v[16:19]\n"
"v_exp_f32 v121,v121\n"
"v_cvt_pk_bf16_f32 v149,v118,v119\n"
"v_mfma_f32_16x16x32_bf16 v[20:23],v[208:211],v[144:147],v[20:23]\n"
"v_exp_f32 v122,v122\n"
"v_mfma_f32_16x16x32_bf16 v[24:27],v[212:215],v[144:147],v[24:27]\n"
"v_exp_f32 v123,v123\n"
"v_cvt_pk_bf16_f32 v150,v120,v121\n"
"v_mfma_f32_16x16x32_bf16 v[28:31],v[216:219],v[144:147],v[28:31]\n"
"v_cvt_pk_bf16_f32 v151,v122,v123\n"
"v_max3_f32 v248,v124,v125,v126\n"
"v_max3_f32 v248,v248,v127,v128\n"
"v_mfma_f32_16x16x32_bf16 v[96:99],a[48:51],v[144:147],v[96:99]\n"
"v_max3_f32 v248,v248,v129,v130\n"
"v_max_f32 v248,v248,v131\n"
"v_cmp_lt_f32 s[82:83],s64,v248\n"
"s_cbranch_vccnz Lk_32x\n"
"Lk_32:\n"
"v_mfma_f32_16x16x32_bf16 v[32:35],v[188:191],v[148:151],v[32:35]\n"
"v_exp_f32 v124,v124\n"
"v_mfma_f32_16x16x32_bf16 v[36:39],v[192:195],v[148:151],v[36:39]\n"
"v_exp_f32 v125,v125\n"
"v_mfma_f32_16x16x32_bf16 v[40:43],v[196:199],v[148:151],v[40:43]\n"
"v_exp_f32 v126,v126\n"
"v_mfma_f32_16x16x32_bf16 v[44:47],v[200:203],v[148:151],v[44:47]\n"
"v_exp_f32 v127,v127\n"
"v_cvt_pk_bf16_f32 v152,v124,v125\n"
"v_mfma_f32_16x16x32_bf16 v[48:51],v[204:207],v[148:151],v[48:51]\n"
"v_exp_f32 v128,v128\n"
"v_mfma_f32_16x16x32_bf16 v[52:55],v[208:211],v[148:151],v[52:55]\n"
"v_exp_f32 v129,v129\n"
"v_mfma_f32_16x16x32_bf16 v[56:59],v[212:215],v[148:151],v[56:59]\n"
"v_cvt_pk_bf16_f32 v153,v126,v127\n"
"v_exp_f32 v130,v130\n"
"v_mfma_f32_16x16x32_bf16 v[60:63],v[216:219],v[148:151],v[60:63]\n"
"v_exp_f32 v131,v131\n"
"v_mfma_f32_16x16x32_bf16 v[100:103],a[48:51],v[148:151],v[100:103]\n"
"v_cvt_pk_bf16_f32 v154,v128,v129\n"
"v_cvt_pk_bf16_f32 v155,v130,v131\n"
"s_cmp_lg_u64 s[82:83],0\n"
"s_cbranch_scc1 Lk_33x\n"
"Lk_33:\n"
"v_mfma_f32_16x16x32_bf16 v[64:67],v[188:191],v[152:155],v[64:67]\n"
"ds_read_b128 v[156:159],v220 offset:16384\n"
"v_mfma_f32_16x16x32_bf16 v[68:71],v[192:195],v[152:155],v[68:71]\n"
"ds_read_b128 v[172:175],v220 offset:20480\n"
"v_mfma_f32_16x16x32_bf16 v[72:75],v[196:199],v[152:155],v[72:75]\n"
"ds_read_b128 v[160:163],v221 offset:16384\n"
"v_mfma_f32_16x16x32_bf16 v[76:79],v[200:203],v[152:155],v[76:79]\n"
"ds_read_b128 v[176:179],v221 offset:20480\n"
"v_mfma_f32_16x16x32_bf16 v[80:83],v[204:207],v[152:155],v[80:83]\n"
"v_mfma_f32_16x16x32_bf16 v[84:87],v[208:211],v[152:155],v[84:87]\n"
"ds_read_b128 v[164:167],v222 offset:16384\n"
"v_mfma_f32_16x16x32_bf16 v[88:91],v[212:215],v[152:155],v[88:91]\n"
"ds_read_b128 v[180:183],v222 offset:20480\n"
"v_mfma_f32_16x16x32_bf16 v[92:95],v[216:219],v[152:155],v[92:95]\n"
"ds_read_b128 v[168:171],v223 offset:16384\n"
"v_mfma_f32_16x16x32_bf16 v[104:107],a[48:51],v[152:155],v[104:107]\n"
"ds_read_b128 v[184:187],v223 offset:20480\n"
"s_sleep 12\n"
"s_waitcnt lgkmcnt(7)\n"
"v_mfma_f32_16x16x32_bf16 v[108:111],v[156:159],a[0:3],v[132:135]\n"
"ds_read_b64_tr_b16 v[188:189],v224 offset:16384\n"
"s_waitcnt lgkmcnt(7)\n"
"v_mfma_f32_16x16x32_bf16 v[112:115],v[172:175],a[0:3],v[132:135]\n"
"ds_read_b64_tr_b16 v[190:191],v224 offset:20480\n"
"ds_read_b64_tr_b16 v[192:193],v225 offset:16384\n"
"s_waitcnt lgkmcnt(8)\n"
"v_mfma_f32_16x16x32_bf16 v[108:111],v[160:163],a[12:15],v[108:111]\n"
"ds_read_b64_tr_b16 v[194:195],v225 offset:20480\n"
"s_waitcnt lgkmcnt(8)\n"
"v_mfma_f32_16x16x32_bf16 v[112:115],v[176:179],a[12:15],v[112:115]\n"
"ds_read_b64_tr_b16 v[196:197],v226 offset:16384\n"
"s_waitcnt lgkmcnt(8)\n"
"v_mfma_f32_16x16x32_bf16 v[108:111],v[164:167],a[24:27],v[108:111]\n"
"ds_read_b64_tr_b16 v[198:199],v226 offset:20480\n"
"s_waitcnt lgkmcnt(8)\n"
"v_mfma_f32_16x16x32_bf16 v[112:115],v[180:183],a[24:27],v[112:115]\n"
"ds_read_b64_tr_b16 v[200:201],v227 offset:16384\n"
"ds_read_b64_tr_b16 v[202:203],v227 offset:20480\n"
"s_waitcnt lgkmcnt(9)\n"
"v_mfma_f32_16x16x32_bf16 v[108:111],v[168:171],a[36:39],v[108:111]\n"
"ds_read_b64_tr_b16 v[204:205],v228 offset:16384\n"
"s_waitcnt lgkmcnt(9)\n"
"v_mfma_f32_16x16x32_bf16 v[112:115],v[184:187],a[36:39],v[112:115]\n"
"ds_read_b64_tr_b16 v[206:207],v228 offset:20480\n"
"v_mfma_f32_16x16x32_bf16 v[116:119],v[156:159],a[4:7],v[136:139]\n"
"s_waitcnt vmcnt(8)\n"
"s_barrier\n"
"s_add_u32 s67,s1,0x3c00\n"
"s_and_b32 s67,s67,0x3fff\n"
"s_add_u32 m0,s66,0x14000\n"
"s_nop 0\n"
"buffer_load_dwordx4 v232,s[24:27],s67 offen nt lds\n"
"v_mfma_f32_16x16x32_bf16 v[120:123],v[172:175],a[4:7],v[136:139]\n"
"buffer_load_dwordx4 v233,s[24:27],s67 offen offset:1024 nt lds\n"
"s_add_u32 m0,s66,0x16000\n"
"s_nop 0\n"
"buffer_load_dwordx4 v236,s[28:31],s67 offen nt lds\n"
"v_mfma_f32_16x16x32_bf16 v[116:119],v[160:163],a[16:19],v[116:119]\n"
"buffer_load_dwordx4 v237,s[28:31],s67 offen offset:1024 nt lds\n"
"ds_read_b64_tr_b16 v[208:209],v229 offset:16384\n"
"ds_read_b64_tr_b16 v[210:211],v229 offset:20480\n"
"v_mfma_f32_16x16x32_bf16 v[120:123],v[176:179],a[16:19],v[120:123]\n"
"ds_read_b64_tr_b16 v[212:213],v230 offset:16384\n"
"ds_read_b64_tr_b16 v[214:215],v230 offset:20480\n"
"ds_read_b64_tr_b16 v[216:217],v231 offset:16384\n"
"s_waitcnt lgkmcnt(9)\n"
"ds_read_b64_tr_b16 v[218:219],v231 offset:20480\n"
"v_mfma_f32_16x16x32_bf16 v[116:119],v[164:167],a[28:31],v[116:119]\n"
"v_max3_f32 v246,v108,v109,v110\n"
"v_max3_f32 v246,v246,v111,v112\n"
"v_max3_f32 v246,v246,v113,v114\n"
"v_mfma_f32_16x16x32_bf16 v[120:123],v[180:183],a[28:31],v[120:123]\n"
"v_max_f32 v246,v246,v115\n"
"v_cmp_lt_f32 s[80:81],s64,v246\n"
"v_exp_f32 v108,v108\n"
"v_mfma_f32_16x16x32_bf16 v[116:119],v[168:171],a[40:43],v[116:119]\n"
"v_exp_f32 v109,v109\n"
"v_mfma_f32_16x16x32_bf16 v[120:123],v[184:187],a[40:43],v[120:123]\n"
"v_exp_f32 v110,v110\n"
"v_exp_f32 v111,v111\n"
"v_mfma_f32_16x16x32_bf16 v[124:127],v[156:159],a[8:11],v[140:143]\n"
"v_cvt_pk_bf16_f32 v144,v108,v109\n"
"v_exp_f32 v112,v112\n"
"v_mfma_f32_16x16x32_bf16 v[128:131],v[172:175],a[8:11],v[140:143]\n"
"v_exp_f32 v113,v113\n"
"v_mfma_f32_16x16x32_bf16 v[124:127],v[160:163],a[20:23],v[124:127]\n"
"v_cvt_pk_bf16_f32 v145,v110,v111\n"
"v_mfma_f32_16x16x32_bf16 v[128:131],v[176:179],a[20:23],v[128:131]\n"
"v_exp_f32 v114,v114\n"
"v_mfma_f32_16x16x32_bf16 v[124:127],v[164:167],a[32:35],v[124:127]\n"
"v_exp_f32 v115,v115\n"
"v_cvt_pk_bf16_f32 v146,v112,v113\n"
"v_mfma_f32_16x16x32_bf16 v[128:131],v[180:183],a[32:35],v[128:131]\n"
"v_cvt_pk_bf16_f32 v147,v114,v115\n"
"v_max3_f32 v247,v116,v117,v118\n"
"v_mfma_f32_16x16x32_bf16 v[124:127],v[168:171],a[44:47],v[124:127]\n"
"v_max3_f32 v247,v247,v119,v120\n"
"v_max3_f32 v247,v247,v121,v122\n"
"v_mfma_f32_16x16x32_bf16 v[128:131],v[184:187],a[44:47],v[128:131]\n"
"v_max_f32 v247,v247,v123\n"
"v_cmp_lt_f32 vcc,s64,v247\n"
"s_cmp_lg_u64 s[80:81],0\n"
"s_cbranch_scc1 Lk_34x\n"
"Lk_34:\n"
"v_mfma_f32_16x16x32_bf16 v[0:3],v[188:191],v[144:147],v[0:3]\n"
"v_exp_f32 v116,v116\n"
"v_mfma_f32_16x16x32_bf16 v[4:7],v[192:195],v[144:147],v[4:7]\n"
"v_exp_f32 v117,v117\n"
"v_exp_f32 v118,v118\n"
"v_mfma_f32_16x16x32_bf16 v[8:11],v[196:199],v[144:147],v[8:11]\n"
"v_exp_f32 v119,v119\n"
"s_waitcnt lgkmcnt(0)\n"
"v_mfma_f32_16x16x32_bf16 v[12:15],v[200:203],v[144:147],v[12:15]\n"
"v_cvt_pk_bf16_f32 v148,v116,v117\n"
"v_exp_f32 v120,v120\n"
"v_mfma_f32_16x16x32_bf16 v[16:19],v[204:207],v[144:147],v[16:19]\n"
"v_exp_f32 v121,v121\n"
"v_cvt_pk_bf16_f32 v149,v118,v119\n"
"v_mfma_f32_16x16x32_bf16 v[20:23],v[208:211],v[144:147],v[20:23]\n"
"v_exp_f32 v122,v122\n"
"v_mfma_f32_16x16x32_bf16 v[24:27],v[212:215],v[144:147],v[24:27]\n"
"v_exp_f32 v123,v123\n"
"v_cvt_pk_bf16_f32 v150,v120,v121\n"
"v_mfma_f32_16x16x32_bf16 v[28:31],v[216:219],v[144:147],v[28:31]\n"
"v_cvt_pk_bf16_f32 v151,v122,v123\n"
"v_max3_f32 v248,v124,v125,v126\n"
"v_max3_f32 v248,v248,v127,v128\n"
"v_mfma_f32_16x16x32_bf16 v[96:99],a[48:51],v[144:147],v[96:99]\n"
"v_max3_f32 v248,v248,v129,v130\n"
"v_max_f32 v248,v248,v131\n"
"v_cmp_lt_f32 s[82:83],s64,v248\n"
"s_cbranch_vccnz Lk_35x\n"
"Lk_35:\n"
"v_mfma_f32_16x16x32_bf16 v[32:35],v[188:191],v[148:151],v[32:35]\n"
"v_exp_f32 v124,v124\n"
"v_mfma_f32_16x16x32_bf16 v[36:39],v[192:195],v[148:151],v[36:39]\n"
"v_exp_f32 v125,v125\n"
"v_mfma_f32_16x16x32_bf16 v[40:43],v[196:199],v[148:151],v[40:43]\n"
"v_exp_f32 v126,v126\n"
"v_mfma_f32_16x16x32_bf16 v[44:47],v[200:203],v[148:151],v[44:47]\n"
"v_exp_f32 v127,v127\n"
"v_cvt_pk_bf16_f32 v152,v124,v125\n"
"v_mfma_f32_16x16x32_bf16 v[48:51],v[204:207],v[148:151],v[48:51]\n"
"v_exp_f32 v128,v128\n"
"v_mfma_f32_16x16x32_bf16 v[52:55],v[208:211],v[148:151],v[52:55]\n"
"v_exp_f32 v129,v129\n"
"v_mfma_f32_16x16x32_bf16 v[56:59],v[212:215],v[148:151],v[56:59]\n"
"v_cvt_pk_bf16_f32 v153,v126,v127\n"
"v_exp_f32 v130,v130\n"
"v_mfma_f32_16x16x32_bf16 v[60:63],v[216:219],v[148:151],v[60:63]\n"
"v_exp_f32 v131,v131\n"
"v_mfma_f32_16x16x32_bf16 v[100:103],a[48:51],v[148:151],v[100:103]\n"
"v_cvt_pk_bf16_f32 v154,v128,v129\n"
"v_cvt_pk_bf16_f32 v155,v130,v131\n"
"s_cmp_lg_u64 s[82:83],0\n"
"s_cbranch_scc1 Lk_36x\n"
"Lk_36:\n"
"v_mfma_f32_16x16x32_bf16 v[64:67],v[188:191],v[152:155],v[64:67]\n"
"ds_read_b128 v[156:159],v220 offset:32768\n"
"v_mfma_f32_16x16x32_bf16 v[68:71],v[192:195],v[152:155],v[68:71]\n"
"ds_read_b128 v[172:175],v220 offset:36864\n"
"v_mfma_f32_16x16x32_bf16 v[72:75],v[196:199],v[152:155],v[72:75]\n"
"ds_read_b128 v[160:163],v221 offset:32768\n"
"v_mfma_f32_16x16x32_bf16 v[76:79],v[200:203],v[152:155],v[76:79]\n"
"ds_read_b128 v[176:179],v221 offset:36864\n"
"v_mfma_f32_16x16x32_bf16 v[80:83],v[204:207],v[152:155],v[80:83]\n"
"v_mfma_f32_16x16x32_bf16 v[84:87],v[208:211],v[152:155],v[84:87]\n"
"ds_read_b128 v[164:167],v222 offset:32768\n"
"v_mfma_f32_16x16x32_bf16 v[88:91],v[212:215],v[152:155],v[88:91]\n"
"ds_read_b128 v[180:183],v222 offset:36864\n"
"v_mfma_f32_16x16x32_bf16 v[92:95],v[216:219],v[152:155],v[92:95]\n"
"ds_read_b128 v[168:171],v223 offset:32768\n"
"v_mfma_f32_16x16x32_bf16 v[104:107],a[48:51],v[152:155],v[104:107]\n"
"ds_read_b128 v[184:187],v223 offset:36864\n"
"s_waitcnt lgkmcnt(7)\n"
"v_mfma_f32_16x16x32_bf16 v[108:111],v[156:159],a[0:3],v[132:135]\n"
"ds_read_b64_tr_b16 v[188:189],v224 offset:32768\n"
"s_waitcnt lgkmcnt(7)\n"
"v_mfma_f32_16x16x32_bf16 v[112:115],v[172:175],a[0:3],v[132:135]\n"
"ds_read_b64_tr_b16 v[190:191],v224 offset:36864\n"
"ds_read_b64_tr_b16 v[192:193],v225 offset:32768\n"
"s_waitcnt lgkmcnt(8)\n"
"v_mfma_f32_16x16x32_bf16 v[108:111],v[160:163],a[12:15],v[108:111]\n"
"ds_read_b64_tr_b16 v[194:195],v225 offset:36864\n"
"s_waitcnt lgkmcnt(8)\n"
"v_mfma_f32_16x16x32_bf16 v[112:115],v[176:179],a[12:15],v[112:115]\n"
"ds_read_b64_tr_b16 v[196:197],v226 offset:32768\n"
"s_waitcnt lgkmcnt(8)\n"
"v_mfma_f32_16x16x32_bf16 v[108:111],v[164:167],a[24:27],v[108:111]\n"
"ds_read_b64_tr_b16 v[198:199],v226 offset:36864\n"
"s_waitcnt lgkmcnt(8)\n"
"v_mfma_f32_16x16x32_bf16 v[112:115],v[180:183],a[24:27],v[112:115]\n"
"ds_read_b64_tr_b16 v[200:201],v227 offset:32768\n"
"ds_read_b64_tr_b16 v[202:203],v227 offset:36864\n"
"s_waitcnt lgkmcnt(9)\n"
"v_mfma_f32_16x16x32_bf16 v[108:111],v[168:171],a[36:39],v[108:111]\n"
"ds_read_b64_tr_b16 v[204:205],v228 offset:32768\n"
"s_waitcnt lgkmcnt(9)\n"
"v_mfma_f32_16x16x32_bf16 v[112:115],v[184:187],a[36:39],v[112:115]\n"
"ds_read_b64_tr_b16 v[206:207],v228 offset:36864\n"
"v_mfma_f32_16x16x32_bf16 v[116:119],v[156:159],a[4:7],v[136:139]\n"
"s_waitcnt vmcnt(8)\n"
"s_barrier\n"
"ds_read_b64_tr_b16 v[208:209],v229 offset:32768\n"
"ds_read_b64_tr_b16 v[210:211],v229 offset:36864\n"
"v_mfma_f32_16x16x32_bf16 v[120:123],v[172:175],a[4:7],v[136:139]\n"
"ds_read_b64_tr_b16 v[212:213],v230 offset:32768\n"
"ds_read_b64_tr_b16 v[214:215],v230 offset:36864\n"
"v_mfma_f32_16x16x32_bf16 v[116:119],v[160:163],a[16:19],v[116:119]\n"
"ds_read_b64_tr_b16 v[216:217],v231 offset:32768\n"
"s_waitcnt lgkmcnt(11)\n"
"ds_read_b64_tr_b16 v[218:219],v231 offset:36864\n"
"v_max3_f32 v246,v108,v109,v110\n"
"v_mfma_f32_16x16x32_bf16 v[120:123],v[176:179],a[16:19],v[120:123]\n"
"v_max3_f32 v246,v246,v111,v112\n"
"v_max3_f32 v246,v246,v113,v114\n"
"v_mfma_f32_16x16x32_bf16 v[116:119],v[164:167],a[28:31],v[116:119]\n"
"v_max_f32 v246,v246,v115\n"
"v_cmp_lt_f32 s[80:81],s64,v246\n"
"v_mfma_f32_16x16x32_bf16 v[120:123],v[180:183],a[28:31],v[120:123]\n"
"v_exp_f32 v108,v108\n"
"v_exp_f32 v109,v109\n"
"v_mfma_f32_16x16x32_bf16 v[116:119],v[168:171],a[40:43],v[116:119]\n"
"v_exp_f32 v110,v110\n"
"v_mfma_f32_16x16x32_bf16 v[120:123],v[184:187],a[40:43],v[120:123]\n"
"v_exp_f32 v111,v111\n"
"v_mfma_f32_16x16x32_bf16 v[124:127],v[156:159],a[8:11],v[140:143]\n"
"v_cvt_pk_bf16_f32 v144,v108,v109\n"
"v_exp_f32 v112,v112\n"
"v_mfma_f32_16x16x32_bf16 v[128:131],v[172:175],a[8:11],v[140:143]\n"
"v_exp_f32 v113,v113\n"
"v_mfma_f32_16x16x32_bf16 v[124:127],v[160:163],a[20:23],v[124:127]\n"
"v_cvt_pk_bf16_f32 v145,v110,v111\n"
"v_mfma_f32_16x16x32_bf16 v[128:131],v[176:179],a[20:23],v[128:131]\n"
"v_exp_f32 v114,v114\n"
"v_mfma_f32_16x16x32_bf16 v[124:127],v[164:167],a[32:35],v[124:127]\n"
"v_exp_f32 v115,v115\n"
"v_cvt_pk_bf16_f32 v146,v112,v113\n"
"v_mfma_f32_16x16x32_bf16 v[128:131],v[180:183],a[32:35],v[128:131]\n"
"v_cvt_pk_bf16_f32 v147,v114,v115\n"
"v_max3_f32 v247,v116,v117,v118\n"
"v_mfma_f32_16x16x32_bf16 v[124:127],v[168:171],a[44:47],v[124:127]\n"
"v_max3_f32 v247,v247,v119,v120\n"
"v_max3_f32 v247,v247,v121,v122\n"
"v_mfma_f32_16x16x32_bf16 v[128:131],v[184:187],a[44:47],v[128:131]\n"
"v_max_f32 v247,v247,v123\n"
"v_cmp_lt_f32 vcc,s64,v247\n"
"s_cmp_lg_u64 s[80:81],0\n"
"s_cbranch_scc1 Lk_37x\n"
"Lk_37:\n"
"v_mfma_f32_16x16x32_bf16 v[0:3],v[188:191],v[144:147],v[0:3]\n"
"v_exp_f32 v116,v116\n"
"v_mfma_f32_16x16x32_bf16 v[4:7],v[192:195],v[144:147],v[4:7]\n"
"v_exp_f32 v117,v117\n"
"v_exp_f32 v118,v118\n"
"s_waitcnt lgkmcnt(0)\n"
"v_mfma_f32_16x16x32_bf16 v[8:11],v[196:199],v[144:147],v[8:11]\n"
"v_exp_f32 v119,v119\n"
"v_mfma_f32_16x16x32_bf16 v[12:15],v[200:203],v[144:147],v[12:15]\n"
"v_cvt_pk_bf16_f32 v148,v116,v117\n"
"v_exp_f32 v120,v120\n"
"v_mfma_f32_16x16x32_bf16 v[16:19],v[204:207],v[144:147],v[16:19]\n"
"v_exp_f32 v121,v121\n"
"v_cvt_pk_bf16_f32 v149,v118,v119\n"
"v_mfma_f32_16x16x32_bf16 v[20:23],v[208:211],v[144:147],v[20:23]\n"
"v_exp_f32 v122,v122\n"
"v_mfma_f32_16x16x32_bf16 v[24:27],v[212:215],v[144:147],v[24:27]\n"
"v_exp_f32 v123,v123\n"
"v_cvt_pk_bf16_f32 v150,v120,v121\n"
"v_mfma_f32_16x16x32_bf16 v[28:31],v[216:219],v[144:147],v[28:31]\n"
"v_cvt_pk_bf16_f32 v151,v122,v123\n"
"v_max3_f32 v248,v124,v125,v126\n"
"v_max3_f32 v248,v248,v127,v128\n"
"v_mfma_f32_16x16x32_bf16 v[96:99],a[48:51],v[144:147],v[96:99]\n"
"v_max3_f32 v248,v248,v129,v130\n"
"v_max_f32 v248,v248,v131\n"
"v_cmp_lt_f32 s[82:83],s64,v248\n"
"s_cbranch_vccnz Lk_38x\n"
"Lk_38:\n"
"v_mfma_f32_16x16x32_bf16 v[32:35],v[188:191],v[148:151],v[32:35]\n"
"v_exp_f32 v124,v124\n"
"v_mfma_f32_16x16x32_bf16 v[36:39],v[192:195],v[148:151],v[36:39]\n"
"v_exp_f32 v125,v125\n"
"v_mfma_f32_16x16x32_bf16 v[40:43],v[196:199],v[148:151],v[40:43]\n"
"v_exp_f32 v126,v126\n"
"v_mfma_f32_16x16x32_bf16 v[44:47],v[200:203],v[148:151],v[44:47]\n"
"v_exp_f32 v127,v127\n"
"v_cvt_pk_bf16_f32 v152,v124,v125\n"
"v_mfma_f32_16x16x32_bf16 v[48:51],v[204:207],v[148:151],v[48:51]\n"
"v_exp_f32 v128,v128\n"
"v_mfma_f32_16x16x32_bf16 v[52:55],v[208:211],v[148:151],v[52:55]\n"
"v_exp_f32 v129,v129\n"
"v_mfma_f32_16x16x32_bf16 v[56:59],v[212:215],v[148:151],v[56:59]\n"
"v_cvt_pk_bf16_f32 v153,v126,v127\n"
"v_exp_f32 v130,v130\n"
"v_mfma_f32_16x16x32_bf16 v[60:63],v[216:219],v[148:151],v[60:63]\n"
"v_exp_f32 v131,v131\n"
"v_mfma_f32_16x16x32_bf16 v[100:103],a[48:51],v[148:151],v[100:103]\n"
"v_cvt_pk_bf16_f32 v154,v128,v129\n"
"v_cvt_pk_bf16_f32 v155,v130,v131\n"
"s_cmp_lg_u64 s[82:83],0\n"
"s_cbranch_scc1 Lk_39x\n"
"Lk_39:\n"
"v_mfma_f32_16x16x32_bf16 v[64:67],v[188:191],v[152:155],v[64:67]\n"
"ds_read_b128 v[156:159],v220 offset:49152\n"
"v_mfma_f32_16x16x32_bf16 v[68:71],v[192:195],v[152:155],v[68:71]\n"
"ds_read_b128 v[172:175],v220 offset:53248\n"
"v_mfma_f32_16x16x32_bf16 v[72:75],v[196:199],v[152:155],v[72:75]\n"
"ds_read_b128 v[160:163],v221 offset:49152\n"
"v_mfma_f32_16x16x32_bf16 v[76:79],v[200:203],v[152:155],v[76:79]\n"
"ds_read_b128 v[176:179],v221 offset:53248\n"
"v_mfma_f32_16x16x32_bf16 v[80:83],v[204:207],v[152:155],v[80:83]\n"
"v_mfma_f32_16x16x32_bf16 v[84:87],v[208:211],v[152:155],v[84:87]\n"
"ds_read_b128 v[164:167],v222 offset:49152\n"
"v_mfma_f32_16x16x32_bf16 v[88:91],v[212:215],v[152:155],v[88:91]\n"
"ds_read_b128 v[180:183],v222 offset:53248\n"
"v_mfma_f32_16x16x32_bf16 v[92:95],v[216:219],v[152:155],v[92:95]\n"
"ds_read_b128 v[168:171],v223 offset:49152\n"
"v_mfma_f32_16x16x32_bf16 v[104:107],a[48:51],v[152:155],v[104:107]\n"
"ds_read_b128 v[184:187],v223 offset:53248\n"
"s_waitcnt lgkmcnt(7)\n"
"v_mfma_f32_16x16x32_bf16 v[108:111],v[156:159],a[0:3],v[132:135]\n"
"ds_read_b64_tr_b16 v[188:189],v224 offset:49152\n"
"s_waitcnt lgkmcnt(7)\n"
"v_mfma_f32_16x16x32_bf16 v[112:115],v[172:175],a[0:3],v[132:135]\n"
"ds_read_b64_tr_b16 v[190:191],v224 offset:53248\n"
"ds_read_b64_tr_b16 v[192:193],v225 offset:49152\n"
"s_waitcnt lgkmcnt(8)\n"
"v_mfma_f32_16x16x32_bf16 v[108:111],v[160:163],a[12:15],v[108:111]\n"
"ds_read_b64_tr_b16 v[194:195],v225 offset:53248\n"
"s_waitcnt lgkmcnt(8)\n"
"v_mfma_f32_16x16x32_bf16 v[112:115],v[176:179],a[12:15],v[112:115]\n"
"ds_read_b64_tr_b16 v[196:197],v226 offset:49152\n"
"s_waitcnt lgkmcnt(8)\n"
"v_mfma_f32_16x16x32_bf16 v[108:111],v[164:167],a[24:27],v[108:111]\n"
"ds_read_b64_tr_b16 v[198:199],v226 offset:53248\n"
"s_waitcnt lgkmcnt(8)\n"
"v_mfma_f32_16x16x32_bf16 v[112:115],v[180:183],a[24:27],v[112:115]\n"
"ds_read_b64_tr_b16 v[200:201],v227 offset:49152\n"
"ds_read_b64_tr_b16 v[202:203],v227 offset:53248\n"
"s_waitcnt lgkmcnt(9)\n"
"v_mfma_f32_16x16x32_bf16 v[108:111],v[168:171],a[36:39],v[108:111]\n"
"ds_read_b64_tr_b16 v[204:205],v228 offset:49152\n"
"s_waitcnt lgkmcnt(9)\n"
"v_mfma_f32_16x16x32_bf16 v[112:115],v[184:187],a[36:39],v[112:115]\n"
"ds_read_b64_tr_b16 v[206:207],v228 offset:53248\n"
"v_mfma_f32_16x16x32_bf16 v[116:119],v[156:159],a[4:7],v[136:139]\n"
"s_waitcnt vmcnt(4)\n"
"s_barrier\n"
"ds_read_b64_tr_b16 v[208:209],v229 offset:49152\n"
"ds_read_b64_tr_b16 v[210:211],v229 offset:53248\n"
"v_mfma_f32_16x16x32_bf16 v[120:123],v[172:175],a[4:7],v[136:139]\n"
"ds_read_b64_tr_b16 v[212:213],v230 offset:49152\n"
"ds_read_b64_tr_b16 v[214:215],v230 offset:53248\n"
"v_mfma_f32_16x16x32_bf16 v[116:119],v[160:163],a[16:19],v[116:119]\n"
"ds_read_b64_tr_b16 v[216:217],v231 offset:49152\n"
"s_waitcnt lgkmcnt(11)\n"
"ds_read_b64_tr_b16 v[218:219],v231 offset:53248\n"
"v_max3_f32 v246,v108,v109,v110\n"
"v_mfma_f32_16x16x32_bf16 v[120:123],v[176:179],a[16:19],v[120:123]\n"
"v_max3_f32 v246,v246,v111,v112\n"
"v_max3_f32 v246,v246,v113,v114\n"
"v_mfma_f32_16x16x32_bf16 v[116:119],v[164:167],a[28:31],v[116:119]\n"
"v_max_f32 v246,v246,v115\n"
"v_cmp_lt_f32 s[80:81],s64,v246\n"
"v_mfma_f32_16x16x32_bf16 v[120:123],v[180:183],a[28:31],v[120:123]\n"
"v_exp_f32 v108,v108\n"
"v_exp_f32 v109,v109\n"
"v_mfma_f32_16x16x32_bf16 v[116:119],v[168:171],a[40:43],v[116:119]\n"
"v_exp_f32 v110,v110\n"
"v_mfma_f32_16x16x32_bf16 v[120:123],v[184:187],a[40:43],v[120:123]\n"
"v_exp_f32 v111,v111\n"
"v_mfma_f32_16x16x32_bf16 v[124:127],v[156:159],a[8:11],v[140:143]\n"
"v_cvt_pk_bf16_f32 v144,v108,v109\n"
"v_exp_f32 v112,v112\n"
"v_mfma_f32_16x16x32_bf16 v[128:131],v[172:175],a[8:11],v[140:143]\n"
"v_exp_f32 v113,v113\n"
"v_mfma_f32_16x16x32_bf16 v[124:127],v[160:163],a[20:23],v[124:127]\n"
"v_cvt_pk_bf16_f32 v145,v110,v111\n"
"v_exp_f32 v114,v114\n"
"v_mfma_f32_16x16x32_bf16 v[128:131],v[176:179],a[20:23],v[128:131]\n"
"v_exp_f32 v115,v115\n"
"v_cvt_pk_bf16_f32 v146,v112,v113\n"
"v_mfma_f32_16x16x32_bf16 v[124:127],v[164:167],a[32:35],v[124:127]\n"
"v_cvt_pk_bf16_f32 v147,v114,v115\n"
"v_mfma_f32_16x16x32_bf16 v[128:131],v[180:183],a[32:35],v[128:131]\n"
"s_add_u32 s67,s59,0\n"
"s_and_b32 s67,s67,15\n"
"s_lshl_b32 s67,s67,15\n"
"s_add_u32 m0,s3,0x18000\n"
"s_nop 0\n"
"buffer_load_dwordx4 v238,s[40:43],s67 offen lds\n"
"buffer_load_dwordx4 v239,s[40:43],s67 offen offset:1024 lds\n"
"v_mfma_f32_16x16x32_bf16 v[124:127],v[168:171],a[44:47],v[124:127]\n"
"v_max3_f32 v247,v116,v117,v118\n"
"v_max3_f32 v247,v247,v119,v120\n"
"v_mfma_f32_16x16x32_bf16 v[128:131],v[184:187],a[44:47],v[128:131]\n"
"v_max3_f32 v247,v247,v121,v122\n"
"v_max_f32 v247,v247,v123\n"
"v_cmp_lt_f32 vcc,s64,v247\n"
"s_cmp_lg_u64 s[80:81],0\n"
"s_cbranch_scc1 Lk_40x\n"
"Lk_40:\n"
"v_mfma_f32_16x16x32_bf16 v[0:3],v[188:191],v[144:147],v[0:3]\n"
"v_exp_f32 v116,v116\n"
"v_mfma_f32_16x16x32_bf16 v[4:7],v[192:195],v[144:147],v[4:7]\n"
"v_exp_f32 v117,v117\n"
"v_exp_f32 v118,v118\n"
"s_waitcnt lgkmcnt(0)\n"
"v_mfma_f32_16x16x32_bf16 v[8:11],v[196:199],v[144:147],v[8:11]\n"
"v_exp_f32 v119,v119\n"
"v_mfma_f32_16x16x32_bf16 v[12:15],v[200:203],v[144:147],v[12:15]\n"
"v_cvt_pk_bf16_f32 v148,v116,v117\n"
"v_exp_f32 v120,v120\n"
"v_mfma_f32_16x16x32_bf16 v[16:19],v[204:207],v[144:147],v[16:19]\n"
"v_exp_f32 v121,v121\n"
"v_cvt_pk_bf16_f32 v149,v118,v119\n"
"v_mfma_f32_16x16x32_bf16 v[20:23],v[208:211],v[144:147],v[20:23]\n"
"v_exp_f32 v122,v122\n"
"v_mfma_f32_16x16x32_bf16 v[24:27],v[212:215],v[144:147],v[24:27]\n"
"v_exp_f32 v123,v123\n"
"v_cvt_pk_bf16_f32 v150,v120,v121\n"
"v_mfma_f32_16x16x32_bf16 v[28:31],v[216:219],v[144:147],v[28:31]\n"
"v_cvt_pk_bf16_f32 v151,v122,v123\n"
"v_max3_f32 v248,v124,v125,v126\n"
"v_max3_f32 v248,v248,v127,v128\n"
"v_mfma_f32_16x16x32_bf16 v[96:99],a[48:51],v[144:147],v[96:99]\n"
"v_max3_f32 v248,v248,v129,v130\n"
"v_max_f32 v248,v248,v131\n"
"v_cmp_lt_f32 s[82:83],s64,v248\n"
"s_cbranch_vccnz Lk_41x\n"
"Lk_41:\n"
"v_mfma_f32_16x16x32_bf16 v[32:35],v[188:191],v[148:151],v[32:35]\n"
"v_exp_f32 v124,v124\n"
"v_mfma_f32_16x16x32_bf16 v[36:39],v[192:195],v[148:151],v[36:39]\n"
"v_exp_f32 v125,v125\n"
"v_exp_f32 v126,v126\n"
"v_mfma_f32_16x16x32_bf16 v[40:43],v[196:199],v[148:151],v[40:43]\n"
"v_exp_f32 v127,v127\n"
"v_mfma_f32_16x16x32_bf16 v[44:47],v[200:203],v[148:151],v[44:47]\n"
"v_cvt_pk_bf16_f32 v152,v124,v125\n"
"v_exp_f32 v128,v128\n"
"v_mfma_f32_16x16x32_bf16 v[48:51],v[204:207],v[148:151],v[48:51]\n"
"v_exp_f32 v129,v129\n"
"v_cvt_pk_bf16_f32 v153,v126,v127\n"
"v_mfma_f32_16x16x32_bf16 v[52:55],v[208:211],v[148:151],v[52:55]\n"
"v_exp_f32 v130,v130\n"
"v_mfma_f32_16x16x32_bf16 v[56:59],v[212:215],v[148:151],v[56:59]\n"
"v_exp_f32 v131,v131\n"
"v_cvt_pk_bf16_f32 v154,v128,v129\n"
"v_mfma_f32_16x16x32_bf16 v[60:63],v[216:219],v[148:151],v[60:63]\n"
"v_cvt_pk_bf16_f32 v155,v130,v131\n"
"buffer_load_dwordx4 v240,s[40:43],s67 offen offset:2048 lds\n"
"buffer_load_dwordx4 v241,s[40:43],s67 offen offset:3072 lds\n"
"v_mfma_f32_16x16x32_bf16 v[100:103],a[48:51],v[148:151],v[100:103]\n"
"s_add_u32 s67,s67,0x4000\n"
"s_add_u32 m0,s3,0x19000\n"
"s_nop 0\n"
"buffer_load_dwordx4 v238,s[40:43],s67 offen lds\n"
"s_cmp_lg_u64 s[82:83],0\n"
"s_cbranch_scc1 Lk_42x\n"
"Lk_42:\n"
"v_mfma_f32_16x16x32_bf16 v[64:67],v[188:191],v[152:155],v[64:67]\n"
"buffer_load_dwordx4 v239,s[40:43],s67 offen offset:1024 lds\n"
"buffer_load_dwordx4 v240,s[40:43],s67 offen offset:2048 lds\n"
"v_mfma_f32_16x16x32_bf16 v[68:71],v[192:195],v[152:155],v[68:71]\n"
"buffer_load_dwordx4 v241,s[40:43],s67 offen offset:3072 lds\n"
"s_mov_b32 s60,0x10000\n"
"v_mfma_f32_16x16x32_bf16 v[72:75],v[196:199],v[152:155],v[72:75]\n"
"v_add_u32 v220,s60,v220\n"
"v_add_u32 v221,s60,v221\n"
"v_mfma_f32_16x16x32_bf16 v[76:79],v[200:203],v[152:155],v[76:79]\n"
"v_add_u32 v222,s60,v222\n"
"v_mfma_f32_16x16x32_bf16 v[80:83],v[204:207],v[152:155],v[80:83]\n"
"v_add_u32 v223,s60,v223\n"
"ds_read_b128 v[156:159],v220\n"
"v_mfma_f32_16x16x32_bf16 v[84:87],v[208:211],v[152:155],v[84:87]\n"
"ds_read_b128 v[172:175],v220 offset:4096\n"
"ds_read_b128 v[160:163],v221\n"
"v_mfma_f32_16x16x32_bf16 v[88:91],v[212:215],v[152:155],v[88:91]\n"
"ds_read_b128 v[176:179],v221 offset:4096\n"
"ds_read_b128 v[164:167],v222\n"
"v_mfma_f32_16x16x32_bf16 v[92:95],v[216:219],v[152:155],v[92:95]\n"
"ds_read_b128 v[180:183],v222 offset:4096\n"
"v_mfma_f32_16x16x32_bf16 v[104:107],a[48:51],v[152:155],v[104:107]\n"
"ds_read_b128 v[168:171],v223\n"
"ds_read_b128 v[184:187],v223 offset:4096\n"
"s_mov_b32 s60,0x10000\n"
"v_add_u32 v224,s60,v224\n"
"v_add_u32 v225,s60,v225\n"
"v_add_u32 v226,s60,v226\n"
"v_add_u32 v227,s60,v227\n"
"v_add_u32 v228,s60,v228\n"
"v_add_u32 v229,s60,v229\n"
"v_add_u32 v230,s60,v230\n"
"v_add_u32 v231,s60,v231\n"
"s_waitcnt lgkmcnt(7)\n"
"v_mfma_f32_16x16x32_bf16 v[108:111],v[156:159],a[0:3],v[132:135]\n"
"s_waitcnt lgkmcnt(6)\n"
"v_mfma_f32_16x16x32_bf16 v[112:115],v[172:175],a[0:3],v[132:135]\n"
"s_add_u32 s67,s59,1\n"
"s_and_b32 s67,s67,15\n"
"s_lshl_b32 s67,s67,15\n"
"s_add_u32 m0,s3,0x1a000\n"
"s_nop 0\n"
"buffer_load_dwordx4 v238,s[40:43],s67 offen lds\n"
"s_waitcnt lgkmcnt(5)\n"
"v_mfma_f32_16x16x32_bf16 v[108:111],v[160:163],a[12:15],v[108:111]\n"
"buffer_load_dwordx4 v239,s[40:43],s67 offen offset:1024 lds\n"
"ds_read_b64_tr_b16 v[188:189],v224\n"
"s_waitcnt lgkmcnt(5)\n"
"v_mfma_f32_16x16x32_bf16 v[112:115],v[176:179],a[12:15],v[112:115]\n"
"ds_read_b64_tr_b16 v[190:191],v224 offset:4096\n"
"ds_read_b64_tr_b16 v[192:193],v225\n"
"s_waitcnt lgkmcnt(6)\n"
"v_mfma_f32_16x16x32_bf16 v[108:111],v[164:167],a[24:27],v[108:111]\n"
"ds_read_b64_tr_b16 v[194:195],v225 offset:4096\n"
"s_waitcnt lgkmcnt(5)\n"
"v_mfma_f32_16x16x32_bf16 v[112:115],v[180:183],a[24:27],v[112:115]\n"
"ds_read_b64_tr_b16 v[196:197],v226\n"
"ds_read_b64_tr_b16 v[198:199],v226 offset:4096\n"
"v_mfma_f32_16x16x32_bf16 v[108:111],v[168:171],a[36:39],v[108:111]\n"
"ds_read_b64_tr_b16 v[200:201],v227\n"
"ds_read_b64_tr_b16 v[202:203],v227 offset:4096\n"
"s_waitcnt lgkmcnt(8)\n"
"v_mfma_f32_16x16x32_bf16 v[112:115],v[184:187],a[36:39],v[112:115]\n"
"ds_read_b64_tr_b16 v[204:205],v228\n"
"ds_read_b64_tr_b16 v[206:207],v228 offset:4096\n"
"v_mfma_f32_16x16x32_bf16 v[116:119],v[156:159],a[4:7],v[136:139]\n"
"s_waitcnt vmcnt(10)\n"
"s_barrier\n"
"ds_read_b64_tr_b16 v[208:209],v229\n"
"ds_read_b64_tr_b16 v[210:211],v229 offset:4096\n"
"v_mfma_f32_16x16x32_bf16 v[120:123],v[172:175],a[4:7],v[136:139]\n"
"ds_read_b64_tr_b16 v[212:213],v230\n"
"ds_read_b64_tr_b16 v[214:215],v230 offset:4096\n"
"v_mfma_f32_16x16x32_bf16 v[116:119],v[160:163],a[16:19],v[116:119]\n"
"ds_read_b64_tr_b16 v[216:217],v231\n"
"s_waitcnt lgkmcnt(14)\n"
"ds_read_b64_tr_b16 v[218:219],v231 offset:4096\n"
"v_max3_f32 v246,v108,v109,v110\n"
"v_mfma_f32_16x16x32_bf16 v[120:123],v[176:179],a[16:19],v[120:123]\n"
"v_max3_f32 v246,v246,v111,v112\n"
"v_max3_f32 v246,v246,v113,v114\n"
"v_mfma_f32_16x16x32_bf16 v[116:119],v[164:167],a[28:31],v[116:119]\n"
"v_max_f32 v246,v246,v115\n"
"v_cmp_lt_f32 s[80:81],s64,v246\n"
"v_mfma_f32_16x16x32_bf16 v[120:123],v[180:183],a[28:31],v[120:123]\n"
"v_exp_f32 v108,v108\n"
"v_exp_f32 v109,v109\n"
"v_mfma_f32_16x16x32_bf16 v[116:119],v[168:171],a[40:43],v[116:119]\n"
"v_exp_f32 v110,v110\n"
"v_mfma_f32_16x16x32_bf16 v[120:123],v[184:187],a[40:43],v[120:123]\n"
"v_exp_f32 v111,v111\n"
"v_mfma_f32_16x16x32_bf16 v[124:127],v[156:159],a[8:11],v[140:143]\n"
"v_cvt_pk_bf16_f32 v144,v108,v109\n"
"v_exp_f32 v112,v112\n"
"v_mfma_f32_16x16x32_bf16 v[128:131],v[172:175],a[8:11],v[140:143]\n"
"v_exp_f32 v113,v113\n"
"v_mfma_f32_16x16x32_bf16 v[124:127],v[160:163],a[20:23],v[124:127]\n"
"v_cvt_pk_bf16_f32 v145,v110,v111\n"
"v_exp_f32 v114,v114\n"
"v_mfma_f32_16x16x32_bf16 v[128:131],v[176:179],a[20:23],v[128:131]\n"
"v_exp_f32 v115,v115\n"
"v_mfma_f32_16x16x32_bf16 v[124:127],v[164:167],a[32:35],v[124:127]\n"
"v_cvt_pk_bf16_f32 v146,v112,v113\n"
"v_cvt_pk_bf16_f32 v147,v114,v115\n"
"v_mfma_f32_16x16x32_bf16 v[128:131],v[180:183],a[32:35],v[128:131]\n"
"buffer_load_dwordx4 v240,s[40:43],s67 offen offset:2048 lds\n"
"buffer_load_dwordx4 v241,s[40:43],s67 offen offset:3072 lds\n"
"v_mfma_f32_16x16x32_bf16 v[124:127],v[168:171],a[44:47],v[124:127]\n"
"v_max3_f32 v247,v116,v117,v118\n"
"v_max3_f32 v247,v247,v119,v120\n"
"v_max3_f32 v247,v247,v121,v122\n"
"v_mfma_f32_16x16x32_bf16 v[128:131],v[184:187],a[44:47],v[128:131]\n"
"v_max_f32 v247,v247,v123\n"
"v_cmp_lt_f32 vcc,s64,v247\n"
"s_cmp_lg_u64 s[80:81],0\n"
"s_cbranch_scc1 Lk_43x\n"
"Lk_43:\n"
"s_waitcnt lgkmcnt(0)\n"
"v_mfma_f32_16x16x32_bf16 v[0:3],v[188:191],v[144:147],v[0:3]\n"
"v_exp_f32 v116,v116\n"
"v_mfma_f32_16x16x32_bf16 v[4:7],v[192:195],v[144:147],v[4:7]\n"
"v_exp_f32 v117,v117\n"
"v_exp_f32 v118,v118\n"
"v_mfma_f32_16x16x32_bf16 v[8:11],v[196:199],v[144:147],v[8:11]\n"
"v_exp_f32 v119,v119\n"
"v_mfma_f32_16x16x32_bf16 v[12:15],v[200:203],v[144:147],v[12:15]\n"
"v_cvt_pk_bf16_f32 v148,v116,v117\n"
"v_exp_f32 v120,v120\n"
"v_mfma_f32_16x16x32_bf16 v[16:19],v[204:207],v[144:147],v[16:19]\n"
"v_exp_f32 v121,v121\n"
"v_cvt_pk_bf16_f32 v149,v118,v119\n"
"v_mfma_f32_16x16x32_bf16 v[20:23],v[208:211],v[144:147],v[20:23]\n"
"v_exp_f32 v122,v122\n"
"v_mfma_f32_16x16x32_bf16 v[24:27],v[212:215],v[144:147],v[24:27]\n"
"v_exp_f32 v123,v123\n"
"v_cvt_pk_bf16_f32 v150,v120,v121\n"
"v_mfma_f32_16x16x32_bf16 v[28:31],v[216:219],v[144:147],v[28:31]\n"
"v_cvt_pk_bf16_f32 v151,v122,v123\n"
"v_max3_f32 v248,v124,v125,v126\n"
"v_max3_f32 v248,v248,v127,v128\n"
"v_mfma_f32_16x16x32_bf16 v[96:99],a[48:51],v[144:147],v[96:99]\n"
"v_max3_f32 v248,v248,v129,v130\n"
"v_max_f32 v248,v248,v131\n"
"v_cmp_lt_f32 s[82:83],s64,v248\n"
"s_cbranch_vccnz Lk_44x\n"
"Lk_44:\n"
"v_mfma_f32_16x16x32_bf16 v[32:35],v[188:191],v[148:151],v[32:35]\n"
"v_exp_f32 v124,v124\n"
"v_mfma_f32_16x16x32_bf16 v[36:39],v[192:195],v[148:151],v[36:39]\n"
"v_exp_f32 v125,v125\n"
"v_exp_f32 v126,v126\n"
"v_mfma_f32_16x16x32_bf16 v[40:43],v[196:199],v[148:151],v[40:43]\n"
"v_exp_f32 v127,v127\n"
"v_mfma_f32_16x16x32_bf16 v[44:47],v[200:203],v[148:151],v[44:47]\n"
"v_cvt_pk_bf16_f32 v152,v124,v125\n"
"v_exp_f32 v128,v128\n"
"v_mfma_f32_16x16x32_bf16 v[48:51],v[204:207],v[148:151],v[48:51]\n"
"v_exp_f32 v129,v129\n"
"v_mfma_f32_16x16x32_bf16 v[52:55],v[208:211],v[148:151],v[52:55]\n"
"v_cvt_pk_bf16_f32 v153,v126,v127\n"
"v_exp_f32 v130,v130\n"
"v_mfma_f32_16x16x32_bf16 v[56:59],v[212:215],v[148:151],v[56:59]\n"
"v_exp_f32 v131,v131\n"
"v_mfma_f32_16x16x32_bf16 v[60:63],v[216:219],v[148:151],v[60:63]\n"
"v_cvt_pk_bf16_f32 v154,v128,v129\n"
"v_cvt_pk_bf16_f32 v155,v130,v131\n"
"v_mfma_f32_16x16x32_bf16 v[100:103],a[48:51],v[148:151],v[100:103]\n"
"s_add_u32 s67,s67,0x4000\n"
"s_add_u32 m0,s3,0x1b000\n"
"s_nop 0\n"
"buffer_load_dwordx4 v238,s[40:43],s67 offen lds\n"
"buffer_load_dwordx4 v239,s[40:43],s67 offen offset:1024 lds\n"
"s_cmp_lg_u64 s[82:83],0\n"
"s_cbranch_scc1 Lk_45x\n"
"Lk_45:\n"
"v_mfma_f32_16x16x32_bf16 v[64:67],v[188:191],v[152:155],v[64:67]\n"
"buffer_load_dwordx4 v240,s[40:43],s67 offen offset:2048 lds\n"
"v_mfma_f32_16x16x32_bf16 v[68:71],v[192:195],v[152:155],v[68:71]\n"
"buffer_load_dwordx4 v241,s[40:43],s67 offen offset:3072 lds\n"
"v_mfma_f32_16x16x32_bf16 v[72:75],v[196:199],v[152:155],v[72:75]\n"
"ds_read_b128 v[156:159],v220 offset:16384\n"
"v_mfma_f32_16x16x32_bf16 v[76:79],v[200:203],v[152:155],v[76:79]\n"
"ds_read_b128 v[172:175],v220 offset:20480\n"
"v_mfma_f32_16x16x32_bf16 v[80:83],v[204:207],v[152:155],v[80:83]\n"
"ds_read_b128 v[160:163],v221 offset:16384\n"
"ds_read_b128 v[176:179],v221 offset:20480\n"
"v_mfma_f32_16x16x32_bf16 v[84:87],v[208:211],v[152:155],v[84:87]\n"
"ds_read_b128 v[164:167],v222 offset:16384\n"
"v_mfma_f32_16x16x32_bf16 v[88:91],v[212:215],v[152:155],v[88:91]\n"
"ds_read_b128 v[180:183],v222 offset:20480\n"
"v_mfma_f32_16x16x32_bf16 v[92:95],v[216:219],v[152:155],v[92:95]\n"
"ds_read_b128 v[168:171],v223 offset:16384\n"
"v_mfma_f32_16x16x32_bf16 v[104:107],a[48:51],v[152:155],v[104:107]\n"
"ds_read_b128 v[184:187],v223 offset:20480\n"
"s_waitcnt lgkmcnt(7)\n"
"v_mfma_f32_16x16x32_bf16 v[108:111],v[156:159],a[0:3],v[132:135]\n"
"ds_read_b64_tr_b16 v[188:189],v224 offset:16384\n"
"s_waitcnt lgkmcnt(7)\n"
"v_mfma_f32_16x16x32_bf16 v[112:115],v[172:175],a[0:3],v[132:135]\n"
"ds_read_b64_tr_b16 v[190:191],v224 offset:20480\n"
"ds_read_b64_tr_b16 v[192:193],v225 offset:16384\n"
"s_waitcnt lgkmcnt(8)\n"
"v_mfma_f32_16x16x32_bf16 v[108:111],v[160:163],a[12:15],v[108:111]\n"
"ds_read_b64_tr_b16 v[194:195],v225 offset:20480\n"
"s_waitcnt lgkmcnt(8)\n"
"v_mfma_f32_16x16x32_bf16 v[112:115],v[176:179],a[12:15],v[112:115]\n"
"ds_read_b64_tr_b16 v[196:197],v226 offset:16384\n"
"s_waitcnt lgkmcnt(8)\n"
"v_mfma_f32_16x16x32_bf16 v[108:111],v[164:167],a[24:27],v[108:111]\n"
"ds_read_b64_tr_b16 v[198:199],v226 offset:20480\n"
"s_waitcnt lgkmcnt(8)\n"
"v_mfma_f32_16x16x32_bf16 v[112:115],v[180:183],a[24:27],v[112:115]\n"
"ds_read_b64_tr_b16 v[200:201],v227 offset:16384\n"
"ds_read_b64_tr_b16 v[202:203],v227 offset:20480\n"
"s_waitcnt lgkmcnt(9)\n"
"v_mfma_f32_16x16x32_bf16 v[108:111],v[168:171],a[36:39],v[108:111]\n"
"ds_read_b64_tr_b16 v[204:205],v228 offset:16384\n"
"s_waitcnt lgkmcnt(9)\n"
"v_mfma_f32_16x16x32_bf16 v[112:115],v[184:187],a[36:39],v[112:115]\n"
"ds_read_b64_tr_b16 v[206:207],v228 offset:20480\n"
"v_mfma_f32_16x16x32_bf16 v[116:119],v[156:159],a[4:7],v[136:139]\n"
"ds_read_b64_tr_b16 v[208:209],v229 offset:16384\n"
"ds_read_b64_tr_b16 v[210:211],v229 offset:20480\n"
"v_mfma_f32_16x16x32_bf16 v[120:123],v[172:175],a[4:7],v[136:139]\n"
"ds_read_b64_tr_b16 v[212:213],v230 offset:16384\n"
"ds_read_b64_tr_b16 v[214:215],v230 offset:20480\n"
"ds_read_b64_tr_b16 v[216:217],v231 offset:16384\n"
"v_mfma_f32_16x16x32_bf16 v[116:119],v[160:163],a[16:19],v[116:119]\n"
"s_waitcnt lgkmcnt(12)\n"
"ds_read_b64_tr_b16 v[218:219],v231 offset:20480\n"
"v_max3_f32 v246,v108,v109,v110\n"
"v_mfma_f32_16x16x32_bf16 v[120:123],v[176:179],a[16:19],v[120:123]\n"
"v_max3_f32 v246,v246,v111,v112\n"
"v_max3_f32 v246,v246,v113,v114\n"
"v_max_f32 v246,v246,v115\n"
"v_mfma_f32_16x16x32_bf16 v[116:119],v[164:167],a[28:31],v[116:119]\n"
"v_cmp_lt_f32 s[80:81],s64,v246\n"
"v_mfma_f32_16x16x32_bf16 v[120:123],v[180:183],a[28:31],v[120:123]\n"
"v_exp_f32 v108,v108\n"
"v_exp_f32 v109,v109\n"
"v_mfma_f32_16x16x32_bf16 v[116:119],v[168:171],a[40:43],v[116:119]\n"
"v_exp_f32 v110,v110\n"
"v_mfma_f32_16x16x32_bf16 v[120:123],v[184:187],a[40:43],v[120:123]\n"
"v_exp_f32 v111,v111\n"
"v_mfma_f32_16x16x32_bf16 v[124:127],v[156:159],a[8:11],v[140:143]\n"
"v_cvt_pk_bf16_f32 v144,v108,v109\n"
"v_exp_f32 v112,v112\n"
"v_mfma_f32_16x16x32_bf16 v[128:131],v[172:175],a[8:11],v[140:143]\n"
"v_exp_f32 v113,v113\n"
"v_mfma_f32_16x16x32_bf16 v[124:127],v[160:163],a[20:23],v[124:127]\n"
"v_cvt_pk_bf16_f32 v145,v110,v111\n"
"v_mfma_f32_16x16x32_bf16 v[128:131],v[176:179],a[20:23],v[128:131]\n"
"v_exp_f32 v114,v114\n"
"v_mfma_f32_16x16x32_bf16 v[124:127],v[164:167],a[32:35],v[124:127]\n"
"v_exp_f32 v115,v115\n"
"v_cvt_pk_bf16_f32 v146,v112,v113\n"
"v_mfma_f32_16x16x32_bf16 v[128:131],v[180:183],a[32:35],v[128:131]\n"
"v_cvt_pk_bf16_f32 v147,v114,v115\n"
"v_max3_f32 v247,v116,v117,v118\n"
"v_mfma_f32_16x16x32_bf16 v[124:127],v[168:171],a[44:47],v[124:127]\n"
"v_max3_f32 v247,v247,v119,v120\n"
"v_max3_f32 v247,v247,v121,v122\n"
"v_mfma_f32_16x16x32_bf16 v[128:131],v[184:187],a[44:47],v[128:131]\n"
"v_max_f32 v247,v247,v123\n"
"v_cmp_lt_f32 vcc,s64,v247\n"
"s_cmp_lg_u64 s[80:81],0\n"
"s_cbranch_scc1 Lk_46x\n"
"Lk_46:\n"
"v_mfma_f32_16x16x32_bf16 v[0:3],v[188:191],v[144:147],v[0:3]\n"
"v_exp_f32 v116,v116\n"
"s_waitcnt lgkmcnt(0)\n"
"v_mfma_f32_16x16x32_bf16 v[4:7],v[192:195],v[144:147],v[4:7]\n"
"v_exp_f32 v117,v117\n"
"v_exp_f32 v118,v118\n"
"v_mfma_f32_16x16x32_bf16 v[8:11],v[196:199],v[144:147],v[8:11]\n"
"v_exp_f32 v119,v119\n"
"v_mfma_f32_16x16x32_bf16 v[12:15],v[200:203],v[144:147],v[12:15]\n"
"v_cvt_pk_bf16_f32 v148,v116,v117\n"
"v_exp_f32 v120,v120\n"
"v_mfma_f32_16x16x32_bf16 v[16:19],v[204:207],v[144:147],v[16:19]\n"
"v_exp_f32 v121,v121\n"
"v_cvt_pk_bf16_f32 v149,v118,v119\n"
"v_mfma_f32_16x16x32_bf16 v[20:23],v[208:211],v[144:147],v[20:23]\n"
"v_exp_f32 v122,v122\n"
"v_mfma_f32_16x16x32_bf16 v[24:27],v[212:215],v[144:147],v[24:27]\n"
"v_exp_f32 v123,v123\n"
"v_cvt_pk_bf16_f32 v150,v120,v121\n"
"v_mfma_f32_16x16x32_bf16 v[28:31],v[216:219],v[144:147],v[28:31]\n"
"v_cvt_pk_bf16_f32 v151,v122,v123\n"
"v_max3_f32 v248,v124,v125,v126\n"
"v_max3_f32 v248,v248,v127,v128\n"
"v_mfma_f32_16x16x32_bf16 v[96:99],a[48:51],v[144:147],v[96:99]\n"
"v_max3_f32 v248,v248,v129,v130\n"
"v_max_f32 v248,v248,v131\n"
"v_cmp_lt_f32 s[82:83],s64,v248\n"
"s_cbranch_vccnz Lk_47x\n"
"Lk_47:\n"
"v_mfma_f32_16x16x32_bf16 v[32:35],v[188:191],v[148:151],v[32:35]\n"
"v_exp_f32 v124,v124\n"
"v_mfma_f32_16x16x32_bf16 v[36:39],v[192:195],v[148:151],v[36:39]\n"
"v_exp_f32 v125,v125\n"
"v_mfma_f32_16x16x32_bf16 v[40:43],v[196:199],v[148:151],v[40:43]\n"
"v_exp_f32 v126,v126\n"
"v_mfma_f32_16x16x32_bf16 v[44:47],v[200:203],v[148:151],v[44:47]\n"
"v_exp_f32 v127,v127\n"
"v_cvt_pk_bf16_f32 v152,v124,v125\n"
"v_mfma_f32_16x16x32_bf16 v[48:51],v[204:207],v[148:151],v[48:51]\n"
"v_exp_f32 v128,v128\n"
"v_mfma_f32_16x16x32_bf16 v[52:55],v[208:211],v[148:151],v[52:55]\n"
"v_exp_f32 v129,v129\n"
"v_mfma_f32_16x16x32_bf16 v[56:59],v[212:215],v[148:151],v[56:59]\n"
"v_cvt_pk_bf16_f32 v153,v126,v127\n"
"v_exp_f32 v130,v130\n"
"v_mfma_f32_16x16x32_bf16 v[60:63],v[216:219],v[148:151],v[60:63]\n"
"v_exp_f32 v131,v131\n"
"v_mfma_f32_16x16x32_bf16 v[100:103],a[48:51],v[148:151],v[100:103]\n"
"v_cvt_pk_bf16_f32 v154,v128,v129\n"
"v_cvt_pk_bf16_f32 v155,v130,v131\n"
"s_cmp_lg_u64 s[82:83],0\n"
"s_cbranch_scc1 Lk_48x\n"
"Lk_48:\n"
"v_mfma_f32_16x16x32_bf16 v[64:67],v[188:191],v[152:155],v[64:67]\n"
"v_mfma_f32_16x16x32_bf16 v[68:71],v[192:195],v[152:155],v[68:71]\n"
"v_mfma_f32_16x16x32_bf16 v[72:75],v[196:199],v[152:155],v[72:75]\n"
"v_mfma_f32_16x16x32_bf16 v[76:79],v[200:203],v[152:155],v[76:79]\n"
"v_mfma_f32_16x16x32_bf16 v[80:83],v[204:207],v[152:155],v[80:83]\n"
"v_mfma_f32_16x16x32_bf16 v[84:87],v[208:211],v[152:155],v[84:87]\n"
"v_mfma_f32_16x16x32_bf16 v[88:91],v[212:215],v[152:155],v[88:91]\n"
"v_mfma_f32_16x16x32_bf16 v[92:95],v[216:219],v[152:155],v[92:95]\n"
"v_mfma_f32_16x16x32_bf16 v[104:107],a[48:51],v[152:155],v[104:107]\n"
"s_mul_i32 s60,s7,0x3000\n"
"v_add_u32 v254,s60,v242\n"
"v_rcp_f32 v250,v96\n"
"s_nop 0\n"
"v_pk_mul_f32 v[0:1],v[0:1],v[250:251] op_sel_hi:[1,0]\n"
"v_pk_mul_f32 v[2:3],v[2:3],v[250:251] op_sel_hi:[1,0]\n"
"v_cvt_pk_bf16_f32 v108,v0,v1\n"
"v_cvt_pk_bf16_f32 v109,v2,v3\n"
"v_pk_mul_f32 v[4:5],v[4:5],v[250:251] op_sel_hi:[1,0]\n"
"v_pk_mul_f32 v[6:7],v[6:7],v[250:251] op_sel_hi:[1,0]\n"
"v_cvt_pk_bf16_f32 v110,v4,v5\n"
"v_cvt_pk_bf16_f32 v111,v6,v7\n"
"ds_write_b128 v254,v[108:111]\n"
"v_pk_mul_f32 v[8:9],v[8:9],v[250:251] op_sel_hi:[1,0]\n"
"v_pk_mul_f32 v[10:11],v[10:11],v[250:251] op_sel_hi:[1,0]\n"
"v_cvt_pk_bf16_f32 v112,v8,v9\n"
"v_cvt_pk_bf16_f32 v113,v10,v11\n"
"v_pk_mul_f32 v[12:13],v[12:13],v[250:251] op_sel_hi:[1,0]\n"
"v_pk_mul_f32 v[14:15],v[14:15],v[250:251] op_sel_hi:[1,0]\n"
"v_cvt_pk_bf16_f32 v114,v12,v13\n"
"v_cvt_pk_bf16_f32 v115,v14,v15\n"
"ds_write_b128 v254,v[112:115] offset:3072\n"
"v_pk_mul_f32 v[16:17],v[16:17],v[250:251] op_sel_hi:[1,0]\n"
"v_pk_mul_f32 v[18:19],v[18:19],v[250:251] op_sel_hi:[1,0]\n"
"v_cvt_pk_bf16_f32 v108,v16,v17\n"
"v_cvt_pk_bf16_f32 v109,v18,v19\n"
"v_pk_mul_f32 v[20:21],v[20:21],v[250:251] op_sel_hi:[1,0]\n"
"v_pk_mul_f32 v[22:23],v[22:23],v[250:251] op_sel_hi:[1,0]\n"
"v_cvt_pk_bf16_f32 v110,v20,v21\n"
"v_cvt_pk_bf16_f32 v111,v22,v23\n"
"ds_write_b128 v254,v[108:111] offset:6144\n"
"v_pk_mul_f32 v[24:25],v[24:25],v[250:251] op_sel_hi:[1,0]\n"
"v_pk_mul_f32 v[26:27],v[26:27],v[250:251] op_sel_hi:[1,0]\n"
"v_cvt_pk_bf16_f32 v112,v24,v25\n"
"v_cvt_pk_bf16_f32 v113,v26,v27\n"
"v_pk_mul_f32 v[28:29],v[28:29],v[250:251] op_sel_hi:[1,0]\n"
"v_pk_mul_f32 v[30:31],v[30:31],v[250:251] op_sel_hi:[1,0]\n"
"v_cvt_pk_bf16_f32 v114,v28,v29\n"
"v_cvt_pk_bf16_f32 v115,v30,v31\n"
"ds_write_b128 v254,v[112:115] offset:9216\n"
"v_rcp_f32 v250,v100\n"
"s_nop 0\n"
"v_pk_mul_f32 v[32:33],v[32:33],v[250:251] op_sel_hi:[1,0]\n"
"v_pk_mul_f32 v[34:35],v[34:35],v[250:251] op_sel_hi:[1,0]\n"
"v_cvt_pk_bf16_f32 v108,v32,v33\n"
"v_cvt_pk_bf16_f32 v109,v34,v35\n"
"v_pk_mul_f32 v[36:37],v[36:37],v[250:251] op_sel_hi:[1,0]\n"
"v_pk_mul_f32 v[38:39],v[38:39],v[250:251] op_sel_hi:[1,0]\n"
"v_cvt_pk_bf16_f32 v110,v36,v37\n"
"v_cvt_pk_bf16_f32 v111,v38,v39\n"
"ds_write_b128 v254,v[108:111] offset:1024\n"
"v_pk_mul_f32 v[40:41],v[40:41],v[250:251] op_sel_hi:[1,0]\n"
"v_pk_mul_f32 v[42:43],v[42:43],v[250:251] op_sel_hi:[1,0]\n"
"v_cvt_pk_bf16_f32 v112,v40,v41\n"
"v_cvt_pk_bf16_f32 v113,v42,v43\n"
"v_pk_mul_f32 v[44:45],v[44:45],v[250:251] op_sel_hi:[1,0]\n"
"v_pk_mul_f32 v[46:47],v[46:47],v[250:251] op_sel_hi:[1,0]\n"
"v_cvt_pk_bf16_f32 v114,v44,v45\n"
"v_cvt_pk_bf16_f32 v115,v46,v47\n"
"ds_write_b128 v254,v[112:115] offset:4096\n"
"v_pk_mul_f32 v[48:49],v[48:49],v[250:251] op_sel_hi:[1,0]\n"
"v_pk_mul_f32 v[50:51],v[50:51],v[250:251] op_sel_hi:[1,0]\n"
"v_cvt_pk_bf16_f32 v108,v48,v49\n"
"v_cvt_pk_bf16_f32 v109,v50,v51\n"
"v_pk_mul_f32 v[52:53],v[52:53],v[250:251] op_sel_hi:[1,0]\n"
"v_pk_mul_f32 v[54:55],v[54:55],v[250:251] op_sel_hi:[1,0]\n"
"v_cvt_pk_bf16_f32 v110,v52,v53\n"
"v_cvt_pk_bf16_f32 v111,v54,v55\n"
"ds_write_b128 v254,v[108:111] offset:7168\n"
"v_pk_mul_f32 v[56:57],v[56:57],v[250:251] op_sel_hi:[1,0]\n"
"v_pk_mul_f32 v[58:59],v[58:59],v[250:251] op_sel_hi:[1,0]\n"
"v_cvt_pk_bf16_f32 v112,v56,v57\n"
"v_cvt_pk_bf16_f32 v113,v58,v59\n"
"v_pk_mul_f32 v[60:61],v[60:61],v[250:251] op_sel_hi:[1,0]\n"
"v_pk_mul_f32 v[62:63],v[62:63],v[250:251] op_sel_hi:[1,0]\n"
"v_cvt_pk_bf16_f32 v114,v60,v61\n"
"v_cvt_pk_bf16_f32 v115,v62,v63\n"
"ds_write_b128 v254,v[112:115] offset:10240\n"
"v_rcp_f32 v250,v104\n"
"s_nop 0\n"
"v_pk_mul_f32 v[64:65],v[64:65],v[250:251] op_sel_hi:[1,0]\n"
"v_pk_mul_f32 v[66:67],v[66:67],v[250:251] op_sel_hi:[1,0]\n"
"v_cvt_pk_bf16_f32 v108,v64,v65\n"
"v_cvt_pk_bf16_f32 v109,v66,v67\n"
"v_pk_mul_f32 v[68:69],v[68:69],v[250:251] op_sel_hi:[1,0]\n"
"v_pk_mul_f32 v[70:71],v[70:71],v[250:251] op_sel_hi:[1,0]\n"
"v_cvt_pk_bf16_f32 v110,v68,v69\n"
"v_cvt_pk_bf16_f32 v111,v70,v71\n"
"ds_write_b128 v254,v[108:111] offset:2048\n"
"v_pk_mul_f32 v[72:73],v[72:73],v[250:251] op_sel_hi:[1,0]\n"
"v_pk_mul_f32 v[74:75],v[74:75],v[250:251] op_sel_hi:[1,0]\n"
"v_cvt_pk_bf16_f32 v112,v72,v73\n"
"v_cvt_pk_bf16_f32 v113,v74,v75\n"
"v_pk_mul_f32 v[76:77],v[76:77],v[250:251] op_sel_hi:[1,0]\n"
"v_pk_mul_f32 v[78:79],v[78:79],v[250:251] op_sel_hi:[1,0]\n"
"v_cvt_pk_bf16_f32 v114,v76,v77\n"
"v_cvt_pk_bf16_f32 v115,v78,v79\n"
"ds_write_b128 v254,v[112:115] offset:5120\n"
"v_pk_mul_f32 v[80:81],v[80:81],v[250:251] op_sel_hi:[1,0]\n"
"v_pk_mul_f32 v[82:83],v[82:83],v[250:251] op_sel_hi:[1,0]\n"
"v_cvt_pk_bf16_f32 v108,v80,v81\n"
"v_cvt_pk_bf16_f32 v109,v82,v83\n"
"v_pk_mul_f32 v[84:85],v[84:85],v[250:251] op_sel_hi:[1,0]\n"
"v_pk_mul_f32 v[86:87],v[86:87],v[250:251] op_sel_hi:[1,0]\n"
"v_cvt_pk_bf16_f32 v110,v84,v85\n"
"v_cvt_pk_bf16_f32 v111,v86,v87\n"
"ds_write_b128 v254,v[108:111] offset:8192\n"
"v_pk_mul_f32 v[88:89],v[88:89],v[250:251] op_sel_hi:[1,0]\n"
"v_pk_mul_f32 v[90:91],v[90:91],v[250:251] op_sel_hi:[1,0]\n"
"v_cvt_pk_bf16_f32 v112,v88,v89\n"
"v_cvt_pk_bf16_f32 v113,v90,v91\n"
"v_pk_mul_f32 v[92:93],v[92:93],v[250:251] op_sel_hi:[1,0]\n"
"v_pk_mul_f32 v[94:95],v[94:95],v[250:251] op_sel_hi:[1,0]\n"
"v_cvt_pk_bf16_f32 v114,v92,v93\n"
"v_cvt_pk_bf16_f32 v115,v94,v95\n"
"ds_write_b128 v254,v[112:115] offset:11264\n"
"s_add_u32 s61,s3,0x18000\n"
"v_lshrrev_b32 v246,2,v243\n"
"v_lshl_or_b32 v246,v244,2,v246\n"
"v_and_b32 v247,7,v246\n"
"v_lshlrev_b32 v247,5,v247\n"
"v_lshl_or_b32 v247,v246,8,v247\n"
"v_and_b32 v248,3,v243\n"
"v_lshl_or_b32 v247,v248,3,v247\n"
"v_add_u32 v247,s61,v247\n"
"v_xor_b32 v224,0,v247\n"
"v_xor_b32 v225,32,v247\n"
"v_xor_b32 v226,64,v247\n"
"v_xor_b32 v227,0x60,v247\n"
"v_xor_b32 v228,0x80,v247\n"
"v_xor_b32 v229,0xa0,v247\n"
"v_xor_b32 v230,0xc0,v247\n"
"v_xor_b32 v231,0xe0,v247\n"
"s_add_u32 s61,s2,0xc000\n"
"v_lshrrev_b32 v246,2,v243\n"
"v_lshl_or_b32 v246,v244,2,v246\n"
"v_and_b32 v247,7,v246\n"
"v_lshlrev_b32 v247,5,v247\n"
"v_lshl_or_b32 v247,v246,8,v247\n"
"v_and_b32 v248,3,v243\n"
"v_lshl_or_b32 v247,v248,3,v247\n"
"v_add_u32 v247,s61,v247\n"
"v_xor_b32 v132,0,v247\n"
"v_xor_b32 v133,32,v247\n"
"v_xor_b32 v134,64,v247\n"
"v_xor_b32 v135,0x60,v247\n"
"v_xor_b32 v136,0x80,v247\n"
"v_xor_b32 v137,0xa0,v247\n"
"v_xor_b32 v138,0xc0,v247\n"
"v_xor_b32 v139,0xe0,v247\n"
"s_waitcnt lgkmcnt(0)\n"
"s_barrier\n"
"s_add_u32 s67,s59,2\n"
"s_and_b32 s67,s67,15\n"
"s_lshl_b32 s67,s67,15\n"
"s_add_u32 m0,s2,0xc000\n"
"s_nop 0\n"
"buffer_load_dwordx4 v238,s[40:43],s67 offen lds\n"
"buffer_load_dwordx4 v239,s[40:43],s67 offen offset:1024 lds\n"
"buffer_load_dwordx4 v240,s[40:43],s67 offen offset:2048 lds\n"
"buffer_load_dwordx4 v241,s[40:43],s67 offen offset:3072 lds\n"
"s_add_u32 s67,s67,0x4000\n"
"s_add_u32 m0,s2,0xd000\n"
"s_nop 0\n"
"buffer_load_dwordx4 v238,s[40:43],s67 offen lds\n"
"buffer_load_dwordx4 v239,s[40:43],s67 offen offset:1024 lds\n"
"buffer_load_dwordx4 v240,s[40:43],s67 offen offset:2048 lds\n"
"buffer_load_dwordx4 v241,s[40:43],s67 offen offset:3072 lds\n"
"s_add_u32 s60,s59,0\n"
"s_and_b32 s60,s60,15\n"
"s_mul_i32 s60,s60,0xc00\n"
"v_add_u32 v140,s60,v242\n"
"ds_read_b128 v[108:111],v140\n"
"ds_read_b128 v[112:115],v140 offset:1024\n"
"ds_read_b128 v[116:119],v140 offset:2048\n"
"s_waitcnt vmcnt(16)\n"
"ds_read_b64_tr_b16 v[188:189],v224\n"
"ds_read_b64_tr_b16 v[190:191],v224 offset:4096\n"
"ds_read_b64_tr_b16 v[192:193],v225\n"
"ds_read_b64_tr_b16 v[194:195],v225 offset:4096\n"
"ds_read_b64_tr_b16 v[196:197],v226\n"
"ds_read_b64_tr_b16 v[198:199],v226 offset:4096\n"
"ds_read_b64_tr_b16 v[200:201],v227\n"
"ds_read_b64_tr_b16 v[202:203],v227 offset:4096\n"
"ds_read_b64_tr_b16 v[204:205],v228\n"
"ds_read_b64_tr_b16 v[206:207],v228 offset:4096\n"
"ds_read_b64_tr_b16 v[208:209],v229\n"
"ds_read_b64_tr_b16 v[210:211],v229 offset:4096\n"
"s_waitcnt lgkmcnt(14)\n"
"ds_read_b64_tr_b16 v[212:213],v230\n"
"s_waitcnt lgkmcnt(14)\n"
"ds_read_b64_tr_b16 v[214:215],v230 offset:4096\n"
"s_waitcnt lgkmcnt(14)\n"
"ds_read_b64_tr_b16 v[216:217],v231\n"
"s_waitcnt lgkmcnt(14)\n"
"ds_read_b64_tr_b16 v[218:219],v231 offset:4096\n"
"s_waitcnt lgkmcnt(14)\n"
"v_mfma_f32_16x16x32_bf16 v[0:3],v[188:191],v[108:111],0\n"
"s_waitcnt lgkmcnt(0)\n"
"v_mfma_f32_16x16x32_bf16 v[4:7],v[188:191],v[112:115],0\n"
"s_add_u32 s67,s59,3\n"
"s_and_b32 s67,s67,15\n"
"s_lshl_b32 s67,s67,15\n"
"s_add_u32 m0,s3,0x18000\n"
"s_nop 0\n"
"buffer_load_dwordx4 v238,s[40:43],s67 offen lds\n"
"v_mfma_f32_16x16x32_bf16 v[8:11],v[188:191],v[116:119],0\n"
"v_mfma_f32_16x16x32_bf16 v[12:15],v[192:195],v[108:111],0\n"
"buffer_load_dwordx4 v239,s[40:43],s67 offen offset:1024 lds\n"
"buffer_load_dwordx4 v240,s[40:43],s67 offen offset:2048 lds\n"
"v_mfma_f32_16x16x32_bf16 v[16:19],v[192:195],v[112:115],0\n"
"buffer_load_dwordx4 v241,s[40:43],s67 offen offset:3072 lds\n"
"v_mfma_f32_16x16x32_bf16 v[20:23],v[192:195],v[116:119],0\n"
"s_add_u32 s67,s67,0x4000\n"
"s_add_u32 m0,s3,0x19000\n"
"s_nop 0\n"
"buffer_load_dwordx4 v238,s[40:43],s67 offen lds\n"
"v_mfma_f32_16x16x32_bf16 v[24:27],v[196:199],v[108:111],0\n"
"buffer_load_dwordx4 v239,s[40:43],s67 offen offset:1024 lds\n"
"v_mfma_f32_16x16x32_bf16 v[28:31],v[196:199],v[112:115],0\n"
"buffer_load_dwordx4 v240,s[40:43],s67 offen offset:2048 lds\n"
"v_mfma_f32_16x16x32_bf16 v[32:35],v[196:199],v[116:119],0\n"
"buffer_load_dwordx4 v241,s[40:43],s67 offen offset:3072 lds\n"
"v_mfma_f32_16x16x32_bf16 v[36:39],v[200:203],v[108:111],0\n"
"s_add_u32 s60,s59,1\n"
"s_and_b32 s60,s60,15\n"
"s_mul_i32 s60,s60,0xc00\n"
"v_add_u32 v140,s60,v242\n"
"ds_read_b128 v[120:123],v140\n"
"v_mfma_f32_16x16x32_bf16 v[40:43],v[200:203],v[112:115],0\n"
"v_mfma_f32_16x16x32_bf16 v[44:47],v[200:203],v[116:119],0\n"
"ds_read_b128 v[124:127],v140 offset:1024\n"
"v_mfma_f32_16x16x32_bf16 v[48:51],v[204:207],v[108:111],0\n"
"ds_read_b128 v[128:131],v140 offset:2048\n"
"v_mfma_f32_16x16x32_bf16 v[52:55],v[204:207],v[112:115],0\n"
"s_waitcnt vmcnt(16)\n"
"ds_read_b64_tr_b16 v[156:157],v224 offset:8192\n"
"ds_read_b64_tr_b16 v[158:159],v224 offset:12288\n"
"v_mfma_f32_16x16x32_bf16 v[56:59],v[204:207],v[116:119],0\n"
"ds_read_b64_tr_b16 v[160:161],v225 offset:8192\n"
"v_mfma_f32_16x16x32_bf16 v[60:63],v[208:211],v[108:111],0\n"
"ds_read_b64_tr_b16 v[162:163],v225 offset:12288\n"
"ds_read_b64_tr_b16 v[164:165],v226 offset:8192\n"
"v_mfma_f32_16x16x32_bf16 v[64:67],v[208:211],v[112:115],0\n"
"ds_read_b64_tr_b16 v[166:167],v226 offset:12288\n"
"v_mfma_f32_16x16x32_bf16 v[68:71],v[208:211],v[116:119],0\n"
"ds_read_b64_tr_b16 v[168:169],v227 offset:8192\n"
"v_mfma_f32_16x16x32_bf16 v[72:75],v[212:215],v[108:111],0\n"
"ds_read_b64_tr_b16 v[170:171],v227 offset:12288\n"
"ds_read_b64_tr_b16 v[172:173],v228 offset:8192\n"
"v_mfma_f32_16x16x32_bf16 v[76:79],v[212:215],v[112:115],0\n"
"ds_read_b64_tr_b16 v[174:175],v228 offset:12288\n"
"v_mfma_f32_16x16x32_bf16 v[80:83],v[212:215],v[116:119],0\n"
"ds_read_b64_tr_b16 v[176:177],v229 offset:8192\n"
"ds_read_b64_tr_b16 v[178:179],v229 offset:12288\n"
"v_mfma_f32_16x16x32_bf16 v[84:87],v[216:219],v[108:111],0\n"
"s_waitcnt lgkmcnt(13)\n"
"ds_read_b64_tr_b16 v[180:181],v230 offset:8192\n"
"v_mfma_f32_16x16x32_bf16 v[88:91],v[216:219],v[112:115],0\n"
"ds_read_b64_tr_b16 v[182:183],v230 offset:12288\n"
"s_waitcnt lgkmcnt(13)\n"
"ds_read_b64_tr_b16 v[184:185],v231 offset:8192\n"
"v_mfma_f32_16x16x32_bf16 v[92:95],v[216:219],v[116:119],0\n"
"ds_read_b64_tr_b16 v[186:187],v231 offset:12288\n"
"s_waitcnt lgkmcnt(13)\n"
"v_mfma_f32_16x16x32_bf16 v[0:3],v[156:159],v[120:123],v[0:3]\n"
"s_waitcnt lgkmcnt(0)\n"
"v_mfma_f32_16x16x32_bf16 v[4:7],v[156:159],v[124:127],v[4:7]\n"
"s_add_u32 s67,s59,4\n"
"s_and_b32 s67,s67,15\n"
"s_lshl_b32 s67,s67,15\n"
"s_add_u32 m0,s3,0x1a000\n"
"s_nop 0\n"
"buffer_load_dwordx4 v238,s[40:43],s67 offen lds\n"
"v_mfma_f32_16x16x32_bf16 v[8:11],v[156:159],v[128:131],v[8:11]\n"
"v_mfma_f32_16x16x32_bf16 v[12:15],v[160:163],v[120:123],v[12:15]\n"
"buffer_load_dwordx4 v239,s[40:43],s67 offen offset:1024 lds\n"
"buffer_load_dwordx4 v240,s[40:43],s67 offen offset:2048 lds\n"
"v_mfma_f32_16x16x32_bf16 v[16:19],v[160:163],v[124:127],v[16:19]\n"
"buffer_load_dwordx4 v241,s[40:43],s67 offen offset:3072 lds\n"
"v_mfma_f32_16x16x32_bf16 v[20:23],v[160:163],v[128:131],v[20:23]\n"
"s_add_u32 s67,s67,0x4000\n"
"s_add_u32 m0,s3,0x1b000\n"
"s_nop 0\n"
"buffer_load_dwordx4 v238,s[40:43],s67 offen lds\n"
"v_mfma_f32_16x16x32_bf16 v[24:27],v[164:167],v[120:123],v[24:27]\n"
"buffer_load_dwordx4 v239,s[40:43],s67 offen offset:1024 lds\n"
"v_mfma_f32_16x16x32_bf16 v[28:31],v[164:167],v[124:127],v[28:31]\n"
"buffer_load_dwordx4 v240,s[40:43],s67 offen offset:2048 lds\n"
"v_mfma_f32_16x16x32_bf16 v[32:35],v[164:167],v[128:131],v[32:35]\n"
"buffer_load_dwordx4 v241,s[40:43],s67 offen offset:3072 lds\n"
"v_mfma_f32_16x16x32_bf16 v[36:39],v[168:171],v[120:123],v[36:39]\n"
"s_add_u32 s60,s59,2\n"
"s_and_b32 s60,s60,15\n"
"s_mul_i32 s60,s60,0xc00\n"
"v_add_u32 v140,s60,v242\n"
"ds_read_b128 v[108:111],v140\n"
"v_mfma_f32_16x16x32_bf16 v[40:43],v[168:171],v[124:127],v[40:43]\n"
"v_mfma_f32_16x16x32_bf16 v[44:47],v[168:171],v[128:131],v[44:47]\n"
"ds_read_b128 v[112:115],v140 offset:1024\n"
"v_mfma_f32_16x16x32_bf16 v[48:51],v[172:175],v[120:123],v[48:51]\n"
"ds_read_b128 v[116:119],v140 offset:2048\n"
"v_mfma_f32_16x16x32_bf16 v[52:55],v[172:175],v[124:127],v[52:55]\n"
"s_waitcnt vmcnt(16)\n"
"ds_read_b64_tr_b16 v[188:189],v132\n"
"ds_read_b64_tr_b16 v[190:191],v132 offset:4096\n"
"v_mfma_f32_16x16x32_bf16 v[56:59],v[172:175],v[128:131],v[56:59]\n"
"ds_read_b64_tr_b16 v[192:193],v133\n"
"v_mfma_f32_16x16x32_bf16 v[60:63],v[176:179],v[120:123],v[60:63]\n"
"ds_read_b64_tr_b16 v[194:195],v133 offset:4096\n"
"ds_read_b64_tr_b16 v[196:197],v134\n"
"v_mfma_f32_16x16x32_bf16 v[64:67],v[176:179],v[124:127],v[64:67]\n"
"ds_read_b64_tr_b16 v[198:199],v134 offset:4096\n"
"v_mfma_f32_16x16x32_bf16 v[68:71],v[176:179],v[128:131],v[68:71]\n"
"ds_read_b64_tr_b16 v[200:201],v135\n"
"v_mfma_f32_16x16x32_bf16 v[72:75],v[180:183],v[120:123],v[72:75]\n"
"ds_read_b64_tr_b16 v[202:203],v135 offset:4096\n"
"ds_read_b64_tr_b16 v[204:205],v136\n"
"v_mfma_f32_16x16x32_bf16 v[76:79],v[180:183],v[124:127],v[76:79]\n"
"ds_read_b64_tr_b16 v[206:207],v136 offset:4096\n"
"v_mfma_f32_16x16x32_bf16 v[80:83],v[180:183],v[128:131],v[80:83]\n"
"ds_read_b64_tr_b16 v[208:209],v137\n"
"ds_read_b64_tr_b16 v[210:211],v137 offset:4096\n"
"v_mfma_f32_16x16x32_bf16 v[84:87],v[184:187],v[120:123],v[84:87]\n"
"s_waitcnt lgkmcnt(13)\n"
"ds_read_b64_tr_b16 v[212:213],v138\n"
"v_mfma_f32_16x16x32_bf16 v[88:91],v[184:187],v[124:127],v[88:91]\n"
"ds_read_b64_tr_b16 v[214:215],v138 offset:4096\n"
"s_waitcnt lgkmcnt(13)\n"
"ds_read_b64_tr_b16 v[216:217],v139\n"
"v_mfma_f32_16x16x32_bf16 v[92:95],v[184:187],v[128:131],v[92:95]\n"
"ds_read_b64_tr_b16 v[218:219],v139 offset:4096\n"
"s_waitcnt lgkmcnt(13)\n"
"v_mfma_f32_16x16x32_bf16 v[0:3],v[188:191],v[108:111],v[0:3]\n"
"s_waitcnt lgkmcnt(0)\n"
"v_mfma_f32_16x16x32_bf16 v[4:7],v[188:191],v[112:115],v[4:7]\n"
"s_add_u32 s67,s59,5\n"
"s_and_b32 s67,s67,15\n"
"s_lshl_b32 s67,s67,15\n"
"s_add_u32 m0,s2,0xc000\n"
"s_nop 0\n"
"buffer_load_dwordx4 v238,s[40:43],s67 offen lds\n"
"v_mfma_f32_16x16x32_bf16 v[8:11],v[188:191],v[116:119],v[8:11]\n"
"v_mfma_f32_16x16x32_bf16 v[12:15],v[192:195],v[108:111],v[12:15]\n"
"buffer_load_dwordx4 v239,s[40:43],s67 offen offset:1024 lds\n"
"buffer_load_dwordx4 v240,s[40:43],s67 offen offset:2048 lds\n"
"v_mfma_f32_16x16x32_bf16 v[16:19],v[192:195],v[112:115],v[16:19]\n"
"buffer_load_dwordx4 v241,s[40:43],s67 offen offset:3072 lds\n"
"v_mfma_f32_16x16x32_bf16 v[20:23],v[192:195],v[116:119],v[20:23]\n"
"s_add_u32 s67,s67,0x4000\n"
"s_add_u32 m0,s2,0xd000\n"
"s_nop 0\n"
"buffer_load_dwordx4 v238,s[40:43],s67 offen lds\n"
"v_mfma_f32_16x16x32_bf16 v[24:27],v[196:199],v[108:111],v[24:27]\n"
"buffer_load_dwordx4 v239,s[40:43],s67 offen offset:1024 lds\n"
"v_mfma_f32_16x16x32_bf16 v[28:31],v[196:199],v[112:115],v[28:31]\n"
"buffer_load_dwordx4 v240,s[40:43],s67 offen offset:2048 lds\n"
"v_mfma_f32_16x16x32_bf16 v[32:35],v[196:199],v[116:119],v[32:35]\n"
"buffer_load_dwordx4 v241,s[40:43],s67 offen offset:3072 lds\n"
"v_mfma_f32_16x16x32_bf16 v[36:39],v[200:203],v[108:111],v[36:39]\n"
"s_add_u32 s60,s59,3\n"
"s_and_b32 s60,s60,15\n"
"s_mul_i32 s60,s60,0xc00\n"
"v_add_u32 v140,s60,v242\n"
"ds_read_b128 v[120:123],v140\n"
"v_mfma_f32_16x16x32_bf16 v[40:43],v[200:203],v[112:115],v[40:43]\n"
"v_mfma_f32_16x16x32_bf16 v[44:47],v[200:203],v[116:119],v[44:47]\n"
"ds_read_b128 v[124:127],v140 offset:1024\n"
"v_mfma_f32_16x16x32_bf16 v[48:51],v[204:207],v[108:111],v[48:51]\n"
"ds_read_b128 v[128:131],v140 offset:2048\n"
"v_mfma_f32_16x16x32_bf16 v[52:55],v[204:207],v[112:115],v[52:55]\n"
"s_waitcnt vmcnt(16)\n"
"ds_read_b64_tr_b16 v[156:157],v224\n"
"ds_read_b64_tr_b16 v[158:159],v224 offset:4096\n"
"v_mfma_f32_16x16x32_bf16 v[56:59],v[204:207],v[116:119],v[56:59]\n"
"ds_read_b64_tr_b16 v[160:161],v225\n"
"v_mfma_f32_16x16x32_bf16 v[60:63],v[208:211],v[108:111],v[60:63]\n"
"ds_read_b64_tr_b16 v[162:163],v225 offset:4096\n"
"ds_read_b64_tr_b16 v[164:165],v226\n"
"v_mfma_f32_16x16x32_bf16 v[64:67],v[208:211],v[112:115],v[64:67]\n"
"ds_read_b64_tr_b16 v[166:167],v226 offset:4096\n"
"v_mfma_f32_16x16x32_bf16 v[68:71],v[208:211],v[116:119],v[68:71]\n"
"ds_read_b64_tr_b16 v[168:169],v227\n"
"v_mfma_f32_16x16x32_bf16 v[72:75],v[212:215],v[108:111],v[72:75]\n"
"ds_read_b64_tr_b16 v[170:171],v227 offset:4096\n"
"ds_read_b64_tr_b16 v[172:173],v228\n"
"v_mfma_f32_16x16x32_bf16 v[76:79],v[212:215],v[112:115],v[76:79]\n"
"ds_read_b64_tr_b16 v[174:175],v228 offset:4096\n"
"v_mfma_f32_16x16x32_bf16 v[80:83],v[212:215],v[116:119],v[80:83]\n"
"ds_read_b64_tr_b16 v[176:177],v229\n"
"ds_read_b64_tr_b16 v[178:179],v229 offset:4096\n"
"v_mfma_f32_16x16x32_bf16 v[84:87],v[216:219],v[108:111],v[84:87]\n"
"s_waitcnt lgkmcnt(13)\n"
"ds_read_b64_tr_b16 v[180:181],v230\n"
"v_mfma_f32_16x16x32_bf16 v[88:91],v[216:219],v[112:115],v[88:91]\n"
"ds_read_b64_tr_b16 v[182:183],v230 offset:4096\n"
"s_waitcnt lgkmcnt(13)\n"
"ds_read_b64_tr_b16 v[184:185],v231\n"
"v_mfma_f32_16x16x32_bf16 v[92:95],v[216:219],v[116:119],v[92:95]\n"
"ds_read_b64_tr_b16 v[186:187],v231 offset:4096\n"
"s_waitcnt lgkmcnt(13)\n"
"v_mfma_f32_16x16x32_bf16 v[0:3],v[156:159],v[120:123],v[0:3]\n"
"s_waitcnt lgkmcnt(0)\n"
"v_mfma_f32_16x16x32_bf16 v[4:7],v[156:159],v[124:127],v[4:7]\n"
"s_add_u32 s67,s59,6\n"
"s_and_b32 s67,s67,15\n"
"s_lshl_b32 s67,s67,15\n"
"s_add_u32 m0,s3,0x18000\n"
"s_nop 0\n"
"buffer_load_dwordx4 v238,s[40:43],s67 offen lds\n"
"v_mfma_f32_16x16x32_bf16 v[8:11],v[156:159],v[128:131],v[8:11]\n"
"v_mfma_f32_16x16x32_bf16 v[12:15],v[160:163],v[120:123],v[12:15]\n"
"buffer_load_dwordx4 v239,s[40:43],s67 offen offset:1024 lds\n"
"buffer_load_dwordx4 v240,s[40:43],s67 offen offset:2048 lds\n"
"v_mfma_f32_16x16x32_bf16 v[16:19],v[160:163],v[124:127],v[16:19]\n"
"buffer_load_dwordx4 v241,s[40:43],s67 offen offset:3072 lds\n"
"v_mfma_f32_16x16x32_bf16 v[20:23],v[160:163],v[128:131],v[20:23]\n"
"s_add_u32 s67,s67,0x4000\n"
"s_add_u32 m0,s3,0x19000\n"
"s_nop 0\n"
"buffer_load_dwordx4 v238,s[40:43],s67 offen lds\n"
"v_mfma_f32_16x16x32_bf16 v[24:27],v[164:167],v[120:123],v[24:27]\n"
"buffer_load_dwordx4 v239,s[40:43],s67 offen offset:1024 lds\n"
"v_mfma_f32_16x16x32_bf16 v[28:31],v[164:167],v[124:127],v[28:31]\n"
"buffer_load_dwordx4 v240,s[40:43],s67 offen offset:2048 lds\n"
"v_mfma_f32_16x16x32_bf16 v[32:35],v[164:167],v[128:131],v[32:35]\n"
"buffer_load_dwordx4 v241,s[40:43],s67 offen offset:3072 lds\n"
"v_mfma_f32_16x16x32_bf16 v[36:39],v[168:171],v[120:123],v[36:39]\n"
"s_add_u32 s60,s59,4\n"
"s_and_b32 s60,s60,15\n"
"s_mul_i32 s60,s60,0xc00\n"
"v_add_u32 v140,s60,v242\n"
"ds_read_b128 v[108:111],v140\n"
"v_mfma_f32_16x16x32_bf16 v[40:43],v[168:171],v[124:127],v[40:43]\n"
"v_mfma_f32_16x16x32_bf16 v[44:47],v[168:171],v[128:131],v[44:47]\n"
"ds_read_b128 v[112:115],v140 offset:1024\n"
"v_mfma_f32_16x16x32_bf16 v[48:51],v[172:175],v[120:123],v[48:51]\n"
"ds_read_b128 v[116:119],v140 offset:2048\n"
"v_mfma_f32_16x16x32_bf16 v[52:55],v[172:175],v[124:127],v[52:55]\n"
"s_waitcnt vmcnt(16)\n"
"ds_read_b64_tr_b16 v[188:189],v224 offset:8192\n"
"ds_read_b64_tr_b16 v[190:191],v224 offset:12288\n"
"v_mfma_f32_16x16x32_bf16 v[56:59],v[172:175],v[128:131],v[56:59]\n"
"ds_read_b64_tr_b16 v[192:193],v225 offset:8192\n"
"v_mfma_f32_16x16x32_bf16 v[60:63],v[176:179],v[120:123],v[60:63]\n"
"ds_read_b64_tr_b16 v[194:195],v225 offset:12288\n"
"ds_read_b64_tr_b16 v[196:197],v226 offset:8192\n"
"v_mfma_f32_16x16x32_bf16 v[64:67],v[176:179],v[124:127],v[64:67]\n"
"ds_read_b64_tr_b16 v[198:199],v226 offset:12288\n"
"v_mfma_f32_16x16x32_bf16 v[68:71],v[176:179],v[128:131],v[68:71]\n"
"ds_read_b64_tr_b16 v[200:201],v227 offset:8192\n"
"v_mfma_f32_16x16x32_bf16 v[72:75],v[180:183],v[120:123],v[72:75]\n"
"ds_read_b64_tr_b16 v[202:203],v227 offset:12288\n"
"ds_read_b64_tr_b16 v[204:205],v228 offset:8192\n"
"v_mfma_f32_16x16x32_bf16 v[76:79],v[180:183],v[124:127],v[76:79]\n"
"ds_read_b64_tr_b16 v[206:207],v228 offset:12288\n"
"v_mfma_f32_16x16x32_bf16 v[80:83],v[180:183],v[128:131],v[80:83]\n"
"ds_read_b64_tr_b16 v[208:209],v229 offset:8192\n"
"ds_read_b64_tr_b16 v[210:211],v229 offset:12288\n"
"v_mfma_f32_16x16x32_bf16 v[84:87],v[184:187],v[120:123],v[84:87]\n"
"s_waitcnt lgkmcnt(13)\n"
"ds_read_b64_tr_b16 v[212:213],v230 offset:8192\n"
"v_mfma_f32_16x16x32_bf16 v[88:91],v[184:187],v[124:127],v[88:91]\n"
"ds_read_b64_tr_b16 v[214:215],v230 offset:12288\n"
"s_waitcnt lgkmcnt(13)\n"
"ds_read_b64_tr_b16 v[216:217],v231 offset:8192\n"
"v_mfma_f32_16x16x32_bf16 v[92:95],v[184:187],v[128:131],v[92:95]\n"
"ds_read_b64_tr_b16 v[218:219],v231 offset:12288\n"
"s_waitcnt lgkmcnt(13)\n"
"v_mfma_f32_16x16x32_bf16 v[0:3],v[188:191],v[108:111],v[0:3]\n"
"s_waitcnt lgkmcnt(0)\n"
"v_mfma_f32_16x16x32_bf16 v[4:7],v[188:191],v[112:115],v[4:7]\n"
"s_add_u32 s67,s59,7\n"
"s_and_b32 s67,s67,15\n"
"s_lshl_b32 s67,s67,15\n"
"s_add_u32 m0,s3,0x1a000\n"
"s_nop 0\n"
"buffer_load_dwordx4 v238,s[40:43],s67 offen lds\n"
"v_mfma_f32_16x16x32_bf16 v[8:11],v[188:191],v[116:119],v[8:11]\n"
"v_mfma_f32_16x16x32_bf16 v[12:15],v[192:195],v[108:111],v[12:15]\n"
"buffer_load_dwordx4 v239,s[40:43],s67 offen offset:1024 lds\n"
"buffer_load_dwordx4 v240,s[40:43],s67 offen offset:2048 lds\n"
"v_mfma_f32_16x16x32_bf16 v[16:19],v[192:195],v[112:115],v[16:19]\n"
"buffer_load_dwordx4 v241,s[40:43],s67 offen offset:3072 lds\n"
"v_mfma_f32_16x16x32_bf16 v[20:23],v[192:195],v[116:119],v[20:23]\n"
"s_add_u32 s67,s67,0x4000\n"
"s_add_u32 m0,s3,0x1b000\n"
"s_nop 0\n"
"buffer_load_dwordx4 v238,s[40:43],s67 offen lds\n"
"v_mfma_f32_16x16x32_bf16 v[24:27],v[196:199],v[108:111],v[24:27]\n"
"buffer_load_dwordx4 v239,s[40:43],s67 offen offset:1024 lds\n"
"v_mfma_f32_16x16x32_bf16 v[28:31],v[196:199],v[112:115],v[28:31]\n"
"buffer_load_dwordx4 v240,s[40:43],s67 offen offset:2048 lds\n"
"v_mfma_f32_16x16x32_bf16 v[32:35],v[196:199],v[116:119],v[32:35]\n"
"buffer_load_dwordx4 v241,s[40:43],s67 offen offset:3072 lds\n"
"v_mfma_f32_16x16x32_bf16 v[36:39],v[200:203],v[108:111],v[36:39]\n"
"s_add_u32 s60,s59,5\n"
"s_and_b32 s60,s60,15\n"
"s_mul_i32 s60,s60,0xc00\n"
"v_add_u32 v140,s60,v242\n"
"ds_read_b128 v[120:123],v140\n"
"v_mfma_f32_16x16x32_bf16 v[40:43],v[200:203],v[112:115],v[40:43]\n"
"v_mfma_f32_16x16x32_bf16 v[44:47],v[200:203],v[116:119],v[44:47]\n"
"ds_read_b128 v[124:127],v140 offset:1024\n"
"v_mfma_f32_16x16x32_bf16 v[48:51],v[204:207],v[108:111],v[48:51]\n"
"ds_read_b128 v[128:131],v140 offset:2048\n"
"v_mfma_f32_16x16x32_bf16 v[52:55],v[204:207],v[112:115],v[52:55]\n"
"s_waitcnt vmcnt(16)\n"
"ds_read_b64_tr_b16 v[156:157],v132\n"
"ds_read_b64_tr_b16 v[158:159],v132 offset:4096\n"
"v_mfma_f32_16x16x32_bf16 v[56:59],v[204:207],v[116:119],v[56:59]\n"
"ds_read_b64_tr_b16 v[160:161],v133\n"
"v_mfma_f32_16x16x32_bf16 v[60:63],v[208:211],v[108:111],v[60:63]\n"
"ds_read_b64_tr_b16 v[162:163],v133 offset:4096\n"
"ds_read_b64_tr_b16 v[164:165],v134\n"
"v_mfma_f32_16x16x32_bf16 v[64:67],v[208:211],v[112:115],v[64:67]\n"
"ds_read_b64_tr_b16 v[166:167],v134 offset:4096\n"
"v_mfma_f32_16x16x32_bf16 v[68:71],v[208:211],v[116:119],v[68:71]\n"
"ds_read_b64_tr_b16 v[168:169],v135\n"
"v_mfma_f32_16x16x32_bf16 v[72:75],v[212:215],v[108:111],v[72:75]\n"
"ds_read_b64_tr_b16 v[170:171],v135 offset:4096\n"
"ds_read_b64_tr_b16 v[172:173],v136\n"
"v_mfma_f32_16x16x32_bf16 v[76:79],v[212:215],v[112:115],v[76:79]\n"
"ds_read_b64_tr_b16 v[174:175],v136 offset:4096\n"
"v_mfma_f32_16x16x32_bf16 v[80:83],v[212:215],v[116:119],v[80:83]\n"
"ds_read_b64_tr_b16 v[176:177],v137\n"
"ds_read_b64_tr_b16 v[178:179],v137 offset:4096\n"
"v_mfma_f32_16x16x32_bf16 v[84:87],v[216:219],v[108:111],v[84:87]\n"
"s_waitcnt lgkmcnt(13)\n"
"ds_read_b64_tr_b16 v[180:181],v138\n"
"v_mfma_f32_16x16x32_bf16 v[88:91],v[216:219],v[112:115],v[88:91]\n"
"ds_read_b64_tr_b16 v[182:183],v138 offset:4096\n"
"s_waitcnt lgkmcnt(13)\n"
"ds_read_b64_tr_b16 v[184:185],v139\n"
"v_mfma_f32_16x16x32_bf16 v[92:95],v[216:219],v[116:119],v[92:95]\n"
"ds_read_b64_tr_b16 v[186:187],v139 offset:4096\n"
"s_waitcnt lgkmcnt(13)\n"
"v_mfma_f32_16x16x32_bf16 v[0:3],v[156:159],v[120:123],v[0:3]\n"
"s_waitcnt lgkmcnt(0)\n"
"v_mfma_f32_16x16x32_bf16 v[4:7],v[156:159],v[124:127],v[4:7]\n"
"s_add_u32 s67,s59,8\n"
"s_and_b32 s67,s67,15\n"
"s_lshl_b32 s67,s67,15\n"
"s_add_u32 m0,s2,0xc000\n"
"s_nop 0\n"
"buffer_load_dwordx4 v238,s[40:43],s67 offen lds\n"
"v_mfma_f32_16x16x32_bf16 v[8:11],v[156:159],v[128:131],v[8:11]\n"
"v_mfma_f32_16x16x32_bf16 v[12:15],v[160:163],v[120:123],v[12:15]\n"
"buffer_load_dwordx4 v239,s[40:43],s67 offen offset:1024 lds\n"
"buffer_load_dwordx4 v240,s[40:43],s67 offen offset:2048 lds\n"
"v_mfma_f32_16x16x32_bf16 v[16:19],v[160:163],v[124:127],v[16:19]\n"
"buffer_load_dwordx4 v241,s[40:43],s67 offen offset:3072 lds\n"
"v_mfma_f32_16x16x32_bf16 v[20:23],v[160:163],v[128:131],v[20:23]\n"
"s_add_u32 s67,s67,0x4000\n"
"s_add_u32 m0,s2,0xd000\n"
"s_nop 0\n"
"buffer_load_dwordx4 v238,s[40:43],s67 offen lds\n"
"v_mfma_f32_16x16x32_bf16 v[24:27],v[164:167],v[120:123],v[24:27]\n"
"buffer_load_dwordx4 v239,s[40:43],s67 offen offset:1024 lds\n"
"v_mfma_f32_16x16x32_bf16 v[28:31],v[164:167],v[124:127],v[28:31]\n"
"buffer_load_dwordx4 v240,s[40:43],s67 offen offset:2048 lds\n"
"v_mfma_f32_16x16x32_bf16 v[32:35],v[164:167],v[128:131],v[32:35]\n"
"buffer_load_dwordx4 v241,s[40:43],s67 offen offset:3072 lds\n"
"v_mfma_f32_16x16x32_bf16 v[36:39],v[168:171],v[120:123],v[36:39]\n"
"s_add_u32 s60,s59,6\n"
"s_and_b32 s60,s60,15\n"
"s_mul_i32 s60,s60,0xc00\n"
"v_add_u32 v140,s60,v242\n"
"ds_read_b128 v[108:111],v140\n"
"v_mfma_f32_16x16x32_bf16 v[40:43],v[168:171],v[124:127],v[40:43]\n"
"v_mfma_f32_16x16x32_bf16 v[44:47],v[168:171],v[128:131],v[44:47]\n"
"ds_read_b128 v[112:115],v140 offset:1024\n"
"v_mfma_f32_16x16x32_bf16 v[48:51],v[172:175],v[120:123],v[48:51]\n"
"ds_read_b128 v[116:119],v140 offset:2048\n"
"v_mfma_f32_16x16x32_bf16 v[52:55],v[172:175],v[124:127],v[52:55]\n"
"s_waitcnt vmcnt(16)\n"
"ds_read_b64_tr_b16 v[188:189],v224\n"
"ds_read_b64_tr_b16 v[190:191],v224 offset:4096\n"
"v_mfma_f32_16x16x32_bf16 v[56:59],v[172:175],v[128:131],v[56:59]\n"
"ds_read_b64_tr_b16 v[192:193],v225\n"
"v_mfma_f32_16x16x32_bf16 v[60:63],v[176:179],v[120:123],v[60:63]\n"
"ds_read_b64_tr_b16 v[194:195],v225 offset:4096\n"
"ds_read_b64_tr_b16 v[196:197],v226\n"
"v_mfma_f32_16x16x32_bf16 v[64:67],v[176:179],v[124:127],v[64:67]\n"
"ds_read_b64_tr_b16 v[198:199],v226 offset:4096\n"
"v_mfma_f32_16x16x32_bf16 v[68:71],v[176:179],v[128:131],v[68:71]\n"
"ds_read_b64_tr_b16 v[200:201],v227\n"
"v_mfma_f32_16x16x32_bf16 v[72:75],v[180:183],v[120:123],v[72:75]\n"
"ds_read_b64_tr_b16 v[202:203],v227 offset:4096\n"
"ds_read_b64_tr_b16 v[204:205],v228\n"
"v_mfma_f32_16x16x32_bf16 v[76:79],v[180:183],v[124:127],v[76:79]\n"
"ds_read_b64_tr_b16 v[206:207],v228 offset:4096\n"
"v_mfma_f32_16x16x32_bf16 v[80:83],v[180:183],v[128:131],v[80:83]\n"
"ds_read_b64_tr_b16 v[208:209],v229\n"
"ds_read_b64_tr_b16 v[210:211],v229 offset:4096\n"
"v_mfma_f32_16x16x32_bf16 v[84:87],v[184:187],v[120:123],v[84:87]\n"
"s_waitcnt lgkmcnt(13)\n"
"ds_read_b64_tr_b16 v[212:213],v230\n"
"v_mfma_f32_16x16x32_bf16 v[88:91],v[184:187],v[124:127],v[88:91]\n"
"ds_read_b64_tr_b16 v[214:215],v230 offset:4096\n"
"s_waitcnt lgkmcnt(13)\n"
"ds_read_b64_tr_b16 v[216:217],v231\n"
"v_mfma_f32_16x16x32_bf16 v[92:95],v[184:187],v[128:131],v[92:95]\n"
"ds_read_b64_tr_b16 v[218:219],v231 offset:4096\n"
"s_waitcnt lgkmcnt(13)\n"
"v_mfma_f32_16x16x32_bf16 v[0:3],v[188:191],v[108:111],v[0:3]\n"
"s_waitcnt lgkmcnt(0)\n"
"v_mfma_f32_16x16x32_bf16 v[4:7],v[188:191],v[112:115],v[4:7]\n"
"s_add_u32 s67,s59,9\n"
"s_and_b32 s67,s67,15\n"
"s_lshl_b32 s67,s67,15\n"
"s_add_u32 m0,s3,0x18000\n"
"s_nop 0\n"
"buffer_load_dwordx4 v238,s[40:43],s67 offen lds\n"
"v_mfma_f32_16x16x32_bf16 v[8:11],v[188:191],v[116:119],v[8:11]\n"
"v_mfma_f32_16x16x32_bf16 v[12:15],v[192:195],v[108:111],v[12:15]\n"
"buffer_load_dwordx4 v239,s[40:43],s67 offen offset:1024 lds\n"
"buffer_load_dwordx4 v240,s[40:43],s67 offen offset:2048 lds\n"
"v_mfma_f32_16x16x32_bf16 v[16:19],v[192:195],v[112:115],v[16:19]\n"
"buffer_load_dwordx4 v241,s[40:43],s67 offen offset:3072 lds\n"
"v_mfma_f32_16x16x32_bf16 v[20:23],v[192:195],v[116:119],v[20:23]\n"
"s_add_u32 s67,s67,0x4000\n"
"s_add_u32 m0,s3,0x19000\n"
"s_nop 0\n"
"buffer_load_dwordx4 v238,s[40:43],s67 offen lds\n"
"v_mfma_f32_16x16x32_bf16 v[24:27],v[196:199],v[108:111],v[24:27]\n"
"buffer_load_dwordx4 v239,s[40:43],s67 offen offset:1024 lds\n"
"v_mfma_f32_16x16x32_bf16 v[28:31],v[196:199],v[112:115],v[28:31]\n"
"buffer_load_dwordx4 v240,s[40:43],s67 offen offset:2048 lds\n"
"v_mfma_f32_16x16x32_bf16 v[32:35],v[196:199],v[116:119],v[32:35]\n"
"buffer_load_dwordx4 v241,s[40:43],s67 offen offset:3072 lds\n"
"v_mfma_f32_16x16x32_bf16 v[36:39],v[200:203],v[108:111],v[36:39]\n"
"s_add_u32 s60,s59,7\n"
"s_and_b32 s60,s60,15\n"
"s_mul_i32 s60,s60,0xc00\n"
"v_add_u32 v140,s60,v242\n"
"ds_read_b128 v[120:123],v140\n"
"v_mfma_f32_16x16x32_bf16 v[40:43],v[200:203],v[112:115],v[40:43]\n"
"v_mfma_f32_16x16x32_bf16 v[44:47],v[200:203],v[116:119],v[44:47]\n"
"ds_read_b128 v[124:127],v140 offset:1024\n"
"v_mfma_f32_16x16x32_bf16 v[48:51],v[204:207],v[108:111],v[48:51]\n"
"ds_read_b128 v[128:131],v140 offset:2048\n"
"v_mfma_f32_16x16x32_bf16 v[52:55],v[204:207],v[112:115],v[52:55]\n"
"s_waitcnt vmcnt(16)\n"
"ds_read_b64_tr_b16 v[156:157],v224 offset:8192\n"
"ds_read_b64_tr_b16 v[158:159],v224 offset:12288\n"
"v_mfma_f32_16x16x32_bf16 v[56:59],v[204:207],v[116:119],v[56:59]\n"
"ds_read_b64_tr_b16 v[160:161],v225 offset:8192\n"
"v_mfma_f32_16x16x32_bf16 v[60:63],v[208:211],v[108:111],v[60:63]\n"
"ds_read_b64_tr_b16 v[162:163],v225 offset:12288\n"
"ds_read_b64_tr_b16 v[164:165],v226 offset:8192\n"
"v_mfma_f32_16x16x32_bf16 v[64:67],v[208:211],v[112:115],v[64:67]\n"
"ds_read_b64_tr_b16 v[166:167],v226 offset:12288\n"
"v_mfma_f32_16x16x32_bf16 v[68:71],v[208:211],v[116:119],v[68:71]\n"
"ds_read_b64_tr_b16 v[168:169],v227 offset:8192\n"
"v_mfma_f32_16x16x32_bf16 v[72:75],v[212:215],v[108:111],v[72:75]\n"
"ds_read_b64_tr_b16 v[170:171],v227 offset:12288\n"
"ds_read_b64_tr_b16 v[172:173],v228 offset:8192\n"
"v_mfma_f32_16x16x32_bf16 v[76:79],v[212:215],v[112:115],v[76:79]\n"
"ds_read_b64_tr_b16 v[174:175],v228 offset:12288\n"
"v_mfma_f32_16x16x32_bf16 v[80:83],v[212:215],v[116:119],v[80:83]\n"
"ds_read_b64_tr_b16 v[176:177],v229 offset:8192\n"
"ds_read_b64_tr_b16 v[178:179],v229 offset:12288\n"
"v_mfma_f32_16x16x32_bf16 v[84:87],v[216:219],v[108:111],v[84:87]\n"
"s_waitcnt lgkmcnt(13)\n"
"ds_read_b64_tr_b16 v[180:181],v230 offset:8192\n"
"v_mfma_f32_16x16x32_bf16 v[88:91],v[216:219],v[112:115],v[88:91]\n"
"ds_read_b64_tr_b16 v[182:183],v230 offset:12288\n"
"s_waitcnt lgkmcnt(13)\n"
"ds_read_b64_tr_b16 v[184:185],v231 offset:8192\n"
"v_mfma_f32_16x16x32_bf16 v[92:95],v[216:219],v[116:119],v[92:95]\n"
"ds_read_b64_tr_b16 v[186:187],v231 offset:12288\n"
"s_waitcnt lgkmcnt(13)\n"
"v_mfma_f32_16x16x32_bf16 v[0:3],v[156:159],v[120:123],v[0:3]\n"
"s_waitcnt lgkmcnt(0)\n"
"v_mfma_f32_16x16x32_bf16 v[4:7],v[156:159],v[124:127],v[4:7]\n"
"s_add_u32 s67,s59,10\n"
"s_and_b32 s67,s67,15\n"
"s_lshl_b32 s67,s67,15\n"
"s_add_u32 m0,s3,0x1a000\n"
"s_nop 0\n"
"buffer_load_dwordx4 v238,s[40:43],s67 offen lds\n"
"v_mfma_f32_16x16x32_bf16 v[8:11],v[156:159],v[128:131],v[8:11]\n"
"v_mfma_f32_16x16x32_bf16 v[12:15],v[160:163],v[120:123],v[12:15]\n"
"buffer_load_dwordx4 v239,s[40:43],s67 offen offset:1024 lds\n"
"buffer_load_dwordx4 v240,s[40:43],s67 offen offset:2048 lds\n"
"v_mfma_f32_16x16x32_bf16 v[16:19],v[160:163],v[124:127],v[16:19]\n"
"buffer_load_dwordx4 v241,s[40:43],s67 offen offset:3072 lds\n"
"v_mfma_f32_16x16x32_bf16 v[20:23],v[160:163],v[128:131],v[20:23]\n"
"s_add_u32 s67,s67,0x4000\n"
"s_add_u32 m0,s3,0x1b000\n"
"s_nop 0\n"
"buffer_load_dwordx4 v238,s[40:43],s67 offen lds\n"
"v_mfma_f32_16x16x32_bf16 v[24:27],v[164:167],v[120:123],v[24:27]\n"
"buffer_load_dwordx4 v239,s[40:43],s67 offen offset:1024 lds\n"
"v_mfma_f32_16x16x32_bf16 v[28:31],v[164:167],v[124:127],v[28:31]\n"
"buffer_load_dwordx4 v240,s[40:43],s67 offen offset:2048 lds\n"
"v_mfma_f32_16x16x32_bf16 v[32:35],v[164:167],v[128:131],v[32:35]\n"
"buffer_load_dwordx4 v241,s[40:43],s67 offen offset:3072 lds\n"
"v_mfma_f32_16x16x32_bf16 v[36:39],v[168:171],v[120:123],v[36:39]\n"
"s_add_u32 s60,s59,8\n"
"s_and_b32 s60,s60,15\n"
"s_mul_i32 s60,s60,0xc00\n"
"v_add_u32 v140,s60,v242\n"
"ds_read_b128 v[108:111],v140\n"
"v_mfma_f32_16x16x32_bf16 v[40:43],v[168:171],v[124:127],v[40:43]\n"
"v_mfma_f32_16x16x32_bf16 v[44:47],v[168:171],v[128:131],v[44:47]\n"
"ds_read_b128 v[112:115],v140 offset:1024\n"
"v_mfma_f32_16x16x32_bf16 v[48:51],v[172:175],v[120:123],v[48:51]\n"
"ds_read_b128 v[116:119],v140 offset:2048\n"
"v_mfma_f32_16x16x32_bf16 v[52:55],v[172:175],v[124:127],v[52:55]\n"
"s_waitcnt vmcnt(16)\n"
"ds_read_b64_tr_b16 v[188:189],v132\n"
"ds_read_b64_tr_b16 v[190:191],v132 offset:4096\n"
"v_mfma_f32_16x16x32_bf16 v[56:59],v[172:175],v[128:131],v[56:59]\n"
"ds_read_b64_tr_b16 v[192:193],v133\n"
"v_mfma_f32_16x16x32_bf16 v[60:63],v[176:179],v[120:123],v[60:63]\n"
"ds_read_b64_tr_b16 v[194:195],v133 offset:4096\n"
"ds_read_b64_tr_b16 v[196:197],v134\n"
"v_mfma_f32_16x16x32_bf16 v[64:67],v[176:179],v[124:127],v[64:67]\n"
"ds_read_b64_tr_b16 v[198:199],v134 offset:4096\n"
"v_mfma_f32_16x16x32_bf16 v[68:71],v[176:179],v[128:131],v[68:71]\n"
"ds_read_b64_tr_b16 v[200:201],v135\n"
"v_mfma_f32_16x16x32_bf16 v[72:75],v[180:183],v[120:123],v[72:75]\n"
"ds_read_b64_tr_b16 v[202:203],v135 offset:4096\n"
"ds_read_b64_tr_b16 v[204:205],v136\n"
"v_mfma_f32_16x16x32_bf16 v[76:79],v[180:183],v[124:127],v[76:79]\n"
"ds_read_b64_tr_b16 v[206:207],v136 offset:4096\n"
"v_mfma_f32_16x16x32_bf16 v[80:83],v[180:183],v[128:131],v[80:83]\n"
"ds_read_b64_tr_b16 v[208:209],v137\n"
"ds_read_b64_tr_b16 v[210:211],v137 offset:4096\n"
"v_mfma_f32_16x16x32_bf16 v[84:87],v[184:187],v[120:123],v[84:87]\n"
"s_waitcnt lgkmcnt(13)\n"
"ds_read_b64_tr_b16 v[212:213],v138\n"
"v_mfma_f32_16x16x32_bf16 v[88:91],v[184:187],v[124:127],v[88:91]\n"
"ds_read_b64_tr_b16 v[214:215],v138 offset:4096\n"
"s_waitcnt lgkmcnt(13)\n"
"ds_read_b64_tr_b16 v[216:217],v139\n"
"v_mfma_f32_16x16x32_bf16 v[92:95],v[184:187],v[128:131],v[92:95]\n"
"ds_read_b64_tr_b16 v[218:219],v139 offset:4096\n"
"s_waitcnt lgkmcnt(13)\n"
"v_mfma_f32_16x16x32_bf16 v[0:3],v[188:191],v[108:111],v[0:3]\n"
"s_waitcnt lgkmcnt(0)\n"
"v_mfma_f32_16x16x32_bf16 v[4:7],v[188:191],v[112:115],v[4:7]\n"
"s_add_u32 s67,s59,11\n"
"s_and_b32 s67,s67,15\n"
"s_lshl_b32 s67,s67,15\n"
"s_add_u32 m0,s2,0xc000\n"
"s_nop 0\n"
"buffer_load_dwordx4 v238,s[40:43],s67 offen lds\n"
"v_mfma_f32_16x16x32_bf16 v[8:11],v[188:191],v[116:119],v[8:11]\n"
"v_mfma_f32_16x16x32_bf16 v[12:15],v[192:195],v[108:111],v[12:15]\n"
"buffer_load_dwordx4 v239,s[40:43],s67 offen offset:1024 lds\n"
"buffer_load_dwordx4 v240,s[40:43],s67 offen offset:2048 lds\n"
"v_mfma_f32_16x16x32_bf16 v[16:19],v[192:195],v[112:115],v[16:19]\n"
"buffer_load_dwordx4 v241,s[40:43],s67 offen offset:3072 lds\n"
"v_mfma_f32_16x16x32_bf16 v[20:23],v[192:195],v[116:119],v[20:23]\n"
"s_add_u32 s67,s67,0x4000\n"
"s_add_u32 m0,s2,0xd000\n"
"s_nop 0\n"
"buffer_load_dwordx4 v238,s[40:43],s67 offen lds\n"
"v_mfma_f32_16x16x32_bf16 v[24:27],v[196:199],v[108:111],v[24:27]\n"
"buffer_load_dwordx4 v239,s[40:43],s67 offen offset:1024 lds\n"
"v_mfma_f32_16x16x32_bf16 v[28:31],v[196:199],v[112:115],v[28:31]\n"
"buffer_load_dwordx4 v240,s[40:43],s67 offen offset:2048 lds\n"
"v_mfma_f32_16x16x32_bf16 v[32:35],v[196:199],v[116:119],v[32:35]\n"
"buffer_load_dwordx4 v241,s[40:43],s67 offen offset:3072 lds\n"
"v_mfma_f32_16x16x32_bf16 v[36:39],v[200:203],v[108:111],v[36:39]\n"
"s_add_u32 s60,s59,9\n"
"s_and_b32 s60,s60,15\n"
"s_mul_i32 s60,s60,0xc00\n"
"v_add_u32 v140,s60,v242\n"
"ds_read_b128 v[120:123],v140\n"
"v_mfma_f32_16x16x32_bf16 v[40:43],v[200:203],v[112:115],v[40:43]\n"
"v_mfma_f32_16x16x32_bf16 v[44:47],v[200:203],v[116:119],v[44:47]\n"
"ds_read_b128 v[124:127],v140 offset:1024\n"
"v_mfma_f32_16x16x32_bf16 v[48:51],v[204:207],v[108:111],v[48:51]\n"
"ds_read_b128 v[128:131],v140 offset:2048\n"
"v_mfma_f32_16x16x32_bf16 v[52:55],v[204:207],v[112:115],v[52:55]\n"
"s_waitcnt vmcnt(16)\n"
"ds_read_b64_tr_b16 v[156:157],v224\n"
"ds_read_b64_tr_b16 v[158:159],v224 offset:4096\n"
"v_mfma_f32_16x16x32_bf16 v[56:59],v[204:207],v[116:119],v[56:59]\n"
"ds_read_b64_tr_b16 v[160:161],v225\n"
"v_mfma_f32_16x16x32_bf16 v[60:63],v[208:211],v[108:111],v[60:63]\n"
"ds_read_b64_tr_b16 v[162:163],v225 offset:4096\n"
"ds_read_b64_tr_b16 v[164:165],v226\n"
"v_mfma_f32_16x16x32_bf16 v[64:67],v[208:211],v[112:115],v[64:67]\n"
"ds_read_b64_tr_b16 v[166:167],v226 offset:4096\n"
"v_mfma_f32_16x16x32_bf16 v[68:71],v[208:211],v[116:119],v[68:71]\n"
"ds_read_b64_tr_b16 v[168:169],v227\n"
"v_mfma_f32_16x16x32_bf16 v[72:75],v[212:215],v[108:111],v[72:75]\n"
"ds_read_b64_tr_b16 v[170:171],v227 offset:4096\n"
"ds_read_b64_tr_b16 v[172:173],v228\n"
"v_mfma_f32_16x16x32_bf16 v[76:79],v[212:215],v[112:115],v[76:79]\n"
"ds_read_b64_tr_b16 v[174:175],v228 offset:4096\n"
"v_mfma_f32_16x16x32_bf16 v[80:83],v[212:215],v[116:119],v[80:83]\n"
"ds_read_b64_tr_b16 v[176:177],v229\n"
"ds_read_b64_tr_b16 v[178:179],v229 offset:4096\n"
"v_mfma_f32_16x16x32_bf16 v[84:87],v[216:219],v[108:111],v[84:87]\n"
"s_waitcnt lgkmcnt(13)\n"
"ds_read_b64_tr_b16 v[180:181],v230\n"
"v_mfma_f32_16x16x32_bf16 v[88:91],v[216:219],v[112:115],v[88:91]\n"
"ds_read_b64_tr_b16 v[182:183],v230 offset:4096\n"
"s_waitcnt lgkmcnt(13)\n"
"ds_read_b64_tr_b16 v[184:185],v231\n"
"v_mfma_f32_16x16x32_bf16 v[92:95],v[216:219],v[116:119],v[92:95]\n"
"ds_read_b64_tr_b16 v[186:187],v231 offset:4096\n"
"s_waitcnt lgkmcnt(13)\n"
"v_mfma_f32_16x16x32_bf16 v[0:3],v[156:159],v[120:123],v[0:3]\n"
"s_waitcnt lgkmcnt(0)\n"
"v_mfma_f32_16x16x32_bf16 v[4:7],v[156:159],v[124:127],v[4:7]\n"
"s_add_u32 s67,s59,12\n"
"s_and_b32 s67,s67,15\n"
"s_lshl_b32 s67,s67,15\n"
"s_add_u32 m0,s3,0x18000\n"
"s_nop 0\n"
"buffer_load_dwordx4 v238,s[40:43],s67 offen lds\n"
"v_mfma_f32_16x16x32_bf16 v[8:11],v[156:159],v[128:131],v[8:11]\n"
"v_mfma_f32_16x16x32_bf16 v[12:15],v[160:163],v[120:123],v[12:15]\n"
"buffer_load_dwordx4 v239,s[40:43],s67 offen offset:1024 lds\n"
"buffer_load_dwordx4 v240,s[40:43],s67 offen offset:2048 lds\n"
"v_mfma_f32_16x16x32_bf16 v[16:19],v[160:163],v[124:127],v[16:19]\n"
"buffer_load_dwordx4 v241,s[40:43],s67 offen offset:3072 lds\n"
"v_mfma_f32_16x16x32_bf16 v[20:23],v[160:163],v[128:131],v[20:23]\n"
"s_add_u32 s67,s67,0x4000\n"
"s_add_u32 m0,s3,0x19000\n"
"s_nop 0\n"
"buffer_load_dwordx4 v238,s[40:43],s67 offen lds\n"
"v_mfma_f32_16x16x32_bf16 v[24:27],v[164:167],v[120:123],v[24:27]\n"
"buffer_load_dwordx4 v239,s[40:43],s67 offen offset:1024 lds\n"
"v_mfma_f32_16x16x32_bf16 v[28:31],v[164:167],v[124:127],v[28:31]\n"
"buffer_load_dwordx4 v240,s[40:43],s67 offen offset:2048 lds\n"
"v_mfma_f32_16x16x32_bf16 v[32:35],v[164:167],v[128:131],v[32:35]\n"
"buffer_load_dwordx4 v241,s[40:43],s67 offen offset:3072 lds\n"
"v_mfma_f32_16x16x32_bf16 v[36:39],v[168:171],v[120:123],v[36:39]\n"
"s_add_u32 s60,s59,10\n"
"s_and_b32 s60,s60,15\n"
"s_mul_i32 s60,s60,0xc00\n"
"v_add_u32 v140,s60,v242\n"
"ds_read_b128 v[108:111],v140\n"
"v_mfma_f32_16x16x32_bf16 v[40:43],v[168:171],v[124:127],v[40:43]\n"
"v_mfma_f32_16x16x32_bf16 v[44:47],v[168:171],v[128:131],v[44:47]\n"
"ds_read_b128 v[112:115],v140 offset:1024\n"
"v_mfma_f32_16x16x32_bf16 v[48:51],v[172:175],v[120:123],v[48:51]\n"
"ds_read_b128 v[116:119],v140 offset:2048\n"
"v_mfma_f32_16x16x32_bf16 v[52:55],v[172:175],v[124:127],v[52:55]\n"
"s_waitcnt vmcnt(16)\n"
"ds_read_b64_tr_b16 v[188:189],v224 offset:8192\n"
"ds_read_b64_tr_b16 v[190:191],v224 offset:12288\n"
"v_mfma_f32_16x16x32_bf16 v[56:59],v[172:175],v[128:131],v[56:59]\n"
"ds_read_b64_tr_b16 v[192:193],v225 offset:8192\n"
"v_mfma_f32_16x16x32_bf16 v[60:63],v[176:179],v[120:123],v[60:63]\n"
"ds_read_b64_tr_b16 v[194:195],v225 offset:12288\n"
"ds_read_b64_tr_b16 v[196:197],v226 offset:8192\n"
"v_mfma_f32_16x16x32_bf16 v[64:67],v[176:179],v[124:127],v[64:67]\n"
"ds_read_b64_tr_b16 v[198:199],v226 offset:12288\n"
"v_mfma_f32_16x16x32_bf16 v[68:71],v[176:179],v[128:131],v[68:71]\n"
"ds_read_b64_tr_b16 v[200:201],v227 offset:8192\n"
"v_mfma_f32_16x16x32_bf16 v[72:75],v[180:183],v[120:123],v[72:75]\n"
"ds_read_b64_tr_b16 v[202:203],v227 offset:12288\n"
"ds_read_b64_tr_b16 v[204:205],v228 offset:8192\n"
"v_mfma_f32_16x16x32_bf16 v[76:79],v[180:183],v[124:127],v[76:79]\n"
"ds_read_b64_tr_b16 v[206:207],v228 offset:12288\n"
"v_mfma_f32_16x16x32_bf16 v[80:83],v[180:183],v[128:131],v[80:83]\n"
"ds_read_b64_tr_b16 v[208:209],v229 offset:8192\n"
"ds_read_b64_tr_b16 v[210:211],v229 offset:12288\n"
"v_mfma_f32_16x16x32_bf16 v[84:87],v[184:187],v[120:123],v[84:87]\n"
"s_waitcnt lgkmcnt(13)\n"
"ds_read_b64_tr_b16 v[212:213],v230 offset:8192\n"
"v_mfma_f32_16x16x32_bf16 v[88:91],v[184:187],v[124:127],v[88:91]\n"
"ds_read_b64_tr_b16 v[214:215],v230 offset:12288\n"
"s_waitcnt lgkmcnt(13)\n"
"ds_read_b64_tr_b16 v[216:217],v231 offset:8192\n"
"v_mfma_f32_16x16x32_bf16 v[92:95],v[184:187],v[128:131],v[92:95]\n"
"ds_read_b64_tr_b16 v[218:219],v231 offset:12288\n"
"s_waitcnt lgkmcnt(13)\n"
"v_mfma_f32_16x16x32_bf16 v[0:3],v[188:191],v[108:111],v[0:3]\n"
"s_waitcnt lgkmcnt(0)\n"
"v_mfma_f32_16x16x32_bf16 v[4:7],v[188:191],v[112:115],v[4:7]\n"
"s_add_u32 s67,s59,13\n"
"s_and_b32 s67,s67,15\n"
"s_lshl_b32 s67,s67,15\n"
"s_add_u32 m0,s3,0x1a000\n"
"s_nop 0\n"
"buffer_load_dwordx4 v238,s[40:43],s67 offen lds\n"
"v_mfma_f32_16x16x32_bf16 v[8:11],v[188:191],v[116:119],v[8:11]\n"
"v_mfma_f32_16x16x32_bf16 v[12:15],v[192:195],v[108:111],v[12:15]\n"
"buffer_load_dwordx4 v239,s[40:43],s67 offen offset:1024 lds\n"
"buffer_load_dwordx4 v240,s[40:43],s67 offen offset:2048 lds\n"
"v_mfma_f32_16x16x32_bf16 v[16:19],v[192:195],v[112:115],v[16:19]\n"
"buffer_load_dwordx4 v241,s[40:43],s67 offen offset:3072 lds\n"
"v_mfma_f32_16x16x32_bf16 v[20:23],v[192:195],v[116:119],v[20:23]\n"
"s_add_u32 s67,s67,0x4000\n"
"s_add_u32 m0,s3,0x1b000\n"
"s_nop 0\n"
"buffer_load_dwordx4 v238,s[40:43],s67 offen lds\n"
"v_mfma_f32_16x16x32_bf16 v[24:27],v[196:199],v[108:111],v[24:27]\n"
"buffer_load_dwordx4 v239,s[40:43],s67 offen offset:1024 lds\n"
"v_mfma_f32_16x16x32_bf16 v[28:31],v[196:199],v[112:115],v[28:31]\n"
"buffer_load_dwordx4 v240,s[40:43],s67 offen offset:2048 lds\n"
"v_mfma_f32_16x16x32_bf16 v[32:35],v[196:199],v[116:119],v[32:35]\n"
"buffer_load_dwordx4 v241,s[40:43],s67 offen offset:3072 lds\n"
"v_mfma_f32_16x16x32_bf16 v[36:39],v[200:203],v[108:111],v[36:39]\n"
"s_add_u32 s60,s59,11\n"
"s_and_b32 s60,s60,15\n"
"s_mul_i32 s60,s60,0xc00\n"
"v_add_u32 v140,s60,v242\n"
"ds_read_b128 v[120:123],v140\n"
"v_mfma_f32_16x16x32_bf16 v[40:43],v[200:203],v[112:115],v[40:43]\n"
"v_mfma_f32_16x16x32_bf16 v[44:47],v[200:203],v[116:119],v[44:47]\n"
"ds_read_b128 v[124:127],v140 offset:1024\n"
"v_mfma_f32_16x16x32_bf16 v[48:51],v[204:207],v[108:111],v[48:51]\n"
"ds_read_b128 v[128:131],v140 offset:2048\n"
"v_mfma_f32_16x16x32_bf16 v[52:55],v[204:207],v[112:115],v[52:55]\n"
"s_waitcnt vmcnt(16)\n"
"ds_read_b64_tr_b16 v[156:157],v132\n"
"ds_read_b64_tr_b16 v[158:159],v132 offset:4096\n"
"v_mfma_f32_16x16x32_bf16 v[56:59],v[204:207],v[116:119],v[56:59]\n"
"ds_read_b64_tr_b16 v[160:161],v133\n"
"v_mfma_f32_16x16x32_bf16 v[60:63],v[208:211],v[108:111],v[60:63]\n"
"ds_read_b64_tr_b16 v[162:163],v133 offset:4096\n"
"ds_read_b64_tr_b16 v[164:165],v134\n"
"v_mfma_f32_16x16x32_bf16 v[64:67],v[208:211],v[112:115],v[64:67]\n"
"ds_read_b64_tr_b16 v[166:167],v134 offset:4096\n"
"v_mfma_f32_16x16x32_bf16 v[68:71],v[208:211],v[116:119],v[68:71]\n"
"ds_read_b64_tr_b16 v[168:169],v135\n"
"v_mfma_f32_16x16x32_bf16 v[72:75],v[212:215],v[108:111],v[72:75]\n"
"ds_read_b64_tr_b16 v[170:171],v135 offset:4096\n"
"ds_read_b64_tr_b16 v[172:173],v136\n"
"v_mfma_f32_16x16x32_bf16 v[76:79],v[212:215],v[112:115],v[76:79]\n"
"ds_read_b64_tr_b16 v[174:175],v136 offset:4096\n"
"v_mfma_f32_16x16x32_bf16 v[80:83],v[212:215],v[116:119],v[80:83]\n"
"ds_read_b64_tr_b16 v[176:177],v137\n"
"ds_read_b64_tr_b16 v[178:179],v137 offset:4096\n"
"v_mfma_f32_16x16x32_bf16 v[84:87],v[216:219],v[108:111],v[84:87]\n"
"s_waitcnt lgkmcnt(13)\n"
"ds_read_b64_tr_b16 v[180:181],v138\n"
"v_mfma_f32_16x16x32_bf16 v[88:91],v[216:219],v[112:115],v[88:91]\n"
"ds_read_b64_tr_b16 v[182:183],v138 offset:4096\n"
"s_waitcnt lgkmcnt(13)\n"
"ds_read_b64_tr_b16 v[184:185],v139\n"
"v_mfma_f32_16x16x32_bf16 v[92:95],v[216:219],v[116:119],v[92:95]\n"
"ds_read_b64_tr_b16 v[186:187],v139 offset:4096\n"
"s_waitcnt lgkmcnt(13)\n"
"v_mfma_f32_16x16x32_bf16 v[0:3],v[156:159],v[120:123],v[0:3]\n"
"s_waitcnt lgkmcnt(0)\n"
"v_mfma_f32_16x16x32_bf16 v[4:7],v[156:159],v[124:127],v[4:7]\n"
"s_add_u32 s67,s59,14\n"
"s_and_b32 s67,s67,15\n"
"s_lshl_b32 s67,s67,15\n"
"s_add_u32 m0,s2,0xc000\n"
"s_nop 0\n"
"buffer_load_dwordx4 v238,s[40:43],s67 offen lds\n"
"v_mfma_f32_16x16x32_bf16 v[8:11],v[156:159],v[128:131],v[8:11]\n"
"v_mfma_f32_16x16x32_bf16 v[12:15],v[160:163],v[120:123],v[12:15]\n"
"buffer_load_dwordx4 v239,s[40:43],s67 offen offset:1024 lds\n"
"buffer_load_dwordx4 v240,s[40:43],s67 offen offset:2048 lds\n"
"v_mfma_f32_16x16x32_bf16 v[16:19],v[160:163],v[124:127],v[16:19]\n"
"buffer_load_dwordx4 v241,s[40:43],s67 offen offset:3072 lds\n"
"v_mfma_f32_16x16x32_bf16 v[20:23],v[160:163],v[128:131],v[20:23]\n"
"s_add_u32 s67,s67,0x4000\n"
"s_add_u32 m0,s2,0xd000\n"
"s_nop 0\n"
"buffer_load_dwordx4 v238,s[40:43],s67 offen lds\n"
"v_mfma_f32_16x16x32_bf16 v[24:27],v[164:167],v[120:123],v[24:27]\n"
"buffer_load_dwordx4 v239,s[40:43],s67 offen offset:1024 lds\n"
"v_mfma_f32_16x16x32_bf16 v[28:31],v[164:167],v[124:127],v[28:31]\n"
"buffer_load_dwordx4 v240,s[40:43],s67 offen offset:2048 lds\n"
"v_mfma_f32_16x16x32_bf16 v[32:35],v[164:167],v[128:131],v[32:35]\n"
"buffer_load_dwordx4 v241,s[40:43],s67 offen offset:3072 lds\n"
"v_mfma_f32_16x16x32_bf16 v[36:39],v[168:171],v[120:123],v[36:39]\n"
"s_add_u32 s60,s59,12\n"
"s_and_b32 s60,s60,15\n"
"s_mul_i32 s60,s60,0xc00\n"
"v_add_u32 v140,s60,v242\n"
"ds_read_b128 v[108:111],v140\n"
"v_mfma_f32_16x16x32_bf16 v[40:43],v[168:171],v[124:127],v[40:43]\n"
"v_mfma_f32_16x16x32_bf16 v[44:47],v[168:171],v[128:131],v[44:47]\n"
"ds_read_b128 v[112:115],v140 offset:1024\n"
"v_mfma_f32_16x16x32_bf16 v[48:51],v[172:175],v[120:123],v[48:51]\n"
"ds_read_b128 v[116:119],v140 offset:2048\n"
"v_mfma_f32_16x16x32_bf16 v[52:55],v[172:175],v[124:127],v[52:55]\n"
"s_waitcnt vmcnt(16)\n"
"ds_read_b64_tr_b16 v[188:189],v224\n"
"ds_read_b64_tr_b16 v[190:191],v224 offset:4096\n"
"v_mfma_f32_16x16x32_bf16 v[56:59],v[172:175],v[128:131],v[56:59]\n"
"ds_read_b64_tr_b16 v[192:193],v225\n"
"v_mfma_f32_16x16x32_bf16 v[60:63],v[176:179],v[120:123],v[60:63]\n"
"ds_read_b64_tr_b16 v[194:195],v225 offset:4096\n"
"ds_read_b64_tr_b16 v[196:197],v226\n"
"v_mfma_f32_16x16x32_bf16 v[64:67],v[176:179],v[124:127],v[64:67]\n"
"ds_read_b64_tr_b16 v[198:199],v226 offset:4096\n"
"v_mfma_f32_16x16x32_bf16 v[68:71],v[176:179],v[128:131],v[68:71]\n"
"ds_read_b64_tr_b16 v[200:201],v227\n"
"v_mfma_f32_16x16x32_bf16 v[72:75],v[180:183],v[120:123],v[72:75]\n"
"ds_read_b64_tr_b16 v[202:203],v227 offset:4096\n"
"ds_read_b64_tr_b16 v[204:205],v228\n"
"v_mfma_f32_16x16x32_bf16 v[76:79],v[180:183],v[124:127],v[76:79]\n"
"ds_read_b64_tr_b16 v[206:207],v228 offset:4096\n"
"v_mfma_f32_16x16x32_bf16 v[80:83],v[180:183],v[128:131],v[80:83]\n"
"ds_read_b64_tr_b16 v[208:209],v229\n"
"ds_read_b64_tr_b16 v[210:211],v229 offset:4096\n"
"v_mfma_f32_16x16x32_bf16 v[84:87],v[184:187],v[120:123],v[84:87]\n"
"s_waitcnt lgkmcnt(13)\n"
"ds_read_b64_tr_b16 v[212:213],v230\n"
"v_mfma_f32_16x16x32_bf16 v[88:91],v[184:187],v[124:127],v[88:91]\n"
"ds_read_b64_tr_b16 v[214:215],v230 offset:4096\n"
"s_waitcnt lgkmcnt(13)\n"
"ds_read_b64_tr_b16 v[216:217],v231\n"
"v_mfma_f32_16x16x32_bf16 v[92:95],v[184:187],v[128:131],v[92:95]\n"
"ds_read_b64_tr_b16 v[218:219],v231 offset:4096\n"
"s_waitcnt lgkmcnt(13)\n"
"v_mfma_f32_16x16x32_bf16 v[0:3],v[188:191],v[108:111],v[0:3]\n"
"s_waitcnt lgkmcnt(0)\n"
"v_mfma_f32_16x16x32_bf16 v[4:7],v[188:191],v[112:115],v[4:7]\n"
"s_add_u32 s67,s59,15\n"
"s_and_b32 s67,s67,15\n"
"s_lshl_b32 s67,s67,15\n"
"s_add_u32 m0,s3,0x18000\n"
"s_nop 0\n"
"buffer_load_dwordx4 v238,s[40:43],s67 offen lds\n"
"v_mfma_f32_16x16x32_bf16 v[8:11],v[188:191],v[116:119],v[8:11]\n"
"v_mfma_f32_16x16x32_bf16 v[12:15],v[192:195],v[108:111],v[12:15]\n"
"buffer_load_dwordx4 v239,s[40:43],s67 offen offset:1024 lds\n"
"buffer_load_dwordx4 v240,s[40:43],s67 offen offset:2048 lds\n"
"v_mfma_f32_16x16x32_bf16 v[16:19],v[192:195],v[112:115],v[16:19]\n"
"buffer_load_dwordx4 v241,s[40:43],s67 offen offset:3072 lds\n"
"v_mfma_f32_16x16x32_bf16 v[20:23],v[192:195],v[116:119],v[20:23]\n"
"s_add_u32 s67,s67,0x4000\n"
"s_add_u32 m0,s3,0x19000\n"
"s_nop 0\n"
"buffer_load_dwordx4 v238,s[40:43],s67 offen lds\n"
"v_mfma_f32_16x16x32_bf16 v[24:27],v[196:199],v[108:111],v[24:27]\n"
"buffer_load_dwordx4 v239,s[40:43],s67 offen offset:1024 lds\n"
"v_mfma_f32_16x16x32_bf16 v[28:31],v[196:199],v[112:115],v[28:31]\n"
"buffer_load_dwordx4 v240,s[40:43],s67 offen offset:2048 lds\n"
"v_mfma_f32_16x16x32_bf16 v[32:35],v[196:199],v[116:119],v[32:35]\n"
"buffer_load_dwordx4 v241,s[40:43],s67 offen offset:3072 lds\n"
"v_mfma_f32_16x16x32_bf16 v[36:39],v[200:203],v[108:111],v[36:39]\n"
"s_add_u32 s60,s59,13\n"
"s_and_b32 s60,s60,15\n"
"s_mul_i32 s60,s60,0xc00\n"
"v_add_u32 v140,s60,v242\n"
"ds_read_b128 v[120:123],v140\n"
"v_mfma_f32_16x16x32_bf16 v[40:43],v[200:203],v[112:115],v[40:43]\n"
"v_mfma_f32_16x16x32_bf16 v[44:47],v[200:203],v[116:119],v[44:47]\n"
"ds_read_b128 v[124:127],v140 offset:1024\n"
"v_mfma_f32_16x16x32_bf16 v[48:51],v[204:207],v[108:111],v[48:51]\n"
"ds_read_b128 v[128:131],v140 offset:2048\n"
"v_mfma_f32_16x16x32_bf16 v[52:55],v[204:207],v[112:115],v[52:55]\n"
"s_waitcnt vmcnt(16)\n"
"ds_read_b64_tr_b16 v[156:157],v224 offset:8192\n"
"ds_read_b64_tr_b16 v[158:159],v224 offset:12288\n"
"v_mfma_f32_16x16x32_bf16 v[56:59],v[204:207],v[116:119],v[56:59]\n"
"ds_read_b64_tr_b16 v[160:161],v225 offset:8192\n"
"v_mfma_f32_16x16x32_bf16 v[60:63],v[208:211],v[108:111],v[60:63]\n"
"ds_read_b64_tr_b16 v[162:163],v225 offset:12288\n"
"ds_read_b64_tr_b16 v[164:165],v226 offset:8192\n"
"v_mfma_f32_16x16x32_bf16 v[64:67],v[208:211],v[112:115],v[64:67]\n"
"ds_read_b64_tr_b16 v[166:167],v226 offset:12288\n"
"v_mfma_f32_16x16x32_bf16 v[68:71],v[208:211],v[116:119],v[68:71]\n"
"ds_read_b64_tr_b16 v[168:169],v227 offset:8192\n"
"v_mfma_f32_16x16x32_bf16 v[72:75],v[212:215],v[108:111],v[72:75]\n"
"ds_read_b64_tr_b16 v[170:171],v227 offset:12288\n"
"ds_read_b64_tr_b16 v[172:173],v228 offset:8192\n"
"v_mfma_f32_16x16x32_bf16 v[76:79],v[212:215],v[112:115],v[76:79]\n"
"ds_read_b64_tr_b16 v[174:175],v228 offset:12288\n"
"v_mfma_f32_16x16x32_bf16 v[80:83],v[212:215],v[116:119],v[80:83]\n"
"ds_read_b64_tr_b16 v[176:177],v229 offset:8192\n"
"ds_read_b64_tr_b16 v[178:179],v229 offset:12288\n"
"v_mfma_f32_16x16x32_bf16 v[84:87],v[216:219],v[108:111],v[84:87]\n"
"s_waitcnt lgkmcnt(13)\n"
"ds_read_b64_tr_b16 v[180:181],v230 offset:8192\n"
"v_mfma_f32_16x16x32_bf16 v[88:91],v[216:219],v[112:115],v[88:91]\n"
"ds_read_b64_tr_b16 v[182:183],v230 offset:12288\n"
"s_waitcnt lgkmcnt(13)\n"
"ds_read_b64_tr_b16 v[184:185],v231 offset:8192\n"
"v_mfma_f32_16x16x32_bf16 v[92:95],v[216:219],v[116:119],v[92:95]\n"
"ds_read_b64_tr_b16 v[186:187],v231 offset:12288\n"
"s_waitcnt lgkmcnt(13)\n"
"v_mfma_f32_16x16x32_bf16 v[0:3],v[156:159],v[120:123],v[0:3]\n"
"v_mfma_f32_16x16x32_bf16 v[4:7],v[156:159],v[124:127],v[4:7]\n"
"v_mfma_f32_16x16x32_bf16 v[8:11],v[156:159],v[128:131],v[8:11]\n"
"s_add_u32 s60,s59,14\n"
"s_and_b32 s60,s60,15\n"
"s_mul_i32 s60,s60,0xc00\n"
"v_add_u32 v140,s60,v242\n"
"ds_read_b128 v[108:111],v140\n"
"s_waitcnt lgkmcnt(10)\n"
"v_mfma_f32_16x16x32_bf16 v[12:15],v[160:163],v[120:123],v[12:15]\n"
"v_mfma_f32_16x16x32_bf16 v[16:19],v[160:163],v[124:127],v[16:19]\n"
"ds_read_b128 v[112:115],v140 offset:1024\n"
"v_mfma_f32_16x16x32_bf16 v[20:23],v[160:163],v[128:131],v[20:23]\n"
"v_mfma_f32_16x16x32_bf16 v[24:27],v[164:167],v[120:123],v[24:27]\n"
"ds_read_b128 v[116:119],v140 offset:2048\n"
"v_mfma_f32_16x16x32_bf16 v[28:31],v[164:167],v[124:127],v[28:31]\n"
"s_waitcnt vmcnt(8)\n"
"ds_read_b64_tr_b16 v[188:189],v132\n"
"v_mfma_f32_16x16x32_bf16 v[32:35],v[164:167],v[128:131],v[32:35]\n"
"ds_read_b64_tr_b16 v[190:191],v132 offset:4096\n"
"s_waitcnt lgkmcnt(6)\n"
"v_mfma_f32_16x16x32_bf16 v[36:39],v[168:171],v[120:123],v[36:39]\n"
"ds_read_b64_tr_b16 v[192:193],v133\n"
"v_mfma_f32_16x16x32_bf16 v[40:43],v[168:171],v[124:127],v[40:43]\n"
"ds_read_b64_tr_b16 v[194:195],v133 offset:4096\n"
"v_mfma_f32_16x16x32_bf16 v[44:47],v[168:171],v[128:131],v[44:47]\n"
"ds_read_b64_tr_b16 v[196:197],v134\n"
"v_mfma_f32_16x16x32_bf16 v[48:51],v[172:175],v[120:123],v[48:51]\n"
"ds_read_b64_tr_b16 v[198:199],v134 offset:4096\n"
"v_mfma_f32_16x16x32_bf16 v[52:55],v[172:175],v[124:127],v[52:55]\n"
"ds_read_b64_tr_b16 v[200:201],v135\n"
"v_mfma_f32_16x16x32_bf16 v[56:59],v[172:175],v[128:131],v[56:59]\n"
"ds_read_b64_tr_b16 v[202:203],v135 offset:4096\n"
"v_mfma_f32_16x16x32_bf16 v[60:63],v[176:179],v[120:123],v[60:63]\n"
"ds_read_b64_tr_b16 v[204:205],v136\n"
"v_mfma_f32_16x16x32_bf16 v[64:67],v[176:179],v[124:127],v[64:67]\n"
"ds_read_b64_tr_b16 v[206:207],v136 offset:4096\n"
"v_mfma_f32_16x16x32_bf16 v[68:71],v[176:179],v[128:131],v[68:71]\n"
"v_mfma_f32_16x16x32_bf16 v[72:75],v[180:183],v[120:123],v[72:75]\n"
"ds_read_b64_tr_b16 v[208:209],v137\n"
"v_mfma_f32_16x16x32_bf16 v[76:79],v[180:183],v[124:127],v[76:79]\n"
"s_waitcnt lgkmcnt(9)\n"
"ds_read_b64_tr_b16 v[210:211],v137 offset:4096\n"
"v_mfma_f32_16x16x32_bf16 v[80:83],v[180:183],v[128:131],v[80:83]\n"
"ds_read_b64_tr_b16 v[212:213],v138\n"
"v_mfma_f32_16x16x32_bf16 v[84:87],v[184:187],v[120:123],v[84:87]\n"
"ds_read_b64_tr_b16 v[214:215],v138 offset:4096\n"
"v_mfma_f32_16x16x32_bf16 v[88:91],v[184:187],v[124:127],v[88:91]\n"
"ds_read_b64_tr_b16 v[216:217],v139\n"
"v_mfma_f32_16x16x32_bf16 v[92:95],v[184:187],v[128:131],v[92:95]\n"
"ds_read_b64_tr_b16 v[218:219],v139 offset:4096\n"
"v_mfma_f32_16x16x32_bf16 v[0:3],v[188:191],v[108:111],v[0:3]\n"
"v_mfma_f32_16x16x32_bf16 v[4:7],v[188:191],v[112:115],v[4:7]\n"
"v_mfma_f32_16x16x32_bf16 v[8:11],v[188:191],v[116:119],v[8:11]\n"
"s_add_u32 s60,s59,15\n"
"s_and_b32 s60,s60,15\n"
"s_mul_i32 s60,s60,0xc00\n"
"v_add_u32 v140,s60,v242\n"
"ds_read_b128 v[120:123],v140\n"
"s_waitcnt lgkmcnt(7)\n"
"v_mfma_f32_16x16x32_bf16 v[12:15],v[192:195],v[108:111],v[12:15]\n"
"v_mfma_f32_16x16x32_bf16 v[16:19],v[192:195],v[112:115],v[16:19]\n"
"ds_read_b128 v[124:127],v140 offset:1024\n"
"v_mfma_f32_16x16x32_bf16 v[20:23],v[192:195],v[116:119],v[20:23]\n"
"v_mfma_f32_16x16x32_bf16 v[24:27],v[196:199],v[108:111],v[24:27]\n"
"ds_read_b128 v[128:131],v140 offset:2048\n"
"v_mfma_f32_16x16x32_bf16 v[28:31],v[196:199],v[112:115],v[28:31]\n"
"s_waitcnt vmcnt(0)\n"
"ds_read_b64_tr_b16 v[156:157],v224\n"
"v_mfma_f32_16x16x32_bf16 v[32:35],v[196:199],v[116:119],v[32:35]\n"
"ds_read_b64_tr_b16 v[158:159],v224 offset:4096\n"
"v_mfma_f32_16x16x32_bf16 v[36:39],v[200:203],v[108:111],v[36:39]\n"
"ds_read_b64_tr_b16 v[160:161],v225\n"
"v_mfma_f32_16x16x32_bf16 v[40:43],v[200:203],v[112:115],v[40:43]\n"
"ds_read_b64_tr_b16 v[162:163],v225 offset:4096\n"
"v_mfma_f32_16x16x32_bf16 v[44:47],v[200:203],v[116:119],v[44:47]\n"
"ds_read_b64_tr_b16 v[164:165],v226\n"
"v_mfma_f32_16x16x32_bf16 v[48:51],v[204:207],v[108:111],v[48:51]\n"
"ds_read_b64_tr_b16 v[166:167],v226 offset:4096\n"
"v_mfma_f32_16x16x32_bf16 v[52:55],v[204:207],v[112:115],v[52:55]\n"
"s_waitcnt lgkmcnt(8)\n"
"ds_read_b64_tr_b16 v[168:169],v227\n"
"v_mfma_f32_16x16x32_bf16 v[56:59],v[204:207],v[116:119],v[56:59]\n"
"ds_read_b64_tr_b16 v[170:171],v227 offset:4096\n"
"v_mfma_f32_16x16x32_bf16 v[60:63],v[208:211],v[108:111],v[60:63]\n"
"ds_read_b64_tr_b16 v[172:173],v228\n"
"v_mfma_f32_16x16x32_bf16 v[64:67],v[208:211],v[112:115],v[64:67]\n"
"ds_read_b64_tr_b16 v[174:175],v228 offset:4096\n"
"v_mfma_f32_16x16x32_bf16 v[68:71],v[208:211],v[116:119],v[68:71]\n"
"v_mfma_f32_16x16x32_bf16 v[72:75],v[212:215],v[108:111],v[72:75]\n"
"ds_read_b64_tr_b16 v[176:177],v229\n"
"v_mfma_f32_16x16x32_bf16 v[76:79],v[212:215],v[112:115],v[76:79]\n"
"ds_read_b64_tr_b16 v[178:179],v229 offset:4096\n"
"v_mfma_f32_16x16x32_bf16 v[80:83],v[212:215],v[116:119],v[80:83]\n"
"ds_read_b64_tr_b16 v[180:181],v230\n"
"v_mfma_f32_16x16x32_bf16 v[84:87],v[216:219],v[108:111],v[84:87]\n"
"s_waitcnt lgkmcnt(9)\n"
"ds_read_b64_tr_b16 v[182:183],v230 offset:4096\n"
"v_mfma_f32_16x16x32_bf16 v[88:91],v[216:219],v[112:115],v[88:91]\n"
"ds_read_b64_tr_b16 v[184:185],v231\n"
"v_mfma_f32_16x16x32_bf16 v[92:95],v[216:219],v[116:119],v[92:95]\n"
"ds_read_b64_tr_b16 v[186:187],v231 offset:4096\n"
"v_mfma_f32_16x16x32_bf16 v[0:3],v[156:159],v[120:123],v[0:3]\n"
"v_mfma_f32_16x16x32_bf16 v[4:7],v[156:159],v[124:127],v[4:7]\n"
"v_mfma_f32_16x16x32_bf16 v[8:11],v[156:159],v[128:131],v[8:11]\n"
"v_mfma_f32_16x16x32_bf16 v[12:15],v[160:163],v[120:123],v[12:15]\n"
"v_mfma_f32_16x16x32_bf16 v[16:19],v[160:163],v[124:127],v[16:19]\n"
"v_mfma_f32_16x16x32_bf16 v[20:23],v[160:163],v[128:131],v[20:23]\n"
"s_waitcnt lgkmcnt(6)\n"
"v_mfma_f32_16x16x32_bf16 v[24:27],v[164:167],v[120:123],v[24:27]\n"
"v_mfma_f32_16x16x32_bf16 v[28:31],v[164:167],v[124:127],v[28:31]\n"
"v_mfma_f32_16x16x32_bf16 v[32:35],v[164:167],v[128:131],v[32:35]\n"
"v_mfma_f32_16x16x32_bf16 v[36:39],v[168:171],v[120:123],v[36:39]\n"
"v_mfma_f32_16x16x32_bf16 v[40:43],v[168:171],v[124:127],v[40:43]\n"
"v_mfma_f32_16x16x32_bf16 v[44:47],v[168:171],v[128:131],v[44:47]\n"
"v_mfma_f32_16x16x32_bf16 v[48:51],v[172:175],v[120:123],v[48:51]\n"
"v_mfma_f32_16x16x32_bf16 v[52:55],v[172:175],v[124:127],v[52:55]\n"
"v_mfma_f32_16x16x32_bf16 v[56:59],v[172:175],v[128:131],v[56:59]\n"
"s_waitcnt lgkmcnt(0)\n"
"v_mfma_f32_16x16x32_bf16 v[60:63],v[176:179],v[120:123],v[60:63]\n"
"v_mfma_f32_16x16x32_bf16 v[64:67],v[176:179],v[124:127],v[64:67]\n"
"v_mfma_f32_16x16x32_bf16 v[68:71],v[176:179],v[128:131],v[68:71]\n"
"v_mfma_f32_16x16x32_bf16 v[72:75],v[180:183],v[120:123],v[72:75]\n"
"v_mfma_f32_16x16x32_bf16 v[76:79],v[180:183],v[124:127],v[76:79]\n"
"v_mfma_f32_16x16x32_bf16 v[80:83],v[180:183],v[128:131],v[80:83]\n"
"v_mfma_f32_16x16x32_bf16 v[84:87],v[184:187],v[120:123],v[84:87]\n"
"v_mfma_f32_16x16x32_bf16 v[88:91],v[184:187],v[124:127],v[88:91]\n"
"v_mfma_f32_16x16x32_bf16 v[92:95],v[184:187],v[128:131],v[92:95]\n"
"s_mul_i32 s67,s56,0xc400\n"
"s_mul_i32 s60,s7,0x3000\n"
"s_add_u32 s67,s67,s60\n"
"v_cvt_pk_bf16_f32 v108,v0,v1\n"
"v_cvt_pk_bf16_f32 v109,v2,v3\n"
"v_cvt_pk_bf16_f32 v110,v12,v13\n"
"v_cvt_pk_bf16_f32 v111,v14,v15\n"
"buffer_store_dwordx4 v[108:111],v242,s[44:47],s67 offen sc1\n"
"s_sleep 4\n"
"v_cvt_pk_bf16_f32 v112,v24,v25\n"
"v_cvt_pk_bf16_f32 v113,v26,v27\n"
"v_cvt_pk_bf16_f32 v114,v36,v37\n"
"v_cvt_pk_bf16_f32 v115,v38,v39\n"
"buffer_store_dwordx4 v[112:115],v242,s[44:47],s67 offen offset:1024 sc1\n"
"s_sleep 4\n"
"v_cvt_pk_bf16_f32 v116,v48,v49\n"
"v_cvt_pk_bf16_f32 v117,v50,v51\n"
"v_cvt_pk_bf16_f32 v118,v60,v61\n"
"v_cvt_pk_bf16_f32 v119,v62,v63\n"
"buffer_store_dwordx4 v[116:119],v242,s[44:47],s67 offen offset:2048 sc1\n"
"s_sleep 4\n"
"v_cvt_pk_bf16_f32 v120,v72,v73\n"
"v_cvt_pk_bf16_f32 v121,v74,v75\n"
"v_cvt_pk_bf16_f32 v122,v84,v85\n"
"v_cvt_pk_bf16_f32 v123,v86,v87\n"
"buffer_store_dwordx4 v[120:123],v242,s[44:47],s67 offen offset:3072 sc1\n"
"s_sleep 4\n"
"s_add_u32 s67,s67,0x1000\n"
"v_cvt_pk_bf16_f32 v108,v4,v5\n"
"v_cvt_pk_bf16_f32 v109,v6,v7\n"
"v_cvt_pk_bf16_f32 v110,v16,v17\n"
"v_cvt_pk_bf16_f32 v111,v18,v19\n"
"buffer_store_dwordx4 v[108:111],v242,s[44:47],s67 offen sc1\n"
"s_sleep 4\n"
"v_cvt_pk_bf16_f32 v112,v28,v29\n"
"v_cvt_pk_bf16_f32 v113,v30,v31\n"
"v_cvt_pk_bf16_f32 v114,v40,v41\n"
"v_cvt_pk_bf16_f32 v115,v42,v43\n"
"buffer_store_dwordx4 v[112:115],v242,s[44:47],s67 offen offset:1024 sc1\n"
"s_sleep 4\n"
"v_cvt_pk_bf16_f32 v116,v52,v53\n"
"v_cvt_pk_bf16_f32 v117,v54,v55\n"
"v_cvt_pk_bf16_f32 v118,v64,v65\n"
"v_cvt_pk_bf16_f32 v119,v66,v67\n"
"buffer_store_dwordx4 v[116:119],v242,s[44:47],s67 offen offset:2048 sc1\n"
"s_sleep 4\n"
"v_cvt_pk_bf16_f32 v120,v76,v77\n"
"v_cvt_pk_bf16_f32 v121,v78,v79\n"
"v_cvt_pk_bf16_f32 v122,v88,v89\n"
"v_cvt_pk_bf16_f32 v123,v90,v91\n"
"buffer_store_dwordx4 v[120:123],v242,s[44:47],s67 offen offset:3072 sc1\n"
"s_sleep 4\n"
"s_add_u32 s67,s67,0x1000\n"
"v_cvt_pk_bf16_f32 v108,v8,v9\n"
"v_cvt_pk_bf16_f32 v109,v10,v11\n"
"v_cvt_pk_bf16_f32 v110,v20,v21\n"
"v_cvt_pk_bf16_f32 v111,v22,v23\n"
"buffer_store_dwordx4 v[108:111],v242,s[44:47],s67 offen sc1\n"
"s_sleep 4\n"
"v_cvt_pk_bf16_f32 v112,v32,v33\n"
"v_cvt_pk_bf16_f32 v113,v34,v35\n"
"v_cvt_pk_bf16_f32 v114,v44,v45\n"
"v_cvt_pk_bf16_f32 v115,v46,v47\n"
"buffer_store_dwordx4 v[112:115],v242,s[44:47],s67 offen offset:1024 sc1\n"
"s_sleep 4\n"
"v_cvt_pk_bf16_f32 v116,v56,v57\n"
"v_cvt_pk_bf16_f32 v117,v58,v59\n"
"v_cvt_pk_bf16_f32 v118,v68,v69\n"
"v_cvt_pk_bf16_f32 v119,v70,v71\n"
"buffer_store_dwordx4 v[116:119],v242,s[44:47],s67 offen offset:2048 sc1\n"
"s_sleep 4\n"
"v_cvt_pk_bf16_f32 v120,v80,v81\n"
"v_cvt_pk_bf16_f32 v121,v82,v83\n"
"v_cvt_pk_bf16_f32 v122,v92,v93\n"
"v_cvt_pk_bf16_f32 v123,v94,v95\n"
"buffer_store_dwordx4 v[120:123],v242,s[44:47],s67 offen offset:3072 sc1\n"
"s_sleep 4\n"
"s_add_u32 s67,s67,0x1000\n"
"v_and_b32 v246,7,v243\n"
"v_lshlrev_b32 v248,2,v246\n"
"v_lshlrev_b32 v246,7,v246\n"
"buffer_load_dword v245,v246,s[52:55],0 offen sc1\n"
"v_and_b32 v251,1,v244\n"
"v_lshlrev_b32 v251,5,v251\n"
"v_lshrrev_b32 v253,1,v244\n"
"v_lshl_or_b32 v251,v253,4,v251\n"
"v_lshl_or_b32 v170,v243,11,v251\n"
"s_mul_i32 s2,s56,6\n"
"s_lshl_b32 s60,s7,1\n"
"s_add_u32 s2,s2,s60\n"
"s_add_u32 s2,s2,0\n"
"s_lshl_b32 s60,s2,10\n"
"s_mov_b32 s8,s60\n"
"s_mul_i32 s62,s2,43\n"
"s_lshr_b32 s62,s62,9\n"
"s_mul_i32 s63,s62,12\n"
"s_sub_u32 s63,s2,s63\n"
"s_lshl_b32 s61,s62,8\n"
"s_and_b32 s62,s63,3\n"
"s_lshl_b32 s62,s62,6\n"
"s_add_u32 s61,s61,s62\n"
"s_lshr_b32 s63,s63,2\n"
"s_lshl_b32 s63,s63,15\n"
"s_add_u32 s61,s61,s63\n"
"s_mul_i32 s63,s57,0x18000\n"
"s_add_u32 s61,s61,s63\n"
"s_add_u32 s61,s61,0\n"
"s_mov_b32 s10,s61\n"
"s_mul_i32 s3,s56,6\n"
"s_lshl_b32 s60,s7,1\n"
"s_add_u32 s3,s3,s60\n"
"s_add_u32 s3,s3,1\n"
"s_lshl_b32 s60,s3,10\n"
"s_mov_b32 s9,s60\n"
"s_mul_i32 s62,s3,43\n"
"s_lshr_b32 s62,s62,9\n"
"s_mul_i32 s63,s62,12\n"
"s_sub_u32 s63,s3,s63\n"
"s_lshl_b32 s61,s62,8\n"
"s_and_b32 s62,s63,3\n"
"s_lshl_b32 s62,s62,6\n"
"s_add_u32 s61,s61,s62\n"
"s_lshr_b32 s63,s63,2\n"
"s_lshl_b32 s63,s63,15\n"
"s_add_u32 s61,s61,s63\n"
"s_mul_i32 s63,s57,0x18000\n"
"s_add_u32 s61,s61,s63\n"
"s_add_u32 s61,s61,0\n"
"s_mov_b32 s11,s61\n"
"v_lshrrev_b32 v251,2,v243\n"
"v_cmp_eq_u32 vcc,v251,v244\n"
"v_and_b32 v251,1,v243\n"
"v_lshlrev_b32 v251,4,v251\n"
"v_mov_b32 v253,0x3f80\n"
"v_lshlrev_b32 v251,v251,v253\n"
"v_cndmask_b32 v251,0,v251,vcc\n"
"v_and_b32 v253,2,v243\n"
"v_cmp_eq_u32 vcc,0,v253\n"
"v_mov_b32 v180,0\n"
"v_mov_b32 v181,0\n"
"v_mov_b32 v184,0\n"
"v_mov_b32 v185,0\n"
"v_cndmask_b32 v182,0,v251,vcc\n"
"v_cndmask_b32 v183,v251,0,vcc\n"
"s_lshl_b32 s61,s68,2\n"
"s_add_u32 s61,s61,0x1000\n"
"s_lshl_b32 s62,s68,7\n"
"s_add_u32 s62,s62,0x1000\n"
"s_waitcnt vmcnt(0)\n"
"s_barrier\n"
"s_cmp_lg_u32 s7,3\n"
"s_cbranch_scc1 Ls_49\n"
"s_mov_b64 exec,0xff\n"
"buffer_store_dword v245,v246,s[52:55],s61 offen sc1\n"
"s_mov_b64 exec,-1\n"
"s_mov_b32 s69,0\n"
"Ll_50:\n"
"buffer_load_dword v250,v248,s[52:55],s62 offen sc1\n"
"s_waitcnt vmcnt(0)\n"
"v_cmp_ne_u32 vcc,s65,v250\n"
"s_cbranch_vccz Ld_51\n"
"s_add_u32 s69,s69,1\n"
"s_and_b32 s60,s69,15\n"
"s_cbranch_scc1 Ll_50\n"
"buffer_load_dword v249,v246,s[52:55],0 offen sc1\n"
"s_waitcnt vmcnt(0)\n"
"v_cmp_ne_u32 vcc,v249,v245\n"
"s_cbranch_vccz Lo_52\n"
"s_mov_b64 exec,0xff\n"
"buffer_store_dword v249,v246,s[52:55],s61 offen sc1\n"
"s_mov_b64 exec,-1\n"
"v_mov_b32 v245,v249\n"
"Lo_52:\n"
"s_cmp_lt_u32 s69,0x80000\n"
"s_cbranch_scc1 Ll_50\n"
"Ld_51:\n"
"s_waitcnt vmcnt(0)\n"
"Ls_49:\n"
"s_barrier\n"
"s_cmp_lg_u32 s7,3\n"
"s_cbranch_scc1 Lr_53\n"
"buffer_load_dword v249,v246,s[52:55],0 offen sc1\n"
"s_waitcnt vmcnt(0)\n"
"v_cmp_ne_u32 vcc,v249,v245\n"
"s_cbranch_vccz Lo_55\n"
"s_mov_b64 exec,0xff\n"
"buffer_store_dword v249,v246,s[52:55],s61 offen sc1\n"
"s_mov_b64 exec,-1\n"
"v_mov_b32 v245,v249\n"
"Lo_55:\n"
"s_waitcnt vmcnt(0)\n"
"s_branch Le_54\n"
"Lr_53:\n"
"buffer_load_dwordx4 v[0:3],v242,s[44:47],s8 offen sc1\n"
"s_add_u32 s8,s8,0xc400\n"
"buffer_load_dwordx4 v[4:7],v242,s[44:47],s8 offen sc1\n"
"s_add_u32 s8,s8,0xc400\n"
"buffer_load_dwordx4 v[8:11],v242,s[44:47],s8 offen sc1\n"
"s_add_u32 s8,s8,0xc400\n"
"buffer_load_dwordx4 v[12:15],v242,s[44:47],s8 offen sc1\n"
"s_add_u32 s8,s8,0xc400\n"
"buffer_load_dwordx4 v[16:19],v242,s[44:47],s8 offen sc1\n"
"s_add_u32 s8,s8,0xc400\n"
"buffer_load_dwordx4 v[20:23],v242,s[44:47],s8 offen sc1\n"
"s_add_u32 s8,s8,0xc400\n"
"buffer_load_dwordx4 v[24:27],v242,s[44:47],s8 offen sc1\n"
"s_add_u32 s8,s8,0xc400\n"
"buffer_load_dwordx4 v[28:31],v242,s[44:47],s8 offen sc1\n"
"buffer_load_dwordx4 v[32:35],v242,s[44:47],s9 offen sc1\n"
"s_add_u32 s9,s9,0xc400\n"
"buffer_load_dwordx4 v[36:39],v242,s[44:47],s9 offen sc1\n"
"s_add_u32 s9,s9,0xc400\n"
"buffer_load_dwordx4 v[40:43],v242,s[44:47],s9 offen sc1\n"
"s_add_u32 s9,s9,0xc400\n"
"buffer_load_dwordx4 v[44:47],v242,s[44:47],s9 offen sc1\n"
"s_add_u32 s9,s9,0xc400\n"
"buffer_load_dwordx4 v[48:51],v242,s[44:47],s9 offen sc1\n"
"s_add_u32 s9,s9,0xc400\n"
"buffer_load_dwordx4 v[52:55],v242,s[44:47],s9 offen sc1\n"
"s_add_u32 s9,s9,0xc400\n"
"buffer_load_dwordx4 v[56:59],v242,s[44:47],s9 offen sc1\n"
"s_add_u32 s9,s9,0xc400\n"
"buffer_load_dwordx4 v[60:63],v242,s[44:47],s9 offen sc1\n"
"s_waitcnt vmcnt(15)\n"
"v_mfma_f32_16x16x32_bf16 v[186:189],v[182:185],v[0:3],0\n"
"v_mfma_f32_16x16x32_bf16 v[190:193],v[180:183],v[0:3],0\n"
"s_waitcnt vmcnt(14)\n"
"v_mfma_f32_16x16x32_bf16 v[186:189],v[182:185],v[4:7],v[186:189]\n"
"v_mfma_f32_16x16x32_bf16 v[190:193],v[180:183],v[4:7],v[190:193]\n"
"s_waitcnt vmcnt(13)\n"
"v_mfma_f32_16x16x32_bf16 v[186:189],v[182:185],v[8:11],v[186:189]\n"
"v_mfma_f32_16x16x32_bf16 v[190:193],v[180:183],v[8:11],v[190:193]\n"
"s_waitcnt vmcnt(12)\n"
"v_mfma_f32_16x16x32_bf16 v[186:189],v[182:185],v[12:15],v[186:189]\n"
"v_mfma_f32_16x16x32_bf16 v[190:193],v[180:183],v[12:15],v[190:193]\n"
"s_waitcnt vmcnt(11)\n"
"v_mfma_f32_16x16x32_bf16 v[186:189],v[182:185],v[16:19],v[186:189]\n"
"v_mfma_f32_16x16x32_bf16 v[190:193],v[180:183],v[16:19],v[190:193]\n"
"s_waitcnt vmcnt(10)\n"
"v_mfma_f32_16x16x32_bf16 v[186:189],v[182:185],v[20:23],v[186:189]\n"
"v_mfma_f32_16x16x32_bf16 v[190:193],v[180:183],v[20:23],v[190:193]\n"
"s_waitcnt vmcnt(9)\n"
"v_mfma_f32_16x16x32_bf16 v[186:189],v[182:185],v[24:27],v[186:189]\n"
"v_mfma_f32_16x16x32_bf16 v[190:193],v[180:183],v[24:27],v[190:193]\n"
"s_waitcnt vmcnt(8)\n"
"v_mfma_f32_16x16x32_bf16 v[186:189],v[182:185],v[28:31],v[186:189]\n"
"v_mfma_f32_16x16x32_bf16 v[190:193],v[180:183],v[28:31],v[190:193]\n"
"s_nop 6\n"
"v_cvt_pk_bf16_f32 v172,v186,v187\n"
"v_cvt_pk_bf16_f32 v173,v188,v189\n"
"v_cvt_pk_bf16_f32 v174,v190,v191\n"
"v_cvt_pk_bf16_f32 v175,v192,v193\n"
"s_nop 0\n"
"v_permlane16_swap_b32 v172,v174\n"
"v_permlane16_swap_b32 v173,v175\n"
"buffer_store_dwordx4 v[172:175],v170,s[48:51],s10 offen sc0 sc1\n"
"s_waitcnt vmcnt(8)\n"
"v_mfma_f32_16x16x32_bf16 v[194:197],v[182:185],v[32:35],0\n"
"v_mfma_f32_16x16x32_bf16 v[198:201],v[180:183],v[32:35],0\n"
"s_waitcnt vmcnt(7)\n"
"v_mfma_f32_16x16x32_bf16 v[194:197],v[182:185],v[36:39],v[194:197]\n"
"v_mfma_f32_16x16x32_bf16 v[198:201],v[180:183],v[36:39],v[198:201]\n"
"s_waitcnt vmcnt(6)\n"
"v_mfma_f32_16x16x32_bf16 v[194:197],v[182:185],v[40:43],v[194:197]\n"
"v_mfma_f32_16x16x32_bf16 v[198:201],v[180:183],v[40:43],v[198:201]\n"
"s_waitcnt vmcnt(5)\n"
"v_mfma_f32_16x16x32_bf16 v[194:197],v[182:185],v[44:47],v[194:197]\n"
"v_mfma_f32_16x16x32_bf16 v[198:201],v[180:183],v[44:47],v[198:201]\n"
"s_waitcnt vmcnt(4)\n"
"v_mfma_f32_16x16x32_bf16 v[194:197],v[182:185],v[48:51],v[194:197]\n"
"v_mfma_f32_16x16x32_bf16 v[198:201],v[180:183],v[48:51],v[198:201]\n"
"s_waitcnt vmcnt(3)\n"
"v_mfma_f32_16x16x32_bf16 v[194:197],v[182:185],v[52:55],v[194:197]\n"
"v_mfma_f32_16x16x32_bf16 v[198:201],v[180:183],v[52:55],v[198:201]\n"
"s_waitcnt vmcnt(2)\n"
"v_mfma_f32_16x16x32_bf16 v[194:197],v[182:185],v[56:59],v[194:197]\n"
"v_mfma_f32_16x16x32_bf16 v[198:201],v[180:183],v[56:59],v[198:201]\n"
"s_waitcnt vmcnt(1)\n"
"v_mfma_f32_16x16x32_bf16 v[194:197],v[182:185],v[60:63],v[194:197]\n"
"v_mfma_f32_16x16x32_bf16 v[198:201],v[180:183],v[60:63],v[198:201]\n"
"s_nop 6\n"
"v_cvt_pk_bf16_f32 v176,v194,v195\n"
"v_cvt_pk_bf16_f32 v177,v196,v197\n"
"v_cvt_pk_bf16_f32 v178,v198,v199\n"
"v_cvt_pk_bf16_f32 v179,v200,v201\n"
"s_nop 0\n"
"v_permlane16_swap_b32 v176,v178\n"
"v_permlane16_swap_b32 v177,v179\n"
"buffer_store_dwordx4 v[176:179],v170,s[48:51],s11 offen sc0 sc1\n"
"s_waitcnt vmcnt(0)\n"
"Le_54:\n"
"s_endpgm\n"
"Lk_4x:\n"
"s_swappc_b64 s[70:71],s[72:73]\n"
"s_branch Lk_4\n"
"Lk_5x:\n"
"s_swappc_b64 s[70:71],s[74:75]\n"
"s_branch Lk_5\n"
"Lk_6x:\n"
"s_swappc_b64 s[70:71],s[76:77]\n"
"s_branch Lk_6\n"
"Lk_7x:\n"
"s_swappc_b64 s[70:71],s[72:73]\n"
"s_branch Lk_7\n"
"Lk_8x:\n"
"s_swappc_b64 s[70:71],s[74:75]\n"
"s_branch Lk_8\n"
"Lk_9x:\n"
"s_swappc_b64 s[70:71],s[76:77]\n"
"s_branch Lk_9\n"
"Lk_10x:\n"
"s_swappc_b64 s[70:71],s[72:73]\n"
"s_branch Lk_10\n"
"Lk_11x:\n"
"s_swappc_b64 s[70:71],s[74:75]\n"
"s_branch Lk_11\n"
"Lk_12x:\n"
"s_swappc_b64 s[70:71],s[76:77]\n"
"s_branch Lk_12\n"
"Lk_13x:\n"
"s_swappc_b64 s[70:71],s[72:73]\n"
"s_branch Lk_13\n"
"Lk_14x:\n"
"s_swappc_b64 s[70:71],s[74:75]\n"
"s_branch Lk_14\n"
"Lk_15x:\n"
"s_swappc_b64 s[70:71],s[76:77]\n"
"s_branch Lk_15\n"
"Lk_16x:\n"
"s_swappc_b64 s[70:71],s[72:73]\n"
"s_branch Lk_16\n"
"Lk_17x:\n"
"s_swappc_b64 s[70:71],s[74:75]\n"
"s_branch Lk_17\n"
"Lk_18x:\n"
"s_swappc_b64 s[70:71],s[76:77]\n"
"s_branch Lk_18\n"
"Lk_19x:\n"
"s_swappc_b64 s[70:71],s[72:73]\n"
"s_branch Lk_19\n"
"Lk_20x:\n"
"s_swappc_b64 s[70:71],s[74:75]\n"
"s_branch Lk_20\n"
"Lk_21x:\n"
"s_swappc_b64 s[70:71],s[76:77]\n"
"s_branch Lk_21\n"
"Lk_22x:\n"
"s_swappc_b64 s[70:71],s[72:73]\n"
"s_branch Lk_22\n"
"Lk_23x:\n"
"s_swappc_b64 s[70:71],s[74:75]\n"
"s_branch Lk_23\n"
"Lk_24x:\n"
"s_swappc_b64 s[70:71],s[76:77]\n"
"s_branch Lk_24\n"
"Lk_25x:\n"
"s_swappc_b64 s[70:71],s[72:73]\n"
"s_branch Lk_25\n"
"Lk_26x:\n"
"s_swappc_b64 s[70:71],s[74:75]\n"
"s_branch Lk_26\n"
"Lk_27x:\n"
"s_swappc_b64 s[70:71],s[76:77]\n"
"s_branch Lk_27\n"
"Lk_28x:\n"
"s_swappc_b64 s[70:71],s[72:73]\n"
"s_branch Lk_28\n"
"Lk_29x:\n"
"s_swappc_b64 s[70:71],s[74:75]\n"
"s_branch Lk_29\n"
"Lk_30x:\n"
"s_swappc_b64 s[70:71],s[76:77]\n"
"s_branch Lk_30\n"
"Lk_31x:\n"
"s_swappc_b64 s[70:71],s[72:73]\n"
"s_branch Lk_31\n"
"Lk_32x:\n"
"s_swappc_b64 s[70:71],s[74:75]\n"
"s_branch Lk_32\n"
"Lk_33x:\n"
"s_swappc_b64 s[70:71],s[76:77]\n"
"s_branch Lk_33\n"
"Lk_34x:\n"
"s_swappc_b64 s[70:71],s[72:73]\n"
"s_branch Lk_34\n"
"Lk_35x:\n"
"s_swappc_b64 s[70:71],s[74:75]\n"
"s_branch Lk_35\n"
"Lk_36x:\n"
"s_swappc_b64 s[70:71],s[76:77]\n"
"s_branch Lk_36\n"
"Lk_37x:\n"
"s_swappc_b64 s[70:71],s[72:73]\n"
"s_branch Lk_37\n"
"Lk_38x:\n"
"s_swappc_b64 s[70:71],s[74:75]\n"
"s_branch Lk_38\n"
"Lk_39x:\n"
"s_swappc_b64 s[70:71],s[76:77]\n"
"s_branch Lk_39\n"
"Lk_40x:\n"
"s_swappc_b64 s[70:71],s[72:73]\n"
"s_branch Lk_40\n"
"Lk_41x:\n"
"s_swappc_b64 s[70:71],s[74:75]\n"
"s_branch Lk_41\n"
"Lk_42x:\n"
"s_swappc_b64 s[70:71],s[76:77]\n"
"s_branch Lk_42\n"
"Lk_43x:\n"
"s_swappc_b64 s[70:71],s[72:73]\n"
"s_branch Lk_43\n"
"Lk_44x:\n"
"s_swappc_b64 s[70:71],s[74:75]\n"
"s_branch Lk_44\n"
"Lk_45x:\n"
"s_swappc_b64 s[70:71],s[76:77]\n"
"s_branch Lk_45\n"
"Lk_46x:\n"
"s_swappc_b64 s[70:71],s[72:73]\n"
"s_branch Lk_46\n"
"Lk_47x:\n"
"s_swappc_b64 s[70:71],s[74:75]\n"
"s_branch Lk_47\n"
"Lk_48x:\n"
"s_swappc_b64 s[70:71],s[76:77]\n"
"s_branch Lk_48\n"
"LsubA0:\n"
"s_nop 7\n"
"v_mov_b32 v249,v246\n"
"s_nop 1\n"
"v_permlane32_swap_b32 v246,v249\n"
"v_max_f32 v246,v246,v249\n"
"v_mov_b32 v249,v246\n"
"s_nop 1\n"
"v_permlane16_swap_b32 v246,v249\n"
"v_max_f32 v246,v246,v249\n"
"v_max_f32 v246,0,v246\n"
"v_sub_f32 v250,0,v246\n"
"v_exp_f32 v250,v250\n"
"v_sub_f32 v132,v132,v246\n"
"v_sub_f32 v133,v133,v246\n"
"v_sub_f32 v134,v134,v246\n"
"v_sub_f32 v135,v135,v246\n"
"s_nop 1\n"
"v_mfma_f32_16x16x32_bf16 v[108:111],v[156:159],a[0:3],v[132:135]\n"
"v_mfma_f32_16x16x32_bf16 v[112:115],v[172:175],a[0:3],v[132:135]\n"
"v_mfma_f32_16x16x32_bf16 v[108:111],v[160:163],a[12:15],v[108:111]\n"
"v_mfma_f32_16x16x32_bf16 v[112:115],v[176:179],a[12:15],v[112:115]\n"
"v_mfma_f32_16x16x32_bf16 v[108:111],v[164:167],a[24:27],v[108:111]\n"
"v_mfma_f32_16x16x32_bf16 v[112:115],v[180:183],a[24:27],v[112:115]\n"
"v_mfma_f32_16x16x32_bf16 v[108:111],v[168:171],a[36:39],v[108:111]\n"
"v_mfma_f32_16x16x32_bf16 v[112:115],v[184:187],a[36:39],v[112:115]\n"
"v_pk_mul_f32 v[0:1],v[0:1],v[250:251] op_sel_hi:[1,0]\n"
"v_pk_mul_f32 v[2:3],v[2:3],v[250:251] op_sel_hi:[1,0]\n"
"v_pk_mul_f32 v[4:5],v[4:5],v[250:251] op_sel_hi:[1,0]\n"
"v_pk_mul_f32 v[6:7],v[6:7],v[250:251] op_sel_hi:[1,0]\n"
"v_pk_mul_f32 v[8:9],v[8:9],v[250:251] op_sel_hi:[1,0]\n"
"v_pk_mul_f32 v[10:11],v[10:11],v[250:251] op_sel_hi:[1,0]\n"
"v_pk_mul_f32 v[12:13],v[12:13],v[250:251] op_sel_hi:[1,0]\n"
"v_pk_mul_f32 v[14:15],v[14:15],v[250:251] op_sel_hi:[1,0]\n"
"v_pk_mul_f32 v[16:17],v[16:17],v[250:251] op_sel_hi:[1,0]\n"
"v_pk_mul_f32 v[18:19],v[18:19],v[250:251] op_sel_hi:[1,0]\n"
"v_pk_mul_f32 v[20:21],v[20:21],v[250:251] op_sel_hi:[1,0]\n"
"v_pk_mul_f32 v[22:23],v[22:23],v[250:251] op_sel_hi:[1,0]\n"
"v_pk_mul_f32 v[24:25],v[24:25],v[250:251] op_sel_hi:[1,0]\n"
"v_pk_mul_f32 v[26:27],v[26:27],v[250:251] op_sel_hi:[1,0]\n"
"v_pk_mul_f32 v[28:29],v[28:29],v[250:251] op_sel_hi:[1,0]\n"
"v_pk_mul_f32 v[30:31],v[30:31],v[250:251] op_sel_hi:[1,0]\n"
"v_pk_mul_f32 v[96:97],v[96:97],v[250:251] op_sel_hi:[1,0]\n"
"v_pk_mul_f32 v[98:99],v[98:99],v[250:251] op_sel_hi:[1,0]\n"
"v_exp_f32 v108,v108\n"
"v_exp_f32 v109,v109\n"
"v_exp_f32 v110,v110\n"
"v_exp_f32 v111,v111\n"
"v_cvt_pk_bf16_f32 v144,v108,v109\n"
"v_exp_f32 v112,v112\n"
"v_exp_f32 v113,v113\n"
"v_cvt_pk_bf16_f32 v145,v110,v111\n"
"v_exp_f32 v114,v114\n"
"v_exp_f32 v115,v115\n"
"v_cvt_pk_bf16_f32 v146,v112,v113\n"
"v_cvt_pk_bf16_f32 v147,v114,v115\n"
"s_nop 1\n"
"s_setpc_b64 s[70:71]\n"
"LsubA1:\n"
"s_nop 7\n"
"v_mov_b32 v249,v247\n"
"s_nop 1\n"
"v_permlane32_swap_b32 v247,v249\n"
"v_max_f32 v247,v247,v249\n"
"v_mov_b32 v249,v247\n"
"s_nop 1\n"
"v_permlane16_swap_b32 v247,v249\n"
"v_max_f32 v247,v247,v249\n"
"v_max_f32 v247,0,v247\n"
"v_sub_f32 v250,0,v247\n"
"v_exp_f32 v250,v250\n"
"v_sub_f32 v136,v136,v247\n"
"v_sub_f32 v137,v137,v247\n"
"v_sub_f32 v138,v138,v247\n"
"v_sub_f32 v139,v139,v247\n"
"s_nop 1\n"
"v_mfma_f32_16x16x32_bf16 v[116:119],v[156:159],a[4:7],v[136:139]\n"
"v_mfma_f32_16x16x32_bf16 v[120:123],v[172:175],a[4:7],v[136:139]\n"
"v_mfma_f32_16x16x32_bf16 v[116:119],v[160:163],a[16:19],v[116:119]\n"
"v_mfma_f32_16x16x32_bf16 v[120:123],v[176:179],a[16:19],v[120:123]\n"
"v_mfma_f32_16x16x32_bf16 v[116:119],v[164:167],a[28:31],v[116:119]\n"
"v_mfma_f32_16x16x32_bf16 v[120:123],v[180:183],a[28:31],v[120:123]\n"
"v_mfma_f32_16x16x32_bf16 v[116:119],v[168:171],a[40:43],v[116:119]\n"
"v_mfma_f32_16x16x32_bf16 v[120:123],v[184:187],a[40:43],v[120:123]\n"
"v_pk_mul_f32 v[32:33],v[32:33],v[250:251] op_sel_hi:[1,0]\n"
"v_pk_mul_f32 v[34:35],v[34:35],v[250:251] op_sel_hi:[1,0]\n"
"v_pk_mul_f32 v[36:37],v[36:37],v[250:251] op_sel_hi:[1,0]\n"
"v_pk_mul_f32 v[38:39],v[38:39],v[250:251] op_sel_hi:[1,0]\n"
"v_pk_mul_f32 v[40:41],v[40:41],v[250:251] op_sel_hi:[1,0]\n"
"v_pk_mul_f32 v[42:43],v[42:43],v[250:251] op_sel_hi:[1,0]\n"
"v_pk_mul_f32 v[44:45],v[44:45],v[250:251] op_sel_hi:[1,0]\n"
"v_pk_mul_f32 v[46:47],v[46:47],v[250:251] op_sel_hi:[1,0]\n"
"v_pk_mul_f32 v[48:49],v[48:49],v[250:251] op_sel_hi:[1,0]\n"
"v_pk_mul_f32 v[50:51],v[50:51],v[250:251] op_sel_hi:[1,0]\n"
"v_pk_mul_f32 v[52:53],v[52:53],v[250:251] op_sel_hi:[1,0]\n"
"v_pk_mul_f32 v[54:55],v[54:55],v[250:251] op_sel_hi:[1,0]\n"
"v_pk_mul_f32 v[56:57],v[56:57],v[250:251] op_sel_hi:[1,0]\n"
"v_pk_mul_f32 v[58:59],v[58:59],v[250:251] op_sel_hi:[1,0]\n"
"v_pk_mul_f32 v[60:61],v[60:61],v[250:251] op_sel_hi:[1,0]\n"
"v_pk_mul_f32 v[62:63],v[62:63],v[250:251] op_sel_hi:[1,0]\n"
"v_pk_mul_f32 v[100:101],v[100:101],v[250:251] op_sel_hi:[1,0]\n"
"v_pk_mul_f32 v[102:103],v[102:103],v[250:251] op_sel_hi:[1,0]\n"
"v_exp_f32 v116,v116\n"
"v_exp_f32 v117,v117\n"
"v_exp_f32 v118,v118\n"
"v_exp_f32 v119,v119\n"
"v_cvt_pk_bf16_f32 v148,v116,v117\n"
"v_exp_f32 v120,v120\n"
"v_exp_f32 v121,v121\n"
"v_cvt_pk_bf16_f32 v149,v118,v119\n"
"v_exp_f32 v122,v122\n"
"v_exp_f32 v123,v123\n"
"v_cvt_pk_bf16_f32 v150,v120,v121\n"
"v_cvt_pk_bf16_f32 v151,v122,v123\n"
"s_nop 1\n"
"s_setpc_b64 s[70:71]\n"
"LsubA2:\n"
"s_nop 7\n"
"v_mov_b32 v249,v248\n"
"s_nop 1\n"
"v_permlane32_swap_b32 v248,v249\n"
"v_max_f32 v248,v248,v249\n"
"v_mov_b32 v249,v248\n"
"s_nop 1\n"
"v_permlane16_swap_b32 v248,v249\n"
"v_max_f32 v248,v248,v249\n"
"v_max_f32 v248,0,v248\n"
"v_sub_f32 v250,0,v248\n"
"v_exp_f32 v250,v250\n"
"v_sub_f32 v140,v140,v248\n"
"v_sub_f32 v141,v141,v248\n"
"v_sub_f32 v142,v142,v248\n"
"v_sub_f32 v143,v143,v248\n"
"s_nop 1\n"
"v_mfma_f32_16x16x32_bf16 v[124:127],v[156:159],a[8:11],v[140:143]\n"
"v_mfma_f32_16x16x32_bf16 v[128:131],v[172:175],a[8:11],v[140:143]\n"
"v_mfma_f32_16x16x32_bf16 v[124:127],v[160:163],a[20:23],v[124:127]\n"
"v_mfma_f32_16x16x32_bf16 v[128:131],v[176:179],a[20:23],v[128:131]\n"
"v_mfma_f32_16x16x32_bf16 v[124:127],v[164:167],a[32:35],v[124:127]\n"
"v_mfma_f32_16x16x32_bf16 v[128:131],v[180:183],a[32:35],v[128:131]\n"
"v_mfma_f32_16x16x32_bf16 v[124:127],v[168:171],a[44:47],v[124:127]\n"
"v_mfma_f32_16x16x32_bf16 v[128:131],v[184:187],a[44:47],v[128:131]\n"
"v_pk_mul_f32 v[64:65],v[64:65],v[250:251] op_sel_hi:[1,0]\n"
"v_pk_mul_f32 v[66:67],v[66:67],v[250:251] op_sel_hi:[1,0]\n"
"v_pk_mul_f32 v[68:69],v[68:69],v[250:251] op_sel_hi:[1,0]\n"
"v_pk_mul_f32 v[70:71],v[70:71],v[250:251] op_sel_hi:[1,0]\n"
"v_pk_mul_f32 v[72:73],v[72:73],v[250:251] op_sel_hi:[1,0]\n"
"v_pk_mul_f32 v[74:75],v[74:75],v[250:251] op_sel_hi:[1,0]\n"
"v_pk_mul_f32 v[76:77],v[76:77],v[250:251] op_sel_hi:[1,0]\n"
"v_pk_mul_f32 v[78:79],v[78:79],v[250:251] op_sel_hi:[1,0]\n"
"v_pk_mul_f32 v[80:81],v[80:81],v[250:251] op_sel_hi:[1,0]\n"
"v_pk_mul_f32 v[82:83],v[82:83],v[250:251] op_sel_hi:[1,0]\n"
"v_pk_mul_f32 v[84:85],v[84:85],v[250:251] op_sel_hi:[1,0]\n"
"v_pk_mul_f32 v[86:87],v[86:87],v[250:251] op_sel_hi:[1,0]\n"
"v_pk_mul_f32 v[88:89],v[88:89],v[250:251] op_sel_hi:[1,0]\n"
"v_pk_mul_f32 v[90:91],v[90:91],v[250:251] op_sel_hi:[1,0]\n"
"v_pk_mul_f32 v[92:93],v[92:93],v[250:251] op_sel_hi:[1,0]\n"
"v_pk_mul_f32 v[94:95],v[94:95],v[250:251] op_sel_hi:[1,0]\n"
"v_pk_mul_f32 v[104:105],v[104:105],v[250:251] op_sel_hi:[1,0]\n"
"v_pk_mul_f32 v[106:107],v[106:107],v[250:251] op_sel_hi:[1,0]\n"
"v_exp_f32 v124,v124\n"
"v_exp_f32 v125,v125\n"
"v_exp_f32 v126,v126\n"
"v_exp_f32 v127,v127\n"
"v_cvt_pk_bf16_f32 v152,v124,v125\n"
"v_exp_f32 v128,v128\n"
"v_exp_f32 v129,v129\n"
"v_cvt_pk_bf16_f32 v153,v126,v127\n"
"v_exp_f32 v130,v130\n"
"v_exp_f32 v131,v131\n"
"v_cvt_pk_bf16_f32 v154,v128,v129\n"
"v_cvt_pk_bf16_f32 v155,v130,v131\n"
"s_nop 1\n"
"s_setpc_b64 s[70:71]\n"
"LpathB:\n"
"s_waitcnt lgkmcnt(0)\n"
"s_lshl_b32 s66,s7,15\n"
"s_mov_b32 s59,s57\n"
"s_mov_b32 s68,s56\n"
"s_add_u32 s36,s14,0\n"
"s_addc_u32 s37,s15,0\n"
"s_and_b32 s37,s37,0xffff\n"
"s_mov_b32 s38,0x400000\n"
"s_mov_b32 s39,0x20000\n"
"s_lshl_b32 s60,s56,10\n"
"s_lshl_b32 s61,s7,8\n"
"s_add_u32 s60,s60,s61\n"
"v_lshlrev_b32 v246,4,v243\n"
"v_lshl_or_b32 v246,v244,16,v246\n"
"v_add_u32 v220,s60,v246\n"
"v_add_u32 v221,0x2000,v220\n"
"v_add_u32 v222,0x4000,v220\n"
"v_add_u32 v223,0x6000,v220\n"
"v_add_u32 v224,0x8000,v220\n"
"v_add_u32 v225,0xa000,v220\n"
"v_add_u32 v226,0xc000,v220\n"
"v_add_u32 v227,0xe000,v220\n"
"s_add_u32 s67,s59,0\n"
"s_and_b32 s67,s67,15\n"
"s_lshl_b32 s67,s67,18\n"
"buffer_load_dwordx4 v[96:99],v220,s[36:39],s67 offen\n"
"buffer_load_dwordx4 v[100:103],v221,s[36:39],s67 offen\n"
"buffer_load_dwordx4 v[104:107],v222,s[36:39],s67 offen\n"
"buffer_load_dwordx4 v[108:111],v223,s[36:39],s67 offen\n"
"buffer_load_dwordx4 v[112:115],v224,s[36:39],s67 offen\n"
"buffer_load_dwordx4 v[116:119],v225,s[36:39],s67 offen\n"
"buffer_load_dwordx4 v[120:123],v226,s[36:39],s67 offen\n"
"buffer_load_dwordx4 v[124:127],v227,s[36:39],s67 offen\n"
"s_add_u32 s67,s59,1\n"
"s_and_b32 s67,s67,15\n"
"s_lshl_b32 s67,s67,18\n"
"buffer_load_dwordx4 v[128:131],v220,s[36:39],s67 offen\n"
"buffer_load_dwordx4 v[132:135],v221,s[36:39],s67 offen\n"
"buffer_load_dwordx4 v[136:139],v222,s[36:39],s67 offen\n"
"buffer_load_dwordx4 v[140:143],v223,s[36:39],s67 offen\n"
"buffer_load_dwordx4 v[144:147],v224,s[36:39],s67 offen\n"
"buffer_load_dwordx4 v[148:151],v225,s[36:39],s67 offen\n"
"buffer_load_dwordx4 v[152:155],v226,s[36:39],s67 offen\n"
"buffer_load_dwordx4 v[156:159],v227,s[36:39],s67 offen\n"
"s_mul_i32 s60,s57,0xc000\n"
"s_add_u32 s32,s8,s60\n"
"s_addc_u32 s33,s9,0\n"
"s_and_b32 s33,s33,0xffff\n"
"s_mov_b32 s34,0xc000\n"
"s_mov_b32 s35,0x20000\n"
"v_and_b32 v250,0x300,v242\n"
"s_and_b32 s61,s7,1\n"
"s_lshl_b32 s61,s61,3\n"
"s_add_u32 s61,s61,0\n"
"s_lshl_b32 s67,s7,13\n"
"s_add_u32 s67,s67,0\n"
"s_add_u32 m0,s67,0x20000\n"
"s_add_u32 s60,s61,0\n"
"v_xor_b32 v246,s60,v243\n"
"v_lshl_or_b32 v246,v246,4,v250\n"
"s_add_u32 s60,s61,1\n"
"v_xor_b32 v247,s60,v243\n"
"v_lshl_or_b32 v247,v247,4,v250\n"
"s_add_u32 s60,s61,2\n"
"v_xor_b32 v248,s60,v243\n"
"v_lshl_or_b32 v248,v248,4,v250\n"
"s_add_u32 s60,s61,3\n"
"v_xor_b32 v249,s60,v243\n"
"v_lshl_or_b32 v249,v249,4,v250\n"
"buffer_load_dwordx4 v246,s[32:35],s67 offen lds\n"
"buffer_load_dwordx4 v247,s[32:35],s67 offen offset:1024 lds\n"
"buffer_load_dwordx4 v248,s[32:35],s67 offen offset:2048 lds\n"
"buffer_load_dwordx4 v249,s[32:35],s67 offen offset:3072 lds\n"
"s_and_b32 s61,s7,1\n"
"s_lshl_b32 s61,s61,3\n"
"s_add_u32 s61,s61,4\n"
"s_lshl_b32 s67,s7,13\n"
"s_add_u32 s67,s67,0x1000\n"
"s_add_u32 m0,s67,0x20000\n"
"s_add_u32 s60,s61,0\n"
"v_xor_b32 v246,s60,v243\n"
"v_lshl_or_b32 v246,v246,4,v250\n"
"s_add_u32 s60,s61,1\n"
"v_xor_b32 v247,s60,v243\n"
"v_lshl_or_b32 v247,v247,4,v250\n"
"s_add_u32 s60,s61,2\n"
"v_xor_b32 v248,s60,v243\n"
"v_lshl_or_b32 v248,v248,4,v250\n"
"s_add_u32 s60,s61,3\n"
"v_xor_b32 v249,s60,v243\n"
"v_lshl_or_b32 v249,v249,4,v250\n"
"buffer_load_dwordx4 v246,s[32:35],s67 offen lds\n"
"buffer_load_dwordx4 v247,s[32:35],s67 offen offset:1024 lds\n"
"buffer_load_dwordx4 v248,s[32:35],s67 offen offset:2048 lds\n"
"buffer_load_dwordx4 v249,s[32:35],s67 offen offset:3072 lds\n"
"s_lshl_b32 s61,s7,2\n"
"s_lshl_b32 s67,s7,12\n"
"s_add_u32 s67,s67,0x8000\n"
"s_add_u32 m0,s66,0x6000\n"
"s_add_u32 s60,s61,0\n"
"v_xor_b32 v246,s60,v243\n"
"v_lshl_or_b32 v246,v246,4,v250\n"
"s_add_u32 s60,s61,1\n"
"v_xor_b32 v247,s60,v243\n"
"v_lshl_or_b32 v247,v247,4,v250\n"
"s_add_u32 s60,s61,2\n"
"v_xor_b32 v248,s60,v243\n"
"v_lshl_or_b32 v248,v248,4,v250\n"
"s_add_u32 s60,s61,3\n"
"v_xor_b32 v249,s60,v243\n"
"v_lshl_or_b32 v249,v249,4,v250\n"
"buffer_load_dwordx4 v246,s[32:35],s67 offen lds\n"
"buffer_load_dwordx4 v247,s[32:35],s67 offen offset:1024 lds\n"
"buffer_load_dwordx4 v248,s[32:35],s67 offen offset:2048 lds\n"
"buffer_load_dwordx4 v249,s[32:35],s67 offen offset:3072 lds\n"
"s_add_u32 s60,s57,16\n"
"s_lshl_b32 s60,s60,13\n"
"s_add_u32 s60,s60,0x2000000\n"
"s_add_u32 s52,s20,s60\n"
"s_addc_u32 s53,s21,0\n"
"s_and_b32 s53,s53,0xffff\n"
"s_mov_b32 s54,0x2000\n"
"s_mov_b32 s55,0x20000\n"
"v_min_u32 v246,8,v243\n"
"v_lshlrev_b32 v246,2,v246\n"
"s_lshl_b32 s60,s68,7\n"
"s_add_u32 s60,s60,0x1000\n"
"buffer_load_dword v245,v246,s[52:55],s60 offen sc1\n"
"s_add_u32 s67,s59,2\n"
"s_and_b32 s67,s67,15\n"
"s_lshl_b32 s67,s67,18\n"
"buffer_load_dwordx4 v[160:163],v220,s[36:39],s67 offen\n"
"buffer_load_dwordx4 v[164:167],v221,s[36:39],s67 offen\n"
"buffer_load_dwordx4 v[168:171],v222,s[36:39],s67 offen\n"
"buffer_load_dwordx4 v[172:175],v223,s[36:39],s67 offen\n"
"buffer_load_dwordx4 v[176:179],v224,s[36:39],s67 offen\n"
"buffer_load_dwordx4 v[180:183],v225,s[36:39],s67 offen\n"
"buffer_load_dwordx4 v[184:187],v226,s[36:39],s67 offen\n"
"buffer_load_dwordx4 v[188:191],v227,s[36:39],s67 offen\n"
"s_lshl_b32 s60,s57,5\n"
"s_lshl_b32 s61,s56,2\n"
"s_add_u32 s60,s60,s61\n"
"s_add_u32 s60,s60,s7\n"
"s_lshl_b32 s60,s60,17\n"
"s_add_u32 s24,s10,s60\n"
"s_addc_u32 s25,s11,0\n"
"s_and_b32 s25,s25,0xffff\n"
"s_mov_b32 s26,0x20000\n"
"s_mov_b32 s27,0x20000\n"
"s_add_u32 s28,s12,s60\n"
"s_addc_u32 s29,s13,0\n"
"s_and_b32 s29,s29,0xffff\n"
"s_mov_b32 s30,0x20000\n"
"s_mov_b32 s31,0x20000\n"
"s_lshl_b32 s60,s56,19\n"
"s_add_u32 s40,s16,s60\n"
"s_addc_u32 s41,s17,0\n"
"s_and_b32 s41,s41,0xffff\n"
"s_mov_b32 s42,0x80000\n"
"s_mov_b32 s43,0x20000\n"
"s_mul_i32 s60,s57,0x62400\n"
"s_add_u32 s60,s60,0x800000\n"
"s_add_u32 s44,s20,s60\n"
"s_addc_u32 s45,s21,0\n"
"s_and_b32 s45,s45,0xffff\n"
"s_mov_b32 s46,0x62000\n"
"s_mov_b32 s47,0x20000\n"
"s_add_u32 s48,s22,0\n"
"s_addc_u32 s49,s23,0\n"
"s_and_b32 s49,s49,0xffff\n"
"s_mov_b32 s50,0x180000\n"
"s_mov_b32 s51,0x20000\n"
"v_or_b32 v246,0,v244\n"
"v_and_b32 v247,10,v246\n"
"v_and_b32 v248,1,v246\n"
"v_lshl_or_b32 v247,v248,2,v247\n"
"v_bfe_u32 v248,v246,2,1\n"
"v_or_b32 v247,v247,v248\n"
"v_xor_b32 v247,v247,v243\n"
"v_lshlrev_b32 v247,4,v247\n"
"v_lshl_or_b32 v232,v244,8,v247\n"
"v_or_b32 v246,4,v244\n"
"v_and_b32 v247,10,v246\n"
"v_and_b32 v248,1,v246\n"
"v_lshl_or_b32 v247,v248,2,v247\n"
"v_bfe_u32 v248,v246,2,1\n"
"v_or_b32 v247,v247,v248\n"
"v_xor_b32 v247,v247,v243\n"
"v_lshlrev_b32 v247,4,v247\n"
"v_lshl_or_b32 v233,v244,8,v247\n"
"v_or_b32 v246,8,v244\n"
"v_and_b32 v247,10,v246\n"
"v_and_b32 v248,1,v246\n"
"v_lshl_or_b32 v247,v248,2,v247\n"
"v_bfe_u32 v248,v246,2,1\n"
"v_or_b32 v247,v247,v248\n"
"v_xor_b32 v247,v247,v243\n"
"v_lshlrev_b32 v247,4,v247\n"
"v_lshl_or_b32 v234,v244,8,v247\n"
"v_or_b32 v246,12,v244\n"
"v_and_b32 v247,10,v246\n"
"v_and_b32 v248,1,v246\n"
"v_lshl_or_b32 v247,v248,2,v247\n"
"v_bfe_u32 v248,v246,2,1\n"
"v_or_b32 v247,v247,v248\n"
"v_xor_b32 v247,v247,v243\n"
"v_lshlrev_b32 v247,4,v247\n"
"v_lshl_or_b32 v235,v244,8,v247\n"
"s_mov_b32 s1,0\n"
"s_bfe_u32 s60,s57,0x10000\n"
"s_lshl_b32 s60,s60,13\n"
"s_or_b32 s1,s1,s60\n"
"s_bfe_u32 s60,s57,0x10001\n"
"s_lshl_b32 s60,s60,11\n"
"s_or_b32 s1,s1,s60\n"
"s_bfe_u32 s60,s57,0x10002\n"
"s_lshl_b32 s60,s60,12\n"
"s_or_b32 s1,s1,s60\n"
"s_bfe_u32 s60,s57,0x10003\n"
"s_lshl_b32 s60,s60,10\n"
"s_or_b32 s1,s1,s60\n"
"v_add_u32 v233,0x3c00,v233\n"
"v_add_u32 v234,0x7800,v234\n"
"v_add_u32 v235,0xb400,v235\n"
"v_add_u32 v254,0x7800,v236\n"
"v_add_u32 v255,0xb400,v237\n"
"v_add_u32 v237,0x3c00,v237\n"
"v_xor_b32 v246,v244,v243\n"
"v_lshlrev_b32 v246,4,v246\n"
"v_lshl_or_b32 v247,v243,10,v246\n"
"v_add_u32 v228,0x20000,v247\n"
"v_and_b32 v247,3,v243\n"
"v_lshl_or_b32 v246,v247,10,v246\n"
"v_lshrrev_b32 v247,2,v243\n"
"v_lshl_or_b32 v246,v247,15,v246\n"
"v_add_u32 v230,0x6000,v246\n"
"s_getpc_b64 s[70:71]\n"
"Lpc_56:\n"
"s_add_u32 s72,s70,LsubB0-Lpc_56\n"
"s_addc_u32 s73,s71,0\n"
"s_add_u32 s74,s70,LsubB1-Lpc_56\n"
"s_addc_u32 s75,s71,0\n"
"s_add_u32 s76,s70,LsubB2-Lpc_56\n"
"s_addc_u32 s77,s71,0\n"
"s_waitcnt vmcnt(9)\n"
"s_barrier\n"
"s_add_u32 s92,s57,15\n"
"s_lshl_b32 s92,s92,15\n"
"s_add_u32 s60,s59,0\n"
"s_and_b32 s60,s60,15\n"
"s_lshl_b32 s60,s60,6\n"
"v_xor_b32 v229,s60,v228\n"
"ds_read_b128 v[200:203],v229\n"
"ds_read_b128 v[204:207],v229 offset:16384\n"
"v_xor_b32 v231,s60,v230\n"
"ds_read_b128 v[208:211],v231\n"
"v_perm_b32 v192,v100,v96,s18\n"
"v_perm_b32 v193,v108,v104,s18\n"
"v_perm_b32 v194,v116,v112,s18\n"
"v_perm_b32 v195,v124,v120,s18\n"
"s_waitcnt lgkmcnt(2)\n"
"s_nop 0\n"
"v_mfma_f32_16x16x32_bf16 v[0:3],v[192:195],v[200:203],0\n"
"v_perm_b32 v196,v100,v96,s19\n"
"v_perm_b32 v197,v108,v104,s19\n"
"v_perm_b32 v198,v116,v112,s19\n"
"s_waitcnt lgkmcnt(1)\n"
"v_mfma_f32_16x16x32_bf16 v[4:7],v[192:195],v[204:207],0\n"
"v_perm_b32 v199,v124,v120,s19\n"
"s_waitcnt lgkmcnt(0)\n"
"v_mfma_f32_16x16x32_bf16 v[8:11],v[192:195],v[208:211],0\n"
"s_add_u32 s60,s59,1\n"
"s_and_b32 s60,s60,15\n"
"s_lshl_b32 s60,s60,6\n"
"v_xor_b32 v229,s60,v228\n"
"ds_read_b128 v[212:215],v229\n"
"v_mfma_f32_16x16x32_bf16 v[12:15],v[196:199],v[200:203],0\n"
"v_perm_b32 v192,v101,v97,s18\n"
"v_perm_b32 v193,v109,v105,s18\n"
"v_mfma_f32_16x16x32_bf16 v[16:19],v[196:199],v[204:207],0\n"
"v_perm_b32 v194,v117,v113,s18\n"
"v_mfma_f32_16x16x32_bf16 v[20:23],v[196:199],v[208:211],0\n"
"v_perm_b32 v195,v125,v121,s18\n"
"ds_read_b128 v[216:219],v229 offset:16384\n"
"s_nop 0\n"
"v_mfma_f32_16x16x32_bf16 v[24:27],v[192:195],v[200:203],0\n"
"v_perm_b32 v196,v101,v97,s19\n"
"v_perm_b32 v197,v109,v105,s19\n"
"v_mfma_f32_16x16x32_bf16 v[28:31],v[192:195],v[204:207],0\n"
"v_perm_b32 v198,v117,v113,s19\n"
"v_perm_b32 v199,v125,v121,s19\n"
"v_mfma_f32_16x16x32_bf16 v[32:35],v[192:195],v[208:211],0\n"
"v_xor_b32 v231,s60,v230\n"
"ds_read_b128 v[238:241],v231\n"
"v_mfma_f32_16x16x32_bf16 v[36:39],v[196:199],v[200:203],0\n"
"v_perm_b32 v192,v102,v98,s18\n"
"v_mfma_f32_16x16x32_bf16 v[40:43],v[196:199],v[204:207],0\n"
"v_perm_b32 v193,v110,v106,s18\n"
"v_perm_b32 v194,v118,v114,s18\n"
"v_mfma_f32_16x16x32_bf16 v[44:47],v[196:199],v[208:211],0\n"
"v_perm_b32 v195,v126,v122,s18\n"
"s_nop 1\n"
"v_mfma_f32_16x16x32_bf16 v[48:51],v[192:195],v[200:203],0\n"
"v_perm_b32 v196,v102,v98,s19\n"
"v_mfma_f32_16x16x32_bf16 v[52:55],v[192:195],v[204:207],0\n"
"v_perm_b32 v197,v110,v106,s19\n"
"v_perm_b32 v198,v118,v114,s19\n"
"v_mfma_f32_16x16x32_bf16 v[56:59],v[192:195],v[208:211],0\n"
"v_perm_b32 v199,v126,v122,s19\n"
"s_nop 1\n"
"v_mfma_f32_16x16x32_bf16 v[60:63],v[196:199],v[200:203],0\n"
"v_perm_b32 v192,v103,v99,s18\n"
"v_mfma_f32_16x16x32_bf16 v[64:67],v[196:199],v[204:207],0\n"
"v_perm_b32 v193,v111,v107,s18\n"
"v_perm_b32 v194,v119,v115,s18\n"
"v_mfma_f32_16x16x32_bf16 v[68:71],v[196:199],v[208:211],0\n"
"v_perm_b32 v195,v127,v123,s18\n"
"s_nop 1\n"
"v_mfma_f32_16x16x32_bf16 v[72:75],v[192:195],v[200:203],0\n"
"v_perm_b32 v196,v103,v99,s19\n"
"v_mfma_f32_16x16x32_bf16 v[76:79],v[192:195],v[204:207],0\n"
"v_perm_b32 v197,v111,v107,s19\n"
"v_perm_b32 v198,v119,v115,s19\n"
"v_mfma_f32_16x16x32_bf16 v[80:83],v[192:195],v[208:211],0\n"
"v_perm_b32 v199,v127,v123,s19\n"
"s_nop 1\n"
"v_mfma_f32_16x16x32_bf16 v[84:87],v[196:199],v[200:203],0\n"
"v_mfma_f32_16x16x32_bf16 v[88:91],v[196:199],v[204:207],0\n"
"v_mfma_f32_16x16x32_bf16 v[92:95],v[196:199],v[208:211],0\n"
"v_perm_b32 v192,v132,v128,s18\n"
"v_perm_b32 v193,v140,v136,s18\n"
"v_perm_b32 v194,v148,v144,s18\n"
"v_perm_b32 v195,v156,v152,s18\n"
"s_waitcnt lgkmcnt(0)\n"
"s_nop 0\n"
"v_mfma_f32_16x16x32_bf16 v[0:3],v[192:195],v[212:215],v[0:3]\n"
"v_perm_b32 v196,v132,v128,s19\n"
"v_perm_b32 v197,v140,v136,s19\n"
"v_perm_b32 v198,v148,v144,s19\n"
"v_mfma_f32_16x16x32_bf16 v[4:7],v[192:195],v[216:219],v[4:7]\n"
"v_perm_b32 v199,v156,v152,s19\n"
"s_add_u32 s60,s59,2\n"
"s_and_b32 s60,s60,15\n"
"s_lshl_b32 s60,s60,6\n"
"v_xor_b32 v229,s60,v228\n"
"ds_read_b128 v[200:203],v229\n"
"v_mfma_f32_16x16x32_bf16 v[8:11],v[192:195],v[238:241],v[8:11]\n"
"ds_read_b128 v[204:207],v229 offset:16384\n"
"v_mfma_f32_16x16x32_bf16 v[12:15],v[196:199],v[212:215],v[12:15]\n"
"v_perm_b32 v192,v133,v129,s18\n"
"v_perm_b32 v193,v141,v137,s18\n"
"v_perm_b32 v194,v149,v145,s18\n"
"v_mfma_f32_16x16x32_bf16 v[16:19],v[196:199],v[216:219],v[16:19]\n"
"v_perm_b32 v195,v157,v153,s18\n"
"v_xor_b32 v231,s60,v230\n"
"ds_read_b128 v[208:211],v231\n"
"v_mfma_f32_16x16x32_bf16 v[20:23],v[196:199],v[238:241],v[20:23]\n"
"s_add_u32 s67,s59,3\n"
"s_and_b32 s67,s67,15\n"
"s_lshl_b32 s67,s67,18\n"
"buffer_load_dwordx4 v[96:99],v220,s[36:39],s67 offen\n"
"v_mfma_f32_16x16x32_bf16 v[24:27],v[192:195],v[212:215],v[24:27]\n"
"v_perm_b32 v196,v133,v129,s19\n"
"v_perm_b32 v197,v141,v137,s19\n"
"v_mfma_f32_16x16x32_bf16 v[28:31],v[192:195],v[216:219],v[28:31]\n"
"v_perm_b32 v198,v149,v145,s19\n"
"v_perm_b32 v199,v157,v153,s19\n"
"v_mfma_f32_16x16x32_bf16 v[32:35],v[192:195],v[238:241],v[32:35]\n"
"buffer_load_dwordx4 v[100:103],v221,s[36:39],s67 offen\n"
"buffer_load_dwordx4 v[104:107],v222,s[36:39],s67 offen\n"
"v_mfma_f32_16x16x32_bf16 v[36:39],v[196:199],v[212:215],v[36:39]\n"
"v_perm_b32 v192,v134,v130,s18\n"
"v_perm_b32 v193,v142,v138,s18\n"
"v_mfma_f32_16x16x32_bf16 v[40:43],v[196:199],v[216:219],v[40:43]\n"
"v_perm_b32 v194,v150,v146,s18\n"
"v_perm_b32 v195,v158,v154,s18\n"
"v_mfma_f32_16x16x32_bf16 v[44:47],v[196:199],v[238:241],v[44:47]\n"
"buffer_load_dwordx4 v[108:111],v223,s[36:39],s67 offen\n"
"buffer_load_dwordx4 v[112:115],v224,s[36:39],s67 offen\n"
"v_mfma_f32_16x16x32_bf16 v[48:51],v[192:195],v[212:215],v[48:51]\n"
"v_perm_b32 v196,v134,v130,s19\n"
"v_perm_b32 v197,v142,v138,s19\n"
"v_mfma_f32_16x16x32_bf16 v[52:55],v[192:195],v[216:219],v[52:55]\n"
"v_perm_b32 v198,v150,v146,s19\n"
"v_perm_b32 v199,v158,v154,s19\n"
"v_mfma_f32_16x16x32_bf16 v[56:59],v[192:195],v[238:241],v[56:59]\n"
"buffer_load_dwordx4 v[116:119],v225,s[36:39],s67 offen\n"
"buffer_load_dwordx4 v[120:123],v226,s[36:39],s67 offen\n"
"v_mfma_f32_16x16x32_bf16 v[60:63],v[196:199],v[212:215],v[60:63]\n"
"v_perm_b32 v192,v135,v131,s18\n"
"v_perm_b32 v193,v143,v139,s18\n"
"v_mfma_f32_16x16x32_bf16 v[64:67],v[196:199],v[216:219],v[64:67]\n"
"v_perm_b32 v194,v151,v147,s18\n"
"v_mfma_f32_16x16x32_bf16 v[68:71],v[196:199],v[238:241],v[68:71]\n"
"v_perm_b32 v195,v159,v155,s18\n"
"buffer_load_dwordx4 v[124:127],v227,s[36:39],s67 offen\n"
"s_nop 0\n"
"v_mfma_f32_16x16x32_bf16 v[72:75],v[192:195],v[212:215],v[72:75]\n"
"v_perm_b32 v196,v135,v131,s19\n"
"v_mfma_f32_16x16x32_bf16 v[76:79],v[192:195],v[216:219],v[76:79]\n"
"v_perm_b32 v197,v143,v139,s19\n"
"v_perm_b32 v198,v151,v147,s19\n"
"v_mfma_f32_16x16x32_bf16 v[80:83],v[192:195],v[238:241],v[80:83]\n"
"v_perm_b32 v199,v159,v155,s19\n"
"s_nop 1\n"
"v_mfma_f32_16x16x32_bf16 v[84:87],v[196:199],v[212:215],v[84:87]\n"
"v_mfma_f32_16x16x32_bf16 v[88:91],v[196:199],v[216:219],v[88:91]\n"
"v_mfma_f32_16x16x32_bf16 v[92:95],v[196:199],v[238:241],v[92:95]\n"
"s_waitcnt vmcnt(14)\n"
"v_perm_b32 v192,v164,v160,s18\n"
"s_waitcnt vmcnt(12)\n"
"v_perm_b32 v193,v172,v168,s18\n"
"s_waitcnt vmcnt(10)\n"
"v_perm_b32 v194,v180,v176,s18\n"
"s_waitcnt vmcnt(8)\n"
"v_perm_b32 v195,v188,v184,s18\n"
"s_waitcnt lgkmcnt(0)\n"
"s_nop 0\n"
"v_mfma_f32_16x16x32_bf16 v[0:3],v[192:195],v[200:203],v[0:3]\n"
"v_perm_b32 v196,v164,v160,s19\n"
"v_perm_b32 v197,v172,v168,s19\n"
"v_perm_b32 v198,v180,v176,s19\n"
"v_mfma_f32_16x16x32_bf16 v[4:7],v[192:195],v[204:207],v[4:7]\n"
"v_perm_b32 v199,v188,v184,s19\n"
"s_add_u32 s60,s59,3\n"
"s_and_b32 s60,s60,15\n"
"s_lshl_b32 s60,s60,6\n"
"v_xor_b32 v229,s60,v228\n"
"ds_read_b128 v[212:215],v229\n"
"v_mfma_f32_16x16x32_bf16 v[8:11],v[192:195],v[208:211],v[8:11]\n"
"ds_read_b128 v[216:219],v229 offset:16384\n"
"v_mfma_f32_16x16x32_bf16 v[12:15],v[196:199],v[200:203],v[12:15]\n"
"v_perm_b32 v192,v165,v161,s18\n"
"v_perm_b32 v193,v173,v169,s18\n"
"v_perm_b32 v194,v181,v177,s18\n"
"v_mfma_f32_16x16x32_bf16 v[16:19],v[196:199],v[204:207],v[16:19]\n"
"v_perm_b32 v195,v189,v185,s18\n"
"v_xor_b32 v231,s60,v230\n"
"ds_read_b128 v[238:241],v231\n"
"v_mfma_f32_16x16x32_bf16 v[20:23],v[196:199],v[208:211],v[20:23]\n"
"s_add_u32 s67,s59,4\n"
"s_and_b32 s67,s67,15\n"
"s_lshl_b32 s67,s67,18\n"
"buffer_load_dwordx4 v[128:131],v220,s[36:39],s67 offen\n"
"v_mfma_f32_16x16x32_bf16 v[24:27],v[192:195],v[200:203],v[24:27]\n"
"v_perm_b32 v196,v165,v161,s19\n"
"v_perm_b32 v197,v173,v169,s19\n"
"v_mfma_f32_16x16x32_bf16 v[28:31],v[192:195],v[204:207],v[28:31]\n"
"v_perm_b32 v198,v181,v177,s19\n"
"v_perm_b32 v199,v189,v185,s19\n"
"v_mfma_f32_16x16x32_bf16 v[32:35],v[192:195],v[208:211],v[32:35]\n"
"buffer_load_dwordx4 v[132:135],v221,s[36:39],s67 offen\n"
"buffer_load_dwordx4 v[136:139],v222,s[36:39],s67 offen\n"
"v_mfma_f32_16x16x32_bf16 v[36:39],v[196:199],v[200:203],v[36:39]\n"
"v_perm_b32 v192,v166,v162,s18\n"
"v_perm_b32 v193,v174,v170,s18\n"
"v_mfma_f32_16x16x32_bf16 v[40:43],v[196:199],v[204:207],v[40:43]\n"
"v_perm_b32 v194,v182,v178,s18\n"
"v_perm_b32 v195,v190,v186,s18\n"
"v_mfma_f32_16x16x32_bf16 v[44:47],v[196:199],v[208:211],v[44:47]\n"
"buffer_load_dwordx4 v[140:143],v223,s[36:39],s67 offen\n"
"buffer_load_dwordx4 v[144:147],v224,s[36:39],s67 offen\n"
"v_mfma_f32_16x16x32_bf16 v[48:51],v[192:195],v[200:203],v[48:51]\n"
"v_perm_b32 v196,v166,v162,s19\n"
"v_perm_b32 v197,v174,v170,s19\n"
"v_mfma_f32_16x16x32_bf16 v[52:55],v[192:195],v[204:207],v[52:55]\n"
"v_perm_b32 v198,v182,v178,s19\n"
"v_perm_b32 v199,v190,v186,s19\n"
"v_mfma_f32_16x16x32_bf16 v[56:59],v[192:195],v[208:211],v[56:59]\n"
"buffer_load_dwordx4 v[148:151],v225,s[36:39],s67 offen\n"
"buffer_load_dwordx4 v[152:155],v226,s[36:39],s67 offen\n"
"v_mfma_f32_16x16x32_bf16 v[60:63],v[196:199],v[200:203],v[60:63]\n"
"v_perm_b32 v192,v167,v163,s18\n"
"v_perm_b32 v193,v175,v171,s18\n"
"v_mfma_f32_16x16x32_bf16 v[64:67],v[196:199],v[204:207],v[64:67]\n"
"v_perm_b32 v194,v183,v179,s18\n"
"v_mfma_f32_16x16x32_bf16 v[68:71],v[196:199],v[208:211],v[68:71]\n"
"v_perm_b32 v195,v191,v187,s18\n"
"buffer_load_dwordx4 v[156:159],v227,s[36:39],s67 offen\n"
"s_nop 0\n"
"v_mfma_f32_16x16x32_bf16 v[72:75],v[192:195],v[200:203],v[72:75]\n"
"v_perm_b32 v196,v167,v163,s19\n"
"v_mfma_f32_16x16x32_bf16 v[76:79],v[192:195],v[204:207],v[76:79]\n"
"v_perm_b32 v197,v175,v171,s19\n"
"v_perm_b32 v198,v183,v179,s19\n"
"v_mfma_f32_16x16x32_bf16 v[80:83],v[192:195],v[208:211],v[80:83]\n"
"v_perm_b32 v199,v191,v187,s19\n"
"s_nop 1\n"
"v_mfma_f32_16x16x32_bf16 v[84:87],v[196:199],v[200:203],v[84:87]\n"
"v_mfma_f32_16x16x32_bf16 v[88:91],v[196:199],v[204:207],v[88:91]\n"
"v_mfma_f32_16x16x32_bf16 v[92:95],v[196:199],v[208:211],v[92:95]\n"
"s_waitcnt vmcnt(14)\n"
"v_perm_b32 v192,v100,v96,s18\n"
"s_waitcnt vmcnt(12)\n"
"v_perm_b32 v193,v108,v104,s18\n"
"s_waitcnt vmcnt(10)\n"
"v_perm_b32 v194,v116,v112,s18\n"
"s_waitcnt vmcnt(8)\n"
"v_perm_b32 v195,v124,v120,s18\n"
"s_waitcnt lgkmcnt(0)\n"
"s_nop 0\n"
"v_mfma_f32_16x16x32_bf16 v[0:3],v[192:195],v[212:215],v[0:3]\n"
"v_perm_b32 v196,v100,v96,s19\n"
"v_perm_b32 v197,v108,v104,s19\n"
"v_perm_b32 v198,v116,v112,s19\n"
"v_mfma_f32_16x16x32_bf16 v[4:7],v[192:195],v[216:219],v[4:7]\n"
"v_perm_b32 v199,v124,v120,s19\n"
"s_add_u32 s60,s59,4\n"
"s_and_b32 s60,s60,15\n"
"s_lshl_b32 s60,s60,6\n"
"v_xor_b32 v229,s60,v228\n"
"ds_read_b128 v[200:203],v229\n"
"v_mfma_f32_16x16x32_bf16 v[8:11],v[192:195],v[238:241],v[8:11]\n"
"ds_read_b128 v[204:207],v229 offset:16384\n"
"v_mfma_f32_16x16x32_bf16 v[12:15],v[196:199],v[212:215],v[12:15]\n"
"v_perm_b32 v192,v101,v97,s18\n"
"v_perm_b32 v193,v109,v105,s18\n"
"v_perm_b32 v194,v117,v113,s18\n"
"v_mfma_f32_16x16x32_bf16 v[16:19],v[196:199],v[216:219],v[16:19]\n"
"v_perm_b32 v195,v125,v121,s18\n"
"v_xor_b32 v231,s60,v230\n"
"ds_read_b128 v[208:211],v231\n"
"v_mfma_f32_16x16x32_bf16 v[20:23],v[196:199],v[238:241],v[20:23]\n"
"s_add_u32 s67,s59,5\n"
"s_and_b32 s67,s67,15\n"
"s_lshl_b32 s67,s67,18\n"
"buffer_load_dwordx4 v[160:163],v220,s[36:39],s67 offen\n"
"v_mfma_f32_16x16x32_bf16 v[24:27],v[192:195],v[212:215],v[24:27]\n"
"v_perm_b32 v196,v101,v97,s19\n"
"v_perm_b32 v197,v109,v105,s19\n"
"v_mfma_f32_16x16x32_bf16 v[28:31],v[192:195],v[216:219],v[28:31]\n"
"v_perm_b32 v198,v117,v113,s19\n"
"v_perm_b32 v199,v125,v121,s19\n"
"v_mfma_f32_16x16x32_bf16 v[32:35],v[192:195],v[238:241],v[32:35]\n"
"buffer_load_dwordx4 v[164:167],v221,s[36:39],s67 offen\n"
"buffer_load_dwordx4 v[168:171],v222,s[36:39],s67 offen\n"
"v_mfma_f32_16x16x32_bf16 v[36:39],v[196:199],v[212:215],v[36:39]\n"
"v_perm_b32 v192,v102,v98,s18\n"
"v_perm_b32 v193,v110,v106,s18\n"
"v_mfma_f32_16x16x32_bf16 v[40:43],v[196:199],v[216:219],v[40:43]\n"
"v_perm_b32 v194,v118,v114,s18\n"
"v_perm_b32 v195,v126,v122,s18\n"
"v_mfma_f32_16x16x32_bf16 v[44:47],v[196:199],v[238:241],v[44:47]\n"
"buffer_load_dwordx4 v[172:175],v223,s[36:39],s67 offen\n"
"buffer_load_dwordx4 v[176:179],v224,s[36:39],s67 offen\n"
"v_mfma_f32_16x16x32_bf16 v[48:51],v[192:195],v[212:215],v[48:51]\n"
"v_perm_b32 v196,v102,v98,s19\n"
"v_perm_b32 v197,v110,v106,s19\n"
"v_mfma_f32_16x16x32_bf16 v[52:55],v[192:195],v[216:219],v[52:55]\n"
"v_perm_b32 v198,v118,v114,s19\n"
"v_perm_b32 v199,v126,v122,s19\n"
"v_mfma_f32_16x16x32_bf16 v[56:59],v[192:195],v[238:241],v[56:59]\n"
"buffer_load_dwordx4 v[180:183],v225,s[36:39],s67 offen\n"
"buffer_load_dwordx4 v[184:187],v226,s[36:39],s67 offen\n"
"v_mfma_f32_16x16x32_bf16 v[60:63],v[196:199],v[212:215],v[60:63]\n"
"v_perm_b32 v192,v103,v99,s18\n"
"v_perm_b32 v193,v111,v107,s18\n"
"v_mfma_f32_16x16x32_bf16 v[64:67],v[196:199],v[216:219],v[64:67]\n"
"v_perm_b32 v194,v119,v115,s18\n"
"v_mfma_f32_16x16x32_bf16 v[68:71],v[196:199],v[238:241],v[68:71]\n"
"v_perm_b32 v195,v127,v123,s18\n"
"buffer_load_dwordx4 v[188:191],v227,s[36:39],s67 offen\n"
"s_nop 0\n"
"v_mfma_f32_16x16x32_bf16 v[72:75],v[192:195],v[212:215],v[72:75]\n"
"v_perm_b32 v196,v103,v99,s19\n"
"v_mfma_f32_16x16x32_bf16 v[76:79],v[192:195],v[216:219],v[76:79]\n"
"v_perm_b32 v197,v111,v107,s19\n"
"v_perm_b32 v198,v119,v115,s19\n"
"v_mfma_f32_16x16x32_bf16 v[80:83],v[192:195],v[238:241],v[80:83]\n"
"v_perm_b32 v199,v127,v123,s19\n"
"s_nop 1\n"
"v_mfma_f32_16x16x32_bf16 v[84:87],v[196:199],v[212:215],v[84:87]\n"
"v_mfma_f32_16x16x32_bf16 v[88:91],v[196:199],v[216:219],v[88:91]\n"
"v_mfma_f32_16x16x32_bf16 v[92:95],v[196:199],v[238:241],v[92:95]\n"
"s_waitcnt vmcnt(14)\n"
"v_perm_b32 v192,v132,v128,s18\n"
"s_waitcnt vmcnt(12)\n"
"v_perm_b32 v193,v140,v136,s18\n"
"s_waitcnt vmcnt(10)\n"
"v_perm_b32 v194,v148,v144,s18\n"
"s_waitcnt vmcnt(8)\n"
"v_perm_b32 v195,v156,v152,s18\n"
"s_waitcnt lgkmcnt(0)\n"
"s_nop 0\n"
"v_mfma_f32_16x16x32_bf16 v[0:3],v[192:195],v[200:203],v[0:3]\n"
"v_perm_b32 v196,v132,v128,s19\n"
"v_perm_b32 v197,v140,v136,s19\n"
"v_perm_b32 v198,v148,v144,s19\n"
"v_mfma_f32_16x16x32_bf16 v[4:7],v[192:195],v[204:207],v[4:7]\n"
"v_perm_b32 v199,v156,v152,s19\n"
"s_add_u32 s60,s59,5\n"
"s_and_b32 s60,s60,15\n"
"s_lshl_b32 s60,s60,6\n"
"v_xor_b32 v229,s60,v228\n"
"ds_read_b128 v[212:215],v229\n"
"v_mfma_f32_16x16x32_bf16 v[8:11],v[192:195],v[208:211],v[8:11]\n"
"ds_read_b128 v[216:219],v229 offset:16384\n"
"v_mfma_f32_16x16x32_bf16 v[12:15],v[196:199],v[200:203],v[12:15]\n"
"v_perm_b32 v192,v133,v129,s18\n"
"v_perm_b32 v193,v141,v137,s18\n"
"v_perm_b32 v194,v149,v145,s18\n"
"v_mfma_f32_16x16x32_bf16 v[16:19],v[196:199],v[204:207],v[16:19]\n"
"v_perm_b32 v195,v157,v153,s18\n"
"v_xor_b32 v231,s60,v230\n"
"ds_read_b128 v[238:241],v231\n"
"v_mfma_f32_16x16x32_bf16 v[20:23],v[196:199],v[208:211],v[20:23]\n"
"s_add_u32 s67,s59,6\n"
"s_and_b32 s67,s67,15\n"
"s_lshl_b32 s67,s67,18\n"
"buffer_load_dwordx4 v[96:99],v220,s[36:39],s67 offen\n"
"v_mfma_f32_16x16x32_bf16 v[24:27],v[192:195],v[200:203],v[24:27]\n"
"v_perm_b32 v196,v133,v129,s19\n"
"v_perm_b32 v197,v141,v137,s19\n"
"v_mfma_f32_16x16x32_bf16 v[28:31],v[192:195],v[204:207],v[28:31]\n"
"v_perm_b32 v198,v149,v145,s19\n"
"v_perm_b32 v199,v157,v153,s19\n"
"v_mfma_f32_16x16x32_bf16 v[32:35],v[192:195],v[208:211],v[32:35]\n"
"buffer_load_dwordx4 v[100:103],v221,s[36:39],s67 offen\n"
"buffer_load_dwordx4 v[104:107],v222,s[36:39],s67 offen\n"
"v_mfma_f32_16x16x32_bf16 v[36:39],v[196:199],v[200:203],v[36:39]\n"
"v_perm_b32 v192,v134,v130,s18\n"
"v_perm_b32 v193,v142,v138,s18\n"
"v_mfma_f32_16x16x32_bf16 v[40:43],v[196:199],v[204:207],v[40:43]\n"
"v_perm_b32 v194,v150,v146,s18\n"
"v_perm_b32 v195,v158,v154,s18\n"
"v_mfma_f32_16x16x32_bf16 v[44:47],v[196:199],v[208:211],v[44:47]\n"
"buffer_load_dwordx4 v[108:111],v223,s[36:39],s67 offen\n"
"buffer_load_dwordx4 v[112:115],v224,s[36:39],s67 offen\n"
"v_mfma_f32_16x16x32_bf16 v[48:51],v[192:195],v[200:203],v[48:51]\n"
"v_perm_b32 v196,v134,v130,s19\n"
"v_perm_b32 v197,v142,v138,s19\n"
"v_mfma_f32_16x16x32_bf16 v[52:55],v[192:195],v[204:207],v[52:55]\n"
"v_perm_b32 v198,v150,v146,s19\n"
"v_perm_b32 v199,v158,v154,s19\n"
"v_mfma_f32_16x16x32_bf16 v[56:59],v[192:195],v[208:211],v[56:59]\n"
"buffer_load_dwordx4 v[116:119],v225,s[36:39],s67 offen\n"
"buffer_load_dwordx4 v[120:123],v226,s[36:39],s67 offen\n"
"v_mfma_f32_16x16x32_bf16 v[60:63],v[196:199],v[200:203],v[60:63]\n"
"v_perm_b32 v192,v135,v131,s18\n"
"v_perm_b32 v193,v143,v139,s18\n"
"v_mfma_f32_16x16x32_bf16 v[64:67],v[196:199],v[204:207],v[64:67]\n"
"v_perm_b32 v194,v151,v147,s18\n"
"v_mfma_f32_16x16x32_bf16 v[68:71],v[196:199],v[208:211],v[68:71]\n"
"v_perm_b32 v195,v159,v155,s18\n"
"buffer_load_dwordx4 v[124:127],v227,s[36:39],s67 offen\n"
"s_nop 0\n"
"v_mfma_f32_16x16x32_bf16 v[72:75],v[192:195],v[200:203],v[72:75]\n"
"v_perm_b32 v196,v135,v131,s19\n"
"v_mfma_f32_16x16x32_bf16 v[76:79],v[192:195],v[204:207],v[76:79]\n"
"v_perm_b32 v197,v143,v139,s19\n"
"v_perm_b32 v198,v151,v147,s19\n"
"v_mfma_f32_16x16x32_bf16 v[80:83],v[192:195],v[208:211],v[80:83]\n"
"v_perm_b32 v199,v159,v155,s19\n"
"s_nop 1\n"
"v_mfma_f32_16x16x32_bf16 v[84:87],v[196:199],v[200:203],v[84:87]\n"
"v_mfma_f32_16x16x32_bf16 v[88:91],v[196:199],v[204:207],v[88:91]\n"
"v_mfma_f32_16x16x32_bf16 v[92:95],v[196:199],v[208:211],v[92:95]\n"
"s_waitcnt vmcnt(14)\n"
"v_perm_b32 v192,v164,v160,s18\n"
"s_waitcnt vmcnt(12)\n"
"v_perm_b32 v193,v172,v168,s18\n"
"s_waitcnt vmcnt(10)\n"
"v_perm_b32 v194,v180,v176,s18\n"
"s_waitcnt vmcnt(8)\n"
"v_perm_b32 v195,v188,v184,s18\n"
"s_waitcnt lgkmcnt(0)\n"
"s_nop 0\n"
"v_mfma_f32_16x16x32_bf16 v[0:3],v[192:195],v[212:215],v[0:3]\n"
"v_perm_b32 v196,v164,v160,s19\n"
"v_perm_b32 v197,v172,v168,s19\n"
"v_perm_b32 v198,v180,v176,s19\n"
"v_mfma_f32_16x16x32_bf16 v[4:7],v[192:195],v[216:219],v[4:7]\n"
"v_perm_b32 v199,v188,v184,s19\n"
"s_add_u32 s60,s59,6\n"
"s_and_b32 s60,s60,15\n"
"s_lshl_b32 s60,s60,6\n"
"v_xor_b32 v229,s60,v228\n"
"ds_read_b128 v[200:203],v229\n"
"v_mfma_f32_16x16x32_bf16 v[8:11],v[192:195],v[238:241],v[8:11]\n"
"ds_read_b128 v[204:207],v229 offset:16384\n"
"v_mfma_f32_16x16x32_bf16 v[12:15],v[196:199],v[212:215],v[12:15]\n"
"v_perm_b32 v192,v165,v161,s18\n"
"v_perm_b32 v193,v173,v169,s18\n"
"v_perm_b32 v194,v181,v177,s18\n"
"v_mfma_f32_16x16x32_bf16 v[16:19],v[196:199],v[216:219],v[16:19]\n"
"v_perm_b32 v195,v189,v185,s18\n"
"v_xor_b32 v231,s60,v230\n"
"ds_read_b128 v[208:211],v231\n"
"v_mfma_f32_16x16x32_bf16 v[20:23],v[196:199],v[238:241],v[20:23]\n"
"s_add_u32 s67,s59,7\n"
"s_and_b32 s67,s67,15\n"
"s_lshl_b32 s67,s67,18\n"
"buffer_load_dwordx4 v[128:131],v220,s[36:39],s67 offen\n"
"v_mfma_f32_16x16x32_bf16 v[24:27],v[192:195],v[212:215],v[24:27]\n"
"v_perm_b32 v196,v165,v161,s19\n"
"v_perm_b32 v197,v173,v169,s19\n"
"v_mfma_f32_16x16x32_bf16 v[28:31],v[192:195],v[216:219],v[28:31]\n"
"v_perm_b32 v198,v181,v177,s19\n"
"v_perm_b32 v199,v189,v185,s19\n"
"v_mfma_f32_16x16x32_bf16 v[32:35],v[192:195],v[238:241],v[32:35]\n"
"buffer_load_dwordx4 v[132:135],v221,s[36:39],s67 offen\n"
"buffer_load_dwordx4 v[136:139],v222,s[36:39],s67 offen\n"
"v_mfma_f32_16x16x32_bf16 v[36:39],v[196:199],v[212:215],v[36:39]\n"
"v_perm_b32 v192,v166,v162,s18\n"
"v_perm_b32 v193,v174,v170,s18\n"
"v_mfma_f32_16x16x32_bf16 v[40:43],v[196:199],v[216:219],v[40:43]\n"
"v_perm_b32 v194,v182,v178,s18\n"
"v_perm_b32 v195,v190,v186,s18\n"
"v_mfma_f32_16x16x32_bf16 v[44:47],v[196:199],v[238:241],v[44:47]\n"
"buffer_load_dwordx4 v[140:143],v223,s[36:39],s67 offen\n"
"buffer_load_dwordx4 v[144:147],v224,s[36:39],s67 offen\n"
"v_mfma_f32_16x16x32_bf16 v[48:51],v[192:195],v[212:215],v[48:51]\n"
"v_perm_b32 v196,v166,v162,s19\n"
"v_perm_b32 v197,v174,v170,s19\n"
"v_mfma_f32_16x16x32_bf16 v[52:55],v[192:195],v[216:219],v[52:55]\n"
"v_perm_b32 v198,v182,v178,s19\n"
"v_perm_b32 v199,v190,v186,s19\n"
"v_mfma_f32_16x16x32_bf16 v[56:59],v[192:195],v[238:241],v[56:59]\n"
"buffer_load_dwordx4 v[148:151],v225,s[36:39],s67 offen\n"
"buffer_load_dwordx4 v[152:155],v226,s[36:39],s67 offen\n"
"v_mfma_f32_16x16x32_bf16 v[60:63],v[196:199],v[212:215],v[60:63]\n"
"v_perm_b32 v192,v167,v163,s18\n"
"v_perm_b32 v193,v175,v171,s18\n"
"v_mfma_f32_16x16x32_bf16 v[64:67],v[196:199],v[216:219],v[64:67]\n"
"v_perm_b32 v194,v183,v179,s18\n"
"v_mfma_f32_16x16x32_bf16 v[68:71],v[196:199],v[238:241],v[68:71]\n"
"v_perm_b32 v195,v191,v187,s18\n"
"buffer_load_dwordx4 v[156:159],v227,s[36:39],s67 offen\n"
"s_nop 0\n"
"v_mfma_f32_16x16x32_bf16 v[72:75],v[192:195],v[212:215],v[72:75]\n"
"v_perm_b32 v196,v167,v163,s19\n"
"v_mfma_f32_16x16x32_bf16 v[76:79],v[192:195],v[216:219],v[76:79]\n"
"v_perm_b32 v197,v175,v171,s19\n"
"v_perm_b32 v198,v183,v179,s19\n"
"v_mfma_f32_16x16x32_bf16 v[80:83],v[192:195],v[238:241],v[80:83]\n"
"v_perm_b32 v199,v191,v187,s19\n"
"s_nop 1\n"
"v_mfma_f32_16x16x32_bf16 v[84:87],v[196:199],v[212:215],v[84:87]\n"
"v_mfma_f32_16x16x32_bf16 v[88:91],v[196:199],v[216:219],v[88:91]\n"
"v_mfma_f32_16x16x32_bf16 v[92:95],v[196:199],v[238:241],v[92:95]\n"
"s_waitcnt vmcnt(14)\n"
"v_perm_b32 v192,v100,v96,s18\n"
"s_waitcnt vmcnt(12)\n"
"v_perm_b32 v193,v108,v104,s18\n"
"s_waitcnt vmcnt(10)\n"
"v_perm_b32 v194,v116,v112,s18\n"
"s_waitcnt vmcnt(8)\n"
"v_perm_b32 v195,v124,v120,s18\n"
"s_waitcnt lgkmcnt(0)\n"
"s_nop 0\n"
"v_mfma_f32_16x16x32_bf16 v[0:3],v[192:195],v[200:203],v[0:3]\n"
"v_perm_b32 v196,v100,v96,s19\n"
"v_perm_b32 v197,v108,v104,s19\n"
"v_perm_b32 v198,v116,v112,s19\n"
"v_mfma_f32_16x16x32_bf16 v[4:7],v[192:195],v[204:207],v[4:7]\n"
"v_perm_b32 v199,v124,v120,s19\n"
"s_add_u32 s60,s59,7\n"
"s_and_b32 s60,s60,15\n"
"s_lshl_b32 s60,s60,6\n"
"v_xor_b32 v229,s60,v228\n"
"ds_read_b128 v[212:215],v229\n"
"v_mfma_f32_16x16x32_bf16 v[8:11],v[192:195],v[208:211],v[8:11]\n"
"ds_read_b128 v[216:219],v229 offset:16384\n"
"v_mfma_f32_16x16x32_bf16 v[12:15],v[196:199],v[200:203],v[12:15]\n"
"v_perm_b32 v192,v101,v97,s18\n"
"v_perm_b32 v193,v109,v105,s18\n"
"v_perm_b32 v194,v117,v113,s18\n"
"v_mfma_f32_16x16x32_bf16 v[16:19],v[196:199],v[204:207],v[16:19]\n"
"v_perm_b32 v195,v125,v121,s18\n"
"v_xor_b32 v231,s60,v230\n"
"ds_read_b128 v[238:241],v231\n"
"v_mfma_f32_16x16x32_bf16 v[20:23],v[196:199],v[208:211],v[20:23]\n"
"s_add_u32 s67,s59,8\n"
"s_and_b32 s67,s67,15\n"
"s_lshl_b32 s67,s67,18\n"
"buffer_load_dwordx4 v[160:163],v220,s[36:39],s67 offen\n"
"v_mfma_f32_16x16x32_bf16 v[24:27],v[192:195],v[200:203],v[24:27]\n"
"v_perm_b32 v196,v101,v97,s19\n"
"v_perm_b32 v197,v109,v105,s19\n"
"v_mfma_f32_16x16x32_bf16 v[28:31],v[192:195],v[204:207],v[28:31]\n"
"v_perm_b32 v198,v117,v113,s19\n"
"v_perm_b32 v199,v125,v121,s19\n"
"v_mfma_f32_16x16x32_bf16 v[32:35],v[192:195],v[208:211],v[32:35]\n"
"buffer_load_dwordx4 v[164:167],v221,s[36:39],s67 offen\n"
"buffer_load_dwordx4 v[168:171],v222,s[36:39],s67 offen\n"
"v_mfma_f32_16x16x32_bf16 v[36:39],v[196:199],v[200:203],v[36:39]\n"
"v_perm_b32 v192,v102,v98,s18\n"
"v_perm_b32 v193,v110,v106,s18\n"
"v_mfma_f32_16x16x32_bf16 v[40:43],v[196:199],v[204:207],v[40:43]\n"
"v_perm_b32 v194,v118,v114,s18\n"
"v_perm_b32 v195,v126,v122,s18\n"
"v_mfma_f32_16x16x32_bf16 v[44:47],v[196:199],v[208:211],v[44:47]\n"
"buffer_load_dwordx4 v[172:175],v223,s[36:39],s67 offen\n"
"buffer_load_dwordx4 v[176:179],v224,s[36:39],s67 offen\n"
"v_mfma_f32_16x16x32_bf16 v[48:51],v[192:195],v[200:203],v[48:51]\n"
"v_perm_b32 v196,v102,v98,s19\n"
"v_perm_b32 v197,v110,v106,s19\n"
"v_mfma_f32_16x16x32_bf16 v[52:55],v[192:195],v[204:207],v[52:55]\n"
"v_perm_b32 v198,v118,v114,s19\n"
"v_perm_b32 v199,v126,v122,s19\n"
"v_mfma_f32_16x16x32_bf16 v[56:59],v[192:195],v[208:211],v[56:59]\n"
"buffer_load_dwordx4 v[180:183],v225,s[36:39],s67 offen\n"
"buffer_load_dwordx4 v[184:187],v226,s[36:39],s67 offen\n"
"v_mfma_f32_16x16x32_bf16 v[60:63],v[196:199],v[200:203],v[60:63]\n"
"v_perm_b32 v192,v103,v99,s18\n"
"v_perm_b32 v193,v111,v107,s18\n"
"v_mfma_f32_16x16x32_bf16 v[64:67],v[196:199],v[204:207],v[64:67]\n"
"v_perm_b32 v194,v119,v115,s18\n"
"v_mfma_f32_16x16x32_bf16 v[68:71],v[196:199],v[208:211],v[68:71]\n"
"v_perm_b32 v195,v127,v123,s18\n"
"buffer_load_dwordx4 v[188:191],v227,s[36:39],s67 offen\n"
"s_nop 0\n"
"v_mfma_f32_16x16x32_bf16 v[72:75],v[192:195],v[200:203],v[72:75]\n"
"v_perm_b32 v196,v103,v99,s19\n"
"v_mfma_f32_16x16x32_bf16 v[76:79],v[192:195],v[204:207],v[76:79]\n"
"v_perm_b32 v197,v111,v107,s19\n"
"v_perm_b32 v198,v119,v115,s19\n"
"v_mfma_f32_16x16x32_bf16 v[80:83],v[192:195],v[208:211],v[80:83]\n"
"v_perm_b32 v199,v127,v123,s19\n"
"s_nop 1\n"
"v_mfma_f32_16x16x32_bf16 v[84:87],v[196:199],v[200:203],v[84:87]\n"
"v_mfma_f32_16x16x32_bf16 v[88:91],v[196:199],v[204:207],v[88:91]\n"
"v_mfma_f32_16x16x32_bf16 v[92:95],v[196:199],v[208:211],v[92:95]\n"
"s_waitcnt vmcnt(14)\n"
"v_perm_b32 v192,v132,v128,s18\n"
"s_waitcnt vmcnt(12)\n"
"v_perm_b32 v193,v140,v136,s18\n"
"s_waitcnt vmcnt(10)\n"
"v_perm_b32 v194,v148,v144,s18\n"
"s_waitcnt vmcnt(8)\n"
"v_perm_b32 v195,v156,v152,s18\n"
"s_waitcnt lgkmcnt(0)\n"
"s_nop 0\n"
"v_mfma_f32_16x16x32_bf16 v[0:3],v[192:195],v[212:215],v[0:3]\n"
"v_perm_b32 v196,v132,v128,s19\n"
"v_perm_b32 v197,v140,v136,s19\n"
"v_perm_b32 v198,v148,v144,s19\n"
"v_mfma_f32_16x16x32_bf16 v[4:7],v[192:195],v[216:219],v[4:7]\n"
"v_perm_b32 v199,v156,v152,s19\n"
"s_add_u32 s60,s59,8\n"
"s_and_b32 s60,s60,15\n"
"s_lshl_b32 s60,s60,6\n"
"v_xor_b32 v229,s60,v228\n"
"ds_read_b128 v[200:203],v229\n"
"v_mfma_f32_16x16x32_bf16 v[8:11],v[192:195],v[238:241],v[8:11]\n"
"ds_read_b128 v[204:207],v229 offset:16384\n"
"v_mfma_f32_16x16x32_bf16 v[12:15],v[196:199],v[212:215],v[12:15]\n"
"v_perm_b32 v192,v133,v129,s18\n"
"v_perm_b32 v193,v141,v137,s18\n"
"v_perm_b32 v194,v149,v145,s18\n"
"v_mfma_f32_16x16x32_bf16 v[16:19],v[196:199],v[216:219],v[16:19]\n"
"v_perm_b32 v195,v157,v153,s18\n"
"v_xor_b32 v231,s60,v230\n"
"ds_read_b128 v[208:211],v231\n"
"v_mfma_f32_16x16x32_bf16 v[20:23],v[196:199],v[238:241],v[20:23]\n"
"s_add_u32 s67,s59,9\n"
"s_and_b32 s67,s67,15\n"
"s_lshl_b32 s67,s67,18\n"
"buffer_load_dwordx4 v[96:99],v220,s[36:39],s67 offen\n"
"v_mfma_f32_16x16x32_bf16 v[24:27],v[192:195],v[212:215],v[24:27]\n"
"v_perm_b32 v196,v133,v129,s19\n"
"v_perm_b32 v197,v141,v137,s19\n"
"v_mfma_f32_16x16x32_bf16 v[28:31],v[192:195],v[216:219],v[28:31]\n"
"v_perm_b32 v198,v149,v145,s19\n"
"v_perm_b32 v199,v157,v153,s19\n"
"v_mfma_f32_16x16x32_bf16 v[32:35],v[192:195],v[238:241],v[32:35]\n"
"buffer_load_dwordx4 v[100:103],v221,s[36:39],s67 offen\n"
"buffer_load_dwordx4 v[104:107],v222,s[36:39],s67 offen\n"
"v_mfma_f32_16x16x32_bf16 v[36:39],v[196:199],v[212:215],v[36:39]\n"
"v_perm_b32 v192,v134,v130,s18\n"
"v_perm_b32 v193,v142,v138,s18\n"
"v_mfma_f32_16x16x32_bf16 v[40:43],v[196:199],v[216:219],v[40:43]\n"
"v_perm_b32 v194,v150,v146,s18\n"
"v_perm_b32 v195,v158,v154,s18\n"
"v_mfma_f32_16x16x32_bf16 v[44:47],v[196:199],v[238:241],v[44:47]\n"
"buffer_load_dwordx4 v[108:111],v223,s[36:39],s67 offen\n"
"buffer_load_dwordx4 v[112:115],v224,s[36:39],s67 offen\n"
"v_mfma_f32_16x16x32_bf16 v[48:51],v[192:195],v[212:215],v[48:51]\n"
"v_perm_b32 v196,v134,v130,s19\n"
"v_perm_b32 v197,v142,v138,s19\n"
"v_mfma_f32_16x16x32_bf16 v[52:55],v[192:195],v[216:219],v[52:55]\n"
"v_perm_b32 v198,v150,v146,s19\n"
"v_perm_b32 v199,v158,v154,s19\n"
"v_mfma_f32_16x16x32_bf16 v[56:59],v[192:195],v[238:241],v[56:59]\n"
"buffer_load_dwordx4 v[116:119],v225,s[36:39],s67 offen\n"
"buffer_load_dwordx4 v[120:123],v226,s[36:39],s67 offen\n"
"v_mfma_f32_16x16x32_bf16 v[60:63],v[196:199],v[212:215],v[60:63]\n"
"v_perm_b32 v192,v135,v131,s18\n"
"v_perm_b32 v193,v143,v139,s18\n"
"v_mfma_f32_16x16x32_bf16 v[64:67],v[196:199],v[216:219],v[64:67]\n"
"v_perm_b32 v194,v151,v147,s18\n"
"v_mfma_f32_16x16x32_bf16 v[68:71],v[196:199],v[238:241],v[68:71]\n"
"v_perm_b32 v195,v159,v155,s18\n"
"buffer_load_dwordx4 v[124:127],v227,s[36:39],s67 offen\n"
"s_nop 0\n"
"v_mfma_f32_16x16x32_bf16 v[72:75],v[192:195],v[212:215],v[72:75]\n"
"v_perm_b32 v196,v135,v131,s19\n"
"v_mfma_f32_16x16x32_bf16 v[76:79],v[192:195],v[216:219],v[76:79]\n"
"v_perm_b32 v197,v143,v139,s19\n"
"v_perm_b32 v198,v151,v147,s19\n"
"v_mfma_f32_16x16x32_bf16 v[80:83],v[192:195],v[238:241],v[80:83]\n"
"v_perm_b32 v199,v159,v155,s19\n"
"s_nop 1\n"
"v_mfma_f32_16x16x32_bf16 v[84:87],v[196:199],v[212:215],v[84:87]\n"
"v_mfma_f32_16x16x32_bf16 v[88:91],v[196:199],v[216:219],v[88:91]\n"
"v_mfma_f32_16x16x32_bf16 v[92:95],v[196:199],v[238:241],v[92:95]\n"
"s_waitcnt vmcnt(14)\n"
"v_perm_b32 v192,v164,v160,s18\n"
"s_waitcnt vmcnt(12)\n"
"v_perm_b32 v193,v172,v168,s18\n"
"s_waitcnt vmcnt(10)\n"
"v_perm_b32 v194,v180,v176,s18\n"
"s_waitcnt vmcnt(8)\n"
"v_perm_b32 v195,v188,v184,s18\n"
"s_waitcnt lgkmcnt(0)\n"
"s_nop 0\n"
"v_mfma_f32_16x16x32_bf16 v[0:3],v[192:195],v[200:203],v[0:3]\n"
"v_perm_b32 v196,v164,v160,s19\n"
"v_perm_b32 v197,v172,v168,s19\n"
"v_perm_b32 v198,v180,v176,s19\n"
"v_mfma_f32_16x16x32_bf16 v[4:7],v[192:195],v[204:207],v[4:7]\n"
"v_perm_b32 v199,v188,v184,s19\n"
"s_add_u32 s60,s59,9\n"
"s_and_b32 s60,s60,15\n"
"s_lshl_b32 s60,s60,6\n"
"v_xor_b32 v229,s60,v228\n"
"ds_read_b128 v[212:215],v229\n"
"v_mfma_f32_16x16x32_bf16 v[8:11],v[192:195],v[208:211],v[8:11]\n"
"ds_read_b128 v[216:219],v229 offset:16384\n"
"v_mfma_f32_16x16x32_bf16 v[12:15],v[196:199],v[200:203],v[12:15]\n"
"v_perm_b32 v192,v165,v161,s18\n"
"v_perm_b32 v193,v173,v169,s18\n"
"v_perm_b32 v194,v181,v177,s18\n"
"v_mfma_f32_16x16x32_bf16 v[16:19],v[196:199],v[204:207],v[16:19]\n"
"v_perm_b32 v195,v189,v185,s18\n"
"v_xor_b32 v231,s60,v230\n"
"ds_read_b128 v[238:241],v231\n"
"v_mfma_f32_16x16x32_bf16 v[20:23],v[196:199],v[208:211],v[20:23]\n"
"s_add_u32 s67,s59,10\n"
"s_and_b32 s67,s67,15\n"
"s_lshl_b32 s67,s67,18\n"
"buffer_load_dwordx4 v[128:131],v220,s[36:39],s67 offen\n"
"v_mfma_f32_16x16x32_bf16 v[24:27],v[192:195],v[200:203],v[24:27]\n"
"v_perm_b32 v196,v165,v161,s19\n"
"v_perm_b32 v197,v173,v169,s19\n"
"v_mfma_f32_16x16x32_bf16 v[28:31],v[192:195],v[204:207],v[28:31]\n"
"v_perm_b32 v198,v181,v177,s19\n"
"v_perm_b32 v199,v189,v185,s19\n"
"v_mfma_f32_16x16x32_bf16 v[32:35],v[192:195],v[208:211],v[32:35]\n"
"buffer_load_dwordx4 v[132:135],v221,s[36:39],s67 offen\n"
"buffer_load_dwordx4 v[136:139],v222,s[36:39],s67 offen\n"
"v_mfma_f32_16x16x32_bf16 v[36:39],v[196:199],v[200:203],v[36:39]\n"
"v_perm_b32 v192,v166,v162,s18\n"
"v_perm_b32 v193,v174,v170,s18\n"
"v_mfma_f32_16x16x32_bf16 v[40:43],v[196:199],v[204:207],v[40:43]\n"
"v_perm_b32 v194,v182,v178,s18\n"
"v_perm_b32 v195,v190,v186,s18\n"
"v_mfma_f32_16x16x32_bf16 v[44:47],v[196:199],v[208:211],v[44:47]\n"
"buffer_load_dwordx4 v[140:143],v223,s[36:39],s67 offen\n"
"buffer_load_dwordx4 v[144:147],v224,s[36:39],s67 offen\n"
"v_mfma_f32_16x16x32_bf16 v[48:51],v[192:195],v[200:203],v[48:51]\n"
"v_perm_b32 v196,v166,v162,s19\n"
"v_perm_b32 v197,v174,v170,s19\n"
"v_mfma_f32_16x16x32_bf16 v[52:55],v[192:195],v[204:207],v[52:55]\n"
"v_perm_b32 v198,v182,v178,s19\n"
"v_perm_b32 v199,v190,v186,s19\n"
"v_mfma_f32_16x16x32_bf16 v[56:59],v[192:195],v[208:211],v[56:59]\n"
"buffer_load_dwordx4 v[148:151],v225,s[36:39],s67 offen\n"
"buffer_load_dwordx4 v[152:155],v226,s[36:39],s67 offen\n"
"v_mfma_f32_16x16x32_bf16 v[60:63],v[196:199],v[200:203],v[60:63]\n"
"v_perm_b32 v192,v167,v163,s18\n"
"v_perm_b32 v193,v175,v171,s18\n"
"v_mfma_f32_16x16x32_bf16 v[64:67],v[196:199],v[204:207],v[64:67]\n"
"v_perm_b32 v194,v183,v179,s18\n"
"v_mfma_f32_16x16x32_bf16 v[68:71],v[196:199],v[208:211],v[68:71]\n"
"v_perm_b32 v195,v191,v187,s18\n"
"buffer_load_dwordx4 v[156:159],v227,s[36:39],s67 offen\n"
"s_nop 0\n"
"v_mfma_f32_16x16x32_bf16 v[72:75],v[192:195],v[200:203],v[72:75]\n"
"v_perm_b32 v196,v167,v163,s19\n"
"v_mfma_f32_16x16x32_bf16 v[76:79],v[192:195],v[204:207],v[76:79]\n"
"v_perm_b32 v197,v175,v171,s19\n"
"v_perm_b32 v198,v183,v179,s19\n"
"v_mfma_f32_16x16x32_bf16 v[80:83],v[192:195],v[208:211],v[80:83]\n"
"v_perm_b32 v199,v191,v187,s19\n"
"s_nop 1\n"
"v_mfma_f32_16x16x32_bf16 v[84:87],v[196:199],v[200:203],v[84:87]\n"
"v_mfma_f32_16x16x32_bf16 v[88:91],v[196:199],v[204:207],v[88:91]\n"
"v_mfma_f32_16x16x32_bf16 v[92:95],v[196:199],v[208:211],v[92:95]\n"
"s_waitcnt vmcnt(14)\n"
"v_perm_b32 v192,v100,v96,s18\n"
"s_waitcnt vmcnt(12)\n"
"v_perm_b32 v193,v108,v104,s18\n"
"s_waitcnt vmcnt(10)\n"
"v_perm_b32 v194,v116,v112,s18\n"
"s_waitcnt vmcnt(8)\n"
"v_perm_b32 v195,v124,v120,s18\n"
"s_waitcnt lgkmcnt(0)\n"
"s_nop 0\n"
"v_mfma_f32_16x16x32_bf16 v[0:3],v[192:195],v[212:215],v[0:3]\n"
"v_perm_b32 v196,v100,v96,s19\n"
"v_perm_b32 v197,v108,v104,s19\n"
"v_perm_b32 v198,v116,v112,s19\n"
"v_mfma_f32_16x16x32_bf16 v[4:7],v[192:195],v[216:219],v[4:7]\n"
"v_perm_b32 v199,v124,v120,s19\n"
"s_add_u32 s60,s59,10\n"
"s_and_b32 s60,s60,15\n"
"s_lshl_b32 s60,s60,6\n"
"v_xor_b32 v229,s60,v228\n"
"ds_read_b128 v[200:203],v229\n"
"v_mfma_f32_16x16x32_bf16 v[8:11],v[192:195],v[238:241],v[8:11]\n"
"ds_read_b128 v[204:207],v229 offset:16384\n"
"v_mfma_f32_16x16x32_bf16 v[12:15],v[196:199],v[212:215],v[12:15]\n"
"v_perm_b32 v192,v101,v97,s18\n"
"v_perm_b32 v193,v109,v105,s18\n"
"v_perm_b32 v194,v117,v113,s18\n"
"v_mfma_f32_16x16x32_bf16 v[16:19],v[196:199],v[216:219],v[16:19]\n"
"v_perm_b32 v195,v125,v121,s18\n"
"v_xor_b32 v231,s60,v230\n"
"ds_read_b128 v[208:211],v231\n"
"v_mfma_f32_16x16x32_bf16 v[20:23],v[196:199],v[238:241],v[20:23]\n"
"s_add_u32 s67,s59,11\n"
"s_and_b32 s67,s67,15\n"
"s_lshl_b32 s67,s67,18\n"
"buffer_load_dwordx4 v[160:163],v220,s[36:39],s67 offen\n"
"v_mfma_f32_16x16x32_bf16 v[24:27],v[192:195],v[212:215],v[24:27]\n"
"v_perm_b32 v196,v101,v97,s19\n"
"v_perm_b32 v197,v109,v105,s19\n"
"v_mfma_f32_16x16x32_bf16 v[28:31],v[192:195],v[216:219],v[28:31]\n"
"v_perm_b32 v198,v117,v113,s19\n"
"v_perm_b32 v199,v125,v121,s19\n"
"v_mfma_f32_16x16x32_bf16 v[32:35],v[192:195],v[238:241],v[32:35]\n"
"buffer_load_dwordx4 v[164:167],v221,s[36:39],s67 offen\n"
"buffer_load_dwordx4 v[168:171],v222,s[36:39],s67 offen\n"
"v_mfma_f32_16x16x32_bf16 v[36:39],v[196:199],v[212:215],v[36:39]\n"
"v_perm_b32 v192,v102,v98,s18\n"
"v_perm_b32 v193,v110,v106,s18\n"
"v_mfma_f32_16x16x32_bf16 v[40:43],v[196:199],v[216:219],v[40:43]\n"
"v_perm_b32 v194,v118,v114,s18\n"
"v_perm_b32 v195,v126,v122,s18\n"
"v_mfma_f32_16x16x32_bf16 v[44:47],v[196:199],v[238:241],v[44:47]\n"
"buffer_load_dwordx4 v[172:175],v223,s[36:39],s67 offen\n"
"buffer_load_dwordx4 v[176:179],v224,s[36:39],s67 offen\n"
"v_mfma_f32_16x16x32_bf16 v[48:51],v[192:195],v[212:215],v[48:51]\n"
"v_perm_b32 v196,v102,v98,s19\n"
"v_perm_b32 v197,v110,v106,s19\n"
"v_mfma_f32_16x16x32_bf16 v[52:55],v[192:195],v[216:219],v[52:55]\n"
"v_perm_b32 v198,v118,v114,s19\n"
"v_perm_b32 v199,v126,v122,s19\n"
"v_mfma_f32_16x16x32_bf16 v[56:59],v[192:195],v[238:241],v[56:59]\n"
"buffer_load_dwordx4 v[180:183],v225,s[36:39],s67 offen\n"
"buffer_load_dwordx4 v[184:187],v226,s[36:39],s67 offen\n"
"v_mfma_f32_16x16x32_bf16 v[60:63],v[196:199],v[212:215],v[60:63]\n"
"v_perm_b32 v192,v103,v99,s18\n"
"v_perm_b32 v193,v111,v107,s18\n"
"v_mfma_f32_16x16x32_bf16 v[64:67],v[196:199],v[216:219],v[64:67]\n"
"v_perm_b32 v194,v119,v115,s18\n"
"v_mfma_f32_16x16x32_bf16 v[68:71],v[196:199],v[238:241],v[68:71]\n"
"v_perm_b32 v195,v127,v123,s18\n"
"buffer_load_dwordx4 v[188:191],v227,s[36:39],s67 offen\n"
"s_nop 0\n"
"v_mfma_f32_16x16x32_bf16 v[72:75],v[192:195],v[212:215],v[72:75]\n"
"v_perm_b32 v196,v103,v99,s19\n"
"v_mfma_f32_16x16x32_bf16 v[76:79],v[192:195],v[216:219],v[76:79]\n"
"v_perm_b32 v197,v111,v107,s19\n"
"v_perm_b32 v198,v119,v115,s19\n"
"v_mfma_f32_16x16x32_bf16 v[80:83],v[192:195],v[238:241],v[80:83]\n"
"v_perm_b32 v199,v127,v123,s19\n"
"s_nop 1\n"
"v_mfma_f32_16x16x32_bf16 v[84:87],v[196:199],v[212:215],v[84:87]\n"
"v_mfma_f32_16x16x32_bf16 v[88:91],v[196:199],v[216:219],v[88:91]\n"
"v_mfma_f32_16x16x32_bf16 v[92:95],v[196:199],v[238:241],v[92:95]\n"
"s_waitcnt vmcnt(14)\n"
"v_perm_b32 v192,v132,v128,s18\n"
"s_waitcnt vmcnt(12)\n"
"v_perm_b32 v193,v140,v136,s18\n"
"s_waitcnt vmcnt(10)\n"
"v_perm_b32 v194,v148,v144,s18\n"
"s_waitcnt vmcnt(8)\n"
"v_perm_b32 v195,v156,v152,s18\n"
"s_waitcnt lgkmcnt(0)\n"
"s_nop 0\n"
"v_mfma_f32_16x16x32_bf16 v[0:3],v[192:195],v[200:203],v[0:3]\n"
"v_perm_b32 v196,v132,v128,s19\n"
"v_perm_b32 v197,v140,v136,s19\n"
"v_perm_b32 v198,v148,v144,s19\n"
"v_mfma_f32_16x16x32_bf16 v[4:7],v[192:195],v[204:207],v[4:7]\n"
"v_perm_b32 v199,v156,v152,s19\n"
"s_add_u32 s60,s59,11\n"
"s_and_b32 s60,s60,15\n"
"s_lshl_b32 s60,s60,6\n"
"v_xor_b32 v229,s60,v228\n"
"ds_read_b128 v[212:215],v229\n"
"v_mfma_f32_16x16x32_bf16 v[8:11],v[192:195],v[208:211],v[8:11]\n"
"ds_read_b128 v[216:219],v229 offset:16384\n"
"v_mfma_f32_16x16x32_bf16 v[12:15],v[196:199],v[200:203],v[12:15]\n"
"v_perm_b32 v192,v133,v129,s18\n"
"v_perm_b32 v193,v141,v137,s18\n"
"v_perm_b32 v194,v149,v145,s18\n"
"v_mfma_f32_16x16x32_bf16 v[16:19],v[196:199],v[204:207],v[16:19]\n"
"v_perm_b32 v195,v157,v153,s18\n"
"v_xor_b32 v231,s60,v230\n"
"ds_read_b128 v[238:241],v231\n"
"v_mfma_f32_16x16x32_bf16 v[20:23],v[196:199],v[208:211],v[20:23]\n"
"s_add_u32 s67,s59,12\n"
"s_and_b32 s67,s67,15\n"
"s_lshl_b32 s67,s67,18\n"
"buffer_load_dwordx4 v[96:99],v220,s[36:39],s67 offen\n"
"v_mfma_f32_16x16x32_bf16 v[24:27],v[192:195],v[200:203],v[24:27]\n"
"v_perm_b32 v196,v133,v129,s19\n"
"v_perm_b32 v197,v141,v137,s19\n"
"v_mfma_f32_16x16x32_bf16 v[28:31],v[192:195],v[204:207],v[28:31]\n"
"v_perm_b32 v198,v149,v145,s19\n"
"v_perm_b32 v199,v157,v153,s19\n"
"v_mfma_f32_16x16x32_bf16 v[32:35],v[192:195],v[208:211],v[32:35]\n"
"buffer_load_dwordx4 v[100:103],v221,s[36:39],s67 offen\n"
"buffer_load_dwordx4 v[104:107],v222,s[36:39],s67 offen\n"
"v_mfma_f32_16x16x32_bf16 v[36:39],v[196:199],v[200:203],v[36:39]\n"
"v_perm_b32 v192,v134,v130,s18\n"
"v_perm_b32 v193,v142,v138,s18\n"
"v_mfma_f32_16x16x32_bf16 v[40:43],v[196:199],v[204:207],v[40:43]\n"
"v_perm_b32 v194,v150,v146,s18\n"
"v_perm_b32 v195,v158,v154,s18\n"
"v_mfma_f32_16x16x32_bf16 v[44:47],v[196:199],v[208:211],v[44:47]\n"
"buffer_load_dwordx4 v[108:111],v223,s[36:39],s67 offen\n"
"buffer_load_dwordx4 v[112:115],v224,s[36:39],s67 offen\n"
"v_mfma_f32_16x16x32_bf16 v[48:51],v[192:195],v[200:203],v[48:51]\n"
"v_perm_b32 v196,v134,v130,s19\n"
"v_perm_b32 v197,v142,v138,s19\n"
"v_mfma_f32_16x16x32_bf16 v[52:55],v[192:195],v[204:207],v[52:55]\n"
"v_perm_b32 v198,v150,v146,s19\n"
"v_perm_b32 v199,v158,v154,s19\n"
"v_mfma_f32_16x16x32_bf16 v[56:59],v[192:195],v[208:211],v[56:59]\n"
"buffer_load_dwordx4 v[116:119],v225,s[36:39],s67 offen\n"
"buffer_load_dwordx4 v[120:123],v226,s[36:39],s67 offen\n"
"v_mfma_f32_16x16x32_bf16 v[60:63],v[196:199],v[200:203],v[60:63]\n"
"v_perm_b32 v192,v135,v131,s18\n"
"v_perm_b32 v193,v143,v139,s18\n"
"v_mfma_f32_16x16x32_bf16 v[64:67],v[196:199],v[204:207],v[64:67]\n"
"v_perm_b32 v194,v151,v147,s18\n"
"v_mfma_f32_16x16x32_bf16 v[68:71],v[196:199],v[208:211],v[68:71]\n"
"v_perm_b32 v195,v159,v155,s18\n"
"buffer_load_dwordx4 v[124:127],v227,s[36:39],s67 offen\n"
"s_nop 0\n"
"v_mfma_f32_16x16x32_bf16 v[72:75],v[192:195],v[200:203],v[72:75]\n"
"v_perm_b32 v196,v135,v131,s19\n"
"v_mfma_f32_16x16x32_bf16 v[76:79],v[192:195],v[204:207],v[76:79]\n"
"v_perm_b32 v197,v143,v139,s19\n"
"v_perm_b32 v198,v151,v147,s19\n"
"v_mfma_f32_16x16x32_bf16 v[80:83],v[192:195],v[208:211],v[80:83]\n"
"v_perm_b32 v199,v159,v155,s19\n"
"s_nop 1\n"
"v_mfma_f32_16x16x32_bf16 v[84:87],v[196:199],v[200:203],v[84:87]\n"
"v_mfma_f32_16x16x32_bf16 v[88:91],v[196:199],v[204:207],v[88:91]\n"
"v_mfma_f32_16x16x32_bf16 v[92:95],v[196:199],v[208:211],v[92:95]\n"
"s_waitcnt vmcnt(14)\n"
"v_perm_b32 v192,v164,v160,s18\n"
"s_waitcnt vmcnt(12)\n"
"v_perm_b32 v193,v172,v168,s18\n"
"s_waitcnt vmcnt(10)\n"
"v_perm_b32 v194,v180,v176,s18\n"
"s_waitcnt vmcnt(8)\n"
"v_perm_b32 v195,v188,v184,s18\n"
"s_waitcnt lgkmcnt(0)\n"
"s_nop 0\n"
"v_mfma_f32_16x16x32_bf16 v[0:3],v[192:195],v[212:215],v[0:3]\n"
"v_perm_b32 v196,v164,v160,s19\n"
"v_perm_b32 v197,v172,v168,s19\n"
"v_perm_b32 v198,v180,v176,s19\n"
"v_mfma_f32_16x16x32_bf16 v[4:7],v[192:195],v[216:219],v[4:7]\n"
"v_perm_b32 v199,v188,v184,s19\n"
"s_add_u32 s60,s59,12\n"
"s_and_b32 s60,s60,15\n"
"s_lshl_b32 s60,s60,6\n"
"v_xor_b32 v229,s60,v228\n"
"ds_read_b128 v[200:203],v229\n"
"v_mfma_f32_16x16x32_bf16 v[8:11],v[192:195],v[238:241],v[8:11]\n"
"ds_read_b128 v[204:207],v229 offset:16384\n"
"v_mfma_f32_16x16x32_bf16 v[12:15],v[196:199],v[212:215],v[12:15]\n"
"v_perm_b32 v192,v165,v161,s18\n"
"v_perm_b32 v193,v173,v169,s18\n"
"v_perm_b32 v194,v181,v177,s18\n"
"v_mfma_f32_16x16x32_bf16 v[16:19],v[196:199],v[216:219],v[16:19]\n"
"v_perm_b32 v195,v189,v185,s18\n"
"v_xor_b32 v231,s60,v230\n"
"ds_read_b128 v[208:211],v231\n"
"v_mfma_f32_16x16x32_bf16 v[20:23],v[196:199],v[238:241],v[20:23]\n"
"s_add_u32 s67,s59,13\n"
"s_and_b32 s67,s67,15\n"
"s_lshl_b32 s67,s67,18\n"
"buffer_load_dwordx4 v[128:131],v220,s[36:39],s67 offen\n"
"v_mfma_f32_16x16x32_bf16 v[24:27],v[192:195],v[212:215],v[24:27]\n"
"v_perm_b32 v196,v165,v161,s19\n"
"v_perm_b32 v197,v173,v169,s19\n"
"v_mfma_f32_16x16x32_bf16 v[28:31],v[192:195],v[216:219],v[28:31]\n"
"v_perm_b32 v198,v181,v177,s19\n"
"v_perm_b32 v199,v189,v185,s19\n"
"v_mfma_f32_16x16x32_bf16 v[32:35],v[192:195],v[238:241],v[32:35]\n"
"buffer_load_dwordx4 v[132:135],v221,s[36:39],s67 offen\n"
"buffer_load_dwordx4 v[136:139],v222,s[36:39],s67 offen\n"
"v_mfma_f32_16x16x32_bf16 v[36:39],v[196:199],v[212:215],v[36:39]\n"
"v_perm_b32 v192,v166,v162,s18\n"
"v_perm_b32 v193,v174,v170,s18\n"
"v_mfma_f32_16x16x32_bf16 v[40:43],v[196:199],v[216:219],v[40:43]\n"
"v_perm_b32 v194,v182,v178,s18\n"
"v_perm_b32 v195,v190,v186,s18\n"
"v_mfma_f32_16x16x32_bf16 v[44:47],v[196:199],v[238:241],v[44:47]\n"
"buffer_load_dwordx4 v[140:143],v223,s[36:39],s67 offen\n"
"buffer_load_dwordx4 v[144:147],v224,s[36:39],s67 offen\n"
"v_mfma_f32_16x16x32_bf16 v[48:51],v[192:195],v[212:215],v[48:51]\n"
"v_perm_b32 v196,v166,v162,s19\n"
"v_perm_b32 v197,v174,v170,s19\n"
"v_mfma_f32_16x16x32_bf16 v[52:55],v[192:195],v[216:219],v[52:55]\n"
"v_perm_b32 v198,v182,v178,s19\n"
"v_perm_b32 v199,v190,v186,s19\n"
"v_mfma_f32_16x16x32_bf16 v[56:59],v[192:195],v[238:241],v[56:59]\n"
"buffer_load_dwordx4 v[148:151],v225,s[36:39],s67 offen\n"
"buffer_load_dwordx4 v[152:155],v226,s[36:39],s67 offen\n"
"v_mfma_f32_16x16x32_bf16 v[60:63],v[196:199],v[212:215],v[60:63]\n"
"v_perm_b32 v192,v167,v163,s18\n"
"v_perm_b32 v193,v175,v171,s18\n"
"v_mfma_f32_16x16x32_bf16 v[64:67],v[196:199],v[216:219],v[64:67]\n"
"v_perm_b32 v194,v183,v179,s18\n"
"v_mfma_f32_16x16x32_bf16 v[68:71],v[196:199],v[238:241],v[68:71]\n"
"v_perm_b32 v195,v191,v187,s18\n"
"buffer_load_dwordx4 v[156:159],v227,s[36:39],s67 offen\n"
"s_nop 0\n"
"v_mfma_f32_16x16x32_bf16 v[72:75],v[192:195],v[212:215],v[72:75]\n"
"v_perm_b32 v196,v167,v163,s19\n"
"v_mfma_f32_16x16x32_bf16 v[76:79],v[192:195],v[216:219],v[76:79]\n"
"v_perm_b32 v197,v175,v171,s19\n"
"v_perm_b32 v198,v183,v179,s19\n"
"v_mfma_f32_16x16x32_bf16 v[80:83],v[192:195],v[238:241],v[80:83]\n"
"v_perm_b32 v199,v191,v187,s19\n"
"s_nop 1\n"
"v_mfma_f32_16x16x32_bf16 v[84:87],v[196:199],v[212:215],v[84:87]\n"
"v_mfma_f32_16x16x32_bf16 v[88:91],v[196:199],v[216:219],v[88:91]\n"
"v_mfma_f32_16x16x32_bf16 v[92:95],v[196:199],v[238:241],v[92:95]\n"
"s_waitcnt vmcnt(14)\n"
"v_perm_b32 v192,v100,v96,s18\n"
"s_waitcnt vmcnt(12)\n"
"v_perm_b32 v193,v108,v104,s18\n"
"s_waitcnt vmcnt(10)\n"
"v_perm_b32 v194,v116,v112,s18\n"
"s_waitcnt vmcnt(8)\n"
"v_perm_b32 v195,v124,v120,s18\n"
"s_waitcnt lgkmcnt(0)\n"
"s_nop 0\n"
"v_mfma_f32_16x16x32_bf16 v[0:3],v[192:195],v[200:203],v[0:3]\n"
"v_perm_b32 v196,v100,v96,s19\n"
"v_perm_b32 v197,v108,v104,s19\n"
"v_perm_b32 v198,v116,v112,s19\n"
"v_mfma_f32_16x16x32_bf16 v[4:7],v[192:195],v[204:207],v[4:7]\n"
"v_perm_b32 v199,v124,v120,s19\n"
"s_add_u32 s60,s59,13\n"
"s_and_b32 s60,s60,15\n"
"s_lshl_b32 s60,s60,6\n"
"v_xor_b32 v229,s60,v228\n"
"ds_read_b128 v[212:215],v229\n"
"v_mfma_f32_16x16x32_bf16 v[8:11],v[192:195],v[208:211],v[8:11]\n"
"ds_read_b128 v[216:219],v229 offset:16384\n"
"v_mfma_f32_16x16x32_bf16 v[12:15],v[196:199],v[200:203],v[12:15]\n"
"v_perm_b32 v192,v101,v97,s18\n"
"v_perm_b32 v193,v109,v105,s18\n"
"v_perm_b32 v194,v117,v113,s18\n"
"v_mfma_f32_16x16x32_bf16 v[16:19],v[196:199],v[204:207],v[16:19]\n"
"v_perm_b32 v195,v125,v121,s18\n"
"v_xor_b32 v231,s60,v230\n"
"ds_read_b128 v[238:241],v231\n"
"v_mfma_f32_16x16x32_bf16 v[20:23],v[196:199],v[208:211],v[20:23]\n"
"s_add_u32 s67,s59,14\n"
"s_and_b32 s67,s67,15\n"
"s_lshl_b32 s67,s67,18\n"
"buffer_load_dwordx4 v[160:163],v220,s[36:39],s67 offen\n"
"v_mfma_f32_16x16x32_bf16 v[24:27],v[192:195],v[200:203],v[24:27]\n"
"v_perm_b32 v196,v101,v97,s19\n"
"v_perm_b32 v197,v109,v105,s19\n"
"v_mfma_f32_16x16x32_bf16 v[28:31],v[192:195],v[204:207],v[28:31]\n"
"v_perm_b32 v198,v117,v113,s19\n"
"v_perm_b32 v199,v125,v121,s19\n"
"v_mfma_f32_16x16x32_bf16 v[32:35],v[192:195],v[208:211],v[32:35]\n"
"buffer_load_dwordx4 v[164:167],v221,s[36:39],s67 offen\n"
"buffer_load_dwordx4 v[168:171],v222,s[36:39],s67 offen\n"
"v_mfma_f32_16x16x32_bf16 v[36:39],v[196:199],v[200:203],v[36:39]\n"
"v_perm_b32 v192,v102,v98,s18\n"
"v_perm_b32 v193,v110,v106,s18\n"
"v_mfma_f32_16x16x32_bf16 v[40:43],v[196:199],v[204:207],v[40:43]\n"
"v_perm_b32 v194,v118,v114,s18\n"
"v_perm_b32 v195,v126,v122,s18\n"
"v_mfma_f32_16x16x32_bf16 v[44:47],v[196:199],v[208:211],v[44:47]\n"
"buffer_load_dwordx4 v[172:175],v223,s[36:39],s67 offen\n"
"buffer_load_dwordx4 v[176:179],v224,s[36:39],s67 offen\n"
"v_mfma_f32_16x16x32_bf16 v[48:51],v[192:195],v[200:203],v[48:51]\n"
"v_perm_b32 v196,v102,v98,s19\n"
"v_perm_b32 v197,v110,v106,s19\n"
"v_mfma_f32_16x16x32_bf16 v[52:55],v[192:195],v[204:207],v[52:55]\n"
"v_perm_b32 v198,v118,v114,s19\n"
"v_perm_b32 v199,v126,v122,s19\n"
"v_mfma_f32_16x16x32_bf16 v[56:59],v[192:195],v[208:211],v[56:59]\n"
"buffer_load_dwordx4 v[180:183],v225,s[36:39],s67 offen\n"
"buffer_load_dwordx4 v[184:187],v226,s[36:39],s67 offen\n"
"v_mfma_f32_16x16x32_bf16 v[60:63],v[196:199],v[200:203],v[60:63]\n"
"v_perm_b32 v192,v103,v99,s18\n"
"v_perm_b32 v193,v111,v107,s18\n"
"v_mfma_f32_16x16x32_bf16 v[64:67],v[196:199],v[204:207],v[64:67]\n"
"v_perm_b32 v194,v119,v115,s18\n"
"v_mfma_f32_16x16x32_bf16 v[68:71],v[196:199],v[208:211],v[68:71]\n"
"v_perm_b32 v195,v127,v123,s18\n"
"buffer_load_dwordx4 v[188:191],v227,s[36:39],s67 offen\n"
"s_nop 0\n"
"v_mfma_f32_16x16x32_bf16 v[72:75],v[192:195],v[200:203],v[72:75]\n"
"v_perm_b32 v196,v103,v99,s19\n"
"v_mfma_f32_16x16x32_bf16 v[76:79],v[192:195],v[204:207],v[76:79]\n"
"v_perm_b32 v197,v111,v107,s19\n"
"v_perm_b32 v198,v119,v115,s19\n"
"v_mfma_f32_16x16x32_bf16 v[80:83],v[192:195],v[208:211],v[80:83]\n"
"v_perm_b32 v199,v127,v123,s19\n"
"s_nop 1\n"
"v_mfma_f32_16x16x32_bf16 v[84:87],v[196:199],v[200:203],v[84:87]\n"
"v_mfma_f32_16x16x32_bf16 v[88:91],v[196:199],v[204:207],v[88:91]\n"
"v_mfma_f32_16x16x32_bf16 v[92:95],v[196:199],v[208:211],v[92:95]\n"
"s_waitcnt vmcnt(14)\n"
"v_perm_b32 v192,v132,v128,s18\n"
"s_waitcnt vmcnt(12)\n"
"v_perm_b32 v193,v140,v136,s18\n"
"s_waitcnt vmcnt(10)\n"
"v_perm_b32 v194,v148,v144,s18\n"
"s_waitcnt vmcnt(8)\n"
"v_perm_b32 v195,v156,v152,s18\n"
"s_waitcnt lgkmcnt(0)\n"
"s_nop 0\n"
"v_mfma_f32_16x16x32_bf16 v[0:3],v[192:195],v[212:215],v[0:3]\n"
"v_perm_b32 v196,v132,v128,s19\n"
"v_perm_b32 v197,v140,v136,s19\n"
"v_perm_b32 v198,v148,v144,s19\n"
"v_mfma_f32_16x16x32_bf16 v[4:7],v[192:195],v[216:219],v[4:7]\n"
"v_perm_b32 v199,v156,v152,s19\n"
"s_add_u32 s60,s59,14\n"
"s_and_b32 s60,s60,15\n"
"s_lshl_b32 s60,s60,6\n"
"v_xor_b32 v229,s60,v228\n"
"ds_read_b128 v[200:203],v229\n"
"v_mfma_f32_16x16x32_bf16 v[8:11],v[192:195],v[238:241],v[8:11]\n"
"ds_read_b128 v[204:207],v229 offset:16384\n"
"v_mfma_f32_16x16x32_bf16 v[12:15],v[196:199],v[212:215],v[12:15]\n"
"v_perm_b32 v192,v133,v129,s18\n"
"v_perm_b32 v193,v141,v137,s18\n"
"v_perm_b32 v194,v149,v145,s18\n"
"v_mfma_f32_16x16x32_bf16 v[16:19],v[196:199],v[216:219],v[16:19]\n"
"v_perm_b32 v195,v157,v153,s18\n"
"v_xor_b32 v231,s60,v230\n"
"ds_read_b128 v[208:211],v231\n"
"v_mfma_f32_16x16x32_bf16 v[20:23],v[196:199],v[238:241],v[20:23]\n"
"s_add_u32 s67,s59,15\n"
"s_and_b32 s67,s67,15\n"
"s_lshl_b32 s67,s67,18\n"
"buffer_load_dwordx4 v[96:99],v220,s[36:39],s67 offen\n"
"v_mfma_f32_16x16x32_bf16 v[24:27],v[192:195],v[212:215],v[24:27]\n"
"v_perm_b32 v196,v133,v129,s19\n"
"v_perm_b32 v197,v141,v137,s19\n"
"v_mfma_f32_16x16x32_bf16 v[28:31],v[192:195],v[216:219],v[28:31]\n"
"v_perm_b32 v198,v149,v145,s19\n"
"v_perm_b32 v199,v157,v153,s19\n"
"v_mfma_f32_16x16x32_bf16 v[32:35],v[192:195],v[238:241],v[32:35]\n"
"buffer_load_dwordx4 v[100:103],v221,s[36:39],s67 offen\n"
"buffer_load_dwordx4 v[104:107],v222,s[36:39],s67 offen\n"
"v_mfma_f32_16x16x32_bf16 v[36:39],v[196:199],v[212:215],v[36:39]\n"
"v_perm_b32 v192,v134,v130,s18\n"
"v_perm_b32 v193,v142,v138,s18\n"
"v_mfma_f32_16x16x32_bf16 v[40:43],v[196:199],v[216:219],v[40:43]\n"
"v_perm_b32 v194,v150,v146,s18\n"
"v_perm_b32 v195,v158,v154,s18\n"
"v_mfma_f32_16x16x32_bf16 v[44:47],v[196:199],v[238:241],v[44:47]\n"
"buffer_load_dwordx4 v[108:111],v223,s[36:39],s67 offen\n"
"buffer_load_dwordx4 v[112:115],v224,s[36:39],s67 offen\n"
"v_mfma_f32_16x16x32_bf16 v[48:51],v[192:195],v[212:215],v[48:51]\n"
"v_perm_b32 v196,v134,v130,s19\n"
"v_perm_b32 v197,v142,v138,s19\n"
"v_mfma_f32_16x16x32_bf16 v[52:55],v[192:195],v[216:219],v[52:55]\n"
"v_perm_b32 v198,v150,v146,s19\n"
"v_perm_b32 v199,v158,v154,s19\n"
"v_mfma_f32_16x16x32_bf16 v[56:59],v[192:195],v[238:241],v[56:59]\n"
"buffer_load_dwordx4 v[116:119],v225,s[36:39],s67 offen\n"
"buffer_load_dwordx4 v[120:123],v226,s[36:39],s67 offen\n"
"v_mfma_f32_16x16x32_bf16 v[60:63],v[196:199],v[212:215],v[60:63]\n"
"v_perm_b32 v192,v135,v131,s18\n"
"v_perm_b32 v193,v143,v139,s18\n"
"v_mfma_f32_16x16x32_bf16 v[64:67],v[196:199],v[216:219],v[64:67]\n"
"v_perm_b32 v194,v151,v147,s18\n"
"v_mfma_f32_16x16x32_bf16 v[68:71],v[196:199],v[238:241],v[68:71]\n"
"v_perm_b32 v195,v159,v155,s18\n"
"buffer_load_dwordx4 v[124:127],v227,s[36:39],s67 offen\n"
"s_nop 0\n"
"v_mfma_f32_16x16x32_bf16 v[72:75],v[192:195],v[212:215],v[72:75]\n"
"v_perm_b32 v196,v135,v131,s19\n"
"v_mfma_f32_16x16x32_bf16 v[76:79],v[192:195],v[216:219],v[76:79]\n"
"v_perm_b32 v197,v143,v139,s19\n"
"v_perm_b32 v198,v151,v147,s19\n"
"v_mfma_f32_16x16x32_bf16 v[80:83],v[192:195],v[238:241],v[80:83]\n"
"v_perm_b32 v199,v159,v155,s19\n"
"s_nop 1\n"
"v_mfma_f32_16x16x32_bf16 v[84:87],v[196:199],v[212:215],v[84:87]\n"
"v_mfma_f32_16x16x32_bf16 v[88:91],v[196:199],v[216:219],v[88:91]\n"
"v_mfma_f32_16x16x32_bf16 v[92:95],v[196:199],v[238:241],v[92:95]\n"
"s_waitcnt vmcnt(14)\n"
"v_perm_b32 v192,v164,v160,s18\n"
"s_waitcnt vmcnt(12)\n"
"v_perm_b32 v193,v172,v168,s18\n"
"s_waitcnt vmcnt(10)\n"
"v_perm_b32 v194,v180,v176,s18\n"
"s_waitcnt vmcnt(8)\n"
"v_perm_b32 v195,v188,v184,s18\n"
"s_waitcnt lgkmcnt(0)\n"
"s_nop 0\n"
"v_mfma_f32_16x16x32_bf16 v[0:3],v[192:195],v[200:203],v[0:3]\n"
"v_perm_b32 v196,v164,v160,s19\n"
"v_perm_b32 v197,v172,v168,s19\n"
"v_perm_b32 v198,v180,v176,s19\n"
"v_mfma_f32_16x16x32_bf16 v[4:7],v[192:195],v[204:207],v[4:7]\n"
"v_perm_b32 v199,v188,v184,s19\n"
"s_add_u32 s60,s59,15\n"
"s_and_b32 s60,s60,15\n"
"s_lshl_b32 s60,s60,6\n"
"v_xor_b32 v229,s60,v228\n"
"ds_read_b128 v[212:215],v229\n"
"v_mfma_f32_16x16x32_bf16 v[8:11],v[192:195],v[208:211],v[8:11]\n"
"ds_read_b128 v[216:219],v229 offset:16384\n"
"v_mfma_f32_16x16x32_bf16 v[12:15],v[196:199],v[200:203],v[12:15]\n"
"v_perm_b32 v192,v165,v161,s18\n"
"v_perm_b32 v193,v173,v169,s18\n"
"v_perm_b32 v194,v181,v177,s18\n"
"v_mfma_f32_16x16x32_bf16 v[16:19],v[196:199],v[204:207],v[16:19]\n"
"v_perm_b32 v195,v189,v185,s18\n"
"v_xor_b32 v231,s60,v230\n"
"ds_read_b128 v[238:241],v231\n"
"v_mfma_f32_16x16x32_bf16 v[20:23],v[196:199],v[208:211],v[20:23]\n"
"s_add_u32 s67,s1,0\n"
"s_and_b32 s67,s67,0x3fff\n"
"s_add_u32 m0,s66,0\n"
"s_nop 0\n"
"buffer_load_dwordx4 v232,s[24:27],s67 offen nt lds\n"
"v_mfma_f32_16x16x32_bf16 v[24:27],v[192:195],v[200:203],v[24:27]\n"
"v_perm_b32 v196,v165,v161,s19\n"
"v_perm_b32 v197,v173,v169,s19\n"
"v_mfma_f32_16x16x32_bf16 v[28:31],v[192:195],v[204:207],v[28:31]\n"
"v_perm_b32 v198,v181,v177,s19\n"
"v_perm_b32 v199,v189,v185,s19\n"
"v_mfma_f32_16x16x32_bf16 v[32:35],v[192:195],v[208:211],v[32:35]\n"
"buffer_load_dwordx4 v233,s[24:27],s67 offen offset:1024 nt lds\n"
"buffer_load_dwordx4 v234,s[24:27],s67 offen offset:2048 nt lds\n"
"v_mfma_f32_16x16x32_bf16 v[36:39],v[196:199],v[200:203],v[36:39]\n"
"v_perm_b32 v192,v166,v162,s18\n"
"v_perm_b32 v193,v174,v170,s18\n"
"v_mfma_f32_16x16x32_bf16 v[40:43],v[196:199],v[204:207],v[40:43]\n"
"v_perm_b32 v194,v182,v178,s18\n"
"v_perm_b32 v195,v190,v186,s18\n"
"buffer_load_dwordx4 v235,s[24:27],s67 offen offset:3072 nt lds\n"
"v_mfma_f32_16x16x32_bf16 v[44:47],v[196:199],v[208:211],v[44:47]\n"
"s_add_u32 s67,s67,0x10000\n"
"s_add_u32 m0,s66,0x1000\n"
"s_nop 0\n"
"buffer_load_dwordx4 v232,s[24:27],s67 offen nt lds\n"
"v_mfma_f32_16x16x32_bf16 v[48:51],v[192:195],v[200:203],v[48:51]\n"
"v_perm_b32 v196,v166,v162,s19\n"
"v_perm_b32 v197,v174,v170,s19\n"
"v_mfma_f32_16x16x32_bf16 v[52:55],v[192:195],v[204:207],v[52:55]\n"
"v_perm_b32 v198,v182,v178,s19\n"
"v_perm_b32 v199,v190,v186,s19\n"
"v_mfma_f32_16x16x32_bf16 v[56:59],v[192:195],v[208:211],v[56:59]\n"
"buffer_load_dwordx4 v233,s[24:27],s67 offen offset:1024 nt lds\n"
"buffer_load_dwordx4 v234,s[24:27],s67 offen offset:2048 nt lds\n"
"v_mfma_f32_16x16x32_bf16 v[60:63],v[196:199],v[200:203],v[60:63]\n"
"v_perm_b32 v192,v167,v163,s18\n"
"v_perm_b32 v193,v175,v171,s18\n"
"v_mfma_f32_16x16x32_bf16 v[64:67],v[196:199],v[204:207],v[64:67]\n"
"v_perm_b32 v194,v183,v179,s18\n"
"v_mfma_f32_16x16x32_bf16 v[68:71],v[196:199],v[208:211],v[68:71]\n"
"v_perm_b32 v195,v191,v187,s18\n"
"buffer_load_dwordx4 v235,s[24:27],s67 offen offset:3072 nt lds\n"
"s_nop 0\n"
"v_mfma_f32_16x16x32_bf16 v[72:75],v[192:195],v[200:203],v[72:75]\n"
"v_perm_b32 v196,v167,v163,s19\n"
"v_mfma_f32_16x16x32_bf16 v[76:79],v[192:195],v[204:207],v[76:79]\n"
"v_perm_b32 v197,v175,v171,s19\n"
"v_perm_b32 v198,v183,v179,s19\n"
"v_mfma_f32_16x16x32_bf16 v[80:83],v[192:195],v[208:211],v[80:83]\n"
"v_perm_b32 v199,v191,v187,s19\n"
"s_nop 1\n"
"v_mfma_f32_16x16x32_bf16 v[84:87],v[196:199],v[200:203],v[84:87]\n"
"v_mfma_f32_16x16x32_bf16 v[88:91],v[196:199],v[204:207],v[88:91]\n"
"v_mfma_f32_16x16x32_bf16 v[92:95],v[196:199],v[208:211],v[92:95]\n"
"s_waitcnt vmcnt(14)\n"
"v_perm_b32 v192,v100,v96,s18\n"
"s_waitcnt vmcnt(12)\n"
"v_perm_b32 v193,v108,v104,s18\n"
"s_waitcnt vmcnt(10)\n"
"v_perm_b32 v194,v116,v112,s18\n"
"s_waitcnt vmcnt(8)\n"
"v_perm_b32 v195,v124,v120,s18\n"
"s_waitcnt lgkmcnt(0)\n"
"s_nop 0\n"
"v_mfma_f32_16x16x32_bf16 v[0:3],v[192:195],v[212:215],v[0:3]\n"
"v_perm_b32 v196,v100,v96,s19\n"
"v_perm_b32 v197,v108,v104,s19\n"
"v_mfma_f32_16x16x32_bf16 v[4:7],v[192:195],v[216:219],v[4:7]\n"
"v_perm_b32 v198,v116,v112,s19\n"
"v_perm_b32 v199,v124,v120,s19\n"
"v_mfma_f32_16x16x32_bf16 v[8:11],v[192:195],v[238:241],v[8:11]\n"
"s_add_u32 s67,s1,0\n"
"s_and_b32 s67,s67,0x3fff\n"
"s_add_u32 m0,s66,0x2000\n"
"s_nop 0\n"
"buffer_load_dwordx4 v236,s[28:31],s67 offen nt lds\n"
"v_mfma_f32_16x16x32_bf16 v[12:15],v[196:199],v[212:215],v[12:15]\n"
"v_perm_b32 v192,v101,v97,s18\n"
"v_perm_b32 v193,v109,v105,s18\n"
"v_mfma_f32_16x16x32_bf16 v[16:19],v[196:199],v[216:219],v[16:19]\n"
"v_perm_b32 v194,v117,v113,s18\n"
"v_mfma_f32_16x16x32_bf16 v[20:23],v[196:199],v[238:241],v[20:23]\n"
"v_perm_b32 v195,v125,v121,s18\n"
"buffer_load_dwordx4 v237,s[28:31],s67 offen offset:1024 nt lds\n"
"s_nop 0\n"
"v_mfma_f32_16x16x32_bf16 v[24:27],v[192:195],v[212:215],v[24:27]\n"
"v_perm_b32 v196,v101,v97,s19\n"
"v_perm_b32 v197,v109,v105,s19\n"
"v_mfma_f32_16x16x32_bf16 v[28:31],v[192:195],v[216:219],v[28:31]\n"
"v_perm_b32 v198,v117,v113,s19\n"
"v_mfma_f32_16x16x32_bf16 v[32:35],v[192:195],v[238:241],v[32:35]\n"
"v_perm_b32 v199,v125,v121,s19\n"
"buffer_load_dwordx4 v254,s[28:31],s67 offen offset:2048 nt lds\n"
"s_nop 0\n"
"v_mfma_f32_16x16x32_bf16 v[36:39],v[196:199],v[212:215],v[36:39]\n"
"v_perm_b32 v192,v102,v98,s18\n"
"v_perm_b32 v193,v110,v106,s18\n"
"v_mfma_f32_16x16x32_bf16 v[40:43],v[196:199],v[216:219],v[40:43]\n"
"v_perm_b32 v194,v118,v114,s18\n"
"v_mfma_f32_16x16x32_bf16 v[44:47],v[196:199],v[238:241],v[44:47]\n"
"v_perm_b32 v195,v126,v122,s18\n"
"buffer_load_dwordx4 v255,s[28:31],s67 offen offset:3072 nt lds\n"
"s_nop 0\n"
"v_mfma_f32_16x16x32_bf16 v[48:51],v[192:195],v[212:215],v[48:51]\n"
"v_perm_b32 v196,v102,v98,s19\n"
"v_perm_b32 v197,v110,v106,s19\n"
"v_mfma_f32_16x16x32_bf16 v[52:55],v[192:195],v[216:219],v[52:55]\n"
"v_perm_b32 v198,v118,v114,s19\n"
"v_perm_b32 v199,v126,v122,s19\n"
"v_mfma_f32_16x16x32_bf16 v[56:59],v[192:195],v[238:241],v[56:59]\n"
"s_add_u32 s67,s67,0x10000\n"
"s_add_u32 m0,s66,0x3000\n"
"s_nop 0\n"
"buffer_load_dwordx4 v236,s[28:31],s67 offen nt lds\n"
"v_mfma_f32_16x16x32_bf16 v[60:63],v[196:199],v[212:215],v[60:63]\n"
"v_perm_b32 v192,v103,v99,s18\n"
"v_perm_b32 v193,v111,v107,s18\n"
"v_mfma_f32_16x16x32_bf16 v[64:67],v[196:199],v[216:219],v[64:67]\n"
"v_perm_b32 v194,v119,v115,s18\n"
"v_mfma_f32_16x16x32_bf16 v[68:71],v[196:199],v[238:241],v[68:71]\n"
"v_perm_b32 v195,v127,v123,s18\n"
"buffer_load_dwordx4 v237,s[28:31],s67 offen offset:1024 nt lds\n"
"s_nop 0\n"
"v_mfma_f32_16x16x32_bf16 v[72:75],v[192:195],v[212:215],v[72:75]\n"
"v_perm_b32 v196,v103,v99,s19\n"
"v_perm_b32 v197,v111,v107,s19\n"
"v_mfma_f32_16x16x32_bf16 v[76:79],v[192:195],v[216:219],v[76:79]\n"
"v_perm_b32 v198,v119,v115,s19\n"
"v_mfma_f32_16x16x32_bf16 v[80:83],v[192:195],v[238:241],v[80:83]\n"
"v_perm_b32 v199,v127,v123,s19\n"
"buffer_load_dwordx4 v254,s[28:31],s67 offen offset:2048 nt lds\n"
"s_nop 0\n"
"v_mfma_f32_16x16x32_bf16 v[84:87],v[196:199],v[212:215],v[84:87]\n"
"v_mfma_f32_16x16x32_bf16 v[88:91],v[196:199],v[216:219],v[88:91]\n"
"buffer_load_dwordx4 v255,s[28:31],s67 offen offset:3072 nt lds\n"
"v_mfma_f32_16x16x32_bf16 v[92:95],v[196:199],v[238:241],v[92:95]\n"
"s_add_u32 s67,s1,0x400\n"
"s_and_b32 s67,s67,0x3fff\n"
"s_add_u32 m0,s66,0x4000\n"
"s_nop 0\n"
"buffer_load_dwordx4 v232,s[24:27],s67 offen nt lds\n"
"buffer_load_dwordx4 v233,s[24:27],s67 offen offset:1024 nt lds\n"
"buffer_load_dwordx4 v234,s[24:27],s67 offen offset:2048 nt lds\n"
"buffer_load_dwordx4 v235,s[24:27],s67 offen offset:3072 nt lds\n"
"s_add_u32 s67,s67,0x10000\n"
"s_add_u32 m0,s66,0x5000\n"
"s_nop 0\n"
"buffer_load_dwordx4 v232,s[24:27],s67 offen nt lds\n"
"buffer_load_dwordx4 v233,s[24:27],s67 offen offset:1024 nt lds\n"
"buffer_load_dwordx4 v234,s[24:27],s67 offen offset:2048 nt lds\n"
"buffer_load_dwordx4 v235,s[24:27],s67 offen offset:3072 nt lds\n"
"s_cmp_lg_u32 s7,3\n"
"s_cbranch_scc1 Ln_57\n"
"v_readfirstlane_b32 s65,v245\n"
"s_nop 1\n"
"Ln_58:\n"
"s_add_u32 s65,s65,1\n"
"v_cmp_eq_u32 vcc,s65,v245\n"
"s_cbranch_vccnz Ln_58\n"
"v_mov_b32 v246,s65\n"
"v_mov_b32 v247,0\n"
"s_lshl_b32 s60,s68,7\n"
"s_mov_b64 exec,1\n"
"buffer_store_dword v246,v247,s[52:55],s60 offen sc1\n"
"s_mov_b64 exec,-1\n"
"Ln_57:\n"
"s_barrier\n"
"s_add_u32 s67,s1,0x400\n"
"s_and_b32 s67,s67,0x3fff\n"
"s_add_u32 m0,s66,0x6000\n"
"s_nop 0\n"
"buffer_load_dwordx4 v236,s[28:31],s67 offen nt lds\n"
"buffer_load_dwordx4 v237,s[28:31],s67 offen offset:1024 nt lds\n"
"buffer_load_dwordx4 v254,s[28:31],s67 offen offset:2048 nt lds\n"
"buffer_load_dwordx4 v255,s[28:31],s67 offen offset:3072 nt lds\n"
"s_add_u32 s67,s67,0x10000\n"
"s_add_u32 m0,s66,0x7000\n"
"s_nop 0\n"
"buffer_load_dwordx4 v236,s[28:31],s67 offen nt lds\n"
"buffer_load_dwordx4 v237,s[28:31],s67 offen offset:1024 nt lds\n"
"buffer_load_dwordx4 v254,s[28:31],s67 offen offset:2048 nt lds\n"
"buffer_load_dwordx4 v255,s[28:31],s67 offen offset:3072 nt lds\n"
"v_mov_b32 v250,0x3e0293ee\n"
"v_pk_mul_f32 v[0:1],v[0:1],v[250:251] op_sel_hi:[1,0]\n"
"v_pk_mul_f32 v[2:3],v[2:3],v[250:251] op_sel_hi:[1,0]\n"
"v_pk_mul_f32 v[12:13],v[12:13],v[250:251] op_sel_hi:[1,0]\n"
"v_pk_mul_f32 v[14:15],v[14:15],v[250:251] op_sel_hi:[1,0]\n"
"v_pk_mul_f32 v[24:25],v[24:25],v[250:251] op_sel_hi:[1,0]\n"
"v_pk_mul_f32 v[26:27],v[26:27],v[250:251] op_sel_hi:[1,0]\n"
"v_pk_mul_f32 v[36:37],v[36:37],v[250:251] op_sel_hi:[1,0]\n"
"v_pk_mul_f32 v[38:39],v[38:39],v[250:251] op_sel_hi:[1,0]\n"
"v_pk_mul_f32 v[48:49],v[48:49],v[250:251] op_sel_hi:[1,0]\n"
"v_pk_mul_f32 v[50:51],v[50:51],v[250:251] op_sel_hi:[1,0]\n"
"v_pk_mul_f32 v[60:61],v[60:61],v[250:251] op_sel_hi:[1,0]\n"
"v_pk_mul_f32 v[62:63],v[62:63],v[250:251] op_sel_hi:[1,0]\n"
"v_pk_mul_f32 v[72:73],v[72:73],v[250:251] op_sel_hi:[1,0]\n"
"v_pk_mul_f32 v[74:75],v[74:75],v[250:251] op_sel_hi:[1,0]\n"
"v_pk_mul_f32 v[84:85],v[84:85],v[250:251] op_sel_hi:[1,0]\n"
"v_pk_mul_f32 v[86:87],v[86:87],v[250:251] op_sel_hi:[1,0]\n"
"v_cvt_pk_bf16_f32 v246,v0,v12\n"
"v_cvt_pk_bf16_f32 v247,v24,v36\n"
"v_cvt_pk_bf16_f32 v248,v48,v60\n"
"v_cvt_pk_bf16_f32 v249,v72,v84\n"
"v_accvgpr_write_b32 a0,v246\n"
"v_accvgpr_write_b32 a1,v247\n"
"v_accvgpr_write_b32 a2,v248\n"
"v_accvgpr_write_b32 a3,v249\n"
"v_cvt_pk_bf16_f32 v246,v1,v13\n"
"v_cvt_pk_bf16_f32 v247,v25,v37\n"
"v_cvt_pk_bf16_f32 v248,v49,v61\n"
"v_cvt_pk_bf16_f32 v249,v73,v85\n"
"v_accvgpr_write_b32 a12,v246\n"
"v_accvgpr_write_b32 a13,v247\n"
"v_accvgpr_write_b32 a14,v248\n"
"v_accvgpr_write_b32 a15,v249\n"
"v_cvt_pk_bf16_f32 v246,v2,v14\n"
"v_cvt_pk_bf16_f32 v247,v26,v38\n"
"v_cvt_pk_bf16_f32 v248,v50,v62\n"
"v_cvt_pk_bf16_f32 v249,v74,v86\n"
"v_accvgpr_write_b32 a24,v246\n"
"v_accvgpr_write_b32 a25,v247\n"
"v_accvgpr_write_b32 a26,v248\n"
"v_accvgpr_write_b32 a27,v249\n"
"v_cvt_pk_bf16_f32 v246,v3,v15\n"
"v_cvt_pk_bf16_f32 v247,v27,v39\n"
"v_cvt_pk_bf16_f32 v248,v51,v63\n"
"v_cvt_pk_bf16_f32 v249,v75,v87\n"
"v_accvgpr_write_b32 a36,v246\n"
"v_accvgpr_write_b32 a37,v247\n"
"v_accvgpr_write_b32 a38,v248\n"
"v_accvgpr_write_b32 a39,v249\n"
"v_pk_mul_f32 v[4:5],v[4:5],v[250:251] op_sel_hi:[1,0]\n"
"v_pk_mul_f32 v[6:7],v[6:7],v[250:251] op_sel_hi:[1,0]\n"
"v_pk_mul_f32 v[16:17],v[16:17],v[250:251] op_sel_hi:[1,0]\n"
"v_pk_mul_f32 v[18:19],v[18:19],v[250:251] op_sel_hi:[1,0]\n"
"v_pk_mul_f32 v[28:29],v[28:29],v[250:251] op_sel_hi:[1,0]\n"
"v_pk_mul_f32 v[30:31],v[30:31],v[250:251] op_sel_hi:[1,0]\n"
"v_pk_mul_f32 v[40:41],v[40:41],v[250:251] op_sel_hi:[1,0]\n"
"v_pk_mul_f32 v[42:43],v[42:43],v[250:251] op_sel_hi:[1,0]\n"
"v_pk_mul_f32 v[52:53],v[52:53],v[250:251] op_sel_hi:[1,0]\n"
"v_pk_mul_f32 v[54:55],v[54:55],v[250:251] op_sel_hi:[1,0]\n"
"v_pk_mul_f32 v[64:65],v[64:65],v[250:251] op_sel_hi:[1,0]\n"
"v_pk_mul_f32 v[66:67],v[66:67],v[250:251] op_sel_hi:[1,0]\n"
"v_pk_mul_f32 v[76:77],v[76:77],v[250:251] op_sel_hi:[1,0]\n"
"v_pk_mul_f32 v[78:79],v[78:79],v[250:251] op_sel_hi:[1,0]\n"
"v_pk_mul_f32 v[88:89],v[88:89],v[250:251] op_sel_hi:[1,0]\n"
"v_pk_mul_f32 v[90:91],v[90:91],v[250:251] op_sel_hi:[1,0]\n"
"v_cvt_pk_bf16_f32 v246,v4,v16\n"
"v_cvt_pk_bf16_f32 v247,v28,v40\n"
"v_cvt_pk_bf16_f32 v248,v52,v64\n"
"v_cvt_pk_bf16_f32 v249,v76,v88\n"
"v_accvgpr_write_b32 a4,v246\n"
"v_accvgpr_write_b32 a5,v247\n"
"v_accvgpr_write_b32 a6,v248\n"
"v_accvgpr_write_b32 a7,v249\n"
"v_cvt_pk_bf16_f32 v246,v5,v17\n"
"v_cvt_pk_bf16_f32 v247,v29,v41\n"
"v_cvt_pk_bf16_f32 v248,v53,v65\n"
"v_cvt_pk_bf16_f32 v249,v77,v89\n"
"v_accvgpr_write_b32 a16,v246\n"
"v_accvgpr_write_b32 a17,v247\n"
"v_accvgpr_write_b32 a18,v248\n"
"v_accvgpr_write_b32 a19,v249\n"
"v_cvt_pk_bf16_f32 v246,v6,v18\n"
"v_cvt_pk_bf16_f32 v247,v30,v42\n"
"v_cvt_pk_bf16_f32 v248,v54,v66\n"
"v_cvt_pk_bf16_f32 v249,v78,v90\n"
"v_accvgpr_write_b32 a28,v246\n"
"v_accvgpr_write_b32 a29,v247\n"
"v_accvgpr_write_b32 a30,v248\n"
"v_accvgpr_write_b32 a31,v249\n"
"v_cvt_pk_bf16_f32 v246,v7,v19\n"
"v_cvt_pk_bf16_f32 v247,v31,v43\n"
"v_cvt_pk_bf16_f32 v248,v55,v67\n"
"v_cvt_pk_bf16_f32 v249,v79,v91\n"
"v_accvgpr_write_b32 a40,v246\n"
"v_accvgpr_write_b32 a41,v247\n"
"v_accvgpr_write_b32 a42,v248\n"
"v_accvgpr_write_b32 a43,v249\n"
"v_pk_mul_f32 v[8:9],v[8:9],v[250:251] op_sel_hi:[1,0]\n"
"v_pk_mul_f32 v[10:11],v[10:11],v[250:251] op_sel_hi:[1,0]\n"
"v_pk_mul_f32 v[20:21],v[20:21],v[250:251] op_sel_hi:[1,0]\n"
"v_pk_mul_f32 v[22:23],v[22:23],v[250:251] op_sel_hi:[1,0]\n"
"v_pk_mul_f32 v[32:33],v[32:33],v[250:251] op_sel_hi:[1,0]\n"
"v_pk_mul_f32 v[34:35],v[34:35],v[250:251] op_sel_hi:[1,0]\n"
"v_pk_mul_f32 v[44:45],v[44:45],v[250:251] op_sel_hi:[1,0]\n"
"v_pk_mul_f32 v[46:47],v[46:47],v[250:251] op_sel_hi:[1,0]\n"
"v_pk_mul_f32 v[56:57],v[56:57],v[250:251] op_sel_hi:[1,0]\n"
"v_pk_mul_f32 v[58:59],v[58:59],v[250:251] op_sel_hi:[1,0]\n"
"v_pk_mul_f32 v[68:69],v[68:69],v[250:251] op_sel_hi:[1,0]\n"
"v_pk_mul_f32 v[70:71],v[70:71],v[250:251] op_sel_hi:[1,0]\n"
"v_pk_mul_f32 v[80:81],v[80:81],v[250:251] op_sel_hi:[1,0]\n"
"v_pk_mul_f32 v[82:83],v[82:83],v[250:251] op_sel_hi:[1,0]\n"
"v_pk_mul_f32 v[92:93],v[92:93],v[250:251] op_sel_hi:[1,0]\n"
"v_pk_mul_f32 v[94:95],v[94:95],v[250:251] op_sel_hi:[1,0]\n"
"v_cvt_pk_bf16_f32 v246,v8,v20\n"
"v_cvt_pk_bf16_f32 v247,v32,v44\n"
"v_cvt_pk_bf16_f32 v248,v56,v68\n"
"v_cvt_pk_bf16_f32 v249,v80,v92\n"
"v_accvgpr_write_b32 a8,v246\n"
"v_accvgpr_write_b32 a9,v247\n"
"v_accvgpr_write_b32 a10,v248\n"
"v_accvgpr_write_b32 a11,v249\n"
"v_cvt_pk_bf16_f32 v246,v9,v21\n"
"v_cvt_pk_bf16_f32 v247,v33,v45\n"
"v_cvt_pk_bf16_f32 v248,v57,v69\n"
"v_cvt_pk_bf16_f32 v249,v81,v93\n"
"v_accvgpr_write_b32 a20,v246\n"
"v_accvgpr_write_b32 a21,v247\n"
"v_accvgpr_write_b32 a22,v248\n"
"v_accvgpr_write_b32 a23,v249\n"
"v_cvt_pk_bf16_f32 v246,v10,v22\n"
"v_cvt_pk_bf16_f32 v247,v34,v46\n"
"v_cvt_pk_bf16_f32 v248,v58,v70\n"
"v_cvt_pk_bf16_f32 v249,v82,v94\n"
"v_accvgpr_write_b32 a32,v246\n"
"v_accvgpr_write_b32 a33,v247\n"
"v_accvgpr_write_b32 a34,v248\n"
"v_accvgpr_write_b32 a35,v249\n"
"v_cvt_pk_bf16_f32 v246,v11,v23\n"
"v_cvt_pk_bf16_f32 v247,v35,v47\n"
"v_cvt_pk_bf16_f32 v248,v59,v71\n"
"v_cvt_pk_bf16_f32 v249,v83,v95\n"
"v_accvgpr_write_b32 a44,v246\n"
"v_accvgpr_write_b32 a45,v247\n"
"v_accvgpr_write_b32 a46,v248\n"
"v_accvgpr_write_b32 a47,v249\n"
"v_and_b32 v246,10,v243\n"
"v_and_b32 v247,1,v243\n"
"v_lshl_or_b32 v246,v247,2,v246\n"
"v_bfe_u32 v247,v243,2,1\n"
"v_or_b32 v246,v246,v247\n"
"v_lshlrev_b32 v247,2,v244\n"
"v_xor_b32 v246,v246,v247\n"
"v_lshlrev_b32 v246,4,v246\n"
"v_lshl_or_b32 v246,v243,8,v246\n"
"v_add_u32 v246,s66,v246\n"
"v_xor_b32 v220,0,v246\n"
"v_xor_b32 v221,16,v246\n"
"v_xor_b32 v222,32,v246\n"
"v_xor_b32 v223,48,v246\n"
"v_lshrrev_b32 v246,2,v243\n"
"v_lshl_or_b32 v246,v244,2,v246\n"
"v_and_b32 v247,7,v246\n"
"v_lshlrev_b32 v247,5,v247\n"
"v_lshl_or_b32 v247,v246,8,v247\n"
"v_and_b32 v248,3,v243\n"
"v_lshl_or_b32 v247,v248,3,v247\n"
"v_add_u32 v247,s66,v247\n"
"v_xor_b32 v224,0,v247\n"
"v_xor_b32 v225,32,v247\n"
"v_xor_b32 v226,64,v247\n"
"v_xor_b32 v227,0x60,v247\n"
"v_xor_b32 v228,0x80,v247\n"
"v_xor_b32 v229,0xa0,v247\n"
"v_xor_b32 v230,0xc0,v247\n"
"v_xor_b32 v231,0xe0,v247\n"
"v_lshlrev_b32 v246,1,v244\n"
"v_xor_b32 v246,v246,v243\n"
"v_xor_b32 v246,0,v246\n"
"v_lshlrev_b32 v246,4,v246\n"
"v_lshl_or_b32 v238,v244,10,v246\n"
"s_lshl_b32 s60,s7,8\n"
"s_add_u32 s60,s60,0\n"
"v_add_u32 v238,s60,v238\n"
"v_lshlrev_b32 v246,1,v244\n"
"v_xor_b32 v246,v246,v243\n"
"v_xor_b32 v246,8,v246\n"
"v_lshlrev_b32 v246,4,v246\n"
"v_lshl_or_b32 v239,v244,10,v246\n"
"s_lshl_b32 s60,s7,8\n"
"s_add_u32 s60,s60,0xc00\n"
"v_add_u32 v239,s60,v239\n"
"v_lshlrev_b32 v246,1,v244\n"
"v_xor_b32 v246,v246,v243\n"
"v_xor_b32 v246,0,v246\n"
"v_lshlrev_b32 v246,4,v246\n"
"v_lshl_or_b32 v240,v244,10,v246\n"
"s_lshl_b32 s60,s7,8\n"
"s_add_u32 s60,s60,0x1800\n"
"v_add_u32 v240,s60,v240\n"
"v_lshlrev_b32 v246,1,v244\n"
"v_xor_b32 v246,v246,v243\n"
"v_xor_b32 v246,8,v246\n"
"v_lshlrev_b32 v246,4,v246\n"
"v_lshl_or_b32 v241,v244,10,v246\n"
"s_lshl_b32 s60,s7,8\n"
"s_add_u32 s60,s60,0x2400\n"
"v_add_u32 v241,s60,v241\n"
"s_waitcnt vmcnt(24)\n"
"ds_read_b128 v[156:159],v220\n"
"ds_read_b128 v[172:175],v220 offset:4096\n"
"ds_read_b128 v[160:163],v221\n"
"ds_read_b128 v[176:179],v221 offset:4096\n"
"ds_read_b128 v[164:167],v222\n"
"ds_read_b128 v[180:183],v222 offset:4096\n"
"ds_read_b128 v[168:171],v223\n"
"ds_read_b128 v[184:187],v223 offset:4096\n"
"s_waitcnt lgkmcnt(7)\n"
"v_mfma_f32_16x16x32_bf16 v[108:111],v[156:159],a[0:3],0\n"
"s_waitcnt lgkmcnt(6)\n"
"v_mfma_f32_16x16x32_bf16 v[112:115],v[172:175],a[0:3],0\n"
"s_waitcnt lgkmcnt(5)\n"
"v_mfma_f32_16x16x32_bf16 v[108:111],v[160:163],a[12:15],v[108:111]\n"
"s_waitcnt lgkmcnt(4)\n"
"v_mfma_f32_16x16x32_bf16 v[112:115],v[176:179],a[12:15],v[112:115]\n"
"s_waitcnt lgkmcnt(3)\n"
"v_mfma_f32_16x16x32_bf16 v[108:111],v[164:167],a[24:27],v[108:111]\n"
"s_waitcnt lgkmcnt(2)\n"
"v_mfma_f32_16x16x32_bf16 v[112:115],v[180:183],a[24:27],v[112:115]\n"
"s_waitcnt lgkmcnt(1)\n"
"v_mfma_f32_16x16x32_bf16 v[108:111],v[168:171],a[36:39],v[108:111]\n"
"s_waitcnt lgkmcnt(0)\n"
"v_mfma_f32_16x16x32_bf16 v[112:115],v[184:187],a[36:39],v[112:115]\n"
"v_mfma_f32_16x16x32_bf16 v[116:119],v[156:159],a[4:7],0\n"
"s_add_u32 s67,s1,0x800\n"
"s_and_b32 s67,s67,0x3fff\n"
"s_add_u32 m0,s66,0\n"
"s_nop 0\n"
"buffer_load_dwordx4 v232,s[24:27],s67 offen nt lds\n"
"buffer_load_dwordx4 v233,s[24:27],s67 offen offset:1024 nt lds\n"
"v_mfma_f32_16x16x32_bf16 v[120:123],v[172:175],a[4:7],0\n"
"buffer_load_dwordx4 v234,s[24:27],s67 offen offset:2048 nt lds\n"
"buffer_load_dwordx4 v235,s[24:27],s67 offen offset:3072 nt lds\n"
"v_max3_f32 v246,v108,v109,v110\n"
"v_max3_f32 v246,v246,v111,v112\n"
"v_max3_f32 v246,v246,v113,v114\n"
"v_mfma_f32_16x16x32_bf16 v[116:119],v[160:163],a[16:19],v[116:119]\n"
"v_max_f32 v246,v246,v115\n"
"v_mov_b32 v249,v246\n"
"s_nop 1\n"
"v_permlane32_swap_b32 v246,v249\n"
"v_max_f32 v246,v246,v249\n"
"v_mfma_f32_16x16x32_bf16 v[120:123],v[176:179],a[16:19],v[120:123]\n"
"v_mov_b32 v249,v246\n"
"s_nop 1\n"
"v_permlane16_swap_b32 v246,v249\n"
"v_max_f32 v246,v246,v249\n"
"v_sub_f32 v108,v108,v246\n"
"v_sub_f32 v109,v109,v246\n"
"v_mfma_f32_16x16x32_bf16 v[116:119],v[164:167],a[28:31],v[116:119]\n"
"v_sub_f32 v110,v110,v246\n"
"v_sub_f32 v111,v111,v246\n"
"v_sub_f32 v112,v112,v246\n"
"v_sub_f32 v113,v113,v246\n"
"v_mfma_f32_16x16x32_bf16 v[120:123],v[180:183],a[28:31],v[120:123]\n"
"v_sub_f32 v114,v114,v246\n"
"v_sub_f32 v115,v115,v246\n"
"v_sub_f32 v132,0,v246\n"
"v_sub_f32 v133,0,v246\n"
"v_sub_f32 v134,0,v246\n"
"v_mfma_f32_16x16x32_bf16 v[116:119],v[168:171],a[40:43],v[116:119]\n"
"v_sub_f32 v135,0,v246\n"
"v_exp_f32 v108,v108\n"
"v_exp_f32 v109,v109\n"
"v_mfma_f32_16x16x32_bf16 v[120:123],v[184:187],a[40:43],v[120:123]\n"
"v_exp_f32 v110,v110\n"
"v_exp_f32 v111,v111\n"
"v_mfma_f32_16x16x32_bf16 v[124:127],v[156:159],a[8:11],0\n"
"v_cvt_pk_bf16_f32 v144,v108,v109\n"
"v_exp_f32 v112,v112\n"
"v_exp_f32 v113,v113\n"
"v_mfma_f32_16x16x32_bf16 v[128:131],v[172:175],a[8:11],0\n"
"v_cvt_pk_bf16_f32 v145,v110,v111\n"
"v_exp_f32 v114,v114\n"
"v_exp_f32 v115,v115\n"
"v_mfma_f32_16x16x32_bf16 v[124:127],v[160:163],a[20:23],v[124:127]\n"
"v_cvt_pk_bf16_f32 v146,v112,v113\n"
"v_cvt_pk_bf16_f32 v147,v114,v115\n"
"s_add_u32 s67,s67,0x10000\n"
"s_add_u32 m0,s66,0x1000\n"
"s_nop 0\n"
"buffer_load_dwordx4 v232,s[24:27],s67 offen nt lds\n"
"v_mfma_f32_16x16x32_bf16 v[128:131],v[176:179],a[20:23],v[128:131]\n"
"buffer_load_dwordx4 v233,s[24:27],s67 offen offset:1024 nt lds\n"
"buffer_load_dwordx4 v234,s[24:27],s67 offen offset:2048 nt lds\n"
"buffer_load_dwordx4 v235,s[24:27],s67 offen offset:3072 nt lds\n"
"s_waitcnt vmcnt(24)\n"
"v_max3_f32 v247,v116,v117,v118\n"
"v_max3_f32 v247,v247,v119,v120\n"
"v_mfma_f32_16x16x32_bf16 v[124:127],v[164:167],a[32:35],v[124:127]\n"
"v_max3_f32 v247,v247,v121,v122\n"
"v_max_f32 v247,v247,v123\n"
"v_mov_b32 v249,v247\n"
"s_nop 1\n"
"v_permlane32_swap_b32 v247,v249\n"
"v_max_f32 v247,v247,v249\n"
"v_mfma_f32_16x16x32_bf16 v[128:131],v[180:183],a[32:35],v[128:131]\n"
"v_mov_b32 v249,v247\n"
"s_nop 1\n"
"v_permlane16_swap_b32 v247,v249\n"
"v_max_f32 v247,v247,v249\n"
"v_sub_f32 v116,v116,v247\n"
"v_sub_f32 v117,v117,v247\n"
"v_mfma_f32_16x16x32_bf16 v[124:127],v[168:171],a[44:47],v[124:127]\n"
"v_sub_f32 v118,v118,v247\n"
"v_sub_f32 v119,v119,v247\n"
"v_sub_f32 v120,v120,v247\n"
"v_sub_f32 v121,v121,v247\n"
"v_sub_f32 v122,v122,v247\n"
"v_mfma_f32_16x16x32_bf16 v[128:131],v[184:187],a[44:47],v[128:131]\n"
"v_sub_f32 v123,v123,v247\n"
"v_sub_f32 v136,0,v247\n"
"v_sub_f32 v137,0,v247\n"
"v_sub_f32 v138,0,v247\n"
"v_sub_f32 v139,0,v247\n"
"ds_read_b64_tr_b16 v[188:189],v224 offset:8192\n"
"ds_read_b64_tr_b16 v[190:191],v224 offset:12288\n"
"ds_read_b64_tr_b16 v[192:193],v225 offset:8192\n"
"ds_read_b64_tr_b16 v[194:195],v225 offset:12288\n"
"ds_read_b64_tr_b16 v[196:197],v226 offset:8192\n"
"ds_read_b64_tr_b16 v[198:199],v226 offset:12288\n"
"ds_read_b64_tr_b16 v[200:201],v227 offset:8192\n"
"ds_read_b64_tr_b16 v[202:203],v227 offset:12288\n"
"ds_read_b64_tr_b16 v[204:205],v228 offset:8192\n"
"ds_read_b64_tr_b16 v[206:207],v228 offset:12288\n"
"ds_read_b64_tr_b16 v[208:209],v229 offset:8192\n"
"ds_read_b64_tr_b16 v[210:211],v229 offset:12288\n"
"ds_read_b64_tr_b16 v[212:213],v230 offset:8192\n"
"ds_read_b64_tr_b16 v[214:215],v230 offset:12288\n"
"ds_read_b64_tr_b16 v[216:217],v231 offset:8192\n"
"s_waitcnt lgkmcnt(14)\n"
"ds_read_b64_tr_b16 v[218:219],v231 offset:12288\n"
"s_waitcnt lgkmcnt(14)\n"
"v_mfma_f32_16x16x32_bf16 v[0:3],v[188:191],v[144:147],0\n"
"v_exp_f32 v116,v116\n"
"v_exp_f32 v117,v117\n"
"s_waitcnt lgkmcnt(12)\n"
"v_mfma_f32_16x16x32_bf16 v[4:7],v[192:195],v[144:147],0\n"
"v_exp_f32 v118,v118\n"
"v_exp_f32 v119,v119\n"
"v_cvt_pk_bf16_f32 v148,v116,v117\n"
"s_waitcnt lgkmcnt(10)\n"
"v_mfma_f32_16x16x32_bf16 v[8:11],v[196:199],v[144:147],0\n"
"v_exp_f32 v120,v120\n"
"v_exp_f32 v121,v121\n"
"v_cvt_pk_bf16_f32 v149,v118,v119\n"
"s_waitcnt lgkmcnt(8)\n"
"v_mfma_f32_16x16x32_bf16 v[12:15],v[200:203],v[144:147],0\n"
"v_exp_f32 v122,v122\n"
"v_exp_f32 v123,v123\n"
"v_cvt_pk_bf16_f32 v150,v120,v121\n"
"s_waitcnt lgkmcnt(5)\n"
"v_mfma_f32_16x16x32_bf16 v[16:19],v[204:207],v[144:147],0\n"
"v_cvt_pk_bf16_f32 v151,v122,v123\n"
"v_max3_f32 v248,v124,v125,v126\n"
"v_max3_f32 v248,v248,v127,v128\n"
"v_max3_f32 v248,v248,v129,v130\n"
"s_waitcnt lgkmcnt(0)\n"
"v_mfma_f32_16x16x32_bf16 v[20:23],v[208:211],v[144:147],0\n"
"v_max_f32 v248,v248,v131\n"
"v_mov_b32 v249,v248\n"
"s_nop 1\n"
"v_permlane32_swap_b32 v248,v249\n"
"v_max_f32 v248,v248,v249\n"
"v_mov_b32 v249,v248\n"
"v_mfma_f32_16x16x32_bf16 v[24:27],v[212:215],v[144:147],0\n"
"s_nop 0\n"
"v_permlane16_swap_b32 v248,v249\n"
"v_max_f32 v248,v248,v249\n"
"v_sub_f32 v124,v124,v248\n"
"v_sub_f32 v125,v125,v248\n"
"v_sub_f32 v126,v126,v248\n"
"v_mfma_f32_16x16x32_bf16 v[28:31],v[216:219],v[144:147],0\n"
"v_sub_f32 v127,v127,v248\n"
"v_sub_f32 v128,v128,v248\n"
"v_sub_f32 v129,v129,v248\n"
"v_sub_f32 v130,v130,v248\n"
"v_mfma_f32_16x16x32_bf16 v[96:99],a[48:51],v[144:147],0\n"
"v_sub_f32 v131,v131,v248\n"
"v_sub_f32 v140,0,v248\n"
"v_sub_f32 v141,0,v248\n"
"v_sub_f32 v142,0,v248\n"
"v_sub_f32 v143,0,v248\n"
"v_mfma_f32_16x16x32_bf16 v[32:35],v[188:191],v[148:151],0\n"
"v_exp_f32 v124,v124\n"
"v_mfma_f32_16x16x32_bf16 v[36:39],v[192:195],v[148:151],0\n"
"v_exp_f32 v125,v125\n"
"v_exp_f32 v126,v126\n"
"v_mfma_f32_16x16x32_bf16 v[40:43],v[196:199],v[148:151],0\n"
"v_exp_f32 v127,v127\n"
"v_cvt_pk_bf16_f32 v152,v124,v125\n"
"v_mfma_f32_16x16x32_bf16 v[44:47],v[200:203],v[148:151],0\n"
"v_exp_f32 v128,v128\n"
"v_mfma_f32_16x16x32_bf16 v[48:51],v[204:207],v[148:151],0\n"
"v_exp_f32 v129,v129\n"
"v_cvt_pk_bf16_f32 v153,v126,v127\n"
"v_mfma_f32_16x16x32_bf16 v[52:55],v[208:211],v[148:151],0\n"
"v_exp_f32 v130,v130\n"
"v_exp_f32 v131,v131\n"
"v_mfma_f32_16x16x32_bf16 v[56:59],v[212:215],v[148:151],0\n"
"v_cvt_pk_bf16_f32 v154,v128,v129\n"
"v_cvt_pk_bf16_f32 v155,v130,v131\n"
"v_mfma_f32_16x16x32_bf16 v[60:63],v[216:219],v[148:151],0\n"
"s_add_u32 s67,s1,0x800\n"
"s_and_b32 s67,s67,0x3fff\n"
"s_add_u32 m0,s66,0x2000\n"
"s_nop 0\n"
"buffer_load_dwordx4 v236,s[28:31],s67 offen nt lds\n"
"v_mfma_f32_16x16x32_bf16 v[100:103],a[48:51],v[148:151],0\n"
"buffer_load_dwordx4 v237,s[28:31],s67 offen offset:1024 nt lds\n"
"buffer_load_dwordx4 v254,s[28:31],s67 offen offset:2048 nt lds\n"
"buffer_load_dwordx4 v255,s[28:31],s67 offen offset:3072 nt lds\n"
"v_mfma_f32_16x16x32_bf16 v[64:67],v[188:191],v[152:155],0\n"
"v_mfma_f32_16x16x32_bf16 v[68:71],v[192:195],v[152:155],0\n"
"s_add_u32 s67,s67,0x10000\n"
"s_add_u32 m0,s66,0x3000\n"
"s_nop 0\n"
"buffer_load_dwordx4 v236,s[28:31],s67 offen nt lds\n"
"v_mfma_f32_16x16x32_bf16 v[72:75],v[196:199],v[152:155],0\n"
"v_mfma_f32_16x16x32_bf16 v[76:79],v[200:203],v[152:155],0\n"
"v_mfma_f32_16x16x32_bf16 v[80:83],v[204:207],v[152:155],0\n"
"buffer_load_dwordx4 v237,s[28:31],s67 offen offset:1024 nt lds\n"
"v_mfma_f32_16x16x32_bf16 v[84:87],v[208:211],v[152:155],0\n"
"v_mfma_f32_16x16x32_bf16 v[88:91],v[212:215],v[152:155],0\n"
"buffer_load_dwordx4 v254,s[28:31],s67 offen offset:2048 nt lds\n"
"v_mfma_f32_16x16x32_bf16 v[92:95],v[216:219],v[152:155],0\n"
"v_mfma_f32_16x16x32_bf16 v[104:107],a[48:51],v[152:155],0\n"
"buffer_load_dwordx4 v255,s[28:31],s67 offen offset:3072 nt lds\n"
"s_waitcnt vmcnt(24)\n"
"ds_read_b128 v[156:159],v220 offset:16384\n"
"ds_read_b128 v[172:175],v220 offset:20480\n"
"ds_read_b128 v[160:163],v221 offset:16384\n"
"ds_read_b128 v[176:179],v221 offset:20480\n"
"ds_read_b128 v[164:167],v222 offset:16384\n"
"ds_read_b128 v[180:183],v222 offset:20480\n"
"ds_read_b128 v[168:171],v223 offset:16384\n"
"ds_read_b128 v[184:187],v223 offset:20480\n"
"s_waitcnt lgkmcnt(7)\n"
"v_mfma_f32_16x16x32_bf16 v[108:111],v[156:159],a[0:3],v[132:135]\n"
"s_waitcnt lgkmcnt(6)\n"
"v_mfma_f32_16x16x32_bf16 v[112:115],v[172:175],a[0:3],v[132:135]\n"
"s_waitcnt lgkmcnt(5)\n"
"v_mfma_f32_16x16x32_bf16 v[108:111],v[160:163],a[12:15],v[108:111]\n"
"s_waitcnt lgkmcnt(4)\n"
"v_mfma_f32_16x16x32_bf16 v[112:115],v[176:179],a[12:15],v[112:115]\n"
"s_waitcnt lgkmcnt(3)\n"
"v_mfma_f32_16x16x32_bf16 v[108:111],v[164:167],a[24:27],v[108:111]\n"
"s_waitcnt lgkmcnt(2)\n"
"v_mfma_f32_16x16x32_bf16 v[112:115],v[180:183],a[24:27],v[112:115]\n"
"s_waitcnt lgkmcnt(1)\n"
"v_mfma_f32_16x16x32_bf16 v[108:111],v[168:171],a[36:39],v[108:111]\n"
"s_waitcnt lgkmcnt(0)\n"
"v_mfma_f32_16x16x32_bf16 v[112:115],v[184:187],a[36:39],v[112:115]\n"
"v_mfma_f32_16x16x32_bf16 v[116:119],v[156:159],a[4:7],v[136:139]\n"
"s_add_u32 s67,s1,0xc00\n"
"s_and_b32 s67,s67,0x3fff\n"
"s_add_u32 m0,s66,0x4000\n"
"s_nop 0\n"
"buffer_load_dwordx4 v232,s[24:27],s67 offen nt lds\n"
"v_mfma_f32_16x16x32_bf16 v[120:123],v[172:175],a[4:7],v[136:139]\n"
"buffer_load_dwordx4 v233,s[24:27],s67 offen offset:1024 nt lds\n"
"buffer_load_dwordx4 v234,s[24:27],s67 offen offset:2048 nt lds\n"
"v_mfma_f32_16x16x32_bf16 v[116:119],v[160:163],a[16:19],v[116:119]\n"
"buffer_load_dwordx4 v235,s[24:27],s67 offen offset:3072 nt lds\n"
"v_max3_f32 v246,v108,v109,v110\n"
"v_mfma_f32_16x16x32_bf16 v[120:123],v[176:179],a[16:19],v[120:123]\n"
"v_max3_f32 v246,v246,v111,v112\n"
"v_max3_f32 v246,v246,v113,v114\n"
"v_mfma_f32_16x16x32_bf16 v[116:119],v[164:167],a[28:31],v[116:119]\n"
"v_max_f32 v246,v246,v115\n"
"v_cmp_lt_f32 s[80:81],s64,v246\n"
"v_mfma_f32_16x16x32_bf16 v[120:123],v[180:183],a[28:31],v[120:123]\n"
"v_exp_f32 v108,v108\n"
"v_exp_f32 v109,v109\n"
"v_mfma_f32_16x16x32_bf16 v[116:119],v[168:171],a[40:43],v[116:119]\n"
"v_exp_f32 v110,v110\n"
"v_mfma_f32_16x16x32_bf16 v[120:123],v[184:187],a[40:43],v[120:123]\n"
"v_exp_f32 v111,v111\n"
"v_mfma_f32_16x16x32_bf16 v[124:127],v[156:159],a[8:11],v[140:143]\n"
"v_cvt_pk_bf16_f32 v144,v108,v109\n"
"v_exp_f32 v112,v112\n"
"v_mfma_f32_16x16x32_bf16 v[128:131],v[172:175],a[8:11],v[140:143]\n"
"v_exp_f32 v113,v113\n"
"v_cvt_pk_bf16_f32 v145,v110,v111\n"
"v_mfma_f32_16x16x32_bf16 v[124:127],v[160:163],a[20:23],v[124:127]\n"
"v_exp_f32 v114,v114\n"
"v_mfma_f32_16x16x32_bf16 v[128:131],v[176:179],a[20:23],v[128:131]\n"
"v_exp_f32 v115,v115\n"
"v_cvt_pk_bf16_f32 v146,v112,v113\n"
"v_mfma_f32_16x16x32_bf16 v[124:127],v[164:167],a[32:35],v[124:127]\n"
"v_cvt_pk_bf16_f32 v147,v114,v115\n"
"s_add_u32 s67,s67,0x10000\n"
"s_add_u32 m0,s66,0x5000\n"
"s_nop 0\n"
"buffer_load_dwordx4 v232,s[24:27],s67 offen nt lds\n"
"v_mfma_f32_16x16x32_bf16 v[128:131],v[180:183],a[32:35],v[128:131]\n"
"buffer_load_dwordx4 v233,s[24:27],s67 offen offset:1024 nt lds\n"
"buffer_load_dwordx4 v234,s[24:27],s67 offen offset:2048 nt lds\n"
"v_mfma_f32_16x16x32_bf16 v[124:127],v[168:171],a[44:47],v[124:127]\n"
"buffer_load_dwordx4 v235,s[24:27],s67 offen offset:3072 nt lds\n"
"s_waitcnt vmcnt(24)\n"
"v_max3_f32 v247,v116,v117,v118\n"
"v_max3_f32 v247,v247,v119,v120\n"
"v_mfma_f32_16x16x32_bf16 v[128:131],v[184:187],a[44:47],v[128:131]\n"
"v_max3_f32 v247,v247,v121,v122\n"
"v_max_f32 v247,v247,v123\n"
"v_cmp_lt_f32 vcc,s64,v247\n"
"ds_read_b64_tr_b16 v[188:189],v224 offset:24576\n"
"ds_read_b64_tr_b16 v[190:191],v224 offset:28672\n"
"ds_read_b64_tr_b16 v[192:193],v225 offset:24576\n"
"ds_read_b64_tr_b16 v[194:195],v225 offset:28672\n"
"ds_read_b64_tr_b16 v[196:197],v226 offset:24576\n"
"ds_read_b64_tr_b16 v[198:199],v226 offset:28672\n"
"ds_read_b64_tr_b16 v[200:201],v227 offset:24576\n"
"ds_read_b64_tr_b16 v[202:203],v227 offset:28672\n"
"ds_read_b64_tr_b16 v[204:205],v228 offset:24576\n"
"ds_read_b64_tr_b16 v[206:207],v228 offset:28672\n"
"ds_read_b64_tr_b16 v[208:209],v229 offset:24576\n"
"ds_read_b64_tr_b16 v[210:211],v229 offset:28672\n"
"ds_read_b64_tr_b16 v[212:213],v230 offset:24576\n"
"ds_read_b64_tr_b16 v[214:215],v230 offset:28672\n"
"ds_read_b64_tr_b16 v[216:217],v231 offset:24576\n"
"s_waitcnt lgkmcnt(14)\n"
"ds_read_b64_tr_b16 v[218:219],v231 offset:28672\n"
"s_cmp_lg_u64 s[80:81],0\n"
"s_cbranch_scc1 Lk_59x\n"
"Lk_59:\n"
"s_waitcnt lgkmcnt(14)\n"
"v_mfma_f32_16x16x32_bf16 v[0:3],v[188:191],v[144:147],v[0:3]\n"
"v_exp_f32 v116,v116\n"
"s_waitcnt lgkmcnt(12)\n"
"v_mfma_f32_16x16x32_bf16 v[4:7],v[192:195],v[144:147],v[4:7]\n"
"v_exp_f32 v117,v117\n"
"v_exp_f32 v118,v118\n"
"s_waitcnt lgkmcnt(10)\n"
"v_mfma_f32_16x16x32_bf16 v[8:11],v[196:199],v[144:147],v[8:11]\n"
"v_exp_f32 v119,v119\n"
"s_waitcnt lgkmcnt(8)\n"
"v_mfma_f32_16x16x32_bf16 v[12:15],v[200:203],v[144:147],v[12:15]\n"
"v_cvt_pk_bf16_f32 v148,v116,v117\n"
"v_exp_f32 v120,v120\n"
"s_waitcnt lgkmcnt(6)\n"
"v_mfma_f32_16x16x32_bf16 v[16:19],v[204:207],v[144:147],v[16:19]\n"
"v_exp_f32 v121,v121\n"
"v_cvt_pk_bf16_f32 v149,v118,v119\n"
"s_waitcnt lgkmcnt(4)\n"
"v_mfma_f32_16x16x32_bf16 v[20:23],v[208:211],v[144:147],v[20:23]\n"
"v_exp_f32 v122,v122\n"
"s_waitcnt lgkmcnt(0)\n"
"v_mfma_f32_16x16x32_bf16 v[24:27],v[212:215],v[144:147],v[24:27]\n"
"v_exp_f32 v123,v123\n"
"v_cvt_pk_bf16_f32 v150,v120,v121\n"
"v_mfma_f32_16x16x32_bf16 v[28:31],v[216:219],v[144:147],v[28:31]\n"
"v_cvt_pk_bf16_f32 v151,v122,v123\n"
"v_max3_f32 v248,v124,v125,v126\n"
"v_max3_f32 v248,v248,v127,v128\n"
"v_mfma_f32_16x16x32_bf16 v[96:99],a[48:51],v[144:147],v[96:99]\n"
"v_max3_f32 v248,v248,v129,v130\n"
"v_max_f32 v248,v248,v131\n"
"v_cmp_lt_f32 s[82:83],s64,v248\n"
"s_cbranch_vccnz Lk_60x\n"
"Lk_60:\n"
"v_mfma_f32_16x16x32_bf16 v[32:35],v[188:191],v[148:151],v[32:35]\n"
"v_exp_f32 v124,v124\n"
"v_mfma_f32_16x16x32_bf16 v[36:39],v[192:195],v[148:151],v[36:39]\n"
"v_exp_f32 v125,v125\n"
"v_exp_f32 v126,v126\n"
"v_mfma_f32_16x16x32_bf16 v[40:43],v[196:199],v[148:151],v[40:43]\n"
"v_exp_f32 v127,v127\n"
"v_cvt_pk_bf16_f32 v152,v124,v125\n"
"v_mfma_f32_16x16x32_bf16 v[44:47],v[200:203],v[148:151],v[44:47]\n"
"v_exp_f32 v128,v128\n"
"v_mfma_f32_16x16x32_bf16 v[48:51],v[204:207],v[148:151],v[48:51]\n"
"v_exp_f32 v129,v129\n"
"v_cvt_pk_bf16_f32 v153,v126,v127\n"
"v_mfma_f32_16x16x32_bf16 v[52:55],v[208:211],v[148:151],v[52:55]\n"
"v_exp_f32 v130,v130\n"
"v_exp_f32 v131,v131\n"
"v_mfma_f32_16x16x32_bf16 v[56:59],v[212:215],v[148:151],v[56:59]\n"
"v_cvt_pk_bf16_f32 v154,v128,v129\n"
"v_cvt_pk_bf16_f32 v155,v130,v131\n"
"v_mfma_f32_16x16x32_bf16 v[60:63],v[216:219],v[148:151],v[60:63]\n"
"s_add_u32 s67,s1,0xc00\n"
"s_and_b32 s67,s67,0x3fff\n"
"s_add_u32 m0,s66,0x6000\n"
"s_nop 0\n"
"buffer_load_dwordx4 v236,s[28:31],s67 offen nt lds\n"
"v_mfma_f32_16x16x32_bf16 v[100:103],a[48:51],v[148:151],v[100:103]\n"
"buffer_load_dwordx4 v237,s[28:31],s67 offen offset:1024 nt lds\n"
"buffer_load_dwordx4 v254,s[28:31],s67 offen offset:2048 nt lds\n"
"buffer_load_dwordx4 v255,s[28:31],s67 offen offset:3072 nt lds\n"
"s_cmp_lg_u64 s[82:83],0\n"
"s_cbranch_scc1 Lk_61x\n"
"Lk_61:\n"
"v_mfma_f32_16x16x32_bf16 v[64:67],v[188:191],v[152:155],v[64:67]\n"
"v_mfma_f32_16x16x32_bf16 v[68:71],v[192:195],v[152:155],v[68:71]\n"
"s_add_u32 s67,s67,0x10000\n"
"s_add_u32 m0,s66,0x7000\n"
"s_nop 0\n"
"buffer_load_dwordx4 v236,s[28:31],s67 offen nt lds\n"
"v_mfma_f32_16x16x32_bf16 v[72:75],v[196:199],v[152:155],v[72:75]\n"
"v_mfma_f32_16x16x32_bf16 v[76:79],v[200:203],v[152:155],v[76:79]\n"
"v_mfma_f32_16x16x32_bf16 v[80:83],v[204:207],v[152:155],v[80:83]\n"
"buffer_load_dwordx4 v237,s[28:31],s67 offen offset:1024 nt lds\n"
"v_mfma_f32_16x16x32_bf16 v[84:87],v[208:211],v[152:155],v[84:87]\n"
"v_mfma_f32_16x16x32_bf16 v[88:91],v[212:215],v[152:155],v[88:91]\n"
"buffer_load_dwordx4 v254,s[28:31],s67 offen offset:2048 nt lds\n"
"v_mfma_f32_16x16x32_bf16 v[92:95],v[216:219],v[152:155],v[92:95]\n"
"v_mfma_f32_16x16x32_bf16 v[104:107],a[48:51],v[152:155],v[104:107]\n"
"buffer_load_dwordx4 v255,s[28:31],s67 offen offset:3072 nt lds\n"
"s_waitcnt vmcnt(24)\n"
"ds_read_b128 v[156:159],v220\n"
"ds_read_b128 v[172:175],v220 offset:4096\n"
"ds_read_b128 v[160:163],v221\n"
"ds_read_b128 v[176:179],v221 offset:4096\n"
"ds_read_b128 v[164:167],v222\n"
"ds_read_b128 v[180:183],v222 offset:4096\n"
"ds_read_b128 v[168:171],v223\n"
"ds_read_b128 v[184:187],v223 offset:4096\n"
"s_waitcnt lgkmcnt(7)\n"
"v_mfma_f32_16x16x32_bf16 v[108:111],v[156:159],a[0:3],v[132:135]\n"
"s_waitcnt lgkmcnt(6)\n"
"v_mfma_f32_16x16x32_bf16 v[112:115],v[172:175],a[0:3],v[132:135]\n"
"s_waitcnt lgkmcnt(5)\n"
"v_mfma_f32_16x16x32_bf16 v[108:111],v[160:163],a[12:15],v[108:111]\n"
"s_waitcnt lgkmcnt(4)\n"
"v_mfma_f32_16x16x32_bf16 v[112:115],v[176:179],a[12:15],v[112:115]\n"
"s_waitcnt lgkmcnt(3)\n"
"v_mfma_f32_16x16x32_bf16 v[108:111],v[164:167],a[24:27],v[108:111]\n"
"s_waitcnt lgkmcnt(2)\n"
"v_mfma_f32_16x16x32_bf16 v[112:115],v[180:183],a[24:27],v[112:115]\n"
"s_waitcnt lgkmcnt(1)\n"
"v_mfma_f32_16x16x32_bf16 v[108:111],v[168:171],a[36:39],v[108:111]\n"
"s_waitcnt lgkmcnt(0)\n"
"v_mfma_f32_16x16x32_bf16 v[112:115],v[184:187],a[36:39],v[112:115]\n"
"v_mfma_f32_16x16x32_bf16 v[116:119],v[156:159],a[4:7],v[136:139]\n"
"s_add_u32 s67,s1,0x1000\n"
"s_and_b32 s67,s67,0x3fff\n"
"s_add_u32 m0,s66,0\n"
"s_nop 0\n"
"buffer_load_dwordx4 v232,s[24:27],s67 offen nt lds\n"
"v_mfma_f32_16x16x32_bf16 v[120:123],v[172:175],a[4:7],v[136:139]\n"
"buffer_load_dwordx4 v233,s[24:27],s67 offen offset:1024 nt lds\n"
"buffer_load_dwordx4 v234,s[24:27],s67 offen offset:2048 nt lds\n"
"v_mfma_f32_16x16x32_bf16 v[116:119],v[160:163],a[16:19],v[116:119]\n"
"buffer_load_dwordx4 v235,s[24:27],s67 offen offset:3072 nt lds\n"
"v_max3_f32 v246,v108,v109,v110\n"
"v_mfma_f32_16x16x32_bf16 v[120:123],v[176:179],a[16:19],v[120:123]\n"
"v_max3_f32 v246,v246,v111,v112\n"
"v_max3_f32 v246,v246,v113,v114\n"
"v_mfma_f32_16x16x32_bf16 v[116:119],v[164:167],a[28:31],v[116:119]\n"
"v_max_f32 v246,v246,v115\n"
"v_cmp_lt_f32 s[80:81],s64,v246\n"
"v_mfma_f32_16x16x32_bf16 v[120:123],v[180:183],a[28:31],v[120:123]\n"
"v_exp_f32 v108,v108\n"
"v_exp_f32 v109,v109\n"
"v_mfma_f32_16x16x32_bf16 v[116:119],v[168:171],a[40:43],v[116:119]\n"
"v_exp_f32 v110,v110\n"
"v_mfma_f32_16x16x32_bf16 v[120:123],v[184:187],a[40:43],v[120:123]\n"
"v_exp_f32 v111,v111\n"
"v_mfma_f32_16x16x32_bf16 v[124:127],v[156:159],a[8:11],v[140:143]\n"
"v_cvt_pk_bf16_f32 v144,v108,v109\n"
"v_exp_f32 v112,v112\n"
"v_mfma_f32_16x16x32_bf16 v[128:131],v[172:175],a[8:11],v[140:143]\n"
"v_exp_f32 v113,v113\n"
"v_cvt_pk_bf16_f32 v145,v110,v111\n"
"v_mfma_f32_16x16x32_bf16 v[124:127],v[160:163],a[20:23],v[124:127]\n"
"v_exp_f32 v114,v114\n"
"v_mfma_f32_16x16x32_bf16 v[128:131],v[176:179],a[20:23],v[128:131]\n"
"v_exp_f32 v115,v115\n"
"v_cvt_pk_bf16_f32 v146,v112,v113\n"
"v_mfma_f32_16x16x32_bf16 v[124:127],v[164:167],a[32:35],v[124:127]\n"
"v_cvt_pk_bf16_f32 v147,v114,v115\n"
"s_add_u32 s67,s67,0x10000\n"
"s_add_u32 m0,s66,0x1000\n"
"s_nop 0\n"
"buffer_load_dwordx4 v232,s[24:27],s67 offen nt lds\n"
"v_mfma_f32_16x16x32_bf16 v[128:131],v[180:183],a[32:35],v[128:131]\n"
"buffer_load_dwordx4 v233,s[24:27],s67 offen offset:1024 nt lds\n"
"buffer_load_dwordx4 v234,s[24:27],s67 offen offset:2048 nt lds\n"
"v_mfma_f32_16x16x32_bf16 v[124:127],v[168:171],a[44:47],v[124:127]\n"
"buffer_load_dwordx4 v235,s[24:27],s67 offen offset:3072 nt lds\n"
"s_waitcnt vmcnt(24)\n"
"v_max3_f32 v247,v116,v117,v118\n"
"v_max3_f32 v247,v247,v119,v120\n"
"v_mfma_f32_16x16x32_bf16 v[128:131],v[184:187],a[44:47],v[128:131]\n"
"v_max3_f32 v247,v247,v121,v122\n"
"v_max_f32 v247,v247,v123\n"
"v_cmp_lt_f32 vcc,s64,v247\n"
"ds_read_b64_tr_b16 v[188:189],v224 offset:8192\n"
"ds_read_b64_tr_b16 v[190:191],v224 offset:12288\n"
"ds_read_b64_tr_b16 v[192:193],v225 offset:8192\n"
"ds_read_b64_tr_b16 v[194:195],v225 offset:12288\n"
"ds_read_b64_tr_b16 v[196:197],v226 offset:8192\n"
"ds_read_b64_tr_b16 v[198:199],v226 offset:12288\n"
"ds_read_b64_tr_b16 v[200:201],v227 offset:8192\n"
"ds_read_b64_tr_b16 v[202:203],v227 offset:12288\n"
"ds_read_b64_tr_b16 v[204:205],v228 offset:8192\n"
"ds_read_b64_tr_b16 v[206:207],v228 offset:12288\n"
"ds_read_b64_tr_b16 v[208:209],v229 offset:8192\n"
"ds_read_b64_tr_b16 v[210:211],v229 offset:12288\n"
"ds_read_b64_tr_b16 v[212:213],v230 offset:8192\n"
"ds_read_b64_tr_b16 v[214:215],v230 offset:12288\n"
"ds_read_b64_tr_b16 v[216:217],v231 offset:8192\n"
"s_waitcnt lgkmcnt(14)\n"
"ds_read_b64_tr_b16 v[218:219],v231 offset:12288\n"
"s_cmp_lg_u64 s[80:81],0\n"
"s_cbranch_scc1 Lk_62x\n"
"Lk_62:\n"
"s_waitcnt lgkmcnt(14)\n"
"v_mfma_f32_16x16x32_bf16 v[0:3],v[188:191],v[144:147],v[0:3]\n"
"v_exp_f32 v116,v116\n"
"s_waitcnt lgkmcnt(12)\n"
"v_mfma_f32_16x16x32_bf16 v[4:7],v[192:195],v[144:147],v[4:7]\n"
"v_exp_f32 v117,v117\n"
"v_exp_f32 v118,v118\n"
"s_waitcnt lgkmcnt(10)\n"
"v_mfma_f32_16x16x32_bf16 v[8:11],v[196:199],v[144:147],v[8:11]\n"
"v_exp_f32 v119,v119\n"
"s_waitcnt lgkmcnt(8)\n"
"v_mfma_f32_16x16x32_bf16 v[12:15],v[200:203],v[144:147],v[12:15]\n"
"v_cvt_pk_bf16_f32 v148,v116,v117\n"
"v_exp_f32 v120,v120\n"
"s_waitcnt lgkmcnt(6)\n"
"v_mfma_f32_16x16x32_bf16 v[16:19],v[204:207],v[144:147],v[16:19]\n"
"v_exp_f32 v121,v121\n"
"v_cvt_pk_bf16_f32 v149,v118,v119\n"
"s_waitcnt lgkmcnt(4)\n"
"v_mfma_f32_16x16x32_bf16 v[20:23],v[208:211],v[144:147],v[20:23]\n"
"v_exp_f32 v122,v122\n"
"s_waitcnt lgkmcnt(0)\n"
"v_mfma_f32_16x16x32_bf16 v[24:27],v[212:215],v[144:147],v[24:27]\n"
"v_exp_f32 v123,v123\n"
"v_cvt_pk_bf16_f32 v150,v120,v121\n"
"v_mfma_f32_16x16x32_bf16 v[28:31],v[216:219],v[144:147],v[28:31]\n"
"v_cvt_pk_bf16_f32 v151,v122,v123\n"
"v_max3_f32 v248,v124,v125,v126\n"
"v_max3_f32 v248,v248,v127,v128\n"
"v_mfma_f32_16x16x32_bf16 v[96:99],a[48:51],v[144:147],v[96:99]\n"
"v_max3_f32 v248,v248,v129,v130\n"
"v_max_f32 v248,v248,v131\n"
"v_cmp_lt_f32 s[82:83],s64,v248\n"
"s_cbranch_vccnz Lk_63x\n"
"Lk_63:\n"
"v_mfma_f32_16x16x32_bf16 v[32:35],v[188:191],v[148:151],v[32:35]\n"
"v_exp_f32 v124,v124\n"
"v_mfma_f32_16x16x32_bf16 v[36:39],v[192:195],v[148:151],v[36:39]\n"
"v_exp_f32 v125,v125\n"
"v_exp_f32 v126,v126\n"
"v_mfma_f32_16x16x32_bf16 v[40:43],v[196:199],v[148:151],v[40:43]\n"
"v_exp_f32 v127,v127\n"
"v_cvt_pk_bf16_f32 v152,v124,v125\n"
"v_mfma_f32_16x16x32_bf16 v[44:47],v[200:203],v[148:151],v[44:47]\n"
"v_exp_f32 v128,v128\n"
"v_mfma_f32_16x16x32_bf16 v[48:51],v[204:207],v[148:151],v[48:51]\n"
"v_exp_f32 v129,v129\n"
"v_cvt_pk_bf16_f32 v153,v126,v127\n"
"v_mfma_f32_16x16x32_bf16 v[52:55],v[208:211],v[148:151],v[52:55]\n"
"v_exp_f32 v130,v130\n"
"v_exp_f32 v131,v131\n"
"v_mfma_f32_16x16x32_bf16 v[56:59],v[212:215],v[148:151],v[56:59]\n"
"v_cvt_pk_bf16_f32 v154,v128,v129\n"
"v_cvt_pk_bf16_f32 v155,v130,v131\n"
"v_mfma_f32_16x16x32_bf16 v[60:63],v[216:219],v[148:151],v[60:63]\n"
"s_add_u32 s67,s1,0x1000\n"
"s_and_b32 s67,s67,0x3fff\n"
"s_add_u32 m0,s66,0x2000\n"
"s_nop 0\n"
"buffer_load_dwordx4 v236,s[28:31],s67 offen nt lds\n"
"v_mfma_f32_16x16x32_bf16 v[100:103],a[48:51],v[148:151],v[100:103]\n"
"buffer_load_dwordx4 v237,s[28:31],s67 offen offset:1024 nt lds\n"
"buffer_load_dwordx4 v254,s[28:31],s67 offen offset:2048 nt lds\n"
"buffer_load_dwordx4 v255,s[28:31],s67 offen offset:3072 nt lds\n"
"s_cmp_lg_u64 s[82:83],0\n"
"s_cbranch_scc1 Lk_64x\n"
"Lk_64:\n"
"v_mfma_f32_16x16x32_bf16 v[64:67],v[188:191],v[152:155],v[64:67]\n"
"v_mfma_f32_16x16x32_bf16 v[68:71],v[192:195],v[152:155],v[68:71]\n"
"s_add_u32 s67,s67,0x10000\n"
"s_add_u32 m0,s66,0x3000\n"
"s_nop 0\n"
"buffer_load_dwordx4 v236,s[28:31],s67 offen nt lds\n"
"v_mfma_f32_16x16x32_bf16 v[72:75],v[196:199],v[152:155],v[72:75]\n"
"v_mfma_f32_16x16x32_bf16 v[76:79],v[200:203],v[152:155],v[76:79]\n"
"v_mfma_f32_16x16x32_bf16 v[80:83],v[204:207],v[152:155],v[80:83]\n"
"buffer_load_dwordx4 v237,s[28:31],s67 offen offset:1024 nt lds\n"
"v_mfma_f32_16x16x32_bf16 v[84:87],v[208:211],v[152:155],v[84:87]\n"
"v_mfma_f32_16x16x32_bf16 v[88:91],v[212:215],v[152:155],v[88:91]\n"
"buffer_load_dwordx4 v254,s[28:31],s67 offen offset:2048 nt lds\n"
"v_mfma_f32_16x16x32_bf16 v[92:95],v[216:219],v[152:155],v[92:95]\n"
"v_mfma_f32_16x16x32_bf16 v[104:107],a[48:51],v[152:155],v[104:107]\n"
"buffer_load_dwordx4 v255,s[28:31],s67 offen offset:3072 nt lds\n"
"s_waitcnt vmcnt(24)\n"
"ds_read_b128 v[156:159],v220 offset:16384\n"
"ds_read_b128 v[172:175],v220 offset:20480\n"
"ds_read_b128 v[160:163],v221 offset:16384\n"
"ds_read_b128 v[176:179],v221 offset:20480\n"
"ds_read_b128 v[164:167],v222 offset:16384\n"
"ds_read_b128 v[180:183],v222 offset:20480\n"
"ds_read_b128 v[168:171],v223 offset:16384\n"
"ds_read_b128 v[184:187],v223 offset:20480\n"
"s_waitcnt lgkmcnt(7)\n"
"v_mfma_f32_16x16x32_bf16 v[108:111],v[156:159],a[0:3],v[132:135]\n"
"s_waitcnt lgkmcnt(6)\n"
"v_mfma_f32_16x16x32_bf16 v[112:115],v[172:175],a[0:3],v[132:135]\n"
"s_waitcnt lgkmcnt(5)\n"
"v_mfma_f32_16x16x32_bf16 v[108:111],v[160:163],a[12:15],v[108:111]\n"
"s_waitcnt lgkmcnt(4)\n"
"v_mfma_f32_16x16x32_bf16 v[112:115],v[176:179],a[12:15],v[112:115]\n"
"s_waitcnt lgkmcnt(3)\n"
"v_mfma_f32_16x16x32_bf16 v[108:111],v[164:167],a[24:27],v[108:111]\n"
"s_waitcnt lgkmcnt(2)\n"
"v_mfma_f32_16x16x32_bf16 v[112:115],v[180:183],a[24:27],v[112:115]\n"
"s_waitcnt lgkmcnt(1)\n"
"v_mfma_f32_16x16x32_bf16 v[108:111],v[168:171],a[36:39],v[108:111]\n"
"s_waitcnt lgkmcnt(0)\n"
"v_mfma_f32_16x16x32_bf16 v[112:115],v[184:187],a[36:39],v[112:115]\n"
"v_mfma_f32_16x16x32_bf16 v[116:119],v[156:159],a[4:7],v[136:139]\n"
"s_add_u32 s67,s1,0x1400\n"
"s_and_b32 s67,s67,0x3fff\n"
"s_add_u32 m0,s66,0x4000\n"
"s_nop 0\n"
"buffer_load_dwordx4 v232,s[24:27],s67 offen nt lds\n"
"v_mfma_f32_16x16x32_bf16 v[120:123],v[172:175],a[4:7],v[136:139]\n"
"buffer_load_dwordx4 v233,s[24:27],s67 offen offset:1024 nt lds\n"
"buffer_load_dwordx4 v234,s[24:27],s67 offen offset:2048 nt lds\n"
"v_mfma_f32_16x16x32_bf16 v[116:119],v[160:163],a[16:19],v[116:119]\n"
"buffer_load_dwordx4 v235,s[24:27],s67 offen offset:3072 nt lds\n"
"v_max3_f32 v246,v108,v109,v110\n"
"v_mfma_f32_16x16x32_bf16 v[120:123],v[176:179],a[16:19],v[120:123]\n"
"v_max3_f32 v246,v246,v111,v112\n"
"v_max3_f32 v246,v246,v113,v114\n"
"v_mfma_f32_16x16x32_bf16 v[116:119],v[164:167],a[28:31],v[116:119]\n"
"v_max_f32 v246,v246,v115\n"
"v_cmp_lt_f32 s[80:81],s64,v246\n"
"v_mfma_f32_16x16x32_bf16 v[120:123],v[180:183],a[28:31],v[120:123]\n"
"v_exp_f32 v108,v108\n"
"v_exp_f32 v109,v109\n"
"v_mfma_f32_16x16x32_bf16 v[116:119],v[168:171],a[40:43],v[116:119]\n"
"v_exp_f32 v110,v110\n"
"v_mfma_f32_16x16x32_bf16 v[120:123],v[184:187],a[40:43],v[120:123]\n"
"v_exp_f32 v111,v111\n"
"v_mfma_f32_16x16x32_bf16 v[124:127],v[156:159],a[8:11],v[140:143]\n"
"v_cvt_pk_bf16_f32 v144,v108,v109\n"
"v_exp_f32 v112,v112\n"
"v_mfma_f32_16x16x32_bf16 v[128:131],v[172:175],a[8:11],v[140:143]\n"
"v_exp_f32 v113,v113\n"
"v_cvt_pk_bf16_f32 v145,v110,v111\n"
"v_mfma_f32_16x16x32_bf16 v[124:127],v[160:163],a[20:23],v[124:127]\n"
"v_exp_f32 v114,v114\n"
"v_mfma_f32_16x16x32_bf16 v[128:131],v[176:179],a[20:23],v[128:131]\n"
"v_exp_f32 v115,v115\n"
"v_cvt_pk_bf16_f32 v146,v112,v113\n"
"v_mfma_f32_16x16x32_bf16 v[124:127],v[164:167],a[32:35],v[124:127]\n"
"v_cvt_pk_bf16_f32 v147,v114,v115\n"
"s_add_u32 s67,s67,0x10000\n"
"s_add_u32 m0,s66,0x5000\n"
"s_nop 0\n"
"buffer_load_dwordx4 v232,s[24:27],s67 offen nt lds\n"
"v_mfma_f32_16x16x32_bf16 v[128:131],v[180:183],a[32:35],v[128:131]\n"
"buffer_load_dwordx4 v233,s[24:27],s67 offen offset:1024 nt lds\n"
"buffer_load_dwordx4 v234,s[24:27],s67 offen offset:2048 nt lds\n"
"v_mfma_f32_16x16x32_bf16 v[124:127],v[168:171],a[44:47],v[124:127]\n"
"buffer_load_dwordx4 v235,s[24:27],s67 offen offset:3072 nt lds\n"
"s_waitcnt vmcnt(24)\n"
"v_max3_f32 v247,v116,v117,v118\n"
"v_max3_f32 v247,v247,v119,v120\n"
"v_mfma_f32_16x16x32_bf16 v[128:131],v[184:187],a[44:47],v[128:131]\n"
"v_max3_f32 v247,v247,v121,v122\n"
"v_max_f32 v247,v247,v123\n"
"v_cmp_lt_f32 vcc,s64,v247\n"
"ds_read_b64_tr_b16 v[188:189],v224 offset:24576\n"
"ds_read_b64_tr_b16 v[190:191],v224 offset:28672\n"
"ds_read_b64_tr_b16 v[192:193],v225 offset:24576\n"
"ds_read_b64_tr_b16 v[194:195],v225 offset:28672\n"
"ds_read_b64_tr_b16 v[196:197],v226 offset:24576\n"
"ds_read_b64_tr_b16 v[198:199],v226 offset:28672\n"
"ds_read_b64_tr_b16 v[200:201],v227 offset:24576\n"
"ds_read_b64_tr_b16 v[202:203],v227 offset:28672\n"
"ds_read_b64_tr_b16 v[204:205],v228 offset:24576\n"
"ds_read_b64_tr_b16 v[206:207],v228 offset:28672\n"
"ds_read_b64_tr_b16 v[208:209],v229 offset:24576\n"
"ds_read_b64_tr_b16 v[210:211],v229 offset:28672\n"
"ds_read_b64_tr_b16 v[212:213],v230 offset:24576\n"
"ds_read_b64_tr_b16 v[214:215],v230 offset:28672\n"
"ds_read_b64_tr_b16 v[216:217],v231 offset:24576\n"
"s_waitcnt lgkmcnt(14)\n"
"ds_read_b64_tr_b16 v[218:219],v231 offset:28672\n"
"s_cmp_lg_u64 s[80:81],0\n"
"s_cbranch_scc1 Lk_65x\n"
"Lk_65:\n"
"s_waitcnt lgkmcnt(14)\n"
"v_mfma_f32_16x16x32_bf16 v[0:3],v[188:191],v[144:147],v[0:3]\n"
"v_exp_f32 v116,v116\n"
"s_waitcnt lgkmcnt(12)\n"
"v_mfma_f32_16x16x32_bf16 v[4:7],v[192:195],v[144:147],v[4:7]\n"
"v_exp_f32 v117,v117\n"
"v_exp_f32 v118,v118\n"
"s_waitcnt lgkmcnt(10)\n"
"v_mfma_f32_16x16x32_bf16 v[8:11],v[196:199],v[144:147],v[8:11]\n"
"v_exp_f32 v119,v119\n"
"s_waitcnt lgkmcnt(8)\n"
"v_mfma_f32_16x16x32_bf16 v[12:15],v[200:203],v[144:147],v[12:15]\n"
"v_cvt_pk_bf16_f32 v148,v116,v117\n"
"v_exp_f32 v120,v120\n"
"s_waitcnt lgkmcnt(6)\n"
"v_mfma_f32_16x16x32_bf16 v[16:19],v[204:207],v[144:147],v[16:19]\n"
"v_exp_f32 v121,v121\n"
"v_cvt_pk_bf16_f32 v149,v118,v119\n"
"s_waitcnt lgkmcnt(4)\n"
"v_mfma_f32_16x16x32_bf16 v[20:23],v[208:211],v[144:147],v[20:23]\n"
"v_exp_f32 v122,v122\n"
"s_waitcnt lgkmcnt(0)\n"
"v_mfma_f32_16x16x32_bf16 v[24:27],v[212:215],v[144:147],v[24:27]\n"
"v_exp_f32 v123,v123\n"
"v_cvt_pk_bf16_f32 v150,v120,v121\n"
"v_mfma_f32_16x16x32_bf16 v[28:31],v[216:219],v[144:147],v[28:31]\n"
"v_cvt_pk_bf16_f32 v151,v122,v123\n"
"v_max3_f32 v248,v124,v125,v126\n"
"v_max3_f32 v248,v248,v127,v128\n"
"v_mfma_f32_16x16x32_bf16 v[96:99],a[48:51],v[144:147],v[96:99]\n"
"v_max3_f32 v248,v248,v129,v130\n"
"v_max_f32 v248,v248,v131\n"
"v_cmp_lt_f32 s[82:83],s64,v248\n"
"s_cbranch_vccnz Lk_66x\n"
"Lk_66:\n"
"v_mfma_f32_16x16x32_bf16 v[32:35],v[188:191],v[148:151],v[32:35]\n"
"v_exp_f32 v124,v124\n"
"v_mfma_f32_16x16x32_bf16 v[36:39],v[192:195],v[148:151],v[36:39]\n"
"v_exp_f32 v125,v125\n"
"v_exp_f32 v126,v126\n"
"v_mfma_f32_16x16x32_bf16 v[40:43],v[196:199],v[148:151],v[40:43]\n"
"v_exp_f32 v127,v127\n"
"v_cvt_pk_bf16_f32 v152,v124,v125\n"
"v_mfma_f32_16x16x32_bf16 v[44:47],v[200:203],v[148:151],v[44:47]\n"
"v_exp_f32 v128,v128\n"
"v_mfma_f32_16x16x32_bf16 v[48:51],v[204:207],v[148:151],v[48:51]\n"
"v_exp_f32 v129,v129\n"
"v_cvt_pk_bf16_f32 v153,v126,v127\n"
"v_mfma_f32_16x16x32_bf16 v[52:55],v[208:211],v[148:151],v[52:55]\n"
"v_exp_f32 v130,v130\n"
"v_exp_f32 v131,v131\n"
"v_mfma_f32_16x16x32_bf16 v[56:59],v[212:215],v[148:151],v[56:59]\n"
"v_cvt_pk_bf16_f32 v154,v128,v129\n"
"v_cvt_pk_bf16_f32 v155,v130,v131\n"
"v_mfma_f32_16x16x32_bf16 v[60:63],v[216:219],v[148:151],v[60:63]\n"
"s_add_u32 s67,s1,0x1400\n"
"s_and_b32 s67,s67,0x3fff\n"
"s_add_u32 m0,s66,0x6000\n"
"s_nop 0\n"
"buffer_load_dwordx4 v236,s[28:31],s67 offen nt lds\n"
"v_mfma_f32_16x16x32_bf16 v[100:103],a[48:51],v[148:151],v[100:103]\n"
"buffer_load_dwordx4 v237,s[28:31],s67 offen offset:1024 nt lds\n"
"buffer_load_dwordx4 v254,s[28:31],s67 offen offset:2048 nt lds\n"
"buffer_load_dwordx4 v255,s[28:31],s67 offen offset:3072 nt lds\n"
"s_cmp_lg_u64 s[82:83],0\n"
"s_cbranch_scc1 Lk_67x\n"
"Lk_67:\n"
"v_mfma_f32_16x16x32_bf16 v[64:67],v[188:191],v[152:155],v[64:67]\n"
"v_mfma_f32_16x16x32_bf16 v[68:71],v[192:195],v[152:155],v[68:71]\n"
"s_add_u32 s67,s67,0x10000\n"
"s_add_u32 m0,s66,0x7000\n"
"s_nop 0\n"
"buffer_load_dwordx4 v236,s[28:31],s67 offen nt lds\n"
"v_mfma_f32_16x16x32_bf16 v[72:75],v[196:199],v[152:155],v[72:75]\n"
"v_mfma_f32_16x16x32_bf16 v[76:79],v[200:203],v[152:155],v[76:79]\n"
"v_mfma_f32_16x16x32_bf16 v[80:83],v[204:207],v[152:155],v[80:83]\n"
"buffer_load_dwordx4 v237,s[28:31],s67 offen offset:1024 nt lds\n"
"v_mfma_f32_16x16x32_bf16 v[84:87],v[208:211],v[152:155],v[84:87]\n"
"v_mfma_f32_16x16x32_bf16 v[88:91],v[212:215],v[152:155],v[88:91]\n"
"buffer_load_dwordx4 v254,s[28:31],s67 offen offset:2048 nt lds\n"
"v_mfma_f32_16x16x32_bf16 v[92:95],v[216:219],v[152:155],v[92:95]\n"
"v_mfma_f32_16x16x32_bf16 v[104:107],a[48:51],v[152:155],v[104:107]\n"
"buffer_load_dwordx4 v255,s[28:31],s67 offen offset:3072 nt lds\n"
"s_waitcnt vmcnt(24)\n"
"ds_read_b128 v[156:159],v220\n"
"ds_read_b128 v[172:175],v220 offset:4096\n"
"ds_read_b128 v[160:163],v221\n"
"ds_read_b128 v[176:179],v221 offset:4096\n"
"ds_read_b128 v[164:167],v222\n"
"ds_read_b128 v[180:183],v222 offset:4096\n"
"ds_read_b128 v[168:171],v223\n"
"ds_read_b128 v[184:187],v223 offset:4096\n"
"s_waitcnt lgkmcnt(7)\n"
"v_mfma_f32_16x16x32_bf16 v[108:111],v[156:159],a[0:3],v[132:135]\n"
"s_waitcnt lgkmcnt(6)\n"
"v_mfma_f32_16x16x32_bf16 v[112:115],v[172:175],a[0:3],v[132:135]\n"
"s_waitcnt lgkmcnt(5)\n"
"v_mfma_f32_16x16x32_bf16 v[108:111],v[160:163],a[12:15],v[108:111]\n"
"s_waitcnt lgkmcnt(4)\n"
"v_mfma_f32_16x16x32_bf16 v[112:115],v[176:179],a[12:15],v[112:115]\n"
"s_waitcnt lgkmcnt(3)\n"
"v_mfma_f32_16x16x32_bf16 v[108:111],v[164:167],a[24:27],v[108:111]\n"
"s_waitcnt lgkmcnt(2)\n"
"v_mfma_f32_16x16x32_bf16 v[112:115],v[180:183],a[24:27],v[112:115]\n"
"s_waitcnt lgkmcnt(1)\n"
"v_mfma_f32_16x16x32_bf16 v[108:111],v[168:171],a[36:39],v[108:111]\n"
"s_waitcnt lgkmcnt(0)\n"
"v_mfma_f32_16x16x32_bf16 v[112:115],v[184:187],a[36:39],v[112:115]\n"
"v_mfma_f32_16x16x32_bf16 v[116:119],v[156:159],a[4:7],v[136:139]\n"
"s_add_u32 s67,s1,0x1800\n"
"s_and_b32 s67,s67,0x3fff\n"
"s_add_u32 m0,s66,0\n"
"s_nop 0\n"
"buffer_load_dwordx4 v232,s[24:27],s67 offen nt lds\n"
"v_mfma_f32_16x16x32_bf16 v[120:123],v[172:175],a[4:7],v[136:139]\n"
"buffer_load_dwordx4 v233,s[24:27],s67 offen offset:1024 nt lds\n"
"buffer_load_dwordx4 v234,s[24:27],s67 offen offset:2048 nt lds\n"
"v_mfma_f32_16x16x32_bf16 v[116:119],v[160:163],a[16:19],v[116:119]\n"
"buffer_load_dwordx4 v235,s[24:27],s67 offen offset:3072 nt lds\n"
"v_max3_f32 v246,v108,v109,v110\n"
"v_mfma_f32_16x16x32_bf16 v[120:123],v[176:179],a[16:19],v[120:123]\n"
"v_max3_f32 v246,v246,v111,v112\n"
"v_max3_f32 v246,v246,v113,v114\n"
"v_mfma_f32_16x16x32_bf16 v[116:119],v[164:167],a[28:31],v[116:119]\n"
"v_max_f32 v246,v246,v115\n"
"v_cmp_lt_f32 s[80:81],s64,v246\n"
"v_mfma_f32_16x16x32_bf16 v[120:123],v[180:183],a[28:31],v[120:123]\n"
"v_exp_f32 v108,v108\n"
"v_exp_f32 v109,v109\n"
"v_mfma_f32_16x16x32_bf16 v[116:119],v[168:171],a[40:43],v[116:119]\n"
"v_exp_f32 v110,v110\n"
"v_mfma_f32_16x16x32_bf16 v[120:123],v[184:187],a[40:43],v[120:123]\n"
"v_exp_f32 v111,v111\n"
"v_mfma_f32_16x16x32_bf16 v[124:127],v[156:159],a[8:11],v[140:143]\n"
"v_cvt_pk_bf16_f32 v144,v108,v109\n"
"v_exp_f32 v112,v112\n"
"v_mfma_f32_16x16x32_bf16 v[128:131],v[172:175],a[8:11],v[140:143]\n"
"v_exp_f32 v113,v113\n"
"v_cvt_pk_bf16_f32 v145,v110,v111\n"
"v_mfma_f32_16x16x32_bf16 v[124:127],v[160:163],a[20:23],v[124:127]\n"
"v_exp_f32 v114,v114\n"
"v_mfma_f32_16x16x32_bf16 v[128:131],v[176:179],a[20:23],v[128:131]\n"
"v_exp_f32 v115,v115\n"
"v_cvt_pk_bf16_f32 v146,v112,v113\n"
"v_mfma_f32_16x16x32_bf16 v[124:127],v[164:167],a[32:35],v[124:127]\n"
"v_cvt_pk_bf16_f32 v147,v114,v115\n"
"s_add_u32 s67,s67,0x10000\n"
"s_add_u32 m0,s66,0x1000\n"
"s_nop 0\n"
"buffer_load_dwordx4 v232,s[24:27],s67 offen nt lds\n"
"v_mfma_f32_16x16x32_bf16 v[128:131],v[180:183],a[32:35],v[128:131]\n"
"buffer_load_dwordx4 v233,s[24:27],s67 offen offset:1024 nt lds\n"
"buffer_load_dwordx4 v234,s[24:27],s67 offen offset:2048 nt lds\n"
"v_mfma_f32_16x16x32_bf16 v[124:127],v[168:171],a[44:47],v[124:127]\n"
"buffer_load_dwordx4 v235,s[24:27],s67 offen offset:3072 nt lds\n"
"s_waitcnt vmcnt(24)\n"
"v_max3_f32 v247,v116,v117,v118\n"
"v_max3_f32 v247,v247,v119,v120\n"
"v_mfma_f32_16x16x32_bf16 v[128:131],v[184:187],a[44:47],v[128:131]\n"
"v_max3_f32 v247,v247,v121,v122\n"
"v_max_f32 v247,v247,v123\n"
"v_cmp_lt_f32 vcc,s64,v247\n"
"ds_read_b64_tr_b16 v[188:189],v224 offset:8192\n"
"ds_read_b64_tr_b16 v[190:191],v224 offset:12288\n"
"ds_read_b64_tr_b16 v[192:193],v225 offset:8192\n"
"ds_read_b64_tr_b16 v[194:195],v225 offset:12288\n"
"ds_read_b64_tr_b16 v[196:197],v226 offset:8192\n"
"ds_read_b64_tr_b16 v[198:199],v226 offset:12288\n"
"ds_read_b64_tr_b16 v[200:201],v227 offset:8192\n"
"ds_read_b64_tr_b16 v[202:203],v227 offset:12288\n"
"ds_read_b64_tr_b16 v[204:205],v228 offset:8192\n"
"ds_read_b64_tr_b16 v[206:207],v228 offset:12288\n"
"ds_read_b64_tr_b16 v[208:209],v229 offset:8192\n"
"ds_read_b64_tr_b16 v[210:211],v229 offset:12288\n"
"ds_read_b64_tr_b16 v[212:213],v230 offset:8192\n"
"ds_read_b64_tr_b16 v[214:215],v230 offset:12288\n"
"ds_read_b64_tr_b16 v[216:217],v231 offset:8192\n"
"s_waitcnt lgkmcnt(14)\n"
"ds_read_b64_tr_b16 v[218:219],v231 offset:12288\n"
"s_cmp_lg_u64 s[80:81],0\n"
"s_cbranch_scc1 Lk_68x\n"
"Lk_68:\n"
"s_waitcnt lgkmcnt(14)\n"
"v_mfma_f32_16x16x32_bf16 v[0:3],v[188:191],v[144:147],v[0:3]\n"
"v_exp_f32 v116,v116\n"
"s_waitcnt lgkmcnt(12)\n"
"v_mfma_f32_16x16x32_bf16 v[4:7],v[192:195],v[144:147],v[4:7]\n"
"v_exp_f32 v117,v117\n"
"v_exp_f32 v118,v118\n"
"s_waitcnt lgkmcnt(10)\n"
"v_mfma_f32_16x16x32_bf16 v[8:11],v[196:199],v[144:147],v[8:11]\n"
"v_exp_f32 v119,v119\n"
"s_waitcnt lgkmcnt(8)\n"
"v_mfma_f32_16x16x32_bf16 v[12:15],v[200:203],v[144:147],v[12:15]\n"
"v_cvt_pk_bf16_f32 v148,v116,v117\n"
"v_exp_f32 v120,v120\n"
"s_waitcnt lgkmcnt(6)\n"
"v_mfma_f32_16x16x32_bf16 v[16:19],v[204:207],v[144:147],v[16:19]\n"
"v_exp_f32 v121,v121\n"
"v_cvt_pk_bf16_f32 v149,v118,v119\n"
"s_waitcnt lgkmcnt(4)\n"
"v_mfma_f32_16x16x32_bf16 v[20:23],v[208:211],v[144:147],v[20:23]\n"
"v_exp_f32 v122,v122\n"
"s_waitcnt lgkmcnt(0)\n"
"v_mfma_f32_16x16x32_bf16 v[24:27],v[212:215],v[144:147],v[24:27]\n"
"v_exp_f32 v123,v123\n"
"v_cvt_pk_bf16_f32 v150,v120,v121\n"
"v_mfma_f32_16x16x32_bf16 v[28:31],v[216:219],v[144:147],v[28:31]\n"
"v_cvt_pk_bf16_f32 v151,v122,v123\n"
"v_max3_f32 v248,v124,v125,v126\n"
"v_max3_f32 v248,v248,v127,v128\n"
"v_mfma_f32_16x16x32_bf16 v[96:99],a[48:51],v[144:147],v[96:99]\n"
"v_max3_f32 v248,v248,v129,v130\n"
"v_max_f32 v248,v248,v131\n"
"v_cmp_lt_f32 s[82:83],s64,v248\n"
"s_cbranch_vccnz Lk_69x\n"
"Lk_69:\n"
"v_mfma_f32_16x16x32_bf16 v[32:35],v[188:191],v[148:151],v[32:35]\n"
"v_exp_f32 v124,v124\n"
"v_mfma_f32_16x16x32_bf16 v[36:39],v[192:195],v[148:151],v[36:39]\n"
"v_exp_f32 v125,v125\n"
"v_exp_f32 v126,v126\n"
"v_mfma_f32_16x16x32_bf16 v[40:43],v[196:199],v[148:151],v[40:43]\n"
"v_exp_f32 v127,v127\n"
"v_cvt_pk_bf16_f32 v152,v124,v125\n"
"v_mfma_f32_16x16x32_bf16 v[44:47],v[200:203],v[148:151],v[44:47]\n"
"v_exp_f32 v128,v128\n"
"v_mfma_f32_16x16x32_bf16 v[48:51],v[204:207],v[148:151],v[48:51]\n"
"v_exp_f32 v129,v129\n"
"v_cvt_pk_bf16_f32 v153,v126,v127\n"
"v_mfma_f32_16x16x32_bf16 v[52:55],v[208:211],v[148:151],v[52:55]\n"
"v_exp_f32 v130,v130\n"
"v_exp_f32 v131,v131\n"
"v_mfma_f32_16x16x32_bf16 v[56:59],v[212:215],v[148:151],v[56:59]\n"
"v_cvt_pk_bf16_f32 v154,v128,v129\n"
"v_cvt_pk_bf16_f32 v155,v130,v131\n"
"v_mfma_f32_16x16x32_bf16 v[60:63],v[216:219],v[148:151],v[60:63]\n"
"s_add_u32 s67,s1,0x1800\n"
"s_and_b32 s67,s67,0x3fff\n"
"s_add_u32 m0,s66,0x2000\n"
"s_nop 0\n"
"buffer_load_dwordx4 v236,s[28:31],s67 offen nt lds\n"
"v_mfma_f32_16x16x32_bf16 v[100:103],a[48:51],v[148:151],v[100:103]\n"
"buffer_load_dwordx4 v237,s[28:31],s67 offen offset:1024 nt lds\n"
"buffer_load_dwordx4 v254,s[28:31],s67 offen offset:2048 nt lds\n"
"buffer_load_dwordx4 v255,s[28:31],s67 offen offset:3072 nt lds\n"
"s_cmp_lg_u64 s[82:83],0\n"
"s_cbranch_scc1 Lk_70x\n"
"Lk_70:\n"
"v_mfma_f32_16x16x32_bf16 v[64:67],v[188:191],v[152:155],v[64:67]\n"
"v_mfma_f32_16x16x32_bf16 v[68:71],v[192:195],v[152:155],v[68:71]\n"
"s_add_u32 s67,s67,0x10000\n"
"s_add_u32 m0,s66,0x3000\n"
"s_nop 0\n"
"buffer_load_dwordx4 v236,s[28:31],s67 offen nt lds\n"
"v_mfma_f32_16x16x32_bf16 v[72:75],v[196:199],v[152:155],v[72:75]\n"
"v_mfma_f32_16x16x32_bf16 v[76:79],v[200:203],v[152:155],v[76:79]\n"
"v_mfma_f32_16x16x32_bf16 v[80:83],v[204:207],v[152:155],v[80:83]\n"
"buffer_load_dwordx4 v237,s[28:31],s67 offen offset:1024 nt lds\n"
"v_mfma_f32_16x16x32_bf16 v[84:87],v[208:211],v[152:155],v[84:87]\n"
"v_mfma_f32_16x16x32_bf16 v[88:91],v[212:215],v[152:155],v[88:91]\n"
"buffer_load_dwordx4 v254,s[28:31],s67 offen offset:2048 nt lds\n"
"v_mfma_f32_16x16x32_bf16 v[92:95],v[216:219],v[152:155],v[92:95]\n"
"v_mfma_f32_16x16x32_bf16 v[104:107],a[48:51],v[152:155],v[104:107]\n"
"buffer_load_dwordx4 v255,s[28:31],s67 offen offset:3072 nt lds\n"
"s_waitcnt vmcnt(24)\n"
"ds_read_b128 v[156:159],v220 offset:16384\n"
"ds_read_b128 v[172:175],v220 offset:20480\n"
"ds_read_b128 v[160:163],v221 offset:16384\n"
"ds_read_b128 v[176:179],v221 offset:20480\n"
"ds_read_b128 v[164:167],v222 offset:16384\n"
"ds_read_b128 v[180:183],v222 offset:20480\n"
"ds_read_b128 v[168:171],v223 offset:16384\n"
"ds_read_b128 v[184:187],v223 offset:20480\n"
"s_waitcnt lgkmcnt(7)\n"
"v_mfma_f32_16x16x32_bf16 v[108:111],v[156:159],a[0:3],v[132:135]\n"
"s_waitcnt lgkmcnt(6)\n"
"v_mfma_f32_16x16x32_bf16 v[112:115],v[172:175],a[0:3],v[132:135]\n"
"s_waitcnt lgkmcnt(5)\n"
"v_mfma_f32_16x16x32_bf16 v[108:111],v[160:163],a[12:15],v[108:111]\n"
"s_waitcnt lgkmcnt(4)\n"
"v_mfma_f32_16x16x32_bf16 v[112:115],v[176:179],a[12:15],v[112:115]\n"
"s_waitcnt lgkmcnt(3)\n"
"v_mfma_f32_16x16x32_bf16 v[108:111],v[164:167],a[24:27],v[108:111]\n"
"s_waitcnt lgkmcnt(2)\n"
"v_mfma_f32_16x16x32_bf16 v[112:115],v[180:183],a[24:27],v[112:115]\n"
"s_waitcnt lgkmcnt(1)\n"
"v_mfma_f32_16x16x32_bf16 v[108:111],v[168:171],a[36:39],v[108:111]\n"
"s_waitcnt lgkmcnt(0)\n"
"v_mfma_f32_16x16x32_bf16 v[112:115],v[184:187],a[36:39],v[112:115]\n"
"v_mfma_f32_16x16x32_bf16 v[116:119],v[156:159],a[4:7],v[136:139]\n"
"s_add_u32 s67,s1,0x1c00\n"
"s_and_b32 s67,s67,0x3fff\n"
"s_add_u32 m0,s66,0x4000\n"
"s_nop 0\n"
"buffer_load_dwordx4 v232,s[24:27],s67 offen nt lds\n"
"v_mfma_f32_16x16x32_bf16 v[120:123],v[172:175],a[4:7],v[136:139]\n"
"buffer_load_dwordx4 v233,s[24:27],s67 offen offset:1024 nt lds\n"
"buffer_load_dwordx4 v234,s[24:27],s67 offen offset:2048 nt lds\n"
"v_mfma_f32_16x16x32_bf16 v[116:119],v[160:163],a[16:19],v[116:119]\n"
"buffer_load_dwordx4 v235,s[24:27],s67 offen offset:3072 nt lds\n"
"v_max3_f32 v246,v108,v109,v110\n"
"v_mfma_f32_16x16x32_bf16 v[120:123],v[176:179],a[16:19],v[120:123]\n"
"v_max3_f32 v246,v246,v111,v112\n"
"v_max3_f32 v246,v246,v113,v114\n"
"v_mfma_f32_16x16x32_bf16 v[116:119],v[164:167],a[28:31],v[116:119]\n"
"v_max_f32 v246,v246,v115\n"
"v_cmp_lt_f32 s[80:81],s64,v246\n"
"v_mfma_f32_16x16x32_bf16 v[120:123],v[180:183],a[28:31],v[120:123]\n"
"v_exp_f32 v108,v108\n"
"v_exp_f32 v109,v109\n"
"v_mfma_f32_16x16x32_bf16 v[116:119],v[168:171],a[40:43],v[116:119]\n"
"v_exp_f32 v110,v110\n"
"v_mfma_f32_16x16x32_bf16 v[120:123],v[184:187],a[40:43],v[120:123]\n"
"v_exp_f32 v111,v111\n"
"v_mfma_f32_16x16x32_bf16 v[124:127],v[156:159],a[8:11],v[140:143]\n"
"v_cvt_pk_bf16_f32 v144,v108,v109\n"
"v_exp_f32 v112,v112\n"
"v_mfma_f32_16x16x32_bf16 v[128:131],v[172:175],a[8:11],v[140:143]\n"
"v_exp_f32 v113,v113\n"
"v_cvt_pk_bf16_f32 v145,v110,v111\n"
"v_mfma_f32_16x16x32_bf16 v[124:127],v[160:163],a[20:23],v[124:127]\n"
"v_exp_f32 v114,v114\n"
"v_mfma_f32_16x16x32_bf16 v[128:131],v[176:179],a[20:23],v[128:131]\n"
"v_exp_f32 v115,v115\n"
"v_cvt_pk_bf16_f32 v146,v112,v113\n"
"v_mfma_f32_16x16x32_bf16 v[124:127],v[164:167],a[32:35],v[124:127]\n"
"v_cvt_pk_bf16_f32 v147,v114,v115\n"
"s_add_u32 s67,s67,0x10000\n"
"s_add_u32 m0,s66,0x5000\n"
"s_nop 0\n"
"buffer_load_dwordx4 v232,s[24:27],s67 offen nt lds\n"
"v_mfma_f32_16x16x32_bf16 v[128:131],v[180:183],a[32:35],v[128:131]\n"
"buffer_load_dwordx4 v233,s[24:27],s67 offen offset:1024 nt lds\n"
"buffer_load_dwordx4 v234,s[24:27],s67 offen offset:2048 nt lds\n"
"v_mfma_f32_16x16x32_bf16 v[124:127],v[168:171],a[44:47],v[124:127]\n"
"buffer_load_dwordx4 v235,s[24:27],s67 offen offset:3072 nt lds\n"
"s_waitcnt vmcnt(24)\n"
"v_max3_f32 v247,v116,v117,v118\n"
"v_max3_f32 v247,v247,v119,v120\n"
"v_mfma_f32_16x16x32_bf16 v[128:131],v[184:187],a[44:47],v[128:131]\n"
"v_max3_f32 v247,v247,v121,v122\n"
"v_max_f32 v247,v247,v123\n"
"v_cmp_lt_f32 vcc,s64,v247\n"
"ds_read_b64_tr_b16 v[188:189],v224 offset:24576\n"
"ds_read_b64_tr_b16 v[190:191],v224 offset:28672\n"
"ds_read_b64_tr_b16 v[192:193],v225 offset:24576\n"
"ds_read_b64_tr_b16 v[194:195],v225 offset:28672\n"
"ds_read_b64_tr_b16 v[196:197],v226 offset:24576\n"
"ds_read_b64_tr_b16 v[198:199],v226 offset:28672\n"
"ds_read_b64_tr_b16 v[200:201],v227 offset:24576\n"
"ds_read_b64_tr_b16 v[202:203],v227 offset:28672\n"
"ds_read_b64_tr_b16 v[204:205],v228 offset:24576\n"
"ds_read_b64_tr_b16 v[206:207],v228 offset:28672\n"
"ds_read_b64_tr_b16 v[208:209],v229 offset:24576\n"
"ds_read_b64_tr_b16 v[210:211],v229 offset:28672\n"
"ds_read_b64_tr_b16 v[212:213],v230 offset:24576\n"
"ds_read_b64_tr_b16 v[214:215],v230 offset:28672\n"
"ds_read_b64_tr_b16 v[216:217],v231 offset:24576\n"
"s_waitcnt lgkmcnt(14)\n"
"ds_read_b64_tr_b16 v[218:219],v231 offset:28672\n"
"s_cmp_lg_u64 s[80:81],0\n"
"s_cbranch_scc1 Lk_71x\n"
"Lk_71:\n"
"s_waitcnt lgkmcnt(14)\n"
"v_mfma_f32_16x16x32_bf16 v[0:3],v[188:191],v[144:147],v[0:3]\n"
"v_exp_f32 v116,v116\n"
"s_waitcnt lgkmcnt(12)\n"
"v_mfma_f32_16x16x32_bf16 v[4:7],v[192:195],v[144:147],v[4:7]\n"
"v_exp_f32 v117,v117\n"
"v_exp_f32 v118,v118\n"
"s_waitcnt lgkmcnt(10)\n"
"v_mfma_f32_16x16x32_bf16 v[8:11],v[196:199],v[144:147],v[8:11]\n"
"v_exp_f32 v119,v119\n"
"s_waitcnt lgkmcnt(8)\n"
"v_mfma_f32_16x16x32_bf16 v[12:15],v[200:203],v[144:147],v[12:15]\n"
"v_cvt_pk_bf16_f32 v148,v116,v117\n"
"v_exp_f32 v120,v120\n"
"s_waitcnt lgkmcnt(6)\n"
"v_mfma_f32_16x16x32_bf16 v[16:19],v[204:207],v[144:147],v[16:19]\n"
"v_exp_f32 v121,v121\n"
"v_cvt_pk_bf16_f32 v149,v118,v119\n"
"s_waitcnt lgkmcnt(4)\n"
"v_mfma_f32_16x16x32_bf16 v[20:23],v[208:211],v[144:147],v[20:23]\n"
"v_exp_f32 v122,v122\n"
"s_waitcnt lgkmcnt(0)\n"
"v_mfma_f32_16x16x32_bf16 v[24:27],v[212:215],v[144:147],v[24:27]\n"
"v_exp_f32 v123,v123\n"
"v_cvt_pk_bf16_f32 v150,v120,v121\n"
"v_mfma_f32_16x16x32_bf16 v[28:31],v[216:219],v[144:147],v[28:31]\n"
"v_cvt_pk_bf16_f32 v151,v122,v123\n"
"v_max3_f32 v248,v124,v125,v126\n"
"v_max3_f32 v248,v248,v127,v128\n"
"v_mfma_f32_16x16x32_bf16 v[96:99],a[48:51],v[144:147],v[96:99]\n"
"v_max3_f32 v248,v248,v129,v130\n"
"v_max_f32 v248,v248,v131\n"
"v_cmp_lt_f32 s[82:83],s64,v248\n"
"s_cbranch_vccnz Lk_72x\n"
"Lk_72:\n"
"v_mfma_f32_16x16x32_bf16 v[32:35],v[188:191],v[148:151],v[32:35]\n"
"v_exp_f32 v124,v124\n"
"v_mfma_f32_16x16x32_bf16 v[36:39],v[192:195],v[148:151],v[36:39]\n"
"v_exp_f32 v125,v125\n"
"v_exp_f32 v126,v126\n"
"v_mfma_f32_16x16x32_bf16 v[40:43],v[196:199],v[148:151],v[40:43]\n"
"v_exp_f32 v127,v127\n"
"v_cvt_pk_bf16_f32 v152,v124,v125\n"
"v_mfma_f32_16x16x32_bf16 v[44:47],v[200:203],v[148:151],v[44:47]\n"
"v_exp_f32 v128,v128\n"
"v_mfma_f32_16x16x32_bf16 v[48:51],v[204:207],v[148:151],v[48:51]\n"
"v_exp_f32 v129,v129\n"
"v_cvt_pk_bf16_f32 v153,v126,v127\n"
"v_mfma_f32_16x16x32_bf16 v[52:55],v[208:211],v[148:151],v[52:55]\n"
"v_exp_f32 v130,v130\n"
"v_exp_f32 v131,v131\n"
"v_mfma_f32_16x16x32_bf16 v[56:59],v[212:215],v[148:151],v[56:59]\n"
"v_cvt_pk_bf16_f32 v154,v128,v129\n"
"v_cvt_pk_bf16_f32 v155,v130,v131\n"
"v_mfma_f32_16x16x32_bf16 v[60:63],v[216:219],v[148:151],v[60:63]\n"
"s_add_u32 s67,s1,0x1c00\n"
"s_and_b32 s67,s67,0x3fff\n"
"s_add_u32 m0,s66,0x6000\n"
"s_nop 0\n"
"buffer_load_dwordx4 v236,s[28:31],s67 offen nt lds\n"
"v_mfma_f32_16x16x32_bf16 v[100:103],a[48:51],v[148:151],v[100:103]\n"
"buffer_load_dwordx4 v237,s[28:31],s67 offen offset:1024 nt lds\n"
"buffer_load_dwordx4 v254,s[28:31],s67 offen offset:2048 nt lds\n"
"buffer_load_dwordx4 v255,s[28:31],s67 offen offset:3072 nt lds\n"
"s_cmp_lg_u64 s[82:83],0\n"
"s_cbranch_scc1 Lk_73x\n"
"Lk_73:\n"
"v_mfma_f32_16x16x32_bf16 v[64:67],v[188:191],v[152:155],v[64:67]\n"
"v_mfma_f32_16x16x32_bf16 v[68:71],v[192:195],v[152:155],v[68:71]\n"
"s_add_u32 s67,s67,0x10000\n"
"s_add_u32 m0,s66,0x7000\n"
"s_nop 0\n"
"buffer_load_dwordx4 v236,s[28:31],s67 offen nt lds\n"
"v_mfma_f32_16x16x32_bf16 v[72:75],v[196:199],v[152:155],v[72:75]\n"
"v_mfma_f32_16x16x32_bf16 v[76:79],v[200:203],v[152:155],v[76:79]\n"
"v_mfma_f32_16x16x32_bf16 v[80:83],v[204:207],v[152:155],v[80:83]\n"
"buffer_load_dwordx4 v237,s[28:31],s67 offen offset:1024 nt lds\n"
"v_mfma_f32_16x16x32_bf16 v[84:87],v[208:211],v[152:155],v[84:87]\n"
"v_mfma_f32_16x16x32_bf16 v[88:91],v[212:215],v[152:155],v[88:91]\n"
"buffer_load_dwordx4 v254,s[28:31],s67 offen offset:2048 nt lds\n"
"v_mfma_f32_16x16x32_bf16 v[92:95],v[216:219],v[152:155],v[92:95]\n"
"v_mfma_f32_16x16x32_bf16 v[104:107],a[48:51],v[152:155],v[104:107]\n"
"buffer_load_dwordx4 v255,s[28:31],s67 offen offset:3072 nt lds\n"
"s_add_u32 s60,s57,0\n"
"s_bfe_u32 s61,s60,0x20002\n"
"s_and_b32 s62,s60,1\n"
"s_mul_i32 s62,s62,0xc00\n"
"s_lshl_b32 s63,s61,13\n"
"s_add_u32 s63,s63,0x20000\n"
"s_lshl_b32 s61,s61,15\n"
"s_add_u32 s61,s61,0x6000\n"
"s_bitcmp1_b32 s60,1\n"
"s_cselect_b32 s61,s61,s63\n"
"s_add_u32 s8,s61,s62\n"
"s_add_u32 s60,s57,1\n"
"s_bfe_u32 s61,s60,0x20002\n"
"s_and_b32 s62,s60,1\n"
"s_mul_i32 s62,s62,0xc00\n"
"s_lshl_b32 s63,s61,13\n"
"s_add_u32 s63,s63,0x20000\n"
"s_lshl_b32 s61,s61,15\n"
"s_add_u32 s61,s61,0x6000\n"
"s_bitcmp1_b32 s60,1\n"
"s_cselect_b32 s61,s61,s63\n"
"s_add_u32 s9,s61,s62\n"
"s_add_u32 s60,s57,2\n"
"s_bfe_u32 s61,s60,0x20002\n"
"s_and_b32 s62,s60,1\n"
"s_mul_i32 s62,s62,0xc00\n"
"s_lshl_b32 s63,s61,13\n"
"s_add_u32 s63,s63,0x20000\n"
"s_lshl_b32 s61,s61,15\n"
"s_add_u32 s61,s61,0x6000\n"
"s_bitcmp1_b32 s60,1\n"
"s_cselect_b32 s61,s61,s63\n"
"s_add_u32 s10,s61,s62\n"
"s_add_u32 s60,s57,3\n"
"s_bfe_u32 s61,s60,0x20002\n"
"s_and_b32 s62,s60,1\n"
"s_mul_i32 s62,s62,0xc00\n"
"s_lshl_b32 s63,s61,13\n"
"s_add_u32 s63,s63,0x20000\n"
"s_lshl_b32 s61,s61,15\n"
"s_add_u32 s61,s61,0x6000\n"
"s_bitcmp1_b32 s60,1\n"
"s_cselect_b32 s61,s61,s63\n"
"s_add_u32 s11,s61,s62\n"
"s_add_u32 s60,s57,4\n"
"s_bfe_u32 s61,s60,0x20002\n"
"s_and_b32 s62,s60,1\n"
"s_mul_i32 s62,s62,0xc00\n"
"s_lshl_b32 s63,s61,13\n"
"s_add_u32 s63,s63,0x20000\n"
"s_lshl_b32 s61,s61,15\n"
"s_add_u32 s61,s61,0x6000\n"
"s_bitcmp1_b32 s60,1\n"
"s_cselect_b32 s61,s61,s63\n"
"s_add_u32 s12,s61,s62\n"
"s_add_u32 s60,s57,5\n"
"s_bfe_u32 s61,s60,0x20002\n"
"s_and_b32 s62,s60,1\n"
"s_mul_i32 s62,s62,0xc00\n"
"s_lshl_b32 s63,s61,13\n"
"s_add_u32 s63,s63,0x20000\n"
"s_lshl_b32 s61,s61,15\n"
"s_add_u32 s61,s61,0x6000\n"
"s_bitcmp1_b32 s60,1\n"
"s_cselect_b32 s61,s61,s63\n"
"s_add_u32 s13,s61,s62\n"
"s_add_u32 s60,s57,6\n"
"s_bfe_u32 s61,s60,0x20002\n"
"s_and_b32 s62,s60,1\n"
"s_mul_i32 s62,s62,0xc00\n"
"s_lshl_b32 s63,s61,13\n"
"s_add_u32 s63,s63,0x20000\n"
"s_lshl_b32 s61,s61,15\n"
"s_add_u32 s61,s61,0x6000\n"
"s_bitcmp1_b32 s60,1\n"
"s_cselect_b32 s61,s61,s63\n"
"s_add_u32 s14,s61,s62\n"
"s_add_u32 s60,s57,7\n"
"s_bfe_u32 s61,s60,0x20002\n"
"s_and_b32 s62,s60,1\n"
"s_mul_i32 s62,s62,0xc00\n"
"s_lshl_b32 s63,s61,13\n"
"s_add_u32 s63,s63,0x20000\n"
"s_lshl_b32 s61,s61,15\n"
"s_add_u32 s61,s61,0x6000\n"
"s_bitcmp1_b32 s60,1\n"
"s_cselect_b32 s61,s61,s63\n"
"s_add_u32 s15,s61,s62\n"
"s_add_u32 s60,s57,8\n"
"s_bfe_u32 s61,s60,0x20002\n"
"s_and_b32 s62,s60,1\n"
"s_mul_i32 s62,s62,0xc00\n"
"s_lshl_b32 s63,s61,13\n"
"s_add_u32 s63,s63,0x20000\n"
"s_lshl_b32 s61,s61,15\n"
"s_add_u32 s61,s61,0x6000\n"
"s_bitcmp1_b32 s60,1\n"
"s_cselect_b32 s61,s61,s63\n"
"s_add_u32 s16,s61,s62\n"
"s_add_u32 s60,s57,9\n"
"s_bfe_u32 s61,s60,0x20002\n"
"s_and_b32 s62,s60,1\n"
"s_mul_i32 s62,s62,0xc00\n"
"s_lshl_b32 s63,s61,13\n"
"s_add_u32 s63,s63,0x20000\n"
"s_lshl_b32 s61,s61,15\n"
"s_add_u32 s61,s61,0x6000\n"
"s_bitcmp1_b32 s60,1\n"
"s_cselect_b32 s61,s61,s63\n"
"s_add_u32 s17,s61,s62\n"
"s_add_u32 s60,s57,10\n"
"s_bfe_u32 s61,s60,0x20002\n"
"s_and_b32 s62,s60,1\n"
"s_mul_i32 s62,s62,0xc00\n"
"s_lshl_b32 s63,s61,13\n"
"s_add_u32 s63,s63,0x20000\n"
"s_lshl_b32 s61,s61,15\n"
"s_add_u32 s61,s61,0x6000\n"
"s_bitcmp1_b32 s60,1\n"
"s_cselect_b32 s61,s61,s63\n"
"s_add_u32 s32,s61,s62\n"
"s_add_u32 s60,s57,11\n"
"s_bfe_u32 s61,s60,0x20002\n"
"s_and_b32 s62,s60,1\n"
"s_mul_i32 s62,s62,0xc00\n"
"s_lshl_b32 s63,s61,13\n"
"s_add_u32 s63,s63,0x20000\n"
"s_lshl_b32 s61,s61,15\n"
"s_add_u32 s61,s61,0x6000\n"
"s_bitcmp1_b32 s60,1\n"
"s_cselect_b32 s61,s61,s63\n"
"s_add_u32 s33,s61,s62\n"
"s_add_u32 s60,s57,12\n"
"s_bfe_u32 s61,s60,0x20002\n"
"s_and_b32 s62,s60,1\n"
"s_mul_i32 s62,s62,0xc00\n"
"s_lshl_b32 s63,s61,13\n"
"s_add_u32 s63,s63,0x20000\n"
"s_lshl_b32 s61,s61,15\n"
"s_add_u32 s61,s61,0x6000\n"
"s_bitcmp1_b32 s60,1\n"
"s_cselect_b32 s61,s61,s63\n"
"s_add_u32 s34,s61,s62\n"
"s_add_u32 s60,s57,13\n"
"s_bfe_u32 s61,s60,0x20002\n"
"s_and_b32 s62,s60,1\n"
"s_mul_i32 s62,s62,0xc00\n"
"s_lshl_b32 s63,s61,13\n"
"s_add_u32 s63,s63,0x20000\n"
"s_lshl_b32 s61,s61,15\n"
"s_add_u32 s61,s61,0x6000\n"
"s_bitcmp1_b32 s60,1\n"
"s_cselect_b32 s61,s61,s63\n"
"s_add_u32 s35,s61,s62\n"
"s_add_u32 s60,s57,14\n"
"s_bfe_u32 s61,s60,0x20002\n"
"s_and_b32 s62,s60,1\n"
"s_mul_i32 s62,s62,0xc00\n"
"s_lshl_b32 s63,s61,13\n"
"s_add_u32 s63,s63,0x20000\n"
"s_lshl_b32 s61,s61,15\n"
"s_add_u32 s61,s61,0x6000\n"
"s_bitcmp1_b32 s60,1\n"
"s_cselect_b32 s61,s61,s63\n"
"s_add_u32 s36,s61,s62\n"
"s_add_u32 s60,s57,15\n"
"s_bfe_u32 s61,s60,0x20002\n"
"s_and_b32 s62,s60,1\n"
"s_mul_i32 s62,s62,0xc00\n"
"s_lshl_b32 s63,s61,13\n"
"s_add_u32 s63,s63,0x20000\n"
"s_lshl_b32 s61,s61,15\n"
"s_add_u32 s61,s61,0x6000\n"
"s_bitcmp1_b32 s60,1\n"
"s_cselect_b32 s61,s61,s63\n"
"s_add_u32 s37,s61,s62\n"
"s_mul_i32 s84,s56,0xc400\n"
"s_mul_i32 s60,s7,0x3000\n"
"s_add_u32 s84,s84,s60\n"
"s_add_u32 s85,s84,0x1000\n"
"s_add_u32 s86,s84,0x2000\n"
"s_waitcnt vmcnt(24)\n"
"ds_read_b128 v[156:159],v220\n"
"ds_read_b128 v[172:175],v220 offset:4096\n"
"ds_read_b128 v[160:163],v221\n"
"ds_read_b128 v[176:179],v221 offset:4096\n"
"ds_read_b128 v[164:167],v222\n"
"ds_read_b128 v[180:183],v222 offset:4096\n"
"ds_read_b128 v[168:171],v223\n"
"ds_read_b128 v[184:187],v223 offset:4096\n"
"s_waitcnt lgkmcnt(7)\n"
"v_mfma_f32_16x16x32_bf16 v[108:111],v[156:159],a[0:3],v[132:135]\n"
"s_waitcnt lgkmcnt(6)\n"
"v_mfma_f32_16x16x32_bf16 v[112:115],v[172:175],a[0:3],v[132:135]\n"
"s_waitcnt lgkmcnt(5)\n"
"v_mfma_f32_16x16x32_bf16 v[108:111],v[160:163],a[12:15],v[108:111]\n"
"s_waitcnt lgkmcnt(4)\n"
"v_mfma_f32_16x16x32_bf16 v[112:115],v[176:179],a[12:15],v[112:115]\n"
"s_waitcnt lgkmcnt(3)\n"
"v_mfma_f32_16x16x32_bf16 v[108:111],v[164:167],a[24:27],v[108:111]\n"
"s_waitcnt lgkmcnt(2)\n"
"v_mfma_f32_16x16x32_bf16 v[112:115],v[180:183],a[24:27],v[112:115]\n"
"s_waitcnt lgkmcnt(1)\n"
"v_mfma_f32_16x16x32_bf16 v[108:111],v[168:171],a[36:39],v[108:111]\n"
"s_waitcnt lgkmcnt(0)\n"
"v_mfma_f32_16x16x32_bf16 v[112:115],v[184:187],a[36:39],v[112:115]\n"
"v_mfma_f32_16x16x32_bf16 v[116:119],v[156:159],a[4:7],v[136:139]\n"
"s_add_u32 s67,s1,0x2000\n"
"s_and_b32 s67,s67,0x3fff\n"
"s_add_u32 m0,s66,0\n"
"s_nop 0\n"
"buffer_load_dwordx4 v232,s[24:27],s67 offen nt lds\n"
"v_mfma_f32_16x16x32_bf16 v[120:123],v[172:175],a[4:7],v[136:139]\n"
"buffer_load_dwordx4 v233,s[24:27],s67 offen offset:1024 nt lds\n"
"buffer_load_dwordx4 v234,s[24:27],s67 offen offset:2048 nt lds\n"
"v_mfma_f32_16x16x32_bf16 v[116:119],v[160:163],a[16:19],v[116:119]\n"
"buffer_load_dwordx4 v235,s[24:27],s67 offen offset:3072 nt lds\n"
"v_max3_f32 v246,v108,v109,v110\n"
"v_mfma_f32_16x16x32_bf16 v[120:123],v[176:179],a[16:19],v[120:123]\n"
"v_max3_f32 v246,v246,v111,v112\n"
"v_max3_f32 v246,v246,v113,v114\n"
"v_mfma_f32_16x16x32_bf16 v[116:119],v[164:167],a[28:31],v[116:119]\n"
"v_max_f32 v246,v246,v115\n"
"v_cmp_lt_f32 s[80:81],s64,v246\n"
"v_mfma_f32_16x16x32_bf16 v[120:123],v[180:183],a[28:31],v[120:123]\n"
"v_exp_f32 v108,v108\n"
"v_exp_f32 v109,v109\n"
"v_mfma_f32_16x16x32_bf16 v[116:119],v[168:171],a[40:43],v[116:119]\n"
"v_exp_f32 v110,v110\n"
"v_mfma_f32_16x16x32_bf16 v[120:123],v[184:187],a[40:43],v[120:123]\n"
"v_exp_f32 v111,v111\n"
"v_mfma_f32_16x16x32_bf16 v[124:127],v[156:159],a[8:11],v[140:143]\n"
"v_cvt_pk_bf16_f32 v144,v108,v109\n"
"v_exp_f32 v112,v112\n"
"v_mfma_f32_16x16x32_bf16 v[128:131],v[172:175],a[8:11],v[140:143]\n"
"v_exp_f32 v113,v113\n"
"v_cvt_pk_bf16_f32 v145,v110,v111\n"
"v_mfma_f32_16x16x32_bf16 v[124:127],v[160:163],a[20:23],v[124:127]\n"
"v_exp_f32 v114,v114\n"
"v_mfma_f32_16x16x32_bf16 v[128:131],v[176:179],a[20:23],v[128:131]\n"
"v_exp_f32 v115,v115\n"
"v_cvt_pk_bf16_f32 v146,v112,v113\n"
"v_mfma_f32_16x16x32_bf16 v[124:127],v[164:167],a[32:35],v[124:127]\n"
"v_cvt_pk_bf16_f32 v147,v114,v115\n"
"s_add_u32 s67,s67,0x10000\n"
"s_add_u32 m0,s66,0x1000\n"
"s_nop 0\n"
"buffer_load_dwordx4 v232,s[24:27],s67 offen nt lds\n"
"v_mfma_f32_16x16x32_bf16 v[128:131],v[180:183],a[32:35],v[128:131]\n"
"buffer_load_dwordx4 v233,s[24:27],s67 offen offset:1024 nt lds\n"
"buffer_load_dwordx4 v234,s[24:27],s67 offen offset:2048 nt lds\n"
"v_mfma_f32_16x16x32_bf16 v[124:127],v[168:171],a[44:47],v[124:127]\n"
"buffer_load_dwordx4 v235,s[24:27],s67 offen offset:3072 nt lds\n"
"s_waitcnt vmcnt(24)\n"
"v_max3_f32 v247,v116,v117,v118\n"
"v_max3_f32 v247,v247,v119,v120\n"
"v_mfma_f32_16x16x32_bf16 v[128:131],v[184:187],a[44:47],v[128:131]\n"
"v_max3_f32 v247,v247,v121,v122\n"
"v_max_f32 v247,v247,v123\n"
"v_cmp_lt_f32 vcc,s64,v247\n"
"ds_read_b64_tr_b16 v[188:189],v224 offset:8192\n"
"ds_read_b64_tr_b16 v[190:191],v224 offset:12288\n"
"ds_read_b64_tr_b16 v[192:193],v225 offset:8192\n"
"ds_read_b64_tr_b16 v[194:195],v225 offset:12288\n"
"ds_read_b64_tr_b16 v[196:197],v226 offset:8192\n"
"ds_read_b64_tr_b16 v[198:199],v226 offset:12288\n"
"ds_read_b64_tr_b16 v[200:201],v227 offset:8192\n"
"ds_read_b64_tr_b16 v[202:203],v227 offset:12288\n"
"ds_read_b64_tr_b16 v[204:205],v228 offset:8192\n"
"ds_read_b64_tr_b16 v[206:207],v228 offset:12288\n"
"ds_read_b64_tr_b16 v[208:209],v229 offset:8192\n"
"ds_read_b64_tr_b16 v[210:211],v229 offset:12288\n"
"ds_read_b64_tr_b16 v[212:213],v230 offset:8192\n"
"ds_read_b64_tr_b16 v[214:215],v230 offset:12288\n"
"ds_read_b64_tr_b16 v[216:217],v231 offset:8192\n"
"s_waitcnt lgkmcnt(14)\n"
"ds_read_b64_tr_b16 v[218:219],v231 offset:12288\n"
"s_cmp_lg_u64 s[80:81],0\n"
"s_cbranch_scc1 Lk_74x\n"
"Lk_74:\n"
"s_waitcnt lgkmcnt(14)\n"
"v_mfma_f32_16x16x32_bf16 v[0:3],v[188:191],v[144:147],v[0:3]\n"
"v_exp_f32 v116,v116\n"
"s_waitcnt lgkmcnt(12)\n"
"v_mfma_f32_16x16x32_bf16 v[4:7],v[192:195],v[144:147],v[4:7]\n"
"v_exp_f32 v117,v117\n"
"v_exp_f32 v118,v118\n"
"s_waitcnt lgkmcnt(10)\n"
"v_mfma_f32_16x16x32_bf16 v[8:11],v[196:199],v[144:147],v[8:11]\n"
"v_exp_f32 v119,v119\n"
"s_waitcnt lgkmcnt(8)\n"
"v_mfma_f32_16x16x32_bf16 v[12:15],v[200:203],v[144:147],v[12:15]\n"
"v_cvt_pk_bf16_f32 v148,v116,v117\n"
"v_exp_f32 v120,v120\n"
"s_waitcnt lgkmcnt(6)\n"
"v_mfma_f32_16x16x32_bf16 v[16:19],v[204:207],v[144:147],v[16:19]\n"
"v_exp_f32 v121,v121\n"
"v_cvt_pk_bf16_f32 v149,v118,v119\n"
"s_waitcnt lgkmcnt(4)\n"
"v_mfma_f32_16x16x32_bf16 v[20:23],v[208:211],v[144:147],v[20:23]\n"
"v_exp_f32 v122,v122\n"
"s_waitcnt lgkmcnt(0)\n"
"v_mfma_f32_16x16x32_bf16 v[24:27],v[212:215],v[144:147],v[24:27]\n"
"v_exp_f32 v123,v123\n"
"v_cvt_pk_bf16_f32 v150,v120,v121\n"
"v_mfma_f32_16x16x32_bf16 v[28:31],v[216:219],v[144:147],v[28:31]\n"
"v_cvt_pk_bf16_f32 v151,v122,v123\n"
"v_max3_f32 v248,v124,v125,v126\n"
"v_max3_f32 v248,v248,v127,v128\n"
"v_mfma_f32_16x16x32_bf16 v[96:99],a[48:51],v[144:147],v[96:99]\n"
"v_max3_f32 v248,v248,v129,v130\n"
"v_max_f32 v248,v248,v131\n"
"v_cmp_lt_f32 s[82:83],s64,v248\n"
"s_cbranch_vccnz Lk_75x\n"
"Lk_75:\n"
"v_mfma_f32_16x16x32_bf16 v[32:35],v[188:191],v[148:151],v[32:35]\n"
"v_exp_f32 v124,v124\n"
"v_mfma_f32_16x16x32_bf16 v[36:39],v[192:195],v[148:151],v[36:39]\n"
"v_exp_f32 v125,v125\n"
"v_exp_f32 v126,v126\n"
"v_mfma_f32_16x16x32_bf16 v[40:43],v[196:199],v[148:151],v[40:43]\n"
"v_exp_f32 v127,v127\n"
"v_cvt_pk_bf16_f32 v152,v124,v125\n"
"v_mfma_f32_16x16x32_bf16 v[44:47],v[200:203],v[148:151],v[44:47]\n"
"v_exp_f32 v128,v128\n"
"v_mfma_f32_16x16x32_bf16 v[48:51],v[204:207],v[148:151],v[48:51]\n"
"v_exp_f32 v129,v129\n"
"v_cvt_pk_bf16_f32 v153,v126,v127\n"
"v_mfma_f32_16x16x32_bf16 v[52:55],v[208:211],v[148:151],v[52:55]\n"
"v_exp_f32 v130,v130\n"
"v_exp_f32 v131,v131\n"
"v_mfma_f32_16x16x32_bf16 v[56:59],v[212:215],v[148:151],v[56:59]\n"
"v_cvt_pk_bf16_f32 v154,v128,v129\n"
"v_cvt_pk_bf16_f32 v155,v130,v131\n"
"v_mfma_f32_16x16x32_bf16 v[60:63],v[216:219],v[148:151],v[60:63]\n"
"s_add_u32 s67,s1,0x2000\n"
"s_and_b32 s67,s67,0x3fff\n"
"s_add_u32 m0,s66,0x2000\n"
"s_nop 0\n"
"buffer_load_dwordx4 v236,s[28:31],s67 offen nt lds\n"
"v_mfma_f32_16x16x32_bf16 v[100:103],a[48:51],v[148:151],v[100:103]\n"
"buffer_load_dwordx4 v237,s[28:31],s67 offen offset:1024 nt lds\n"
"buffer_load_dwordx4 v254,s[28:31],s67 offen offset:2048 nt lds\n"
"buffer_load_dwordx4 v255,s[28:31],s67 offen offset:3072 nt lds\n"
"s_cmp_lg_u64 s[82:83],0\n"
"s_cbranch_scc1 Lk_76x\n"
"Lk_76:\n"
"v_mfma_f32_16x16x32_bf16 v[64:67],v[188:191],v[152:155],v[64:67]\n"
"v_mfma_f32_16x16x32_bf16 v[68:71],v[192:195],v[152:155],v[68:71]\n"
"s_add_u32 s67,s67,0x10000\n"
"s_add_u32 m0,s66,0x3000\n"
"s_nop 0\n"
"buffer_load_dwordx4 v236,s[28:31],s67 offen nt lds\n"
"v_mfma_f32_16x16x32_bf16 v[72:75],v[196:199],v[152:155],v[72:75]\n"
"v_mfma_f32_16x16x32_bf16 v[76:79],v[200:203],v[152:155],v[76:79]\n"
"v_mfma_f32_16x16x32_bf16 v[80:83],v[204:207],v[152:155],v[80:83]\n"
"buffer_load_dwordx4 v237,s[28:31],s67 offen offset:1024 nt lds\n"
"v_mfma_f32_16x16x32_bf16 v[84:87],v[208:211],v[152:155],v[84:87]\n"
"v_mfma_f32_16x16x32_bf16 v[88:91],v[212:215],v[152:155],v[88:91]\n"
"buffer_load_dwordx4 v254,s[28:31],s67 offen offset:2048 nt lds\n"
"v_mfma_f32_16x16x32_bf16 v[92:95],v[216:219],v[152:155],v[92:95]\n"
"v_mfma_f32_16x16x32_bf16 v[104:107],a[48:51],v[152:155],v[104:107]\n"
"buffer_load_dwordx4 v255,s[28:31],s67 offen offset:3072 nt lds\n"
"s_waitcnt vmcnt(24)\n"
"ds_read_b128 v[156:159],v220 offset:16384\n"
"ds_read_b128 v[172:175],v220 offset:20480\n"
"ds_read_b128 v[160:163],v221 offset:16384\n"
"ds_read_b128 v[176:179],v221 offset:20480\n"
"ds_read_b128 v[164:167],v222 offset:16384\n"
"ds_read_b128 v[180:183],v222 offset:20480\n"
"ds_read_b128 v[168:171],v223 offset:16384\n"
"ds_read_b128 v[184:187],v223 offset:20480\n"
"s_waitcnt lgkmcnt(7)\n"
"v_mfma_f32_16x16x32_bf16 v[108:111],v[156:159],a[0:3],v[132:135]\n"
"s_waitcnt lgkmcnt(6)\n"
"v_mfma_f32_16x16x32_bf16 v[112:115],v[172:175],a[0:3],v[132:135]\n"
"s_waitcnt lgkmcnt(5)\n"
"v_mfma_f32_16x16x32_bf16 v[108:111],v[160:163],a[12:15],v[108:111]\n"
"s_waitcnt lgkmcnt(4)\n"
"v_mfma_f32_16x16x32_bf16 v[112:115],v[176:179],a[12:15],v[112:115]\n"
"s_waitcnt lgkmcnt(3)\n"
"v_mfma_f32_16x16x32_bf16 v[108:111],v[164:167],a[24:27],v[108:111]\n"
"s_waitcnt lgkmcnt(2)\n"
"v_mfma_f32_16x16x32_bf16 v[112:115],v[180:183],a[24:27],v[112:115]\n"
"s_waitcnt lgkmcnt(1)\n"
"v_mfma_f32_16x16x32_bf16 v[108:111],v[168:171],a[36:39],v[108:111]\n"
"s_waitcnt lgkmcnt(0)\n"
"v_mfma_f32_16x16x32_bf16 v[112:115],v[184:187],a[36:39],v[112:115]\n"
"v_mfma_f32_16x16x32_bf16 v[116:119],v[156:159],a[4:7],v[136:139]\n"
"s_add_u32 s67,s1,0x2400\n"
"s_and_b32 s67,s67,0x3fff\n"
"s_add_u32 m0,s66,0x4000\n"
"s_nop 0\n"
"buffer_load_dwordx4 v232,s[24:27],s67 offen nt lds\n"
"v_mfma_f32_16x16x32_bf16 v[120:123],v[172:175],a[4:7],v[136:139]\n"
"buffer_load_dwordx4 v233,s[24:27],s67 offen offset:1024 nt lds\n"
"buffer_load_dwordx4 v234,s[24:27],s67 offen offset:2048 nt lds\n"
"v_mfma_f32_16x16x32_bf16 v[116:119],v[160:163],a[16:19],v[116:119]\n"
"buffer_load_dwordx4 v235,s[24:27],s67 offen offset:3072 nt lds\n"
"v_max3_f32 v246,v108,v109,v110\n"
"v_mfma_f32_16x16x32_bf16 v[120:123],v[176:179],a[16:19],v[120:123]\n"
"v_max3_f32 v246,v246,v111,v112\n"
"v_max3_f32 v246,v246,v113,v114\n"
"v_mfma_f32_16x16x32_bf16 v[116:119],v[164:167],a[28:31],v[116:119]\n"
"v_max_f32 v246,v246,v115\n"
"v_cmp_lt_f32 s[80:81],s64,v246\n"
"v_mfma_f32_16x16x32_bf16 v[120:123],v[180:183],a[28:31],v[120:123]\n"
"v_exp_f32 v108,v108\n"
"v_exp_f32 v109,v109\n"
"v_mfma_f32_16x16x32_bf16 v[116:119],v[168:171],a[40:43],v[116:119]\n"
"v_exp_f32 v110,v110\n"
"v_mfma_f32_16x16x32_bf16 v[120:123],v[184:187],a[40:43],v[120:123]\n"
"v_exp_f32 v111,v111\n"
"v_mfma_f32_16x16x32_bf16 v[124:127],v[156:159],a[8:11],v[140:143]\n"
"v_cvt_pk_bf16_f32 v144,v108,v109\n"
"v_exp_f32 v112,v112\n"
"v_mfma_f32_16x16x32_bf16 v[128:131],v[172:175],a[8:11],v[140:143]\n"
"v_exp_f32 v113,v113\n"
"v_cvt_pk_bf16_f32 v145,v110,v111\n"
"v_mfma_f32_16x16x32_bf16 v[124:127],v[160:163],a[20:23],v[124:127]\n"
"v_exp_f32 v114,v114\n"
"v_mfma_f32_16x16x32_bf16 v[128:131],v[176:179],a[20:23],v[128:131]\n"
"v_exp_f32 v115,v115\n"
"v_cvt_pk_bf16_f32 v146,v112,v113\n"
"v_mfma_f32_16x16x32_bf16 v[124:127],v[164:167],a[32:35],v[124:127]\n"
"v_cvt_pk_bf16_f32 v147,v114,v115\n"
"s_add_u32 s67,s67,0x10000\n"
"s_add_u32 m0,s66,0x5000\n"
"s_nop 0\n"
"buffer_load_dwordx4 v232,s[24:27],s67 offen nt lds\n"
"v_mfma_f32_16x16x32_bf16 v[128:131],v[180:183],a[32:35],v[128:131]\n"
"buffer_load_dwordx4 v233,s[24:27],s67 offen offset:1024 nt lds\n"
"buffer_load_dwordx4 v234,s[24:27],s67 offen offset:2048 nt lds\n"
"v_mfma_f32_16x16x32_bf16 v[124:127],v[168:171],a[44:47],v[124:127]\n"
"buffer_load_dwordx4 v235,s[24:27],s67 offen offset:3072 nt lds\n"
"s_waitcnt vmcnt(24)\n"
"v_max3_f32 v247,v116,v117,v118\n"
"v_max3_f32 v247,v247,v119,v120\n"
"v_mfma_f32_16x16x32_bf16 v[128:131],v[184:187],a[44:47],v[128:131]\n"
"v_max3_f32 v247,v247,v121,v122\n"
"v_max_f32 v247,v247,v123\n"
"v_cmp_lt_f32 vcc,s64,v247\n"
"ds_read_b64_tr_b16 v[188:189],v224 offset:24576\n"
"ds_read_b64_tr_b16 v[190:191],v224 offset:28672\n"
"ds_read_b64_tr_b16 v[192:193],v225 offset:24576\n"
"ds_read_b64_tr_b16 v[194:195],v225 offset:28672\n"
"ds_read_b64_tr_b16 v[196:197],v226 offset:24576\n"
"ds_read_b64_tr_b16 v[198:199],v226 offset:28672\n"
"ds_read_b64_tr_b16 v[200:201],v227 offset:24576\n"
"ds_read_b64_tr_b16 v[202:203],v227 offset:28672\n"
"ds_read_b64_tr_b16 v[204:205],v228 offset:24576\n"
"ds_read_b64_tr_b16 v[206:207],v228 offset:28672\n"
"ds_read_b64_tr_b16 v[208:209],v229 offset:24576\n"
"ds_read_b64_tr_b16 v[210:211],v229 offset:28672\n"
"ds_read_b64_tr_b16 v[212:213],v230 offset:24576\n"
"ds_read_b64_tr_b16 v[214:215],v230 offset:28672\n"
"ds_read_b64_tr_b16 v[216:217],v231 offset:24576\n"
"s_waitcnt lgkmcnt(14)\n"
"ds_read_b64_tr_b16 v[218:219],v231 offset:28672\n"
"s_cmp_lg_u64 s[80:81],0\n"
"s_cbranch_scc1 Lk_77x\n"
"Lk_77:\n"
"s_waitcnt lgkmcnt(14)\n"
"v_mfma_f32_16x16x32_bf16 v[0:3],v[188:191],v[144:147],v[0:3]\n"
"v_exp_f32 v116,v116\n"
"s_waitcnt lgkmcnt(12)\n"
"v_mfma_f32_16x16x32_bf16 v[4:7],v[192:195],v[144:147],v[4:7]\n"
"v_exp_f32 v117,v117\n"
"v_exp_f32 v118,v118\n"
"s_waitcnt lgkmcnt(10)\n"
"v_mfma_f32_16x16x32_bf16 v[8:11],v[196:199],v[144:147],v[8:11]\n"
"v_exp_f32 v119,v119\n"
"s_waitcnt lgkmcnt(8)\n"
"v_mfma_f32_16x16x32_bf16 v[12:15],v[200:203],v[144:147],v[12:15]\n"
"v_cvt_pk_bf16_f32 v148,v116,v117\n"
"v_exp_f32 v120,v120\n"
"s_waitcnt lgkmcnt(6)\n"
"v_mfma_f32_16x16x32_bf16 v[16:19],v[204:207],v[144:147],v[16:19]\n"
"v_exp_f32 v121,v121\n"
"v_cvt_pk_bf16_f32 v149,v118,v119\n"
"s_waitcnt lgkmcnt(4)\n"
"v_mfma_f32_16x16x32_bf16 v[20:23],v[208:211],v[144:147],v[20:23]\n"
"v_exp_f32 v122,v122\n"
"s_waitcnt lgkmcnt(0)\n"
"v_mfma_f32_16x16x32_bf16 v[24:27],v[212:215],v[144:147],v[24:27]\n"
"v_exp_f32 v123,v123\n"
"v_cvt_pk_bf16_f32 v150,v120,v121\n"
"v_mfma_f32_16x16x32_bf16 v[28:31],v[216:219],v[144:147],v[28:31]\n"
"v_cvt_pk_bf16_f32 v151,v122,v123\n"
"v_max3_f32 v248,v124,v125,v126\n"
"v_max3_f32 v248,v248,v127,v128\n"
"v_mfma_f32_16x16x32_bf16 v[96:99],a[48:51],v[144:147],v[96:99]\n"
"v_max3_f32 v248,v248,v129,v130\n"
"v_max_f32 v248,v248,v131\n"
"v_cmp_lt_f32 s[82:83],s64,v248\n"
"s_cbranch_vccnz Lk_78x\n"
"Lk_78:\n"
"v_mfma_f32_16x16x32_bf16 v[32:35],v[188:191],v[148:151],v[32:35]\n"
"v_exp_f32 v124,v124\n"
"v_mfma_f32_16x16x32_bf16 v[36:39],v[192:195],v[148:151],v[36:39]\n"
"v_exp_f32 v125,v125\n"
"v_exp_f32 v126,v126\n"
"v_mfma_f32_16x16x32_bf16 v[40:43],v[196:199],v[148:151],v[40:43]\n"
"v_exp_f32 v127,v127\n"
"v_cvt_pk_bf16_f32 v152,v124,v125\n"
"v_mfma_f32_16x16x32_bf16 v[44:47],v[200:203],v[148:151],v[44:47]\n"
"v_exp_f32 v128,v128\n"
"v_mfma_f32_16x16x32_bf16 v[48:51],v[204:207],v[148:151],v[48:51]\n"
"v_exp_f32 v129,v129\n"
"v_cvt_pk_bf16_f32 v153,v126,v127\n"
"v_mfma_f32_16x16x32_bf16 v[52:55],v[208:211],v[148:151],v[52:55]\n"
"v_exp_f32 v130,v130\n"
"v_exp_f32 v131,v131\n"
"v_mfma_f32_16x16x32_bf16 v[56:59],v[212:215],v[148:151],v[56:59]\n"
"v_cvt_pk_bf16_f32 v154,v128,v129\n"
"v_cvt_pk_bf16_f32 v155,v130,v131\n"
"v_mfma_f32_16x16x32_bf16 v[60:63],v[216:219],v[148:151],v[60:63]\n"
"s_add_u32 s67,s1,0x2400\n"
"s_and_b32 s67,s67,0x3fff\n"
"s_add_u32 m0,s66,0x6000\n"
"s_nop 0\n"
"buffer_load_dwordx4 v236,s[28:31],s67 offen nt lds\n"
"v_mfma_f32_16x16x32_bf16 v[100:103],a[48:51],v[148:151],v[100:103]\n"
"buffer_load_dwordx4 v237,s[28:31],s67 offen offset:1024 nt lds\n"
"buffer_load_dwordx4 v254,s[28:31],s67 offen offset:2048 nt lds\n"
"buffer_load_dwordx4 v255,s[28:31],s67 offen offset:3072 nt lds\n"
"s_cmp_lg_u64 s[82:83],0\n"
"s_cbranch_scc1 Lk_79x\n"
"Lk_79:\n"
"v_mfma_f32_16x16x32_bf16 v[64:67],v[188:191],v[152:155],v[64:67]\n"
"v_mfma_f32_16x16x32_bf16 v[68:71],v[192:195],v[152:155],v[68:71]\n"
"s_add_u32 s67,s67,0x10000\n"
"s_add_u32 m0,s66,0x7000\n"
"s_nop 0\n"
"buffer_load_dwordx4 v236,s[28:31],s67 offen nt lds\n"
"v_mfma_f32_16x16x32_bf16 v[72:75],v[196:199],v[152:155],v[72:75]\n"
"v_mfma_f32_16x16x32_bf16 v[76:79],v[200:203],v[152:155],v[76:79]\n"
"v_mfma_f32_16x16x32_bf16 v[80:83],v[204:207],v[152:155],v[80:83]\n"
"buffer_load_dwordx4 v237,s[28:31],s67 offen offset:1024 nt lds\n"
"v_mfma_f32_16x16x32_bf16 v[84:87],v[208:211],v[152:155],v[84:87]\n"
"v_mfma_f32_16x16x32_bf16 v[88:91],v[212:215],v[152:155],v[88:91]\n"
"buffer_load_dwordx4 v254,s[28:31],s67 offen offset:2048 nt lds\n"
"v_mfma_f32_16x16x32_bf16 v[92:95],v[216:219],v[152:155],v[92:95]\n"
"v_mfma_f32_16x16x32_bf16 v[104:107],a[48:51],v[152:155],v[104:107]\n"
"buffer_load_dwordx4 v255,s[28:31],s67 offen offset:3072 nt lds\n"
"s_waitcnt vmcnt(24)\n"
"ds_read_b128 v[156:159],v220\n"
"ds_read_b128 v[172:175],v220 offset:4096\n"
"ds_read_b128 v[160:163],v221\n"
"ds_read_b128 v[176:179],v221 offset:4096\n"
"ds_read_b128 v[164:167],v222\n"
"ds_read_b128 v[180:183],v222 offset:4096\n"
"ds_read_b128 v[168:171],v223\n"
"ds_read_b128 v[184:187],v223 offset:4096\n"
"s_waitcnt lgkmcnt(7)\n"
"v_mfma_f32_16x16x32_bf16 v[108:111],v[156:159],a[0:3],v[132:135]\n"
"s_waitcnt lgkmcnt(6)\n"
"v_mfma_f32_16x16x32_bf16 v[112:115],v[172:175],a[0:3],v[132:135]\n"
"s_waitcnt lgkmcnt(5)\n"
"v_mfma_f32_16x16x32_bf16 v[108:111],v[160:163],a[12:15],v[108:111]\n"
"s_waitcnt lgkmcnt(4)\n"
"v_mfma_f32_16x16x32_bf16 v[112:115],v[176:179],a[12:15],v[112:115]\n"
"s_waitcnt lgkmcnt(3)\n"
"v_mfma_f32_16x16x32_bf16 v[108:111],v[164:167],a[24:27],v[108:111]\n"
"s_waitcnt lgkmcnt(2)\n"
"v_mfma_f32_16x16x32_bf16 v[112:115],v[180:183],a[24:27],v[112:115]\n"
"s_waitcnt lgkmcnt(1)\n"
"v_mfma_f32_16x16x32_bf16 v[108:111],v[168:171],a[36:39],v[108:111]\n"
"s_waitcnt lgkmcnt(0)\n"
"v_mfma_f32_16x16x32_bf16 v[112:115],v[184:187],a[36:39],v[112:115]\n"
"v_mfma_f32_16x16x32_bf16 v[116:119],v[156:159],a[4:7],v[136:139]\n"
"s_add_u32 s67,s1,0x2800\n"
"s_and_b32 s67,s67,0x3fff\n"
"s_add_u32 m0,s66,0\n"
"s_nop 0\n"
"buffer_load_dwordx4 v232,s[24:27],s67 offen nt lds\n"
"v_mfma_f32_16x16x32_bf16 v[120:123],v[172:175],a[4:7],v[136:139]\n"
"buffer_load_dwordx4 v233,s[24:27],s67 offen offset:1024 nt lds\n"
"buffer_load_dwordx4 v234,s[24:27],s67 offen offset:2048 nt lds\n"
"v_mfma_f32_16x16x32_bf16 v[116:119],v[160:163],a[16:19],v[116:119]\n"
"buffer_load_dwordx4 v235,s[24:27],s67 offen offset:3072 nt lds\n"
"v_max3_f32 v246,v108,v109,v110\n"
"v_mfma_f32_16x16x32_bf16 v[120:123],v[176:179],a[16:19],v[120:123]\n"
"v_max3_f32 v246,v246,v111,v112\n"
"v_max3_f32 v246,v246,v113,v114\n"
"v_mfma_f32_16x16x32_bf16 v[116:119],v[164:167],a[28:31],v[116:119]\n"
"v_max_f32 v246,v246,v115\n"
"v_cmp_lt_f32 s[80:81],s64,v246\n"
"v_mfma_f32_16x16x32_bf16 v[120:123],v[180:183],a[28:31],v[120:123]\n"
"v_exp_f32 v108,v108\n"
"v_exp_f32 v109,v109\n"
"v_mfma_f32_16x16x32_bf16 v[116:119],v[168:171],a[40:43],v[116:119]\n"
"v_exp_f32 v110,v110\n"
"v_mfma_f32_16x16x32_bf16 v[120:123],v[184:187],a[40:43],v[120:123]\n"
"v_exp_f32 v111,v111\n"
"v_mfma_f32_16x16x32_bf16 v[124:127],v[156:159],a[8:11],v[140:143]\n"
"v_cvt_pk_bf16_f32 v144,v108,v109\n"
"v_exp_f32 v112,v112\n"
"v_mfma_f32_16x16x32_bf16 v[128:131],v[172:175],a[8:11],v[140:143]\n"
"v_exp_f32 v113,v113\n"
"v_cvt_pk_bf16_f32 v145,v110,v111\n"
"v_mfma_f32_16x16x32_bf16 v[124:127],v[160:163],a[20:23],v[124:127]\n"
"v_exp_f32 v114,v114\n"
"v_mfma_f32_16x16x32_bf16 v[128:131],v[176:179],a[20:23],v[128:131]\n"
"v_exp_f32 v115,v115\n"
"v_cvt_pk_bf16_f32 v146,v112,v113\n"
"v_mfma_f32_16x16x32_bf16 v[124:127],v[164:167],a[32:35],v[124:127]\n"
"v_cvt_pk_bf16_f32 v147,v114,v115\n"
"s_add_u32 s67,s67,0x10000\n"
"s_add_u32 m0,s66,0x1000\n"
"s_nop 0\n"
"buffer_load_dwordx4 v232,s[24:27],s67 offen nt lds\n"
"v_mfma_f32_16x16x32_bf16 v[128:131],v[180:183],a[32:35],v[128:131]\n"
"buffer_load_dwordx4 v233,s[24:27],s67 offen offset:1024 nt lds\n"
"buffer_load_dwordx4 v234,s[24:27],s67 offen offset:2048 nt lds\n"
"v_mfma_f32_16x16x32_bf16 v[124:127],v[168:171],a[44:47],v[124:127]\n"
"buffer_load_dwordx4 v235,s[24:27],s67 offen offset:3072 nt lds\n"
"s_waitcnt vmcnt(24)\n"
"v_max3_f32 v247,v116,v117,v118\n"
"v_max3_f32 v247,v247,v119,v120\n"
"v_mfma_f32_16x16x32_bf16 v[128:131],v[184:187],a[44:47],v[128:131]\n"
"v_max3_f32 v247,v247,v121,v122\n"
"v_max_f32 v247,v247,v123\n"
"v_cmp_lt_f32 vcc,s64,v247\n"
"ds_read_b64_tr_b16 v[188:189],v224 offset:8192\n"
"ds_read_b64_tr_b16 v[190:191],v224 offset:12288\n"
"ds_read_b64_tr_b16 v[192:193],v225 offset:8192\n"
"ds_read_b64_tr_b16 v[194:195],v225 offset:12288\n"
"ds_read_b64_tr_b16 v[196:197],v226 offset:8192\n"
"ds_read_b64_tr_b16 v[198:199],v226 offset:12288\n"
"ds_read_b64_tr_b16 v[200:201],v227 offset:8192\n"
"ds_read_b64_tr_b16 v[202:203],v227 offset:12288\n"
"ds_read_b64_tr_b16 v[204:205],v228 offset:8192\n"
"ds_read_b64_tr_b16 v[206:207],v228 offset:12288\n"
"ds_read_b64_tr_b16 v[208:209],v229 offset:8192\n"
"ds_read_b64_tr_b16 v[210:211],v229 offset:12288\n"
"ds_read_b64_tr_b16 v[212:213],v230 offset:8192\n"
"ds_read_b64_tr_b16 v[214:215],v230 offset:12288\n"
"ds_read_b64_tr_b16 v[216:217],v231 offset:8192\n"
"s_waitcnt lgkmcnt(14)\n"
"ds_read_b64_tr_b16 v[218:219],v231 offset:12288\n"
"s_cmp_lg_u64 s[80:81],0\n"
"s_cbranch_scc1 Lk_80x\n"
"Lk_80:\n"
"s_waitcnt lgkmcnt(14)\n"
"v_mfma_f32_16x16x32_bf16 v[0:3],v[188:191],v[144:147],v[0:3]\n"
"v_exp_f32 v116,v116\n"
"s_waitcnt lgkmcnt(12)\n"
"v_mfma_f32_16x16x32_bf16 v[4:7],v[192:195],v[144:147],v[4:7]\n"
"v_exp_f32 v117,v117\n"
"v_exp_f32 v118,v118\n"
"s_waitcnt lgkmcnt(10)\n"
"v_mfma_f32_16x16x32_bf16 v[8:11],v[196:199],v[144:147],v[8:11]\n"
"v_exp_f32 v119,v119\n"
"s_waitcnt lgkmcnt(8)\n"
"v_mfma_f32_16x16x32_bf16 v[12:15],v[200:203],v[144:147],v[12:15]\n"
"v_cvt_pk_bf16_f32 v148,v116,v117\n"
"v_exp_f32 v120,v120\n"
"s_waitcnt lgkmcnt(6)\n"
"v_mfma_f32_16x16x32_bf16 v[16:19],v[204:207],v[144:147],v[16:19]\n"
"v_exp_f32 v121,v121\n"
"v_cvt_pk_bf16_f32 v149,v118,v119\n"
"s_waitcnt lgkmcnt(4)\n"
"v_mfma_f32_16x16x32_bf16 v[20:23],v[208:211],v[144:147],v[20:23]\n"
"v_exp_f32 v122,v122\n"
"s_waitcnt lgkmcnt(0)\n"
"v_mfma_f32_16x16x32_bf16 v[24:27],v[212:215],v[144:147],v[24:27]\n"
"v_exp_f32 v123,v123\n"
"v_cvt_pk_bf16_f32 v150,v120,v121\n"
"v_mfma_f32_16x16x32_bf16 v[28:31],v[216:219],v[144:147],v[28:31]\n"
"v_cvt_pk_bf16_f32 v151,v122,v123\n"
"v_max3_f32 v248,v124,v125,v126\n"
"v_max3_f32 v248,v248,v127,v128\n"
"v_mfma_f32_16x16x32_bf16 v[96:99],a[48:51],v[144:147],v[96:99]\n"
"v_max3_f32 v248,v248,v129,v130\n"
"v_max_f32 v248,v248,v131\n"
"v_cmp_lt_f32 s[82:83],s64,v248\n"
"s_cbranch_vccnz Lk_81x\n"
"Lk_81:\n"
"v_mfma_f32_16x16x32_bf16 v[32:35],v[188:191],v[148:151],v[32:35]\n"
"v_exp_f32 v124,v124\n"
"v_mfma_f32_16x16x32_bf16 v[36:39],v[192:195],v[148:151],v[36:39]\n"
"v_exp_f32 v125,v125\n"
"v_exp_f32 v126,v126\n"
"v_mfma_f32_16x16x32_bf16 v[40:43],v[196:199],v[148:151],v[40:43]\n"
"v_exp_f32 v127,v127\n"
"v_cvt_pk_bf16_f32 v152,v124,v125\n"
"v_mfma_f32_16x16x32_bf16 v[44:47],v[200:203],v[148:151],v[44:47]\n"
"v_exp_f32 v128,v128\n"
"v_mfma_f32_16x16x32_bf16 v[48:51],v[204:207],v[148:151],v[48:51]\n"
"v_exp_f32 v129,v129\n"
"v_cvt_pk_bf16_f32 v153,v126,v127\n"
"v_mfma_f32_16x16x32_bf16 v[52:55],v[208:211],v[148:151],v[52:55]\n"
"v_exp_f32 v130,v130\n"
"v_exp_f32 v131,v131\n"
"v_mfma_f32_16x16x32_bf16 v[56:59],v[212:215],v[148:151],v[56:59]\n"
"v_cvt_pk_bf16_f32 v154,v128,v129\n"
"v_cvt_pk_bf16_f32 v155,v130,v131\n"
"v_mfma_f32_16x16x32_bf16 v[60:63],v[216:219],v[148:151],v[60:63]\n"
"s_add_u32 s67,s1,0x2800\n"
"s_and_b32 s67,s67,0x3fff\n"
"s_add_u32 m0,s66,0x2000\n"
"s_nop 0\n"
"buffer_load_dwordx4 v236,s[28:31],s67 offen nt lds\n"
"v_mfma_f32_16x16x32_bf16 v[100:103],a[48:51],v[148:151],v[100:103]\n"
"buffer_load_dwordx4 v237,s[28:31],s67 offen offset:1024 nt lds\n"
"buffer_load_dwordx4 v254,s[28:31],s67 offen offset:2048 nt lds\n"
"buffer_load_dwordx4 v255,s[28:31],s67 offen offset:3072 nt lds\n"
"s_cmp_lg_u64 s[82:83],0\n"
"s_cbranch_scc1 Lk_82x\n"
"Lk_82:\n"
"v_mfma_f32_16x16x32_bf16 v[64:67],v[188:191],v[152:155],v[64:67]\n"
"v_mfma_f32_16x16x32_bf16 v[68:71],v[192:195],v[152:155],v[68:71]\n"
"s_add_u32 s67,s67,0x10000\n"
"s_add_u32 m0,s66,0x3000\n"
"s_nop 0\n"
"buffer_load_dwordx4 v236,s[28:31],s67 offen nt lds\n"
"v_mfma_f32_16x16x32_bf16 v[72:75],v[196:199],v[152:155],v[72:75]\n"
"v_mfma_f32_16x16x32_bf16 v[76:79],v[200:203],v[152:155],v[76:79]\n"
"v_mfma_f32_16x16x32_bf16 v[80:83],v[204:207],v[152:155],v[80:83]\n"
"buffer_load_dwordx4 v237,s[28:31],s67 offen offset:1024 nt lds\n"
"v_mfma_f32_16x16x32_bf16 v[84:87],v[208:211],v[152:155],v[84:87]\n"
"v_mfma_f32_16x16x32_bf16 v[88:91],v[212:215],v[152:155],v[88:91]\n"
"buffer_load_dwordx4 v254,s[28:31],s67 offen offset:2048 nt lds\n"
"v_mfma_f32_16x16x32_bf16 v[92:95],v[216:219],v[152:155],v[92:95]\n"
"v_mfma_f32_16x16x32_bf16 v[104:107],a[48:51],v[152:155],v[104:107]\n"
"buffer_load_dwordx4 v255,s[28:31],s67 offen offset:3072 nt lds\n"
"s_waitcnt vmcnt(24)\n"
"ds_read_b128 v[156:159],v220 offset:16384\n"
"ds_read_b128 v[172:175],v220 offset:20480\n"
"ds_read_b128 v[160:163],v221 offset:16384\n"
"ds_read_b128 v[176:179],v221 offset:20480\n"
"ds_read_b128 v[164:167],v222 offset:16384\n"
"ds_read_b128 v[180:183],v222 offset:20480\n"
"ds_read_b128 v[168:171],v223 offset:16384\n"
"ds_read_b128 v[184:187],v223 offset:20480\n"
"s_waitcnt lgkmcnt(7)\n"
"v_mfma_f32_16x16x32_bf16 v[108:111],v[156:159],a[0:3],v[132:135]\n"
"s_waitcnt lgkmcnt(6)\n"
"v_mfma_f32_16x16x32_bf16 v[112:115],v[172:175],a[0:3],v[132:135]\n"
"s_waitcnt lgkmcnt(5)\n"
"v_mfma_f32_16x16x32_bf16 v[108:111],v[160:163],a[12:15],v[108:111]\n"
"s_waitcnt lgkmcnt(4)\n"
"v_mfma_f32_16x16x32_bf16 v[112:115],v[176:179],a[12:15],v[112:115]\n"
"s_waitcnt lgkmcnt(3)\n"
"v_mfma_f32_16x16x32_bf16 v[108:111],v[164:167],a[24:27],v[108:111]\n"
"s_waitcnt lgkmcnt(2)\n"
"v_mfma_f32_16x16x32_bf16 v[112:115],v[180:183],a[24:27],v[112:115]\n"
"s_waitcnt lgkmcnt(1)\n"
"v_mfma_f32_16x16x32_bf16 v[108:111],v[168:171],a[36:39],v[108:111]\n"
"s_waitcnt lgkmcnt(0)\n"
"v_mfma_f32_16x16x32_bf16 v[112:115],v[184:187],a[36:39],v[112:115]\n"
"v_mfma_f32_16x16x32_bf16 v[116:119],v[156:159],a[4:7],v[136:139]\n"
"s_add_u32 s67,s1,0x2c00\n"
"s_and_b32 s67,s67,0x3fff\n"
"s_add_u32 m0,s66,0x4000\n"
"s_nop 0\n"
"buffer_load_dwordx4 v232,s[24:27],s67 offen nt lds\n"
"v_mfma_f32_16x16x32_bf16 v[120:123],v[172:175],a[4:7],v[136:139]\n"
"buffer_load_dwordx4 v233,s[24:27],s67 offen offset:1024 nt lds\n"
"buffer_load_dwordx4 v234,s[24:27],s67 offen offset:2048 nt lds\n"
"v_mfma_f32_16x16x32_bf16 v[116:119],v[160:163],a[16:19],v[116:119]\n"
"buffer_load_dwordx4 v235,s[24:27],s67 offen offset:3072 nt lds\n"
"v_max3_f32 v246,v108,v109,v110\n"
"v_mfma_f32_16x16x32_bf16 v[120:123],v[176:179],a[16:19],v[120:123]\n"
"v_max3_f32 v246,v246,v111,v112\n"
"v_max3_f32 v246,v246,v113,v114\n"
"v_mfma_f32_16x16x32_bf16 v[116:119],v[164:167],a[28:31],v[116:119]\n"
"v_max_f32 v246,v246,v115\n"
"v_cmp_lt_f32 s[80:81],s64,v246\n"
"v_mfma_f32_16x16x32_bf16 v[120:123],v[180:183],a[28:31],v[120:123]\n"
"v_exp_f32 v108,v108\n"
"v_exp_f32 v109,v109\n"
"v_mfma_f32_16x16x32_bf16 v[116:119],v[168:171],a[40:43],v[116:119]\n"
"v_exp_f32 v110,v110\n"
"v_mfma_f32_16x16x32_bf16 v[120:123],v[184:187],a[40:43],v[120:123]\n"
"v_exp_f32 v111,v111\n"
"v_mfma_f32_16x16x32_bf16 v[124:127],v[156:159],a[8:11],v[140:143]\n"
"v_cvt_pk_bf16_f32 v144,v108,v109\n"
"v_exp_f32 v112,v112\n"
"v_mfma_f32_16x16x32_bf16 v[128:131],v[172:175],a[8:11],v[140:143]\n"
"v_exp_f32 v113,v113\n"
"v_cvt_pk_bf16_f32 v145,v110,v111\n"
"v_mfma_f32_16x16x32_bf16 v[124:127],v[160:163],a[20:23],v[124:127]\n"
"v_exp_f32 v114,v114\n"
"v_mfma_f32_16x16x32_bf16 v[128:131],v[176:179],a[20:23],v[128:131]\n"
"v_exp_f32 v115,v115\n"
"v_cvt_pk_bf16_f32 v146,v112,v113\n"
"v_mfma_f32_16x16x32_bf16 v[124:127],v[164:167],a[32:35],v[124:127]\n"
"v_cvt_pk_bf16_f32 v147,v114,v115\n"
"s_add_u32 s67,s67,0x10000\n"
"s_add_u32 m0,s66,0x5000\n"
"s_nop 0\n"
"buffer_load_dwordx4 v232,s[24:27],s67 offen nt lds\n"
"v_mfma_f32_16x16x32_bf16 v[128:131],v[180:183],a[32:35],v[128:131]\n"
"buffer_load_dwordx4 v233,s[24:27],s67 offen offset:1024 nt lds\n"
"buffer_load_dwordx4 v234,s[24:27],s67 offen offset:2048 nt lds\n"
"v_mfma_f32_16x16x32_bf16 v[124:127],v[168:171],a[44:47],v[124:127]\n"
"buffer_load_dwordx4 v235,s[24:27],s67 offen offset:3072 nt lds\n"
"s_waitcnt vmcnt(24)\n"
"v_max3_f32 v247,v116,v117,v118\n"
"v_max3_f32 v247,v247,v119,v120\n"
"v_mfma_f32_16x16x32_bf16 v[128:131],v[184:187],a[44:47],v[128:131]\n"
"v_max3_f32 v247,v247,v121,v122\n"
"v_max_f32 v247,v247,v123\n"
"v_cmp_lt_f32 vcc,s64,v247\n"
"ds_read_b64_tr_b16 v[188:189],v224 offset:24576\n"
"ds_read_b64_tr_b16 v[190:191],v224 offset:28672\n"
"ds_read_b64_tr_b16 v[192:193],v225 offset:24576\n"
"ds_read_b64_tr_b16 v[194:195],v225 offset:28672\n"
"ds_read_b64_tr_b16 v[196:197],v226 offset:24576\n"
"ds_read_b64_tr_b16 v[198:199],v226 offset:28672\n"
"ds_read_b64_tr_b16 v[200:201],v227 offset:24576\n"
"ds_read_b64_tr_b16 v[202:203],v227 offset:28672\n"
"ds_read_b64_tr_b16 v[204:205],v228 offset:24576\n"
"ds_read_b64_tr_b16 v[206:207],v228 offset:28672\n"
"ds_read_b64_tr_b16 v[208:209],v229 offset:24576\n"
"ds_read_b64_tr_b16 v[210:211],v229 offset:28672\n"
"ds_read_b64_tr_b16 v[212:213],v230 offset:24576\n"
"ds_read_b64_tr_b16 v[214:215],v230 offset:28672\n"
"ds_read_b64_tr_b16 v[216:217],v231 offset:24576\n"
"s_waitcnt lgkmcnt(14)\n"
"ds_read_b64_tr_b16 v[218:219],v231 offset:28672\n"
"s_cmp_lg_u64 s[80:81],0\n"
"s_cbranch_scc1 Lk_83x\n"
"Lk_83:\n"
"s_waitcnt lgkmcnt(14)\n"
"v_mfma_f32_16x16x32_bf16 v[0:3],v[188:191],v[144:147],v[0:3]\n"
"v_exp_f32 v116,v116\n"
"s_waitcnt lgkmcnt(12)\n"
"v_mfma_f32_16x16x32_bf16 v[4:7],v[192:195],v[144:147],v[4:7]\n"
"v_exp_f32 v117,v117\n"
"v_exp_f32 v118,v118\n"
"s_waitcnt lgkmcnt(10)\n"
"v_mfma_f32_16x16x32_bf16 v[8:11],v[196:199],v[144:147],v[8:11]\n"
"v_exp_f32 v119,v119\n"
"s_waitcnt lgkmcnt(8)\n"
"v_mfma_f32_16x16x32_bf16 v[12:15],v[200:203],v[144:147],v[12:15]\n"
"v_cvt_pk_bf16_f32 v148,v116,v117\n"
"v_exp_f32 v120,v120\n"
"s_waitcnt lgkmcnt(6)\n"
"v_mfma_f32_16x16x32_bf16 v[16:19],v[204:207],v[144:147],v[16:19]\n"
"v_exp_f32 v121,v121\n"
"v_cvt_pk_bf16_f32 v149,v118,v119\n"
"s_waitcnt lgkmcnt(4)\n"
"v_mfma_f32_16x16x32_bf16 v[20:23],v[208:211],v[144:147],v[20:23]\n"
"v_exp_f32 v122,v122\n"
"s_waitcnt lgkmcnt(0)\n"
"v_mfma_f32_16x16x32_bf16 v[24:27],v[212:215],v[144:147],v[24:27]\n"
"v_exp_f32 v123,v123\n"
"v_cvt_pk_bf16_f32 v150,v120,v121\n"
"v_mfma_f32_16x16x32_bf16 v[28:31],v[216:219],v[144:147],v[28:31]\n"
"v_cvt_pk_bf16_f32 v151,v122,v123\n"
"v_max3_f32 v248,v124,v125,v126\n"
"v_max3_f32 v248,v248,v127,v128\n"
"v_mfma_f32_16x16x32_bf16 v[96:99],a[48:51],v[144:147],v[96:99]\n"
"v_max3_f32 v248,v248,v129,v130\n"
"v_max_f32 v248,v248,v131\n"
"v_cmp_lt_f32 s[82:83],s64,v248\n"
"s_cbranch_vccnz Lk_84x\n"
"Lk_84:\n"
"v_mfma_f32_16x16x32_bf16 v[32:35],v[188:191],v[148:151],v[32:35]\n"
"v_exp_f32 v124,v124\n"
"v_mfma_f32_16x16x32_bf16 v[36:39],v[192:195],v[148:151],v[36:39]\n"
"v_exp_f32 v125,v125\n"
"v_exp_f32 v126,v126\n"
"v_mfma_f32_16x16x32_bf16 v[40:43],v[196:199],v[148:151],v[40:43]\n"
"v_exp_f32 v127,v127\n"
"v_cvt_pk_bf16_f32 v152,v124,v125\n"
"v_mfma_f32_16x16x32_bf16 v[44:47],v[200:203],v[148:151],v[44:47]\n"
"v_exp_f32 v128,v128\n"
"v_mfma_f32_16x16x32_bf16 v[48:51],v[204:207],v[148:151],v[48:51]\n"
"v_exp_f32 v129,v129\n"
"v_cvt_pk_bf16_f32 v153,v126,v127\n"
"v_mfma_f32_16x16x32_bf16 v[52:55],v[208:211],v[148:151],v[52:55]\n"
"v_exp_f32 v130,v130\n"
"v_exp_f32 v131,v131\n"
"v_mfma_f32_16x16x32_bf16 v[56:59],v[212:215],v[148:151],v[56:59]\n"
"v_cvt_pk_bf16_f32 v154,v128,v129\n"
"v_cvt_pk_bf16_f32 v155,v130,v131\n"
"v_mfma_f32_16x16x32_bf16 v[60:63],v[216:219],v[148:151],v[60:63]\n"
"s_add_u32 s67,s1,0x2c00\n"
"s_and_b32 s67,s67,0x3fff\n"
"s_add_u32 m0,s66,0x6000\n"
"s_nop 0\n"
"buffer_load_dwordx4 v236,s[28:31],s67 offen nt lds\n"
"v_mfma_f32_16x16x32_bf16 v[100:103],a[48:51],v[148:151],v[100:103]\n"
"buffer_load_dwordx4 v237,s[28:31],s67 offen offset:1024 nt lds\n"
"buffer_load_dwordx4 v254,s[28:31],s67 offen offset:2048 nt lds\n"
"buffer_load_dwordx4 v255,s[28:31],s67 offen offset:3072 nt lds\n"
"s_cmp_lg_u64 s[82:83],0\n"
"s_cbranch_scc1 Lk_85x\n"
"Lk_85:\n"
"v_mfma_f32_16x16x32_bf16 v[64:67],v[188:191],v[152:155],v[64:67]\n"
"v_mfma_f32_16x16x32_bf16 v[68:71],v[192:195],v[152:155],v[68:71]\n"
"s_add_u32 s67,s67,0x10000\n"
"s_add_u32 m0,s66,0x7000\n"
"s_nop 0\n"
"buffer_load_dwordx4 v236,s[28:31],s67 offen nt lds\n"
"v_mfma_f32_16x16x32_bf16 v[72:75],v[196:199],v[152:155],v[72:75]\n"
"v_mfma_f32_16x16x32_bf16 v[76:79],v[200:203],v[152:155],v[76:79]\n"
"v_mfma_f32_16x16x32_bf16 v[80:83],v[204:207],v[152:155],v[80:83]\n"
"buffer_load_dwordx4 v237,s[28:31],s67 offen offset:1024 nt lds\n"
"v_mfma_f32_16x16x32_bf16 v[84:87],v[208:211],v[152:155],v[84:87]\n"
"v_mfma_f32_16x16x32_bf16 v[88:91],v[212:215],v[152:155],v[88:91]\n"
"buffer_load_dwordx4 v254,s[28:31],s67 offen offset:2048 nt lds\n"
"v_mfma_f32_16x16x32_bf16 v[92:95],v[216:219],v[152:155],v[92:95]\n"
"v_mfma_f32_16x16x32_bf16 v[104:107],a[48:51],v[152:155],v[104:107]\n"
"buffer_load_dwordx4 v255,s[28:31],s67 offen offset:3072 nt lds\n"
"s_waitcnt vmcnt(24)\n"
"ds_read_b128 v[156:159],v220\n"
"ds_read_b128 v[172:175],v220 offset:4096\n"
"ds_read_b128 v[160:163],v221\n"
"ds_read_b128 v[176:179],v221 offset:4096\n"
"ds_read_b128 v[164:167],v222\n"
"ds_read_b128 v[180:183],v222 offset:4096\n"
"ds_read_b128 v[168:171],v223\n"
"ds_read_b128 v[184:187],v223 offset:4096\n"
"s_waitcnt lgkmcnt(7)\n"
"v_mfma_f32_16x16x32_bf16 v[108:111],v[156:159],a[0:3],v[132:135]\n"
"s_waitcnt lgkmcnt(6)\n"
"v_mfma_f32_16x16x32_bf16 v[112:115],v[172:175],a[0:3],v[132:135]\n"
"s_waitcnt lgkmcnt(5)\n"
"v_mfma_f32_16x16x32_bf16 v[108:111],v[160:163],a[12:15],v[108:111]\n"
"s_waitcnt lgkmcnt(4)\n"
"v_mfma_f32_16x16x32_bf16 v[112:115],v[176:179],a[12:15],v[112:115]\n"
"s_waitcnt lgkmcnt(3)\n"
"v_mfma_f32_16x16x32_bf16 v[108:111],v[164:167],a[24:27],v[108:111]\n"
"s_waitcnt lgkmcnt(2)\n"
"v_mfma_f32_16x16x32_bf16 v[112:115],v[180:183],a[24:27],v[112:115]\n"
"s_waitcnt lgkmcnt(1)\n"
"v_mfma_f32_16x16x32_bf16 v[108:111],v[168:171],a[36:39],v[108:111]\n"
"s_waitcnt lgkmcnt(0)\n"
"v_mfma_f32_16x16x32_bf16 v[112:115],v[184:187],a[36:39],v[112:115]\n"
"v_mfma_f32_16x16x32_bf16 v[116:119],v[156:159],a[4:7],v[136:139]\n"
"s_add_u32 s67,s1,0x3000\n"
"s_and_b32 s67,s67,0x3fff\n"
"s_add_u32 m0,s66,0\n"
"s_nop 0\n"
"buffer_load_dwordx4 v232,s[24:27],s67 offen nt lds\n"
"v_mfma_f32_16x16x32_bf16 v[120:123],v[172:175],a[4:7],v[136:139]\n"
"buffer_load_dwordx4 v233,s[24:27],s67 offen offset:1024 nt lds\n"
"buffer_load_dwordx4 v234,s[24:27],s67 offen offset:2048 nt lds\n"
"v_mfma_f32_16x16x32_bf16 v[116:119],v[160:163],a[16:19],v[116:119]\n"
"buffer_load_dwordx4 v235,s[24:27],s67 offen offset:3072 nt lds\n"
"v_max3_f32 v246,v108,v109,v110\n"
"v_mfma_f32_16x16x32_bf16 v[120:123],v[176:179],a[16:19],v[120:123]\n"
"v_max3_f32 v246,v246,v111,v112\n"
"v_max3_f32 v246,v246,v113,v114\n"
"v_mfma_f32_16x16x32_bf16 v[116:119],v[164:167],a[28:31],v[116:119]\n"
"v_max_f32 v246,v246,v115\n"
"v_cmp_lt_f32 s[80:81],s64,v246\n"
"v_mfma_f32_16x16x32_bf16 v[120:123],v[180:183],a[28:31],v[120:123]\n"
"v_exp_f32 v108,v108\n"
"v_exp_f32 v109,v109\n"
"v_mfma_f32_16x16x32_bf16 v[116:119],v[168:171],a[40:43],v[116:119]\n"
"v_exp_f32 v110,v110\n"
"v_mfma_f32_16x16x32_bf16 v[120:123],v[184:187],a[40:43],v[120:123]\n"
"v_exp_f32 v111,v111\n"
"v_mfma_f32_16x16x32_bf16 v[124:127],v[156:159],a[8:11],v[140:143]\n"
"v_cvt_pk_bf16_f32 v144,v108,v109\n"
"v_exp_f32 v112,v112\n"
"v_mfma_f32_16x16x32_bf16 v[128:131],v[172:175],a[8:11],v[140:143]\n"
"v_exp_f32 v113,v113\n"
"v_cvt_pk_bf16_f32 v145,v110,v111\n"
"v_mfma_f32_16x16x32_bf16 v[124:127],v[160:163],a[20:23],v[124:127]\n"
"v_exp_f32 v114,v114\n"
"v_mfma_f32_16x16x32_bf16 v[128:131],v[176:179],a[20:23],v[128:131]\n"
"v_exp_f32 v115,v115\n"
"v_cvt_pk_bf16_f32 v146,v112,v113\n"
"v_mfma_f32_16x16x32_bf16 v[124:127],v[164:167],a[32:35],v[124:127]\n"
"v_cvt_pk_bf16_f32 v147,v114,v115\n"
"s_add_u32 s67,s67,0x10000\n"
"s_add_u32 m0,s66,0x1000\n"
"s_nop 0\n"
"buffer_load_dwordx4 v232,s[24:27],s67 offen nt lds\n"
"v_mfma_f32_16x16x32_bf16 v[128:131],v[180:183],a[32:35],v[128:131]\n"
"buffer_load_dwordx4 v233,s[24:27],s67 offen offset:1024 nt lds\n"
"buffer_load_dwordx4 v234,s[24:27],s67 offen offset:2048 nt lds\n"
"v_mfma_f32_16x16x32_bf16 v[124:127],v[168:171],a[44:47],v[124:127]\n"
"buffer_load_dwordx4 v235,s[24:27],s67 offen offset:3072 nt lds\n"
"s_waitcnt vmcnt(24)\n"
"v_max3_f32 v247,v116,v117,v118\n"
"v_max3_f32 v247,v247,v119,v120\n"
"v_mfma_f32_16x16x32_bf16 v[128:131],v[184:187],a[44:47],v[128:131]\n"
"v_max3_f32 v247,v247,v121,v122\n"
"v_max_f32 v247,v247,v123\n"
"v_cmp_lt_f32 vcc,s64,v247\n"
"ds_read_b64_tr_b16 v[188:189],v224 offset:8192\n"
"ds_read_b64_tr_b16 v[190:191],v224 offset:12288\n"
"ds_read_b64_tr_b16 v[192:193],v225 offset:8192\n"
"ds_read_b64_tr_b16 v[194:195],v225 offset:12288\n"
"ds_read_b64_tr_b16 v[196:197],v226 offset:8192\n"
"ds_read_b64_tr_b16 v[198:199],v226 offset:12288\n"
"ds_read_b64_tr_b16 v[200:201],v227 offset:8192\n"
"ds_read_b64_tr_b16 v[202:203],v227 offset:12288\n"
"ds_read_b64_tr_b16 v[204:205],v228 offset:8192\n"
"ds_read_b64_tr_b16 v[206:207],v228 offset:12288\n"
"ds_read_b64_tr_b16 v[208:209],v229 offset:8192\n"
"ds_read_b64_tr_b16 v[210:211],v229 offset:12288\n"
"ds_read_b64_tr_b16 v[212:213],v230 offset:8192\n"
"ds_read_b64_tr_b16 v[214:215],v230 offset:12288\n"
"ds_read_b64_tr_b16 v[216:217],v231 offset:8192\n"
"s_waitcnt lgkmcnt(14)\n"
"ds_read_b64_tr_b16 v[218:219],v231 offset:12288\n"
"s_cmp_lg_u64 s[80:81],0\n"
"s_cbranch_scc1 Lk_86x\n"
"Lk_86:\n"
"s_waitcnt lgkmcnt(14)\n"
"v_mfma_f32_16x16x32_bf16 v[0:3],v[188:191],v[144:147],v[0:3]\n"
"v_exp_f32 v116,v116\n"
"s_waitcnt lgkmcnt(12)\n"
"v_mfma_f32_16x16x32_bf16 v[4:7],v[192:195],v[144:147],v[4:7]\n"
"v_exp_f32 v117,v117\n"
"v_exp_f32 v118,v118\n"
"s_waitcnt lgkmcnt(10)\n"
"v_mfma_f32_16x16x32_bf16 v[8:11],v[196:199],v[144:147],v[8:11]\n"
"v_exp_f32 v119,v119\n"
"s_waitcnt lgkmcnt(8)\n"
"v_mfma_f32_16x16x32_bf16 v[12:15],v[200:203],v[144:147],v[12:15]\n"
"v_cvt_pk_bf16_f32 v148,v116,v117\n"
"v_exp_f32 v120,v120\n"
"s_waitcnt lgkmcnt(6)\n"
"v_mfma_f32_16x16x32_bf16 v[16:19],v[204:207],v[144:147],v[16:19]\n"
"v_exp_f32 v121,v121\n"
"v_cvt_pk_bf16_f32 v149,v118,v119\n"
"s_waitcnt lgkmcnt(4)\n"
"v_mfma_f32_16x16x32_bf16 v[20:23],v[208:211],v[144:147],v[20:23]\n"
"v_exp_f32 v122,v122\n"
"s_waitcnt lgkmcnt(0)\n"
"v_mfma_f32_16x16x32_bf16 v[24:27],v[212:215],v[144:147],v[24:27]\n"
"v_exp_f32 v123,v123\n"
"v_cvt_pk_bf16_f32 v150,v120,v121\n"
"v_mfma_f32_16x16x32_bf16 v[28:31],v[216:219],v[144:147],v[28:31]\n"
"v_cvt_pk_bf16_f32 v151,v122,v123\n"
"v_max3_f32 v248,v124,v125,v126\n"
"v_max3_f32 v248,v248,v127,v128\n"
"v_mfma_f32_16x16x32_bf16 v[96:99],a[48:51],v[144:147],v[96:99]\n"
"v_max3_f32 v248,v248,v129,v130\n"
"v_max_f32 v248,v248,v131\n"
"v_cmp_lt_f32 s[82:83],s64,v248\n"
"s_cbranch_vccnz Lk_87x\n"
"Lk_87:\n"
"v_mfma_f32_16x16x32_bf16 v[32:35],v[188:191],v[148:151],v[32:35]\n"
"v_exp_f32 v124,v124\n"
"v_mfma_f32_16x16x32_bf16 v[36:39],v[192:195],v[148:151],v[36:39]\n"
"v_exp_f32 v125,v125\n"
"v_exp_f32 v126,v126\n"
"v_mfma_f32_16x16x32_bf16 v[40:43],v[196:199],v[148:151],v[40:43]\n"
"v_exp_f32 v127,v127\n"
"v_cvt_pk_bf16_f32 v152,v124,v125\n"
"v_mfma_f32_16x16x32_bf16 v[44:47],v[200:203],v[148:151],v[44:47]\n"
"v_exp_f32 v128,v128\n"
"v_mfma_f32_16x16x32_bf16 v[48:51],v[204:207],v[148:151],v[48:51]\n"
"v_exp_f32 v129,v129\n"
"v_cvt_pk_bf16_f32 v153,v126,v127\n"
"v_mfma_f32_16x16x32_bf16 v[52:55],v[208:211],v[148:151],v[52:55]\n"
"v_exp_f32 v130,v130\n"
"v_exp_f32 v131,v131\n"
"v_mfma_f32_16x16x32_bf16 v[56:59],v[212:215],v[148:151],v[56:59]\n"
"v_cvt_pk_bf16_f32 v154,v128,v129\n"
"v_cvt_pk_bf16_f32 v155,v130,v131\n"
"v_mfma_f32_16x16x32_bf16 v[60:63],v[216:219],v[148:151],v[60:63]\n"
"s_add_u32 s67,s1,0x3000\n"
"s_and_b32 s67,s67,0x3fff\n"
"s_add_u32 m0,s66,0x2000\n"
"s_nop 0\n"
"buffer_load_dwordx4 v236,s[28:31],s67 offen nt lds\n"
"v_mfma_f32_16x16x32_bf16 v[100:103],a[48:51],v[148:151],v[100:103]\n"
"buffer_load_dwordx4 v237,s[28:31],s67 offen offset:1024 nt lds\n"
"buffer_load_dwordx4 v254,s[28:31],s67 offen offset:2048 nt lds\n"
"buffer_load_dwordx4 v255,s[28:31],s67 offen offset:3072 nt lds\n"
"s_cmp_lg_u64 s[82:83],0\n"
"s_cbranch_scc1 Lk_88x\n"
"Lk_88:\n"
"v_mfma_f32_16x16x32_bf16 v[64:67],v[188:191],v[152:155],v[64:67]\n"
"v_mfma_f32_16x16x32_bf16 v[68:71],v[192:195],v[152:155],v[68:71]\n"
"s_add_u32 s67,s67,0x10000\n"
"s_add_u32 m0,s66,0x3000\n"
"s_nop 0\n"
"buffer_load_dwordx4 v236,s[28:31],s67 offen nt lds\n"
"v_mfma_f32_16x16x32_bf16 v[72:75],v[196:199],v[152:155],v[72:75]\n"
"v_mfma_f32_16x16x32_bf16 v[76:79],v[200:203],v[152:155],v[76:79]\n"
"v_mfma_f32_16x16x32_bf16 v[80:83],v[204:207],v[152:155],v[80:83]\n"
"buffer_load_dwordx4 v237,s[28:31],s67 offen offset:1024 nt lds\n"
"v_mfma_f32_16x16x32_bf16 v[84:87],v[208:211],v[152:155],v[84:87]\n"
"v_mfma_f32_16x16x32_bf16 v[88:91],v[212:215],v[152:155],v[88:91]\n"
"buffer_load_dwordx4 v254,s[28:31],s67 offen offset:2048 nt lds\n"
"v_mfma_f32_16x16x32_bf16 v[92:95],v[216:219],v[152:155],v[92:95]\n"
"v_mfma_f32_16x16x32_bf16 v[104:107],a[48:51],v[152:155],v[104:107]\n"
"buffer_load_dwordx4 v255,s[28:31],s67 offen offset:3072 nt lds\n"
"s_waitcnt vmcnt(24)\n"
"ds_read_b128 v[156:159],v220 offset:16384\n"
"ds_read_b128 v[172:175],v220 offset:20480\n"
"ds_read_b128 v[160:163],v221 offset:16384\n"
"ds_read_b128 v[176:179],v221 offset:20480\n"
"ds_read_b128 v[164:167],v222 offset:16384\n"
"ds_read_b128 v[180:183],v222 offset:20480\n"
"ds_read_b128 v[168:171],v223 offset:16384\n"
"ds_read_b128 v[184:187],v223 offset:20480\n"
"s_waitcnt lgkmcnt(7)\n"
"v_mfma_f32_16x16x32_bf16 v[108:111],v[156:159],a[0:3],v[132:135]\n"
"s_waitcnt lgkmcnt(6)\n"
"v_mfma_f32_16x16x32_bf16 v[112:115],v[172:175],a[0:3],v[132:135]\n"
"s_waitcnt lgkmcnt(5)\n"
"v_mfma_f32_16x16x32_bf16 v[108:111],v[160:163],a[12:15],v[108:111]\n"
"s_waitcnt lgkmcnt(4)\n"
"v_mfma_f32_16x16x32_bf16 v[112:115],v[176:179],a[12:15],v[112:115]\n"
"s_waitcnt lgkmcnt(3)\n"
"v_mfma_f32_16x16x32_bf16 v[108:111],v[164:167],a[24:27],v[108:111]\n"
"s_waitcnt lgkmcnt(2)\n"
"v_mfma_f32_16x16x32_bf16 v[112:115],v[180:183],a[24:27],v[112:115]\n"
"s_waitcnt lgkmcnt(1)\n"
"v_mfma_f32_16x16x32_bf16 v[108:111],v[168:171],a[36:39],v[108:111]\n"
"s_waitcnt lgkmcnt(0)\n"
"v_mfma_f32_16x16x32_bf16 v[112:115],v[184:187],a[36:39],v[112:115]\n"
"v_mfma_f32_16x16x32_bf16 v[116:119],v[156:159],a[4:7],v[136:139]\n"
"s_add_u32 s67,s1,0x3400\n"
"s_and_b32 s67,s67,0x3fff\n"
"s_add_u32 m0,s66,0x4000\n"
"s_nop 0\n"
"buffer_load_dwordx4 v232,s[24:27],s67 offen nt lds\n"
"v_mfma_f32_16x16x32_bf16 v[120:123],v[172:175],a[4:7],v[136:139]\n"
"buffer_load_dwordx4 v233,s[24:27],s67 offen offset:1024 nt lds\n"
"buffer_load_dwordx4 v234,s[24:27],s67 offen offset:2048 nt lds\n"
"v_mfma_f32_16x16x32_bf16 v[116:119],v[160:163],a[16:19],v[116:119]\n"
"buffer_load_dwordx4 v235,s[24:27],s67 offen offset:3072 nt lds\n"
"v_max3_f32 v246,v108,v109,v110\n"
"v_mfma_f32_16x16x32_bf16 v[120:123],v[176:179],a[16:19],v[120:123]\n"
"v_max3_f32 v246,v246,v111,v112\n"
"v_max3_f32 v246,v246,v113,v114\n"
"v_mfma_f32_16x16x32_bf16 v[116:119],v[164:167],a[28:31],v[116:119]\n"
"v_max_f32 v246,v246,v115\n"
"v_cmp_lt_f32 s[80:81],s64,v246\n"
"v_mfma_f32_16x16x32_bf16 v[120:123],v[180:183],a[28:31],v[120:123]\n"
"v_exp_f32 v108,v108\n"
"v_exp_f32 v109,v109\n"
"v_mfma_f32_16x16x32_bf16 v[116:119],v[168:171],a[40:43],v[116:119]\n"
"v_exp_f32 v110,v110\n"
"v_mfma_f32_16x16x32_bf16 v[120:123],v[184:187],a[40:43],v[120:123]\n"
"v_exp_f32 v111,v111\n"
"v_mfma_f32_16x16x32_bf16 v[124:127],v[156:159],a[8:11],v[140:143]\n"
"v_cvt_pk_bf16_f32 v144,v108,v109\n"
"v_exp_f32 v112,v112\n"
"v_mfma_f32_16x16x32_bf16 v[128:131],v[172:175],a[8:11],v[140:143]\n"
"v_exp_f32 v113,v113\n"
"v_cvt_pk_bf16_f32 v145,v110,v111\n"
"v_mfma_f32_16x16x32_bf16 v[124:127],v[160:163],a[20:23],v[124:127]\n"
"v_exp_f32 v114,v114\n"
"v_mfma_f32_16x16x32_bf16 v[128:131],v[176:179],a[20:23],v[128:131]\n"
"v_exp_f32 v115,v115\n"
"v_cvt_pk_bf16_f32 v146,v112,v113\n"
"v_mfma_f32_16x16x32_bf16 v[124:127],v[164:167],a[32:35],v[124:127]\n"
"v_cvt_pk_bf16_f32 v147,v114,v115\n"
"s_add_u32 s67,s67,0x10000\n"
"s_add_u32 m0,s66,0x5000\n"
"s_nop 0\n"
"buffer_load_dwordx4 v232,s[24:27],s67 offen nt lds\n"
"v_mfma_f32_16x16x32_bf16 v[128:131],v[180:183],a[32:35],v[128:131]\n"
"buffer_load_dwordx4 v233,s[24:27],s67 offen offset:1024 nt lds\n"
"buffer_load_dwordx4 v234,s[24:27],s67 offen offset:2048 nt lds\n"
"v_mfma_f32_16x16x32_bf16 v[124:127],v[168:171],a[44:47],v[124:127]\n"
"buffer_load_dwordx4 v235,s[24:27],s67 offen offset:3072 nt lds\n"
"s_waitcnt vmcnt(24)\n"
"v_max3_f32 v247,v116,v117,v118\n"
"v_max3_f32 v247,v247,v119,v120\n"
"v_mfma_f32_16x16x32_bf16 v[128:131],v[184:187],a[44:47],v[128:131]\n"
"v_max3_f32 v247,v247,v121,v122\n"
"v_max_f32 v247,v247,v123\n"
"v_cmp_lt_f32 vcc,s64,v247\n"
"ds_read_b64_tr_b16 v[188:189],v224 offset:24576\n"
"ds_read_b64_tr_b16 v[190:191],v224 offset:28672\n"
"ds_read_b64_tr_b16 v[192:193],v225 offset:24576\n"
"ds_read_b64_tr_b16 v[194:195],v225 offset:28672\n"
"ds_read_b64_tr_b16 v[196:197],v226 offset:24576\n"
"ds_read_b64_tr_b16 v[198:199],v226 offset:28672\n"
"ds_read_b64_tr_b16 v[200:201],v227 offset:24576\n"
"ds_read_b64_tr_b16 v[202:203],v227 offset:28672\n"
"ds_read_b64_tr_b16 v[204:205],v228 offset:24576\n"
"ds_read_b64_tr_b16 v[206:207],v228 offset:28672\n"
"ds_read_b64_tr_b16 v[208:209],v229 offset:24576\n"
"ds_read_b64_tr_b16 v[210:211],v229 offset:28672\n"
"ds_read_b64_tr_b16 v[212:213],v230 offset:24576\n"
"ds_read_b64_tr_b16 v[214:215],v230 offset:28672\n"
"ds_read_b64_tr_b16 v[216:217],v231 offset:24576\n"
"s_waitcnt lgkmcnt(14)\n"
"ds_read_b64_tr_b16 v[218:219],v231 offset:28672\n"
"s_cmp_lg_u64 s[80:81],0\n"
"s_cbranch_scc1 Lk_89x\n"
"Lk_89:\n"
"s_waitcnt lgkmcnt(14)\n"
"v_mfma_f32_16x16x32_bf16 v[0:3],v[188:191],v[144:147],v[0:3]\n"
"v_exp_f32 v116,v116\n"
"s_waitcnt lgkmcnt(12)\n"
"v_mfma_f32_16x16x32_bf16 v[4:7],v[192:195],v[144:147],v[4:7]\n"
"v_exp_f32 v117,v117\n"
"v_exp_f32 v118,v118\n"
"s_waitcnt lgkmcnt(10)\n"
"v_mfma_f32_16x16x32_bf16 v[8:11],v[196:199],v[144:147],v[8:11]\n"
"v_exp_f32 v119,v119\n"
"s_waitcnt lgkmcnt(8)\n"
"v_mfma_f32_16x16x32_bf16 v[12:15],v[200:203],v[144:147],v[12:15]\n"
"v_cvt_pk_bf16_f32 v148,v116,v117\n"
"v_exp_f32 v120,v120\n"
"s_waitcnt lgkmcnt(6)\n"
"v_mfma_f32_16x16x32_bf16 v[16:19],v[204:207],v[144:147],v[16:19]\n"
"v_exp_f32 v121,v121\n"
"v_cvt_pk_bf16_f32 v149,v118,v119\n"
"s_waitcnt lgkmcnt(4)\n"
"v_mfma_f32_16x16x32_bf16 v[20:23],v[208:211],v[144:147],v[20:23]\n"
"v_exp_f32 v122,v122\n"
"s_waitcnt lgkmcnt(0)\n"
"v_mfma_f32_16x16x32_bf16 v[24:27],v[212:215],v[144:147],v[24:27]\n"
"v_exp_f32 v123,v123\n"
"v_cvt_pk_bf16_f32 v150,v120,v121\n"
"v_mfma_f32_16x16x32_bf16 v[28:31],v[216:219],v[144:147],v[28:31]\n"
"v_cvt_pk_bf16_f32 v151,v122,v123\n"
"v_max3_f32 v248,v124,v125,v126\n"
"v_max3_f32 v248,v248,v127,v128\n"
"v_mfma_f32_16x16x32_bf16 v[96:99],a[48:51],v[144:147],v[96:99]\n"
"v_max3_f32 v248,v248,v129,v130\n"
"v_max_f32 v248,v248,v131\n"
"v_cmp_lt_f32 s[82:83],s64,v248\n"
"s_cbranch_vccnz Lk_90x\n"
"Lk_90:\n"
"v_mfma_f32_16x16x32_bf16 v[32:35],v[188:191],v[148:151],v[32:35]\n"
"v_exp_f32 v124,v124\n"
"v_mfma_f32_16x16x32_bf16 v[36:39],v[192:195],v[148:151],v[36:39]\n"
"v_exp_f32 v125,v125\n"
"v_exp_f32 v126,v126\n"
"v_mfma_f32_16x16x32_bf16 v[40:43],v[196:199],v[148:151],v[40:43]\n"
"v_exp_f32 v127,v127\n"
"v_cvt_pk_bf16_f32 v152,v124,v125\n"
"v_mfma_f32_16x16x32_bf16 v[44:47],v[200:203],v[148:151],v[44:47]\n"
"v_exp_f32 v128,v128\n"
"v_mfma_f32_16x16x32_bf16 v[48:51],v[204:207],v[148:151],v[48:51]\n"
"v_exp_f32 v129,v129\n"
"v_cvt_pk_bf16_f32 v153,v126,v127\n"
"v_mfma_f32_16x16x32_bf16 v[52:55],v[208:211],v[148:151],v[52:55]\n"
"v_exp_f32 v130,v130\n"
"v_exp_f32 v131,v131\n"
"v_mfma_f32_16x16x32_bf16 v[56:59],v[212:215],v[148:151],v[56:59]\n"
"v_cvt_pk_bf16_f32 v154,v128,v129\n"
"v_cvt_pk_bf16_f32 v155,v130,v131\n"
"v_mfma_f32_16x16x32_bf16 v[60:63],v[216:219],v[148:151],v[60:63]\n"
"s_add_u32 s67,s1,0x3400\n"
"s_and_b32 s67,s67,0x3fff\n"
"s_add_u32 m0,s66,0x6000\n"
"s_nop 0\n"
"buffer_load_dwordx4 v236,s[28:31],s67 offen nt lds\n"
"v_mfma_f32_16x16x32_bf16 v[100:103],a[48:51],v[148:151],v[100:103]\n"
"buffer_load_dwordx4 v237,s[28:31],s67 offen offset:1024 nt lds\n"
"buffer_load_dwordx4 v254,s[28:31],s67 offen offset:2048 nt lds\n"
"buffer_load_dwordx4 v255,s[28:31],s67 offen offset:3072 nt lds\n"
"s_cmp_lg_u64 s[82:83],0\n"
"s_cbranch_scc1 Lk_91x\n"
"Lk_91:\n"
"v_mfma_f32_16x16x32_bf16 v[64:67],v[188:191],v[152:155],v[64:67]\n"
"v_mfma_f32_16x16x32_bf16 v[68:71],v[192:195],v[152:155],v[68:71]\n"
"s_add_u32 s67,s67,0x10000\n"
"s_add_u32 m0,s66,0x7000\n"
"s_nop 0\n"
"buffer_load_dwordx4 v236,s[28:31],s67 offen nt lds\n"
"v_mfma_f32_16x16x32_bf16 v[72:75],v[196:199],v[152:155],v[72:75]\n"
"v_mfma_f32_16x16x32_bf16 v[76:79],v[200:203],v[152:155],v[76:79]\n"
"v_mfma_f32_16x16x32_bf16 v[80:83],v[204:207],v[152:155],v[80:83]\n"
"buffer_load_dwordx4 v237,s[28:31],s67 offen offset:1024 nt lds\n"
"v_mfma_f32_16x16x32_bf16 v[84:87],v[208:211],v[152:155],v[84:87]\n"
"v_mfma_f32_16x16x32_bf16 v[88:91],v[212:215],v[152:155],v[88:91]\n"
"buffer_load_dwordx4 v254,s[28:31],s67 offen offset:2048 nt lds\n"
"v_mfma_f32_16x16x32_bf16 v[92:95],v[216:219],v[152:155],v[92:95]\n"
"v_mfma_f32_16x16x32_bf16 v[104:107],a[48:51],v[152:155],v[104:107]\n"
"buffer_load_dwordx4 v255,s[28:31],s67 offen offset:3072 nt lds\n"
"s_waitcnt vmcnt(24)\n"
"ds_read_b128 v[156:159],v220\n"
"ds_read_b128 v[172:175],v220 offset:4096\n"
"ds_read_b128 v[160:163],v221\n"
"ds_read_b128 v[176:179],v221 offset:4096\n"
"ds_read_b128 v[164:167],v222\n"
"ds_read_b128 v[180:183],v222 offset:4096\n"
"ds_read_b128 v[168:171],v223\n"
"ds_read_b128 v[184:187],v223 offset:4096\n"
"s_waitcnt lgkmcnt(7)\n"
"v_mfma_f32_16x16x32_bf16 v[108:111],v[156:159],a[0:3],v[132:135]\n"
"s_waitcnt lgkmcnt(6)\n"
"v_mfma_f32_16x16x32_bf16 v[112:115],v[172:175],a[0:3],v[132:135]\n"
"s_waitcnt lgkmcnt(5)\n"
"v_mfma_f32_16x16x32_bf16 v[108:111],v[160:163],a[12:15],v[108:111]\n"
"s_waitcnt lgkmcnt(4)\n"
"v_mfma_f32_16x16x32_bf16 v[112:115],v[176:179],a[12:15],v[112:115]\n"
"s_waitcnt lgkmcnt(3)\n"
"v_mfma_f32_16x16x32_bf16 v[108:111],v[164:167],a[24:27],v[108:111]\n"
"s_waitcnt lgkmcnt(2)\n"
"v_mfma_f32_16x16x32_bf16 v[112:115],v[180:183],a[24:27],v[112:115]\n"
"s_waitcnt lgkmcnt(1)\n"
"v_mfma_f32_16x16x32_bf16 v[108:111],v[168:171],a[36:39],v[108:111]\n"
"s_waitcnt lgkmcnt(0)\n"
"v_mfma_f32_16x16x32_bf16 v[112:115],v[184:187],a[36:39],v[112:115]\n"
"v_mfma_f32_16x16x32_bf16 v[116:119],v[156:159],a[4:7],v[136:139]\n"
"s_add_u32 s67,s1,0x3800\n"
"s_and_b32 s67,s67,0x3fff\n"
"s_add_u32 m0,s66,0\n"
"s_nop 0\n"
"buffer_load_dwordx4 v232,s[24:27],s67 offen nt lds\n"
"v_mfma_f32_16x16x32_bf16 v[120:123],v[172:175],a[4:7],v[136:139]\n"
"buffer_load_dwordx4 v233,s[24:27],s67 offen offset:1024 nt lds\n"
"buffer_load_dwordx4 v234,s[24:27],s67 offen offset:2048 nt lds\n"
"v_mfma_f32_16x16x32_bf16 v[116:119],v[160:163],a[16:19],v[116:119]\n"
"buffer_load_dwordx4 v235,s[24:27],s67 offen offset:3072 nt lds\n"
"v_max3_f32 v246,v108,v109,v110\n"
"v_mfma_f32_16x16x32_bf16 v[120:123],v[176:179],a[16:19],v[120:123]\n"
"v_max3_f32 v246,v246,v111,v112\n"
"v_max3_f32 v246,v246,v113,v114\n"
"v_mfma_f32_16x16x32_bf16 v[116:119],v[164:167],a[28:31],v[116:119]\n"
"v_max_f32 v246,v246,v115\n"
"v_cmp_lt_f32 s[80:81],s64,v246\n"
"v_mfma_f32_16x16x32_bf16 v[120:123],v[180:183],a[28:31],v[120:123]\n"
"v_exp_f32 v108,v108\n"
"v_exp_f32 v109,v109\n"
"v_mfma_f32_16x16x32_bf16 v[116:119],v[168:171],a[40:43],v[116:119]\n"
"v_exp_f32 v110,v110\n"
"v_mfma_f32_16x16x32_bf16 v[120:123],v[184:187],a[40:43],v[120:123]\n"
"v_exp_f32 v111,v111\n"
"v_mfma_f32_16x16x32_bf16 v[124:127],v[156:159],a[8:11],v[140:143]\n"
"v_cvt_pk_bf16_f32 v144,v108,v109\n"
"v_exp_f32 v112,v112\n"
"v_mfma_f32_16x16x32_bf16 v[128:131],v[172:175],a[8:11],v[140:143]\n"
"v_exp_f32 v113,v113\n"
"v_cvt_pk_bf16_f32 v145,v110,v111\n"
"v_mfma_f32_16x16x32_bf16 v[124:127],v[160:163],a[20:23],v[124:127]\n"
"v_exp_f32 v114,v114\n"
"v_mfma_f32_16x16x32_bf16 v[128:131],v[176:179],a[20:23],v[128:131]\n"
"v_exp_f32 v115,v115\n"
"v_cvt_pk_bf16_f32 v146,v112,v113\n"
"v_mfma_f32_16x16x32_bf16 v[124:127],v[164:167],a[32:35],v[124:127]\n"
"v_cvt_pk_bf16_f32 v147,v114,v115\n"
"s_add_u32 s67,s67,0x10000\n"
"s_add_u32 m0,s66,0x1000\n"
"s_nop 0\n"
"buffer_load_dwordx4 v232,s[24:27],s67 offen nt lds\n"
"v_mfma_f32_16x16x32_bf16 v[128:131],v[180:183],a[32:35],v[128:131]\n"
"buffer_load_dwordx4 v233,s[24:27],s67 offen offset:1024 nt lds\n"
"buffer_load_dwordx4 v234,s[24:27],s67 offen offset:2048 nt lds\n"
"v_mfma_f32_16x16x32_bf16 v[124:127],v[168:171],a[44:47],v[124:127]\n"
"buffer_load_dwordx4 v235,s[24:27],s67 offen offset:3072 nt lds\n"
"s_waitcnt vmcnt(24)\n"
"v_max3_f32 v247,v116,v117,v118\n"
"v_max3_f32 v247,v247,v119,v120\n"
"v_mfma_f32_16x16x32_bf16 v[128:131],v[184:187],a[44:47],v[128:131]\n"
"v_max3_f32 v247,v247,v121,v122\n"
"v_max_f32 v247,v247,v123\n"
"v_cmp_lt_f32 vcc,s64,v247\n"
"ds_read_b64_tr_b16 v[188:189],v224 offset:8192\n"
"ds_read_b64_tr_b16 v[190:191],v224 offset:12288\n"
"ds_read_b64_tr_b16 v[192:193],v225 offset:8192\n"
"ds_read_b64_tr_b16 v[194:195],v225 offset:12288\n"
"ds_read_b64_tr_b16 v[196:197],v226 offset:8192\n"
"ds_read_b64_tr_b16 v[198:199],v226 offset:12288\n"
"ds_read_b64_tr_b16 v[200:201],v227 offset:8192\n"
"ds_read_b64_tr_b16 v[202:203],v227 offset:12288\n"
"ds_read_b64_tr_b16 v[204:205],v228 offset:8192\n"
"ds_read_b64_tr_b16 v[206:207],v228 offset:12288\n"
"ds_read_b64_tr_b16 v[208:209],v229 offset:8192\n"
"ds_read_b64_tr_b16 v[210:211],v229 offset:12288\n"
"ds_read_b64_tr_b16 v[212:213],v230 offset:8192\n"
"ds_read_b64_tr_b16 v[214:215],v230 offset:12288\n"
"ds_read_b64_tr_b16 v[216:217],v231 offset:8192\n"
"s_waitcnt lgkmcnt(14)\n"
"ds_read_b64_tr_b16 v[218:219],v231 offset:12288\n"
"s_cmp_lg_u64 s[80:81],0\n"
"s_cbranch_scc1 Lk_92x\n"
"Lk_92:\n"
"s_waitcnt lgkmcnt(14)\n"
"v_mfma_f32_16x16x32_bf16 v[0:3],v[188:191],v[144:147],v[0:3]\n"
"v_exp_f32 v116,v116\n"
"s_waitcnt lgkmcnt(12)\n"
"v_mfma_f32_16x16x32_bf16 v[4:7],v[192:195],v[144:147],v[4:7]\n"
"v_exp_f32 v117,v117\n"
"v_exp_f32 v118,v118\n"
"s_waitcnt lgkmcnt(10)\n"
"v_mfma_f32_16x16x32_bf16 v[8:11],v[196:199],v[144:147],v[8:11]\n"
"v_exp_f32 v119,v119\n"
"s_waitcnt lgkmcnt(8)\n"
"v_mfma_f32_16x16x32_bf16 v[12:15],v[200:203],v[144:147],v[12:15]\n"
"v_cvt_pk_bf16_f32 v148,v116,v117\n"
"v_exp_f32 v120,v120\n"
"s_waitcnt lgkmcnt(6)\n"
"v_mfma_f32_16x16x32_bf16 v[16:19],v[204:207],v[144:147],v[16:19]\n"
"v_exp_f32 v121,v121\n"
"v_cvt_pk_bf16_f32 v149,v118,v119\n"
"s_waitcnt lgkmcnt(4)\n"
"v_mfma_f32_16x16x32_bf16 v[20:23],v[208:211],v[144:147],v[20:23]\n"
"v_exp_f32 v122,v122\n"
"s_waitcnt lgkmcnt(0)\n"
"v_mfma_f32_16x16x32_bf16 v[24:27],v[212:215],v[144:147],v[24:27]\n"
"v_exp_f32 v123,v123\n"
"v_cvt_pk_bf16_f32 v150,v120,v121\n"
"v_mfma_f32_16x16x32_bf16 v[28:31],v[216:219],v[144:147],v[28:31]\n"
"v_cvt_pk_bf16_f32 v151,v122,v123\n"
"v_max3_f32 v248,v124,v125,v126\n"
"v_max3_f32 v248,v248,v127,v128\n"
"v_mfma_f32_16x16x32_bf16 v[96:99],a[48:51],v[144:147],v[96:99]\n"
"v_max3_f32 v248,v248,v129,v130\n"
"v_max_f32 v248,v248,v131\n"
"v_cmp_lt_f32 s[82:83],s64,v248\n"
"s_cbranch_vccnz Lk_93x\n"
"Lk_93:\n"
"v_mfma_f32_16x16x32_bf16 v[32:35],v[188:191],v[148:151],v[32:35]\n"
"v_exp_f32 v124,v124\n"
"v_mfma_f32_16x16x32_bf16 v[36:39],v[192:195],v[148:151],v[36:39]\n"
"v_exp_f32 v125,v125\n"
"v_exp_f32 v126,v126\n"
"v_mfma_f32_16x16x32_bf16 v[40:43],v[196:199],v[148:151],v[40:43]\n"
"v_exp_f32 v127,v127\n"
"v_cvt_pk_bf16_f32 v152,v124,v125\n"
"v_mfma_f32_16x16x32_bf16 v[44:47],v[200:203],v[148:151],v[44:47]\n"
"v_exp_f32 v128,v128\n"
"v_mfma_f32_16x16x32_bf16 v[48:51],v[204:207],v[148:151],v[48:51]\n"
"v_exp_f32 v129,v129\n"
"v_cvt_pk_bf16_f32 v153,v126,v127\n"
"v_mfma_f32_16x16x32_bf16 v[52:55],v[208:211],v[148:151],v[52:55]\n"
"v_exp_f32 v130,v130\n"
"v_exp_f32 v131,v131\n"
"v_mfma_f32_16x16x32_bf16 v[56:59],v[212:215],v[148:151],v[56:59]\n"
"v_cvt_pk_bf16_f32 v154,v128,v129\n"
"v_cvt_pk_bf16_f32 v155,v130,v131\n"
"v_mfma_f32_16x16x32_bf16 v[60:63],v[216:219],v[148:151],v[60:63]\n"
"s_add_u32 s67,s1,0x3800\n"
"s_and_b32 s67,s67,0x3fff\n"
"s_add_u32 m0,s66,0x2000\n"
"s_nop 0\n"
"buffer_load_dwordx4 v236,s[28:31],s67 offen nt lds\n"
"v_mfma_f32_16x16x32_bf16 v[100:103],a[48:51],v[148:151],v[100:103]\n"
"buffer_load_dwordx4 v237,s[28:31],s67 offen offset:1024 nt lds\n"
"buffer_load_dwordx4 v254,s[28:31],s67 offen offset:2048 nt lds\n"
"buffer_load_dwordx4 v255,s[28:31],s67 offen offset:3072 nt lds\n"
"s_cmp_lg_u64 s[82:83],0\n"
"s_cbranch_scc1 Lk_94x\n"
"Lk_94:\n"
"v_mfma_f32_16x16x32_bf16 v[64:67],v[188:191],v[152:155],v[64:67]\n"
"v_mfma_f32_16x16x32_bf16 v[68:71],v[192:195],v[152:155],v[68:71]\n"
"s_add_u32 s67,s67,0x10000\n"
"s_add_u32 m0,s66,0x3000\n"
"s_nop 0\n"
"buffer_load_dwordx4 v236,s[28:31],s67 offen nt lds\n"
"v_mfma_f32_16x16x32_bf16 v[72:75],v[196:199],v[152:155],v[72:75]\n"
"v_mfma_f32_16x16x32_bf16 v[76:79],v[200:203],v[152:155],v[76:79]\n"
"v_mfma_f32_16x16x32_bf16 v[80:83],v[204:207],v[152:155],v[80:83]\n"
"buffer_load_dwordx4 v237,s[28:31],s67 offen offset:1024 nt lds\n"
"v_mfma_f32_16x16x32_bf16 v[84:87],v[208:211],v[152:155],v[84:87]\n"
"v_mfma_f32_16x16x32_bf16 v[88:91],v[212:215],v[152:155],v[88:91]\n"
"buffer_load_dwordx4 v254,s[28:31],s67 offen offset:2048 nt lds\n"
"v_mfma_f32_16x16x32_bf16 v[92:95],v[216:219],v[152:155],v[92:95]\n"
"v_mfma_f32_16x16x32_bf16 v[104:107],a[48:51],v[152:155],v[104:107]\n"
"buffer_load_dwordx4 v255,s[28:31],s67 offen offset:3072 nt lds\n"
"s_waitcnt vmcnt(24)\n"
"s_barrier\n"
"ds_read_b128 v[156:159],v220 offset:16384\n"
"ds_read_b128 v[172:175],v220 offset:20480\n"
"ds_read_b128 v[160:163],v221 offset:16384\n"
"ds_read_b128 v[176:179],v221 offset:20480\n"
"ds_read_b128 v[164:167],v222 offset:16384\n"
"ds_read_b128 v[180:183],v222 offset:20480\n"
"ds_read_b128 v[168:171],v223 offset:16384\n"
"ds_read_b128 v[184:187],v223 offset:20480\n"
"s_waitcnt lgkmcnt(7)\n"
"v_mfma_f32_16x16x32_bf16 v[108:111],v[156:159],a[0:3],v[132:135]\n"
"s_waitcnt lgkmcnt(6)\n"
"v_mfma_f32_16x16x32_bf16 v[112:115],v[172:175],a[0:3],v[132:135]\n"
"s_waitcnt lgkmcnt(5)\n"
"v_mfma_f32_16x16x32_bf16 v[108:111],v[160:163],a[12:15],v[108:111]\n"
"s_waitcnt lgkmcnt(4)\n"
"v_mfma_f32_16x16x32_bf16 v[112:115],v[176:179],a[12:15],v[112:115]\n"
"s_waitcnt lgkmcnt(3)\n"
"v_mfma_f32_16x16x32_bf16 v[108:111],v[164:167],a[24:27],v[108:111]\n"
"s_waitcnt lgkmcnt(2)\n"
"v_mfma_f32_16x16x32_bf16 v[112:115],v[180:183],a[24:27],v[112:115]\n"
"s_waitcnt lgkmcnt(1)\n"
"v_mfma_f32_16x16x32_bf16 v[108:111],v[168:171],a[36:39],v[108:111]\n"
"s_waitcnt lgkmcnt(0)\n"
"v_mfma_f32_16x16x32_bf16 v[112:115],v[184:187],a[36:39],v[112:115]\n"
"v_mfma_f32_16x16x32_bf16 v[116:119],v[156:159],a[4:7],v[136:139]\n"
"s_add_u32 s67,s1,0x3c00\n"
"s_and_b32 s67,s67,0x3fff\n"
"s_add_u32 m0,s66,0x4000\n"
"s_nop 0\n"
"buffer_load_dwordx4 v232,s[24:27],s67 offen nt lds\n"
"v_mfma_f32_16x16x32_bf16 v[120:123],v[172:175],a[4:7],v[136:139]\n"
"buffer_load_dwordx4 v233,s[24:27],s67 offen offset:1024 nt lds\n"
"buffer_load_dwordx4 v234,s[24:27],s67 offen offset:2048 nt lds\n"
"v_mfma_f32_16x16x32_bf16 v[116:119],v[160:163],a[16:19],v[116:119]\n"
"buffer_load_dwordx4 v235,s[24:27],s67 offen offset:3072 nt lds\n"
"v_max3_f32 v246,v108,v109,v110\n"
"v_mfma_f32_16x16x32_bf16 v[120:123],v[176:179],a[16:19],v[120:123]\n"
"v_max3_f32 v246,v246,v111,v112\n"
"v_max3_f32 v246,v246,v113,v114\n"
"v_mfma_f32_16x16x32_bf16 v[116:119],v[164:167],a[28:31],v[116:119]\n"
"v_max_f32 v246,v246,v115\n"
"v_cmp_lt_f32 s[80:81],s64,v246\n"
"v_mfma_f32_16x16x32_bf16 v[120:123],v[180:183],a[28:31],v[120:123]\n"
"v_exp_f32 v108,v108\n"
"v_exp_f32 v109,v109\n"
"v_mfma_f32_16x16x32_bf16 v[116:119],v[168:171],a[40:43],v[116:119]\n"
"v_exp_f32 v110,v110\n"
"v_mfma_f32_16x16x32_bf16 v[120:123],v[184:187],a[40:43],v[120:123]\n"
"v_exp_f32 v111,v111\n"
"v_mfma_f32_16x16x32_bf16 v[124:127],v[156:159],a[8:11],v[140:143]\n"
"v_cvt_pk_bf16_f32 v144,v108,v109\n"
"v_exp_f32 v112,v112\n"
"v_mfma_f32_16x16x32_bf16 v[128:131],v[172:175],a[8:11],v[140:143]\n"
"v_exp_f32 v113,v113\n"
"v_cvt_pk_bf16_f32 v145,v110,v111\n"
"v_mfma_f32_16x16x32_bf16 v[124:127],v[160:163],a[20:23],v[124:127]\n"
"v_exp_f32 v114,v114\n"
"v_mfma_f32_16x16x32_bf16 v[128:131],v[176:179],a[20:23],v[128:131]\n"
"v_exp_f32 v115,v115\n"
"v_cvt_pk_bf16_f32 v146,v112,v113\n"
"v_mfma_f32_16x16x32_bf16 v[124:127],v[164:167],a[32:35],v[124:127]\n"
"v_cvt_pk_bf16_f32 v147,v114,v115\n"
"s_add_u32 s67,s67,0x10000\n"
"s_add_u32 m0,s66,0x5000\n"
"s_nop 0\n"
"buffer_load_dwordx4 v232,s[24:27],s67 offen nt lds\n"
"v_mfma_f32_16x16x32_bf16 v[128:131],v[180:183],a[32:35],v[128:131]\n"
"buffer_load_dwordx4 v233,s[24:27],s67 offen offset:1024 nt lds\n"
"buffer_load_dwordx4 v234,s[24:27],s67 offen offset:2048 nt lds\n"
"v_mfma_f32_16x16x32_bf16 v[124:127],v[168:171],a[44:47],v[124:127]\n"
"buffer_load_dwordx4 v235,s[24:27],s67 offen offset:3072 nt lds\n"
"s_waitcnt vmcnt(24)\n"
"v_max3_f32 v247,v116,v117,v118\n"
"v_max3_f32 v247,v247,v119,v120\n"
"v_mfma_f32_16x16x32_bf16 v[128:131],v[184:187],a[44:47],v[128:131]\n"
"v_max3_f32 v247,v247,v121,v122\n"
"v_max_f32 v247,v247,v123\n"
"v_cmp_lt_f32 vcc,s64,v247\n"
"ds_read_b64_tr_b16 v[188:189],v224 offset:24576\n"
"ds_read_b64_tr_b16 v[190:191],v224 offset:28672\n"
"ds_read_b64_tr_b16 v[192:193],v225 offset:24576\n"
"ds_read_b64_tr_b16 v[194:195],v225 offset:28672\n"
"ds_read_b64_tr_b16 v[196:197],v226 offset:24576\n"
"ds_read_b64_tr_b16 v[198:199],v226 offset:28672\n"
"ds_read_b64_tr_b16 v[200:201],v227 offset:24576\n"
"ds_read_b64_tr_b16 v[202:203],v227 offset:28672\n"
"ds_read_b64_tr_b16 v[204:205],v228 offset:24576\n"
"ds_read_b64_tr_b16 v[206:207],v228 offset:28672\n"
"ds_read_b64_tr_b16 v[208:209],v229 offset:24576\n"
"ds_read_b64_tr_b16 v[210:211],v229 offset:28672\n"
"ds_read_b64_tr_b16 v[212:213],v230 offset:24576\n"
"ds_read_b64_tr_b16 v[214:215],v230 offset:28672\n"
"ds_read_b64_tr_b16 v[216:217],v231 offset:24576\n"
"s_waitcnt lgkmcnt(14)\n"
"ds_read_b64_tr_b16 v[218:219],v231 offset:28672\n"
"s_cmp_lg_u64 s[80:81],0\n"
"s_cbranch_scc1 Lk_95x\n"
"Lk_95:\n"
"s_waitcnt lgkmcnt(14)\n"
"v_mfma_f32_16x16x32_bf16 v[0:3],v[188:191],v[144:147],v[0:3]\n"
"v_exp_f32 v116,v116\n"
"s_waitcnt lgkmcnt(12)\n"
"v_mfma_f32_16x16x32_bf16 v[4:7],v[192:195],v[144:147],v[4:7]\n"
"v_exp_f32 v117,v117\n"
"v_exp_f32 v118,v118\n"
"s_waitcnt lgkmcnt(10)\n"
"v_mfma_f32_16x16x32_bf16 v[8:11],v[196:199],v[144:147],v[8:11]\n"
"v_exp_f32 v119,v119\n"
"s_waitcnt lgkmcnt(8)\n"
"v_mfma_f32_16x16x32_bf16 v[12:15],v[200:203],v[144:147],v[12:15]\n"
"v_cvt_pk_bf16_f32 v148,v116,v117\n"
"v_exp_f32 v120,v120\n"
"s_waitcnt lgkmcnt(6)\n"
"v_mfma_f32_16x16x32_bf16 v[16:19],v[204:207],v[144:147],v[16:19]\n"
"v_exp_f32 v121,v121\n"
"v_cvt_pk_bf16_f32 v149,v118,v119\n"
"s_waitcnt lgkmcnt(4)\n"
"v_mfma_f32_16x16x32_bf16 v[20:23],v[208:211],v[144:147],v[20:23]\n"
"v_exp_f32 v122,v122\n"
"s_waitcnt lgkmcnt(0)\n"
"v_mfma_f32_16x16x32_bf16 v[24:27],v[212:215],v[144:147],v[24:27]\n"
"v_exp_f32 v123,v123\n"
"v_cvt_pk_bf16_f32 v150,v120,v121\n"
"v_mfma_f32_16x16x32_bf16 v[28:31],v[216:219],v[144:147],v[28:31]\n"
"v_cvt_pk_bf16_f32 v151,v122,v123\n"
"v_max3_f32 v248,v124,v125,v126\n"
"v_max3_f32 v248,v248,v127,v128\n"
"v_mfma_f32_16x16x32_bf16 v[96:99],a[48:51],v[144:147],v[96:99]\n"
"v_max3_f32 v248,v248,v129,v130\n"
"v_max_f32 v248,v248,v131\n"
"v_cmp_lt_f32 s[82:83],s64,v248\n"
"s_cbranch_vccnz Lk_96x\n"
"Lk_96:\n"
"v_mfma_f32_16x16x32_bf16 v[32:35],v[188:191],v[148:151],v[32:35]\n"
"v_exp_f32 v124,v124\n"
"v_mfma_f32_16x16x32_bf16 v[36:39],v[192:195],v[148:151],v[36:39]\n"
"v_exp_f32 v125,v125\n"
"v_exp_f32 v126,v126\n"
"v_mfma_f32_16x16x32_bf16 v[40:43],v[196:199],v[148:151],v[40:43]\n"
"v_exp_f32 v127,v127\n"
"v_cvt_pk_bf16_f32 v152,v124,v125\n"
"v_mfma_f32_16x16x32_bf16 v[44:47],v[200:203],v[148:151],v[44:47]\n"
"v_exp_f32 v128,v128\n"
"v_mfma_f32_16x16x32_bf16 v[48:51],v[204:207],v[148:151],v[48:51]\n"
"v_exp_f32 v129,v129\n"
"v_cvt_pk_bf16_f32 v153,v126,v127\n"
"v_mfma_f32_16x16x32_bf16 v[52:55],v[208:211],v[148:151],v[52:55]\n"
"v_exp_f32 v130,v130\n"
"v_exp_f32 v131,v131\n"
"v_mfma_f32_16x16x32_bf16 v[56:59],v[212:215],v[148:151],v[56:59]\n"
"v_cvt_pk_bf16_f32 v154,v128,v129\n"
"v_cvt_pk_bf16_f32 v155,v130,v131\n"
"v_mfma_f32_16x16x32_bf16 v[60:63],v[216:219],v[148:151],v[60:63]\n"
"s_add_u32 s67,s1,0x3c00\n"
"s_and_b32 s67,s67,0x3fff\n"
"s_add_u32 m0,s66,0x6000\n"
"s_nop 0\n"
"buffer_load_dwordx4 v236,s[28:31],s67 offen nt lds\n"
"v_mfma_f32_16x16x32_bf16 v[100:103],a[48:51],v[148:151],v[100:103]\n"
"buffer_load_dwordx4 v237,s[28:31],s67 offen offset:1024 nt lds\n"
"buffer_load_dwordx4 v254,s[28:31],s67 offen offset:2048 nt lds\n"
"buffer_load_dwordx4 v255,s[28:31],s67 offen offset:3072 nt lds\n"
"s_cmp_lg_u64 s[82:83],0\n"
"s_cbranch_scc1 Lk_97x\n"
"Lk_97:\n"
"v_mfma_f32_16x16x32_bf16 v[64:67],v[188:191],v[152:155],v[64:67]\n"
"v_mfma_f32_16x16x32_bf16 v[68:71],v[192:195],v[152:155],v[68:71]\n"
"s_add_u32 s67,s67,0x10000\n"
"s_add_u32 m0,s66,0x7000\n"
"s_nop 0\n"
"buffer_load_dwordx4 v236,s[28:31],s67 offen nt lds\n"
"v_mfma_f32_16x16x32_bf16 v[72:75],v[196:199],v[152:155],v[72:75]\n"
"v_mfma_f32_16x16x32_bf16 v[76:79],v[200:203],v[152:155],v[76:79]\n"
"v_mfma_f32_16x16x32_bf16 v[80:83],v[204:207],v[152:155],v[80:83]\n"
"buffer_load_dwordx4 v237,s[28:31],s67 offen offset:1024 nt lds\n"
"v_mfma_f32_16x16x32_bf16 v[84:87],v[208:211],v[152:155],v[84:87]\n"
"v_mfma_f32_16x16x32_bf16 v[88:91],v[212:215],v[152:155],v[88:91]\n"
"buffer_load_dwordx4 v254,s[28:31],s67 offen offset:2048 nt lds\n"
"v_mfma_f32_16x16x32_bf16 v[92:95],v[216:219],v[152:155],v[92:95]\n"
"v_mfma_f32_16x16x32_bf16 v[104:107],a[48:51],v[152:155],v[104:107]\n"
"buffer_load_dwordx4 v255,s[28:31],s67 offen offset:3072 nt lds\n"
"s_waitcnt vmcnt(24)\n"
"s_barrier\n"
"ds_read_b128 v[156:159],v220\n"
"ds_read_b128 v[172:175],v220 offset:4096\n"
"ds_read_b128 v[160:163],v221\n"
"ds_read_b128 v[176:179],v221 offset:4096\n"
"ds_read_b128 v[164:167],v222\n"
"ds_read_b128 v[180:183],v222 offset:4096\n"
"ds_read_b128 v[168:171],v223\n"
"ds_read_b128 v[184:187],v223 offset:4096\n"
"s_waitcnt lgkmcnt(7)\n"
"v_mfma_f32_16x16x32_bf16 v[108:111],v[156:159],a[0:3],v[132:135]\n"
"s_waitcnt lgkmcnt(6)\n"
"v_mfma_f32_16x16x32_bf16 v[112:115],v[172:175],a[0:3],v[132:135]\n"
"s_waitcnt lgkmcnt(5)\n"
"v_mfma_f32_16x16x32_bf16 v[108:111],v[160:163],a[12:15],v[108:111]\n"
"s_waitcnt lgkmcnt(4)\n"
"v_mfma_f32_16x16x32_bf16 v[112:115],v[176:179],a[12:15],v[112:115]\n"
"s_waitcnt lgkmcnt(3)\n"
"v_mfma_f32_16x16x32_bf16 v[108:111],v[164:167],a[24:27],v[108:111]\n"
"s_waitcnt lgkmcnt(2)\n"
"v_mfma_f32_16x16x32_bf16 v[112:115],v[180:183],a[24:27],v[112:115]\n"
"s_waitcnt lgkmcnt(1)\n"
"v_mfma_f32_16x16x32_bf16 v[108:111],v[168:171],a[36:39],v[108:111]\n"
"s_waitcnt lgkmcnt(0)\n"
"v_mfma_f32_16x16x32_bf16 v[112:115],v[184:187],a[36:39],v[112:115]\n"
"v_mfma_f32_16x16x32_bf16 v[116:119],v[156:159],a[4:7],v[136:139]\n"
"s_nop 4\n"
"v_max3_f32 v246,v108,v109,v110\n"
"s_nop 0\n"
"v_max3_f32 v246,v246,v111,v112\n"
"v_mfma_f32_16x16x32_bf16 v[120:123],v[172:175],a[4:7],v[136:139]\n"
"v_max3_f32 v246,v246,v113,v114\n"
"v_mfma_f32_16x16x32_bf16 v[116:119],v[160:163],a[16:19],v[116:119]\n"
"v_max_f32 v246,v246,v115\n"
"v_cmp_lt_f32 s[80:81],s64,v246\n"
"v_mfma_f32_16x16x32_bf16 v[120:123],v[176:179],a[16:19],v[120:123]\n"
"v_exp_f32 v108,v108\n"
"v_mfma_f32_16x16x32_bf16 v[116:119],v[164:167],a[28:31],v[116:119]\n"
"v_exp_f32 v109,v109\n"
"v_mfma_f32_16x16x32_bf16 v[120:123],v[180:183],a[28:31],v[120:123]\n"
"v_mfma_f32_16x16x32_bf16 v[116:119],v[168:171],a[40:43],v[116:119]\n"
"v_exp_f32 v110,v110\n"
"v_mfma_f32_16x16x32_bf16 v[120:123],v[184:187],a[40:43],v[120:123]\n"
"v_exp_f32 v111,v111\n"
"v_mfma_f32_16x16x32_bf16 v[124:127],v[156:159],a[8:11],v[140:143]\n"
"v_cvt_pk_bf16_f32 v144,v108,v109\n"
"v_exp_f32 v112,v112\n"
"v_mfma_f32_16x16x32_bf16 v[128:131],v[172:175],a[8:11],v[140:143]\n"
"v_exp_f32 v113,v113\n"
"v_mfma_f32_16x16x32_bf16 v[124:127],v[160:163],a[20:23],v[124:127]\n"
"v_cvt_pk_bf16_f32 v145,v110,v111\n"
"v_mfma_f32_16x16x32_bf16 v[128:131],v[176:179],a[20:23],v[128:131]\n"
"v_exp_f32 v114,v114\n"
"v_mfma_f32_16x16x32_bf16 v[124:127],v[164:167],a[32:35],v[124:127]\n"
"v_exp_f32 v115,v115\n"
"v_cvt_pk_bf16_f32 v146,v112,v113\n"
"v_mfma_f32_16x16x32_bf16 v[128:131],v[180:183],a[32:35],v[128:131]\n"
"v_cvt_pk_bf16_f32 v147,v114,v115\n"
"s_waitcnt vmcnt(16)\n"
"v_max3_f32 v247,v116,v117,v118\n"
"v_mfma_f32_16x16x32_bf16 v[124:127],v[168:171],a[44:47],v[124:127]\n"
"v_max3_f32 v247,v247,v119,v120\n"
"v_max3_f32 v247,v247,v121,v122\n"
"v_mfma_f32_16x16x32_bf16 v[128:131],v[184:187],a[44:47],v[128:131]\n"
"v_max_f32 v247,v247,v123\n"
"v_cmp_lt_f32 vcc,s64,v247\n"
"ds_read_b64_tr_b16 v[188:189],v224 offset:8192\n"
"ds_read_b64_tr_b16 v[190:191],v224 offset:12288\n"
"ds_read_b64_tr_b16 v[192:193],v225 offset:8192\n"
"ds_read_b64_tr_b16 v[194:195],v225 offset:12288\n"
"ds_read_b64_tr_b16 v[196:197],v226 offset:8192\n"
"ds_read_b64_tr_b16 v[198:199],v226 offset:12288\n"
"ds_read_b64_tr_b16 v[200:201],v227 offset:8192\n"
"ds_read_b64_tr_b16 v[202:203],v227 offset:12288\n"
"ds_read_b64_tr_b16 v[204:205],v228 offset:8192\n"
"ds_read_b64_tr_b16 v[206:207],v228 offset:12288\n"
"ds_read_b64_tr_b16 v[208:209],v229 offset:8192\n"
"ds_read_b64_tr_b16 v[210:211],v229 offset:12288\n"
"ds_read_b64_tr_b16 v[212:213],v230 offset:8192\n"
"ds_read_b64_tr_b16 v[214:215],v230 offset:12288\n"
"ds_read_b64_tr_b16 v[216:217],v231 offset:8192\n"
"s_waitcnt lgkmcnt(14)\n"
"ds_read_b64_tr_b16 v[218:219],v231 offset:12288\n"
"s_cmp_lg_u64 s[80:81],0\n"
"s_cbranch_scc1 Lk_98x\n"
"Lk_98:\n"
"s_waitcnt lgkmcnt(14)\n"
"v_mfma_f32_16x16x32_bf16 v[0:3],v[188:191],v[144:147],v[0:3]\n"
"v_exp_f32 v116,v116\n"
"s_waitcnt lgkmcnt(12)\n"
"v_mfma_f32_16x16x32_bf16 v[4:7],v[192:195],v[144:147],v[4:7]\n"
"v_exp_f32 v117,v117\n"
"v_exp_f32 v118,v118\n"
"s_waitcnt lgkmcnt(10)\n"
"v_mfma_f32_16x16x32_bf16 v[8:11],v[196:199],v[144:147],v[8:11]\n"
"v_exp_f32 v119,v119\n"
"s_waitcnt lgkmcnt(8)\n"
"v_mfma_f32_16x16x32_bf16 v[12:15],v[200:203],v[144:147],v[12:15]\n"
"v_cvt_pk_bf16_f32 v148,v116,v117\n"
"v_exp_f32 v120,v120\n"
"s_waitcnt lgkmcnt(6)\n"
"v_mfma_f32_16x16x32_bf16 v[16:19],v[204:207],v[144:147],v[16:19]\n"
"v_exp_f32 v121,v121\n"
"v_cvt_pk_bf16_f32 v149,v118,v119\n"
"s_waitcnt lgkmcnt(4)\n"
"v_mfma_f32_16x16x32_bf16 v[20:23],v[208:211],v[144:147],v[20:23]\n"
"v_exp_f32 v122,v122\n"
"s_waitcnt lgkmcnt(0)\n"
"v_mfma_f32_16x16x32_bf16 v[24:27],v[212:215],v[144:147],v[24:27]\n"
"v_exp_f32 v123,v123\n"
"v_cvt_pk_bf16_f32 v150,v120,v121\n"
"v_mfma_f32_16x16x32_bf16 v[28:31],v[216:219],v[144:147],v[28:31]\n"
"v_cvt_pk_bf16_f32 v151,v122,v123\n"
"v_max3_f32 v248,v124,v125,v126\n"
"v_max3_f32 v248,v248,v127,v128\n"
"v_mfma_f32_16x16x32_bf16 v[96:99],a[48:51],v[144:147],v[96:99]\n"
"v_max3_f32 v248,v248,v129,v130\n"
"v_max_f32 v248,v248,v131\n"
"v_cmp_lt_f32 s[82:83],s64,v248\n"
"s_cbranch_vccnz Lk_99x\n"
"Lk_99:\n"
"v_mfma_f32_16x16x32_bf16 v[32:35],v[188:191],v[148:151],v[32:35]\n"
"v_exp_f32 v124,v124\n"
"v_mfma_f32_16x16x32_bf16 v[36:39],v[192:195],v[148:151],v[36:39]\n"
"v_exp_f32 v125,v125\n"
"v_mfma_f32_16x16x32_bf16 v[40:43],v[196:199],v[148:151],v[40:43]\n"
"v_exp_f32 v126,v126\n"
"v_mfma_f32_16x16x32_bf16 v[44:47],v[200:203],v[148:151],v[44:47]\n"
"v_exp_f32 v127,v127\n"
"v_cvt_pk_bf16_f32 v152,v124,v125\n"
"v_mfma_f32_16x16x32_bf16 v[48:51],v[204:207],v[148:151],v[48:51]\n"
"v_exp_f32 v128,v128\n"
"v_mfma_f32_16x16x32_bf16 v[52:55],v[208:211],v[148:151],v[52:55]\n"
"v_exp_f32 v129,v129\n"
"v_mfma_f32_16x16x32_bf16 v[56:59],v[212:215],v[148:151],v[56:59]\n"
"v_cvt_pk_bf16_f32 v153,v126,v127\n"
"v_exp_f32 v130,v130\n"
"v_mfma_f32_16x16x32_bf16 v[60:63],v[216:219],v[148:151],v[60:63]\n"
"v_exp_f32 v131,v131\n"
"v_mfma_f32_16x16x32_bf16 v[100:103],a[48:51],v[148:151],v[100:103]\n"
"v_cvt_pk_bf16_f32 v154,v128,v129\n"
"v_cvt_pk_bf16_f32 v155,v130,v131\n"
"s_cmp_lg_u64 s[82:83],0\n"
"s_cbranch_scc1 Lk_100x\n"
"Lk_100:\n"
"v_mfma_f32_16x16x32_bf16 v[64:67],v[188:191],v[152:155],v[64:67]\n"
"v_mfma_f32_16x16x32_bf16 v[68:71],v[192:195],v[152:155],v[68:71]\n"
"v_mfma_f32_16x16x32_bf16 v[72:75],v[196:199],v[152:155],v[72:75]\n"
"v_mfma_f32_16x16x32_bf16 v[76:79],v[200:203],v[152:155],v[76:79]\n"
"v_mfma_f32_16x16x32_bf16 v[80:83],v[204:207],v[152:155],v[80:83]\n"
"v_mfma_f32_16x16x32_bf16 v[84:87],v[208:211],v[152:155],v[84:87]\n"
"v_mfma_f32_16x16x32_bf16 v[88:91],v[212:215],v[152:155],v[88:91]\n"
"v_mfma_f32_16x16x32_bf16 v[92:95],v[216:219],v[152:155],v[92:95]\n"
"v_mfma_f32_16x16x32_bf16 v[104:107],a[48:51],v[152:155],v[104:107]\n"
"s_waitcnt vmcnt(8)\n"
"ds_read_b128 v[156:159],v220 offset:16384\n"
"ds_read_b128 v[172:175],v220 offset:20480\n"
"ds_read_b128 v[160:163],v221 offset:16384\n"
"ds_read_b128 v[176:179],v221 offset:20480\n"
"ds_read_b128 v[164:167],v222 offset:16384\n"
"ds_read_b128 v[180:183],v222 offset:20480\n"
"ds_read_b128 v[168:171],v223 offset:16384\n"
"ds_read_b128 v[184:187],v223 offset:20480\n"
"s_waitcnt lgkmcnt(7)\n"
"v_mfma_f32_16x16x32_bf16 v[108:111],v[156:159],a[0:3],v[132:135]\n"
"s_waitcnt lgkmcnt(6)\n"
"v_mfma_f32_16x16x32_bf16 v[112:115],v[172:175],a[0:3],v[132:135]\n"
"s_waitcnt lgkmcnt(5)\n"
"v_mfma_f32_16x16x32_bf16 v[108:111],v[160:163],a[12:15],v[108:111]\n"
"s_waitcnt lgkmcnt(4)\n"
"v_mfma_f32_16x16x32_bf16 v[112:115],v[176:179],a[12:15],v[112:115]\n"
"s_waitcnt lgkmcnt(3)\n"
"v_mfma_f32_16x16x32_bf16 v[108:111],v[164:167],a[24:27],v[108:111]\n"
"s_waitcnt lgkmcnt(2)\n"
"v_mfma_f32_16x16x32_bf16 v[112:115],v[180:183],a[24:27],v[112:115]\n"
"s_waitcnt lgkmcnt(1)\n"
"v_mfma_f32_16x16x32_bf16 v[108:111],v[168:171],a[36:39],v[108:111]\n"
"s_waitcnt lgkmcnt(0)\n"
"v_mfma_f32_16x16x32_bf16 v[112:115],v[184:187],a[36:39],v[112:115]\n"
"v_mfma_f32_16x16x32_bf16 v[116:119],v[156:159],a[4:7],v[136:139]\n"
"s_add_u32 s92,s92,0x8000\n"
"s_and_b32 s92,s92,0x7ffff\n"
"s_add_u32 m0,s66,0\n"
"s_nop 0\n"
"buffer_load_dwordx4 v238,s[40:43],s92 offen lds\n"
"v_mfma_f32_16x16x32_bf16 v[120:123],v[172:175],a[4:7],v[136:139]\n"
"buffer_load_dwordx4 v239,s[40:43],s92 offen offset:1024 lds\n"
"buffer_load_dwordx4 v240,s[40:43],s92 offen offset:2048 lds\n"
"v_mfma_f32_16x16x32_bf16 v[116:119],v[160:163],a[16:19],v[116:119]\n"
"buffer_load_dwordx4 v241,s[40:43],s92 offen offset:3072 lds\n"
"v_max3_f32 v246,v108,v109,v110\n"
"v_mfma_f32_16x16x32_bf16 v[120:123],v[176:179],a[16:19],v[120:123]\n"
"v_max3_f32 v246,v246,v111,v112\n"
"v_max3_f32 v246,v246,v113,v114\n"
"v_mfma_f32_16x16x32_bf16 v[116:119],v[164:167],a[28:31],v[116:119]\n"
"v_max_f32 v246,v246,v115\n"
"v_cmp_lt_f32 s[80:81],s64,v246\n"
"v_mfma_f32_16x16x32_bf16 v[120:123],v[180:183],a[28:31],v[120:123]\n"
"v_exp_f32 v108,v108\n"
"v_exp_f32 v109,v109\n"
"v_mfma_f32_16x16x32_bf16 v[116:119],v[168:171],a[40:43],v[116:119]\n"
"v_exp_f32 v110,v110\n"
"v_mfma_f32_16x16x32_bf16 v[120:123],v[184:187],a[40:43],v[120:123]\n"
"v_exp_f32 v111,v111\n"
"v_mfma_f32_16x16x32_bf16 v[124:127],v[156:159],a[8:11],v[140:143]\n"
"v_cvt_pk_bf16_f32 v144,v108,v109\n"
"v_exp_f32 v112,v112\n"
"v_mfma_f32_16x16x32_bf16 v[128:131],v[172:175],a[8:11],v[140:143]\n"
"v_exp_f32 v113,v113\n"
"v_cvt_pk_bf16_f32 v145,v110,v111\n"
"v_mfma_f32_16x16x32_bf16 v[124:127],v[160:163],a[20:23],v[124:127]\n"
"v_exp_f32 v114,v114\n"
"v_mfma_f32_16x16x32_bf16 v[128:131],v[176:179],a[20:23],v[128:131]\n"
"v_exp_f32 v115,v115\n"
"v_cvt_pk_bf16_f32 v146,v112,v113\n"
"v_mfma_f32_16x16x32_bf16 v[124:127],v[164:167],a[32:35],v[124:127]\n"
"v_cvt_pk_bf16_f32 v147,v114,v115\n"
"s_add_u32 s67,s92,0x4000\n"
"s_add_u32 m0,s66,0x1000\n"
"s_nop 0\n"
"buffer_load_dwordx4 v238,s[40:43],s67 offen lds\n"
"v_mfma_f32_16x16x32_bf16 v[128:131],v[180:183],a[32:35],v[128:131]\n"
"buffer_load_dwordx4 v239,s[40:43],s67 offen offset:1024 lds\n"
"buffer_load_dwordx4 v240,s[40:43],s67 offen offset:2048 lds\n"
"v_mfma_f32_16x16x32_bf16 v[124:127],v[168:171],a[44:47],v[124:127]\n"
"buffer_load_dwordx4 v241,s[40:43],s67 offen offset:3072 lds\n"
"s_waitcnt vmcnt(8)\n"
"v_max3_f32 v247,v116,v117,v118\n"
"v_max3_f32 v247,v247,v119,v120\n"
"v_mfma_f32_16x16x32_bf16 v[128:131],v[184:187],a[44:47],v[128:131]\n"
"v_max3_f32 v247,v247,v121,v122\n"
"v_max_f32 v247,v247,v123\n"
"v_cmp_lt_f32 vcc,s64,v247\n"
"ds_read_b64_tr_b16 v[188:189],v224 offset:24576\n"
"ds_read_b64_tr_b16 v[190:191],v224 offset:28672\n"
"ds_read_b64_tr_b16 v[192:193],v225 offset:24576\n"
"ds_read_b64_tr_b16 v[194:195],v225 offset:28672\n"
"ds_read_b64_tr_b16 v[196:197],v226 offset:24576\n"
"ds_read_b64_tr_b16 v[198:199],v226 offset:28672\n"
"ds_read_b64_tr_b16 v[200:201],v227 offset:24576\n"
"ds_read_b64_tr_b16 v[202:203],v227 offset:28672\n"
"ds_read_b64_tr_b16 v[204:205],v228 offset:24576\n"
"ds_read_b64_tr_b16 v[206:207],v228 offset:28672\n"
"ds_read_b64_tr_b16 v[208:209],v229 offset:24576\n"
"ds_read_b64_tr_b16 v[210:211],v229 offset:28672\n"
"ds_read_b64_tr_b16 v[212:213],v230 offset:24576\n"
"ds_read_b64_tr_b16 v[214:215],v230 offset:28672\n"
"ds_read_b64_tr_b16 v[216:217],v231 offset:24576\n"
"s_waitcnt lgkmcnt(14)\n"
"ds_read_b64_tr_b16 v[218:219],v231 offset:28672\n"
"s_cmp_lg_u64 s[80:81],0\n"
"s_cbranch_scc1 Lk_101x\n"
"Lk_101:\n"
"s_waitcnt lgkmcnt(14)\n"
"v_mfma_f32_16x16x32_bf16 v[0:3],v[188:191],v[144:147],v[0:3]\n"
"v_exp_f32 v116,v116\n"
"s_waitcnt lgkmcnt(12)\n"
"v_mfma_f32_16x16x32_bf16 v[4:7],v[192:195],v[144:147],v[4:7]\n"
"v_exp_f32 v117,v117\n"
"v_exp_f32 v118,v118\n"
"s_waitcnt lgkmcnt(10)\n"
"v_mfma_f32_16x16x32_bf16 v[8:11],v[196:199],v[144:147],v[8:11]\n"
"v_exp_f32 v119,v119\n"
"s_waitcnt lgkmcnt(8)\n"
"v_mfma_f32_16x16x32_bf16 v[12:15],v[200:203],v[144:147],v[12:15]\n"
"v_cvt_pk_bf16_f32 v148,v116,v117\n"
"v_exp_f32 v120,v120\n"
"s_waitcnt lgkmcnt(6)\n"
"v_mfma_f32_16x16x32_bf16 v[16:19],v[204:207],v[144:147],v[16:19]\n"
"v_exp_f32 v121,v121\n"
"v_cvt_pk_bf16_f32 v149,v118,v119\n"
"s_waitcnt lgkmcnt(4)\n"
"v_mfma_f32_16x16x32_bf16 v[20:23],v[208:211],v[144:147],v[20:23]\n"
"v_exp_f32 v122,v122\n"
"s_waitcnt lgkmcnt(0)\n"
"v_mfma_f32_16x16x32_bf16 v[24:27],v[212:215],v[144:147],v[24:27]\n"
"v_exp_f32 v123,v123\n"
"v_cvt_pk_bf16_f32 v150,v120,v121\n"
"v_mfma_f32_16x16x32_bf16 v[28:31],v[216:219],v[144:147],v[28:31]\n"
"v_cvt_pk_bf16_f32 v151,v122,v123\n"
"v_max3_f32 v248,v124,v125,v126\n"
"v_max3_f32 v248,v248,v127,v128\n"
"v_mfma_f32_16x16x32_bf16 v[96:99],a[48:51],v[144:147],v[96:99]\n"
"v_max3_f32 v248,v248,v129,v130\n"
"v_max_f32 v248,v248,v131\n"
"v_cmp_lt_f32 s[82:83],s64,v248\n"
"s_cbranch_vccnz Lk_102x\n"
"Lk_102:\n"
"v_mfma_f32_16x16x32_bf16 v[32:35],v[188:191],v[148:151],v[32:35]\n"
"v_exp_f32 v124,v124\n"
"v_exp_f32 v125,v125\n"
"v_mfma_f32_16x16x32_bf16 v[36:39],v[192:195],v[148:151],v[36:39]\n"
"v_exp_f32 v126,v126\n"
"v_exp_f32 v127,v127\n"
"v_mfma_f32_16x16x32_bf16 v[40:43],v[196:199],v[148:151],v[40:43]\n"
"v_cvt_pk_bf16_f32 v152,v124,v125\n"
"v_exp_f32 v128,v128\n"
"v_mfma_f32_16x16x32_bf16 v[44:47],v[200:203],v[148:151],v[44:47]\n"
"v_exp_f32 v129,v129\n"
"v_cvt_pk_bf16_f32 v153,v126,v127\n"
"v_mfma_f32_16x16x32_bf16 v[48:51],v[204:207],v[148:151],v[48:51]\n"
"v_exp_f32 v130,v130\n"
"v_exp_f32 v131,v131\n"
"v_mfma_f32_16x16x32_bf16 v[52:55],v[208:211],v[148:151],v[52:55]\n"
"v_cvt_pk_bf16_f32 v154,v128,v129\n"
"v_cvt_pk_bf16_f32 v155,v130,v131\n"
"v_mfma_f32_16x16x32_bf16 v[56:59],v[212:215],v[148:151],v[56:59]\n"
"v_mfma_f32_16x16x32_bf16 v[60:63],v[216:219],v[148:151],v[60:63]\n"
"s_add_u32 s92,s92,0x8000\n"
"s_and_b32 s92,s92,0x7ffff\n"
"s_add_u32 m0,s66,0x2000\n"
"s_nop 0\n"
"buffer_load_dwordx4 v238,s[40:43],s92 offen lds\n"
"buffer_load_dwordx4 v239,s[40:43],s92 offen offset:1024 lds\n"
"buffer_load_dwordx4 v240,s[40:43],s92 offen offset:2048 lds\n"
"buffer_load_dwordx4 v241,s[40:43],s92 offen offset:3072 lds\n"
"s_add_u32 s67,s92,0x4000\n"
"s_add_u32 m0,s66,0x3000\n"
"s_nop 0\n"
"buffer_load_dwordx4 v238,s[40:43],s67 offen lds\n"
"buffer_load_dwordx4 v239,s[40:43],s67 offen offset:1024 lds\n"
"buffer_load_dwordx4 v240,s[40:43],s67 offen offset:2048 lds\n"
"buffer_load_dwordx4 v241,s[40:43],s67 offen offset:3072 lds\n"
"v_mfma_f32_16x16x32_bf16 v[100:103],a[48:51],v[148:151],v[100:103]\n"
"s_cmp_lg_u64 s[82:83],0\n"
"s_cbranch_scc1 Lk_103x\n"
"Lk_103:\n"
"v_mfma_f32_16x16x32_bf16 v[64:67],v[188:191],v[152:155],v[64:67]\n"
"v_mfma_f32_16x16x32_bf16 v[68:71],v[192:195],v[152:155],v[68:71]\n"
"s_add_u32 s92,s92,0x8000\n"
"s_and_b32 s92,s92,0x7ffff\n"
"s_add_u32 m0,s66,0x4000\n"
"s_nop 0\n"
"buffer_load_dwordx4 v238,s[40:43],s92 offen lds\n"
"v_mfma_f32_16x16x32_bf16 v[72:75],v[196:199],v[152:155],v[72:75]\n"
"buffer_load_dwordx4 v239,s[40:43],s92 offen offset:1024 lds\n"
"v_mfma_f32_16x16x32_bf16 v[76:79],v[200:203],v[152:155],v[76:79]\n"
"buffer_load_dwordx4 v240,s[40:43],s92 offen offset:2048 lds\n"
"v_mfma_f32_16x16x32_bf16 v[80:83],v[204:207],v[152:155],v[80:83]\n"
"buffer_load_dwordx4 v241,s[40:43],s92 offen offset:3072 lds\n"
"v_mfma_f32_16x16x32_bf16 v[84:87],v[208:211],v[152:155],v[84:87]\n"
"s_add_u32 s67,s92,0x4000\n"
"s_add_u32 m0,s66,0x5000\n"
"s_nop 0\n"
"buffer_load_dwordx4 v238,s[40:43],s67 offen lds\n"
"v_mfma_f32_16x16x32_bf16 v[88:91],v[212:215],v[152:155],v[88:91]\n"
"v_mfma_f32_16x16x32_bf16 v[92:95],v[216:219],v[152:155],v[92:95]\n"
"buffer_load_dwordx4 v239,s[40:43],s67 offen offset:1024 lds\n"
"buffer_load_dwordx4 v240,s[40:43],s67 offen offset:2048 lds\n"
"v_mfma_f32_16x16x32_bf16 v[104:107],a[48:51],v[152:155],v[104:107]\n"
"buffer_load_dwordx4 v241,s[40:43],s67 offen offset:3072 lds\n"
"s_lshl_b32 s60,s7,13\n"
"s_add_u32 s60,s60,0x20000\n"
"v_add_u32 v220,s60,v242\n"
"s_add_u32 s60,s66,0x4800\n"
"v_add_u32 v221,s60,v242\n"
"v_rcp_f32 v250,v96\n"
"s_nop 0\n"
"v_pk_mul_f32 v[0:1],v[0:1],v[250:251] op_sel_hi:[1,0]\n"
"v_pk_mul_f32 v[2:3],v[2:3],v[250:251] op_sel_hi:[1,0]\n"
"v_cvt_pk_bf16_f32 v108,v0,v1\n"
"v_cvt_pk_bf16_f32 v109,v2,v3\n"
"v_pk_mul_f32 v[4:5],v[4:5],v[250:251] op_sel_hi:[1,0]\n"
"v_pk_mul_f32 v[6:7],v[6:7],v[250:251] op_sel_hi:[1,0]\n"
"v_cvt_pk_bf16_f32 v110,v4,v5\n"
"v_cvt_pk_bf16_f32 v111,v6,v7\n"
"ds_write_b128 v220,v[108:111]\n"
"v_pk_mul_f32 v[8:9],v[8:9],v[250:251] op_sel_hi:[1,0]\n"
"v_pk_mul_f32 v[10:11],v[10:11],v[250:251] op_sel_hi:[1,0]\n"
"v_cvt_pk_bf16_f32 v112,v8,v9\n"
"v_cvt_pk_bf16_f32 v113,v10,v11\n"
"v_pk_mul_f32 v[12:13],v[12:13],v[250:251] op_sel_hi:[1,0]\n"
"v_pk_mul_f32 v[14:15],v[14:15],v[250:251] op_sel_hi:[1,0]\n"
"v_cvt_pk_bf16_f32 v114,v12,v13\n"
"v_cvt_pk_bf16_f32 v115,v14,v15\n"
"ds_write_b128 v220,v[112:115] offset:3072\n"
"v_pk_mul_f32 v[16:17],v[16:17],v[250:251] op_sel_hi:[1,0]\n"
"v_pk_mul_f32 v[18:19],v[18:19],v[250:251] op_sel_hi:[1,0]\n"
"v_cvt_pk_bf16_f32 v108,v16,v17\n"
"v_cvt_pk_bf16_f32 v109,v18,v19\n"
"v_pk_mul_f32 v[20:21],v[20:21],v[250:251] op_sel_hi:[1,0]\n"
"v_pk_mul_f32 v[22:23],v[22:23],v[250:251] op_sel_hi:[1,0]\n"
"v_cvt_pk_bf16_f32 v110,v20,v21\n"
"v_cvt_pk_bf16_f32 v111,v22,v23\n"
"ds_write_b128 v221,v[108:111] offset:6144\n"
"v_pk_mul_f32 v[24:25],v[24:25],v[250:251] op_sel_hi:[1,0]\n"
"v_pk_mul_f32 v[26:27],v[26:27],v[250:251] op_sel_hi:[1,0]\n"
"v_cvt_pk_bf16_f32 v112,v24,v25\n"
"v_cvt_pk_bf16_f32 v113,v26,v27\n"
"v_pk_mul_f32 v[28:29],v[28:29],v[250:251] op_sel_hi:[1,0]\n"
"v_pk_mul_f32 v[30:31],v[30:31],v[250:251] op_sel_hi:[1,0]\n"
"v_cvt_pk_bf16_f32 v114,v28,v29\n"
"v_cvt_pk_bf16_f32 v115,v30,v31\n"
"ds_write_b128 v221,v[112:115] offset:9216\n"
"v_rcp_f32 v250,v100\n"
"s_nop 0\n"
"v_pk_mul_f32 v[32:33],v[32:33],v[250:251] op_sel_hi:[1,0]\n"
"v_pk_mul_f32 v[34:35],v[34:35],v[250:251] op_sel_hi:[1,0]\n"
"v_cvt_pk_bf16_f32 v108,v32,v33\n"
"v_cvt_pk_bf16_f32 v109,v34,v35\n"
"v_pk_mul_f32 v[36:37],v[36:37],v[250:251] op_sel_hi:[1,0]\n"
"v_pk_mul_f32 v[38:39],v[38:39],v[250:251] op_sel_hi:[1,0]\n"
"v_cvt_pk_bf16_f32 v110,v36,v37\n"
"v_cvt_pk_bf16_f32 v111,v38,v39\n"
"ds_write_b128 v220,v[108:111] offset:1024\n"
"v_pk_mul_f32 v[40:41],v[40:41],v[250:251] op_sel_hi:[1,0]\n"
"v_pk_mul_f32 v[42:43],v[42:43],v[250:251] op_sel_hi:[1,0]\n"
"v_cvt_pk_bf16_f32 v112,v40,v41\n"
"v_cvt_pk_bf16_f32 v113,v42,v43\n"
"v_pk_mul_f32 v[44:45],v[44:45],v[250:251] op_sel_hi:[1,0]\n"
"v_pk_mul_f32 v[46:47],v[46:47],v[250:251] op_sel_hi:[1,0]\n"
"v_cvt_pk_bf16_f32 v114,v44,v45\n"
"v_cvt_pk_bf16_f32 v115,v46,v47\n"
"ds_write_b128 v220,v[112:115] offset:4096\n"
"v_pk_mul_f32 v[48:49],v[48:49],v[250:251] op_sel_hi:[1,0]\n"
"v_pk_mul_f32 v[50:51],v[50:51],v[250:251] op_sel_hi:[1,0]\n"
"v_cvt_pk_bf16_f32 v108,v48,v49\n"
"v_cvt_pk_bf16_f32 v109,v50,v51\n"
"v_pk_mul_f32 v[52:53],v[52:53],v[250:251] op_sel_hi:[1,0]\n"
"v_pk_mul_f32 v[54:55],v[54:55],v[250:251] op_sel_hi:[1,0]\n"
"v_cvt_pk_bf16_f32 v110,v52,v53\n"
"v_cvt_pk_bf16_f32 v111,v54,v55\n"
"ds_write_b128 v221,v[108:111] offset:7168\n"
"v_pk_mul_f32 v[56:57],v[56:57],v[250:251] op_sel_hi:[1,0]\n"
"v_pk_mul_f32 v[58:59],v[58:59],v[250:251] op_sel_hi:[1,0]\n"
"v_cvt_pk_bf16_f32 v112,v56,v57\n"
"v_cvt_pk_bf16_f32 v113,v58,v59\n"
"v_pk_mul_f32 v[60:61],v[60:61],v[250:251] op_sel_hi:[1,0]\n"
"v_pk_mul_f32 v[62:63],v[62:63],v[250:251] op_sel_hi:[1,0]\n"
"v_cvt_pk_bf16_f32 v114,v60,v61\n"
"v_cvt_pk_bf16_f32 v115,v62,v63\n"
"ds_write_b128 v221,v[112:115] offset:10240\n"
"v_rcp_f32 v250,v104\n"
"s_nop 0\n"
"v_pk_mul_f32 v[64:65],v[64:65],v[250:251] op_sel_hi:[1,0]\n"
"v_pk_mul_f32 v[66:67],v[66:67],v[250:251] op_sel_hi:[1,0]\n"
"v_cvt_pk_bf16_f32 v108,v64,v65\n"
"v_cvt_pk_bf16_f32 v109,v66,v67\n"
"v_pk_mul_f32 v[68:69],v[68:69],v[250:251] op_sel_hi:[1,0]\n"
"v_pk_mul_f32 v[70:71],v[70:71],v[250:251] op_sel_hi:[1,0]\n"
"v_cvt_pk_bf16_f32 v110,v68,v69\n"
"v_cvt_pk_bf16_f32 v111,v70,v71\n"
"ds_write_b128 v220,v[108:111] offset:2048\n"
"v_pk_mul_f32 v[72:73],v[72:73],v[250:251] op_sel_hi:[1,0]\n"
"v_pk_mul_f32 v[74:75],v[74:75],v[250:251] op_sel_hi:[1,0]\n"
"v_cvt_pk_bf16_f32 v112,v72,v73\n"
"v_cvt_pk_bf16_f32 v113,v74,v75\n"
"v_pk_mul_f32 v[76:77],v[76:77],v[250:251] op_sel_hi:[1,0]\n"
"v_pk_mul_f32 v[78:79],v[78:79],v[250:251] op_sel_hi:[1,0]\n"
"v_cvt_pk_bf16_f32 v114,v76,v77\n"
"v_cvt_pk_bf16_f32 v115,v78,v79\n"
"ds_write_b128 v220,v[112:115] offset:5120\n"
"v_pk_mul_f32 v[80:81],v[80:81],v[250:251] op_sel_hi:[1,0]\n"
"v_pk_mul_f32 v[82:83],v[82:83],v[250:251] op_sel_hi:[1,0]\n"
"v_cvt_pk_bf16_f32 v108,v80,v81\n"
"v_cvt_pk_bf16_f32 v109,v82,v83\n"
"v_pk_mul_f32 v[84:85],v[84:85],v[250:251] op_sel_hi:[1,0]\n"
"v_pk_mul_f32 v[86:87],v[86:87],v[250:251] op_sel_hi:[1,0]\n"
"v_cvt_pk_bf16_f32 v110,v84,v85\n"
"v_cvt_pk_bf16_f32 v111,v86,v87\n"
"ds_write_b128 v221,v[108:111] offset:8192\n"
"v_pk_mul_f32 v[88:89],v[88:89],v[250:251] op_sel_hi:[1,0]\n"
"v_pk_mul_f32 v[90:91],v[90:91],v[250:251] op_sel_hi:[1,0]\n"
"v_cvt_pk_bf16_f32 v112,v88,v89\n"
"v_cvt_pk_bf16_f32 v113,v90,v91\n"
"v_pk_mul_f32 v[92:93],v[92:93],v[250:251] op_sel_hi:[1,0]\n"
"v_pk_mul_f32 v[94:95],v[94:95],v[250:251] op_sel_hi:[1,0]\n"
"v_cvt_pk_bf16_f32 v114,v92,v93\n"
"v_cvt_pk_bf16_f32 v115,v94,v95\n"
"ds_write_b128 v221,v[112:115] offset:11264\n"
"v_add_u32 v120,s8,v242\n"
"v_add_u32 v121,s9,v242\n"
"v_add_u32 v122,s10,v242\n"
"v_add_u32 v123,s11,v242\n"
"v_add_u32 v124,s12,v242\n"
"v_add_u32 v125,s13,v242\n"
"v_add_u32 v126,s14,v242\n"
"v_add_u32 v127,s15,v242\n"
"v_add_u32 v128,s16,v242\n"
"v_add_u32 v129,s17,v242\n"
"v_add_u32 v130,s32,v242\n"
"v_add_u32 v131,s33,v242\n"
"v_add_u32 v132,s34,v242\n"
"v_add_u32 v133,s35,v242\n"
"v_add_u32 v134,s36,v242\n"
"v_add_u32 v135,s37,v242\n"
"s_waitcnt vmcnt(16)\n"
"ds_read_b64_tr_b16 a[128:129],v224\n"
"ds_read_b64_tr_b16 a[130:131],v224 offset:4096\n"
"ds_read_b64_tr_b16 a[132:133],v225\n"
"s_waitcnt lgkmcnt(6)\n"
"ds_read_b64_tr_b16 a[134:135],v225 offset:4096\n"
"ds_read_b64_tr_b16 a[136:137],v226\n"
"ds_read_b64_tr_b16 a[138:139],v226 offset:4096\n"
"ds_read_b64_tr_b16 a[140:141],v227\n"
"ds_read_b64_tr_b16 a[142:143],v227 offset:4096\n"
"ds_read_b64_tr_b16 a[144:145],v228\n"
"ds_read_b64_tr_b16 a[146:147],v228 offset:4096\n"
"ds_read_b64_tr_b16 a[148:149],v229\n"
"ds_read_b64_tr_b16 a[150:151],v229 offset:4096\n"
"s_waitcnt lgkmcnt(14)\n"
"ds_read_b64_tr_b16 a[152:153],v230\n"
"s_waitcnt lgkmcnt(14)\n"
"ds_read_b64_tr_b16 a[154:155],v230 offset:4096\n"
"s_waitcnt lgkmcnt(14)\n"
"ds_read_b64_tr_b16 a[156:157],v231\n"
"s_waitcnt lgkmcnt(14)\n"
"ds_read_b64_tr_b16 a[158:159],v231 offset:4096\n"
"s_waitcnt lgkmcnt(0)\n"
"s_barrier\n"
"ds_read_b128 a[0:3],v120\n"
"ds_read_b128 a[4:7],v120 offset:1024\n"
"ds_read_b128 a[8:11],v120 offset:2048\n"
"s_waitcnt lgkmcnt(2)\n"
"v_mfma_f32_16x16x32_bf16 v[0:3],a[128:131],a[0:3],0\n"
"s_waitcnt lgkmcnt(1)\n"
"v_mfma_f32_16x16x32_bf16 v[4:7],a[128:131],a[4:7],0\n"
"s_waitcnt lgkmcnt(0)\n"
"v_mfma_f32_16x16x32_bf16 v[8:11],a[128:131],a[8:11],0\n"
"v_mfma_f32_16x16x32_bf16 v[12:15],a[132:135],a[0:3],0\n"
"v_mfma_f32_16x16x32_bf16 v[16:19],a[132:135],a[4:7],0\n"
"s_add_u32 s92,s92,0x8000\n"
"s_and_b32 s92,s92,0x7ffff\n"
"s_add_u32 m0,s66,0\n"
"s_nop 0\n"
"buffer_load_dwordx4 v238,s[40:43],s92 offen lds\n"
"v_mfma_f32_16x16x32_bf16 v[20:23],a[132:135],a[8:11],0\n"
"buffer_load_dwordx4 v239,s[40:43],s92 offen offset:1024 lds\n"
"v_mfma_f32_16x16x32_bf16 v[24:27],a[136:139],a[0:3],0\n"
"buffer_load_dwordx4 v240,s[40:43],s92 offen offset:2048 lds\n"
"buffer_load_dwordx4 v241,s[40:43],s92 offen offset:3072 lds\n"
"v_mfma_f32_16x16x32_bf16 v[28:31],a[136:139],a[4:7],0\n"
"v_mfma_f32_16x16x32_bf16 v[32:35],a[136:139],a[8:11],0\n"
"s_add_u32 s67,s92,0x4000\n"
"s_add_u32 m0,s66,0x1000\n"
"s_nop 0\n"
"buffer_load_dwordx4 v238,s[40:43],s67 offen lds\n"
"v_mfma_f32_16x16x32_bf16 v[36:39],a[140:143],a[0:3],0\n"
"buffer_load_dwordx4 v239,s[40:43],s67 offen offset:1024 lds\n"
"buffer_load_dwordx4 v240,s[40:43],s67 offen offset:2048 lds\n"
"v_mfma_f32_16x16x32_bf16 v[40:43],a[140:143],a[4:7],0\n"
"buffer_load_dwordx4 v241,s[40:43],s67 offen offset:3072 lds\n"
"v_mfma_f32_16x16x32_bf16 v[44:47],a[140:143],a[8:11],0\n"
"ds_read_b128 a[12:15],v121\n"
"ds_read_b128 a[16:19],v121 offset:1024\n"
"v_mfma_f32_16x16x32_bf16 v[48:51],a[144:147],a[0:3],0\n"
"ds_read_b128 a[20:23],v121 offset:2048\n"
"v_mfma_f32_16x16x32_bf16 v[52:55],a[144:147],a[4:7],0\n"
"s_waitcnt vmcnt(16)\n"
"ds_read_b64_tr_b16 a[160:161],v224 offset:8192\n"
"v_mfma_f32_16x16x32_bf16 v[56:59],a[144:147],a[8:11],0\n"
"ds_read_b64_tr_b16 a[162:163],v224 offset:12288\n"
"ds_read_b64_tr_b16 a[164:165],v225 offset:8192\n"
"v_mfma_f32_16x16x32_bf16 v[60:63],a[148:151],a[0:3],0\n"
"ds_read_b64_tr_b16 a[166:167],v225 offset:12288\n"
"v_mfma_f32_16x16x32_bf16 v[64:67],a[148:151],a[4:7],0\n"
"ds_read_b64_tr_b16 a[168:169],v226 offset:8192\n"
"ds_read_b64_tr_b16 a[170:171],v226 offset:12288\n"
"v_mfma_f32_16x16x32_bf16 v[68:71],a[148:151],a[8:11],0\n"
"ds_read_b64_tr_b16 a[172:173],v227 offset:8192\n"
"v_mfma_f32_16x16x32_bf16 v[72:75],a[152:155],a[0:3],0\n"
"ds_read_b64_tr_b16 a[174:175],v227 offset:12288\n"
"ds_read_b64_tr_b16 a[176:177],v228 offset:8192\n"
"v_mfma_f32_16x16x32_bf16 v[76:79],a[152:155],a[4:7],0\n"
"ds_read_b64_tr_b16 a[178:179],v228 offset:12288\n"
"v_mfma_f32_16x16x32_bf16 v[80:83],a[152:155],a[8:11],0\n"
"ds_read_b64_tr_b16 a[180:181],v229 offset:8192\n"
"ds_read_b64_tr_b16 a[182:183],v229 offset:12288\n"
"v_mfma_f32_16x16x32_bf16 v[84:87],a[156:159],a[0:3],0\n"
"s_waitcnt lgkmcnt(13)\n"
"ds_read_b64_tr_b16 a[184:185],v230 offset:8192\n"
"v_mfma_f32_16x16x32_bf16 v[88:91],a[156:159],a[4:7],0\n"
"ds_read_b64_tr_b16 a[186:187],v230 offset:12288\n"
"s_waitcnt lgkmcnt(13)\n"
"ds_read_b64_tr_b16 a[188:189],v231 offset:8192\n"
"v_mfma_f32_16x16x32_bf16 v[92:95],a[156:159],a[8:11],0\n"
"ds_read_b64_tr_b16 a[190:191],v231 offset:12288\n"
"s_waitcnt lgkmcnt(13)\n"
"v_mfma_f32_16x16x32_bf16 v[0:3],a[160:163],a[12:15],v[0:3]\n"
"v_mfma_f32_16x16x32_bf16 v[4:7],a[160:163],a[16:19],v[4:7]\n"
"v_mfma_f32_16x16x32_bf16 v[8:11],a[160:163],a[20:23],v[8:11]\n"
"s_waitcnt lgkmcnt(10)\n"
"v_mfma_f32_16x16x32_bf16 v[12:15],a[164:167],a[12:15],v[12:15]\n"
"s_waitcnt lgkmcnt(0)\n"
"v_mfma_f32_16x16x32_bf16 v[16:19],a[164:167],a[16:19],v[16:19]\n"
"s_add_u32 s92,s92,0x8000\n"
"s_and_b32 s92,s92,0x7ffff\n"
"s_add_u32 m0,s66,0x2000\n"
"s_nop 0\n"
"buffer_load_dwordx4 v238,s[40:43],s92 offen lds\n"
"v_mfma_f32_16x16x32_bf16 v[20:23],a[164:167],a[20:23],v[20:23]\n"
"buffer_load_dwordx4 v239,s[40:43],s92 offen offset:1024 lds\n"
"v_mfma_f32_16x16x32_bf16 v[24:27],a[168:171],a[12:15],v[24:27]\n"
"buffer_load_dwordx4 v240,s[40:43],s92 offen offset:2048 lds\n"
"buffer_load_dwordx4 v241,s[40:43],s92 offen offset:3072 lds\n"
"v_mfma_f32_16x16x32_bf16 v[28:31],a[168:171],a[16:19],v[28:31]\n"
"v_mfma_f32_16x16x32_bf16 v[32:35],a[168:171],a[20:23],v[32:35]\n"
"s_add_u32 s67,s92,0x4000\n"
"s_add_u32 m0,s66,0x3000\n"
"s_nop 0\n"
"buffer_load_dwordx4 v238,s[40:43],s67 offen lds\n"
"v_mfma_f32_16x16x32_bf16 v[36:39],a[172:175],a[12:15],v[36:39]\n"
"buffer_load_dwordx4 v239,s[40:43],s67 offen offset:1024 lds\n"
"buffer_load_dwordx4 v240,s[40:43],s67 offen offset:2048 lds\n"
"v_mfma_f32_16x16x32_bf16 v[40:43],a[172:175],a[16:19],v[40:43]\n"
"buffer_load_dwordx4 v241,s[40:43],s67 offen offset:3072 lds\n"
"v_mfma_f32_16x16x32_bf16 v[44:47],a[172:175],a[20:23],v[44:47]\n"
"ds_read_b128 a[24:27],v122\n"
"ds_read_b128 a[28:31],v122 offset:1024\n"
"v_mfma_f32_16x16x32_bf16 v[48:51],a[176:179],a[12:15],v[48:51]\n"
"ds_read_b128 a[32:35],v122 offset:2048\n"
"v_mfma_f32_16x16x32_bf16 v[52:55],a[176:179],a[16:19],v[52:55]\n"
"s_waitcnt vmcnt(16)\n"
"ds_read_b64_tr_b16 a[128:129],v224 offset:16384\n"
"v_mfma_f32_16x16x32_bf16 v[56:59],a[176:179],a[20:23],v[56:59]\n"
"ds_read_b64_tr_b16 a[130:131],v224 offset:20480\n"
"ds_read_b64_tr_b16 a[132:133],v225 offset:16384\n"
"v_mfma_f32_16x16x32_bf16 v[60:63],a[180:183],a[12:15],v[60:63]\n"
"ds_read_b64_tr_b16 a[134:135],v225 offset:20480\n"
"v_mfma_f32_16x16x32_bf16 v[64:67],a[180:183],a[16:19],v[64:67]\n"
"ds_read_b64_tr_b16 a[136:137],v226 offset:16384\n"
"ds_read_b64_tr_b16 a[138:139],v226 offset:20480\n"
"v_mfma_f32_16x16x32_bf16 v[68:71],a[180:183],a[20:23],v[68:71]\n"
"ds_read_b64_tr_b16 a[140:141],v227 offset:16384\n"
"v_mfma_f32_16x16x32_bf16 v[72:75],a[184:187],a[12:15],v[72:75]\n"
"ds_read_b64_tr_b16 a[142:143],v227 offset:20480\n"
"ds_read_b64_tr_b16 a[144:145],v228 offset:16384\n"
"v_mfma_f32_16x16x32_bf16 v[76:79],a[184:187],a[16:19],v[76:79]\n"
"ds_read_b64_tr_b16 a[146:147],v228 offset:20480\n"
"v_mfma_f32_16x16x32_bf16 v[80:83],a[184:187],a[20:23],v[80:83]\n"
"ds_read_b64_tr_b16 a[148:149],v229 offset:16384\n"
"ds_read_b64_tr_b16 a[150:151],v229 offset:20480\n"
"v_mfma_f32_16x16x32_bf16 v[84:87],a[188:191],a[12:15],v[84:87]\n"
"s_waitcnt lgkmcnt(13)\n"
"ds_read_b64_tr_b16 a[152:153],v230 offset:16384\n"
"v_mfma_f32_16x16x32_bf16 v[88:91],a[188:191],a[16:19],v[88:91]\n"
"ds_read_b64_tr_b16 a[154:155],v230 offset:20480\n"
"s_waitcnt lgkmcnt(13)\n"
"ds_read_b64_tr_b16 a[156:157],v231 offset:16384\n"
"v_mfma_f32_16x16x32_bf16 v[92:95],a[188:191],a[20:23],v[92:95]\n"
"ds_read_b64_tr_b16 a[158:159],v231 offset:20480\n"
"s_waitcnt lgkmcnt(13)\n"
"v_mfma_f32_16x16x32_bf16 v[0:3],a[128:131],a[24:27],v[0:3]\n"
"v_mfma_f32_16x16x32_bf16 v[4:7],a[128:131],a[28:31],v[4:7]\n"
"v_mfma_f32_16x16x32_bf16 v[8:11],a[128:131],a[32:35],v[8:11]\n"
"s_waitcnt lgkmcnt(10)\n"
"v_mfma_f32_16x16x32_bf16 v[12:15],a[132:135],a[24:27],v[12:15]\n"
"s_waitcnt lgkmcnt(0)\n"
"v_mfma_f32_16x16x32_bf16 v[16:19],a[132:135],a[28:31],v[16:19]\n"
"s_add_u32 s92,s92,0x8000\n"
"s_and_b32 s92,s92,0x7ffff\n"
"s_add_u32 m0,s66,0x4000\n"
"s_nop 0\n"
"buffer_load_dwordx4 v238,s[40:43],s92 offen lds\n"
"v_mfma_f32_16x16x32_bf16 v[20:23],a[132:135],a[32:35],v[20:23]\n"
"buffer_load_dwordx4 v239,s[40:43],s92 offen offset:1024 lds\n"
"v_mfma_f32_16x16x32_bf16 v[24:27],a[136:139],a[24:27],v[24:27]\n"
"buffer_load_dwordx4 v240,s[40:43],s92 offen offset:2048 lds\n"
"buffer_load_dwordx4 v241,s[40:43],s92 offen offset:3072 lds\n"
"v_mfma_f32_16x16x32_bf16 v[28:31],a[136:139],a[28:31],v[28:31]\n"
"v_mfma_f32_16x16x32_bf16 v[32:35],a[136:139],a[32:35],v[32:35]\n"
"s_add_u32 s67,s92,0x4000\n"
"s_add_u32 m0,s66,0x5000\n"
"s_nop 0\n"
"buffer_load_dwordx4 v238,s[40:43],s67 offen lds\n"
"v_mfma_f32_16x16x32_bf16 v[36:39],a[140:143],a[24:27],v[36:39]\n"
"buffer_load_dwordx4 v239,s[40:43],s67 offen offset:1024 lds\n"
"buffer_load_dwordx4 v240,s[40:43],s67 offen offset:2048 lds\n"
"v_mfma_f32_16x16x32_bf16 v[40:43],a[140:143],a[28:31],v[40:43]\n"
"buffer_load_dwordx4 v241,s[40:43],s67 offen offset:3072 lds\n"
"v_mfma_f32_16x16x32_bf16 v[44:47],a[140:143],a[32:35],v[44:47]\n"
"ds_read_b128 a[36:39],v123\n"
"ds_read_b128 a[40:43],v123 offset:1024\n"
"v_mfma_f32_16x16x32_bf16 v[48:51],a[144:147],a[24:27],v[48:51]\n"
"ds_read_b128 a[44:47],v123 offset:2048\n"
"v_mfma_f32_16x16x32_bf16 v[52:55],a[144:147],a[28:31],v[52:55]\n"
"s_waitcnt vmcnt(16)\n"
"ds_read_b64_tr_b16 a[160:161],v224\n"
"v_mfma_f32_16x16x32_bf16 v[56:59],a[144:147],a[32:35],v[56:59]\n"
"ds_read_b64_tr_b16 a[162:163],v224 offset:4096\n"
"ds_read_b64_tr_b16 a[164:165],v225\n"
"v_mfma_f32_16x16x32_bf16 v[60:63],a[148:151],a[24:27],v[60:63]\n"
"ds_read_b64_tr_b16 a[166:167],v225 offset:4096\n"
"v_mfma_f32_16x16x32_bf16 v[64:67],a[148:151],a[28:31],v[64:67]\n"
"ds_read_b64_tr_b16 a[168:169],v226\n"
"ds_read_b64_tr_b16 a[170:171],v226 offset:4096\n"
"v_mfma_f32_16x16x32_bf16 v[68:71],a[148:151],a[32:35],v[68:71]\n"
"ds_read_b64_tr_b16 a[172:173],v227\n"
"v_mfma_f32_16x16x32_bf16 v[72:75],a[152:155],a[24:27],v[72:75]\n"
"ds_read_b64_tr_b16 a[174:175],v227 offset:4096\n"
"ds_read_b64_tr_b16 a[176:177],v228\n"
"v_mfma_f32_16x16x32_bf16 v[76:79],a[152:155],a[28:31],v[76:79]\n"
"ds_read_b64_tr_b16 a[178:179],v228 offset:4096\n"
"v_mfma_f32_16x16x32_bf16 v[80:83],a[152:155],a[32:35],v[80:83]\n"
"ds_read_b64_tr_b16 a[180:181],v229\n"
"ds_read_b64_tr_b16 a[182:183],v229 offset:4096\n"
"v_mfma_f32_16x16x32_bf16 v[84:87],a[156:159],a[24:27],v[84:87]\n"
"s_waitcnt lgkmcnt(13)\n"
"ds_read_b64_tr_b16 a[184:185],v230\n"
"v_mfma_f32_16x16x32_bf16 v[88:91],a[156:159],a[28:31],v[88:91]\n"
"ds_read_b64_tr_b16 a[186:187],v230 offset:4096\n"
"s_waitcnt lgkmcnt(13)\n"
"ds_read_b64_tr_b16 a[188:189],v231\n"
"v_mfma_f32_16x16x32_bf16 v[92:95],a[156:159],a[32:35],v[92:95]\n"
"ds_read_b64_tr_b16 a[190:191],v231 offset:4096\n"
"s_waitcnt lgkmcnt(13)\n"
"v_mfma_f32_16x16x32_bf16 v[0:3],a[160:163],a[36:39],v[0:3]\n"
"v_mfma_f32_16x16x32_bf16 v[4:7],a[160:163],a[40:43],v[4:7]\n"
"v_mfma_f32_16x16x32_bf16 v[8:11],a[160:163],a[44:47],v[8:11]\n"
"s_waitcnt lgkmcnt(10)\n"
"v_mfma_f32_16x16x32_bf16 v[12:15],a[164:167],a[36:39],v[12:15]\n"
"s_waitcnt lgkmcnt(0)\n"
"v_mfma_f32_16x16x32_bf16 v[16:19],a[164:167],a[40:43],v[16:19]\n"
"s_add_u32 s92,s92,0x8000\n"
"s_and_b32 s92,s92,0x7ffff\n"
"s_add_u32 m0,s66,0\n"
"s_nop 0\n"
"buffer_load_dwordx4 v238,s[40:43],s92 offen lds\n"
"v_mfma_f32_16x16x32_bf16 v[20:23],a[164:167],a[44:47],v[20:23]\n"
"buffer_load_dwordx4 v239,s[40:43],s92 offen offset:1024 lds\n"
"v_mfma_f32_16x16x32_bf16 v[24:27],a[168:171],a[36:39],v[24:27]\n"
"buffer_load_dwordx4 v240,s[40:43],s92 offen offset:2048 lds\n"
"buffer_load_dwordx4 v241,s[40:43],s92 offen offset:3072 lds\n"
"v_mfma_f32_16x16x32_bf16 v[28:31],a[168:171],a[40:43],v[28:31]\n"
"v_mfma_f32_16x16x32_bf16 v[32:35],a[168:171],a[44:47],v[32:35]\n"
"s_add_u32 s67,s92,0x4000\n"
"s_add_u32 m0,s66,0x1000\n"
"s_nop 0\n"
"buffer_load_dwordx4 v238,s[40:43],s67 offen lds\n"
"v_mfma_f32_16x16x32_bf16 v[36:39],a[172:175],a[36:39],v[36:39]\n"
"buffer_load_dwordx4 v239,s[40:43],s67 offen offset:1024 lds\n"
"buffer_load_dwordx4 v240,s[40:43],s67 offen offset:2048 lds\n"
"v_mfma_f32_16x16x32_bf16 v[40:43],a[172:175],a[40:43],v[40:43]\n"
"buffer_load_dwordx4 v241,s[40:43],s67 offen offset:3072 lds\n"
"v_mfma_f32_16x16x32_bf16 v[44:47],a[172:175],a[44:47],v[44:47]\n"
"ds_read_b128 a[0:3],v124\n"
"ds_read_b128 a[4:7],v124 offset:1024\n"
"v_mfma_f32_16x16x32_bf16 v[48:51],a[176:179],a[36:39],v[48:51]\n"
"ds_read_b128 a[8:11],v124 offset:2048\n"
"v_mfma_f32_16x16x32_bf16 v[52:55],a[176:179],a[40:43],v[52:55]\n"
"s_waitcnt vmcnt(16)\n"
"ds_read_b64_tr_b16 a[128:129],v224 offset:8192\n"
"v_mfma_f32_16x16x32_bf16 v[56:59],a[176:179],a[44:47],v[56:59]\n"
"ds_read_b64_tr_b16 a[130:131],v224 offset:12288\n"
"ds_read_b64_tr_b16 a[132:133],v225 offset:8192\n"
"v_mfma_f32_16x16x32_bf16 v[60:63],a[180:183],a[36:39],v[60:63]\n"
"ds_read_b64_tr_b16 a[134:135],v225 offset:12288\n"
"v_mfma_f32_16x16x32_bf16 v[64:67],a[180:183],a[40:43],v[64:67]\n"
"ds_read_b64_tr_b16 a[136:137],v226 offset:8192\n"
"ds_read_b64_tr_b16 a[138:139],v226 offset:12288\n"
"v_mfma_f32_16x16x32_bf16 v[68:71],a[180:183],a[44:47],v[68:71]\n"
"ds_read_b64_tr_b16 a[140:141],v227 offset:8192\n"
"v_mfma_f32_16x16x32_bf16 v[72:75],a[184:187],a[36:39],v[72:75]\n"
"ds_read_b64_tr_b16 a[142:143],v227 offset:12288\n"
"ds_read_b64_tr_b16 a[144:145],v228 offset:8192\n"
"v_mfma_f32_16x16x32_bf16 v[76:79],a[184:187],a[40:43],v[76:79]\n"
"ds_read_b64_tr_b16 a[146:147],v228 offset:12288\n"
"v_mfma_f32_16x16x32_bf16 v[80:83],a[184:187],a[44:47],v[80:83]\n"
"ds_read_b64_tr_b16 a[148:149],v229 offset:8192\n"
"ds_read_b64_tr_b16 a[150:151],v229 offset:12288\n"
"v_mfma_f32_16x16x32_bf16 v[84:87],a[188:191],a[36:39],v[84:87]\n"
"s_waitcnt lgkmcnt(13)\n"
"ds_read_b64_tr_b16 a[152:153],v230 offset:8192\n"
"v_mfma_f32_16x16x32_bf16 v[88:91],a[188:191],a[40:43],v[88:91]\n"
"ds_read_b64_tr_b16 a[154:155],v230 offset:12288\n"
"s_waitcnt lgkmcnt(13)\n"
"ds_read_b64_tr_b16 a[156:157],v231 offset:8192\n"
"v_mfma_f32_16x16x32_bf16 v[92:95],a[188:191],a[44:47],v[92:95]\n"
"ds_read_b64_tr_b16 a[158:159],v231 offset:12288\n"
"s_waitcnt lgkmcnt(13)\n"
"v_mfma_f32_16x16x32_bf16 v[0:3],a[128:131],a[0:3],v[0:3]\n"
"v_mfma_f32_16x16x32_bf16 v[4:7],a[128:131],a[4:7],v[4:7]\n"
"v_mfma_f32_16x16x32_bf16 v[8:11],a[128:131],a[8:11],v[8:11]\n"
"s_waitcnt lgkmcnt(10)\n"
"v_mfma_f32_16x16x32_bf16 v[12:15],a[132:135],a[0:3],v[12:15]\n"
"s_waitcnt lgkmcnt(0)\n"
"v_mfma_f32_16x16x32_bf16 v[16:19],a[132:135],a[4:7],v[16:19]\n"
"s_add_u32 s92,s92,0x8000\n"
"s_and_b32 s92,s92,0x7ffff\n"
"s_add_u32 m0,s66,0x2000\n"
"s_nop 0\n"
"buffer_load_dwordx4 v238,s[40:43],s92 offen lds\n"
"v_mfma_f32_16x16x32_bf16 v[20:23],a[132:135],a[8:11],v[20:23]\n"
"buffer_load_dwordx4 v239,s[40:43],s92 offen offset:1024 lds\n"
"v_mfma_f32_16x16x32_bf16 v[24:27],a[136:139],a[0:3],v[24:27]\n"
"buffer_load_dwordx4 v240,s[40:43],s92 offen offset:2048 lds\n"
"buffer_load_dwordx4 v241,s[40:43],s92 offen offset:3072 lds\n"
"v_mfma_f32_16x16x32_bf16 v[28:31],a[136:139],a[4:7],v[28:31]\n"
"v_mfma_f32_16x16x32_bf16 v[32:35],a[136:139],a[8:11],v[32:35]\n"
"s_add_u32 s67,s92,0x4000\n"
"s_add_u32 m0,s66,0x3000\n"
"s_nop 0\n"
"buffer_load_dwordx4 v238,s[40:43],s67 offen lds\n"
"v_mfma_f32_16x16x32_bf16 v[36:39],a[140:143],a[0:3],v[36:39]\n"
"buffer_load_dwordx4 v239,s[40:43],s67 offen offset:1024 lds\n"
"buffer_load_dwordx4 v240,s[40:43],s67 offen offset:2048 lds\n"
"v_mfma_f32_16x16x32_bf16 v[40:43],a[140:143],a[4:7],v[40:43]\n"
"buffer_load_dwordx4 v241,s[40:43],s67 offen offset:3072 lds\n"
"v_mfma_f32_16x16x32_bf16 v[44:47],a[140:143],a[8:11],v[44:47]\n"
"ds_read_b128 a[12:15],v125\n"
"ds_read_b128 a[16:19],v125 offset:1024\n"
"v_mfma_f32_16x16x32_bf16 v[48:51],a[144:147],a[0:3],v[48:51]\n"
"ds_read_b128 a[20:23],v125 offset:2048\n"
"v_mfma_f32_16x16x32_bf16 v[52:55],a[144:147],a[4:7],v[52:55]\n"
"s_waitcnt vmcnt(16)\n"
"ds_read_b64_tr_b16 a[160:161],v224 offset:16384\n"
"v_mfma_f32_16x16x32_bf16 v[56:59],a[144:147],a[8:11],v[56:59]\n"
"ds_read_b64_tr_b16 a[162:163],v224 offset:20480\n"
"ds_read_b64_tr_b16 a[164:165],v225 offset:16384\n"
"v_mfma_f32_16x16x32_bf16 v[60:63],a[148:151],a[0:3],v[60:63]\n"
"ds_read_b64_tr_b16 a[166:167],v225 offset:20480\n"
"v_mfma_f32_16x16x32_bf16 v[64:67],a[148:151],a[4:7],v[64:67]\n"
"ds_read_b64_tr_b16 a[168:169],v226 offset:16384\n"
"ds_read_b64_tr_b16 a[170:171],v226 offset:20480\n"
"v_mfma_f32_16x16x32_bf16 v[68:71],a[148:151],a[8:11],v[68:71]\n"
"ds_read_b64_tr_b16 a[172:173],v227 offset:16384\n"
"v_mfma_f32_16x16x32_bf16 v[72:75],a[152:155],a[0:3],v[72:75]\n"
"ds_read_b64_tr_b16 a[174:175],v227 offset:20480\n"
"ds_read_b64_tr_b16 a[176:177],v228 offset:16384\n"
"v_mfma_f32_16x16x32_bf16 v[76:79],a[152:155],a[4:7],v[76:79]\n"
"ds_read_b64_tr_b16 a[178:179],v228 offset:20480\n"
"v_mfma_f32_16x16x32_bf16 v[80:83],a[152:155],a[8:11],v[80:83]\n"
"ds_read_b64_tr_b16 a[180:181],v229 offset:16384\n"
"ds_read_b64_tr_b16 a[182:183],v229 offset:20480\n"
"v_mfma_f32_16x16x32_bf16 v[84:87],a[156:159],a[0:3],v[84:87]\n"
"s_waitcnt lgkmcnt(13)\n"
"ds_read_b64_tr_b16 a[184:185],v230 offset:16384\n"
"v_mfma_f32_16x16x32_bf16 v[88:91],a[156:159],a[4:7],v[88:91]\n"
"ds_read_b64_tr_b16 a[186:187],v230 offset:20480\n"
"s_waitcnt lgkmcnt(13)\n"
"ds_read_b64_tr_b16 a[188:189],v231 offset:16384\n"
"v_mfma_f32_16x16x32_bf16 v[92:95],a[156:159],a[8:11],v[92:95]\n"
"ds_read_b64_tr_b16 a[190:191],v231 offset:20480\n"
"s_waitcnt lgkmcnt(13)\n"
"v_mfma_f32_16x16x32_bf16 v[0:3],a[160:163],a[12:15],v[0:3]\n"
"v_mfma_f32_16x16x32_bf16 v[4:7],a[160:163],a[16:19],v[4:7]\n"
"v_mfma_f32_16x16x32_bf16 v[8:11],a[160:163],a[20:23],v[8:11]\n"
"s_waitcnt lgkmcnt(10)\n"
"v_mfma_f32_16x16x32_bf16 v[12:15],a[164:167],a[12:15],v[12:15]\n"
"s_waitcnt lgkmcnt(0)\n"
"v_mfma_f32_16x16x32_bf16 v[16:19],a[164:167],a[16:19],v[16:19]\n"
"s_add_u32 s92,s92,0x8000\n"
"s_and_b32 s92,s92,0x7ffff\n"
"s_add_u32 m0,s66,0x4000\n"
"s_nop 0\n"
"buffer_load_dwordx4 v238,s[40:43],s92 offen lds\n"
"v_mfma_f32_16x16x32_bf16 v[20:23],a[164:167],a[20:23],v[20:23]\n"
"buffer_load_dwordx4 v239,s[40:43],s92 offen offset:1024 lds\n"
"v_mfma_f32_16x16x32_bf16 v[24:27],a[168:171],a[12:15],v[24:27]\n"
"buffer_load_dwordx4 v240,s[40:43],s92 offen offset:2048 lds\n"
"buffer_load_dwordx4 v241,s[40:43],s92 offen offset:3072 lds\n"
"v_mfma_f32_16x16x32_bf16 v[28:31],a[168:171],a[16:19],v[28:31]\n"
"v_mfma_f32_16x16x32_bf16 v[32:35],a[168:171],a[20:23],v[32:35]\n"
"s_add_u32 s67,s92,0x4000\n"
"s_add_u32 m0,s66,0x5000\n"
"s_nop 0\n"
"buffer_load_dwordx4 v238,s[40:43],s67 offen lds\n"
"v_mfma_f32_16x16x32_bf16 v[36:39],a[172:175],a[12:15],v[36:39]\n"
"buffer_load_dwordx4 v239,s[40:43],s67 offen offset:1024 lds\n"
"buffer_load_dwordx4 v240,s[40:43],s67 offen offset:2048 lds\n"
"v_mfma_f32_16x16x32_bf16 v[40:43],a[172:175],a[16:19],v[40:43]\n"
"buffer_load_dwordx4 v241,s[40:43],s67 offen offset:3072 lds\n"
"v_mfma_f32_16x16x32_bf16 v[44:47],a[172:175],a[20:23],v[44:47]\n"
"ds_read_b128 a[24:27],v126\n"
"ds_read_b128 a[28:31],v126 offset:1024\n"
"v_mfma_f32_16x16x32_bf16 v[48:51],a[176:179],a[12:15],v[48:51]\n"
"ds_read_b128 a[32:35],v126 offset:2048\n"
"v_mfma_f32_16x16x32_bf16 v[52:55],a[176:179],a[16:19],v[52:55]\n"
"s_waitcnt vmcnt(16)\n"
"ds_read_b64_tr_b16 a[128:129],v224\n"
"v_mfma_f32_16x16x32_bf16 v[56:59],a[176:179],a[20:23],v[56:59]\n"
"ds_read_b64_tr_b16 a[130:131],v224 offset:4096\n"
"ds_read_b64_tr_b16 a[132:133],v225\n"
"v_mfma_f32_16x16x32_bf16 v[60:63],a[180:183],a[12:15],v[60:63]\n"
"ds_read_b64_tr_b16 a[134:135],v225 offset:4096\n"
"v_mfma_f32_16x16x32_bf16 v[64:67],a[180:183],a[16:19],v[64:67]\n"
"ds_read_b64_tr_b16 a[136:137],v226\n"
"ds_read_b64_tr_b16 a[138:139],v226 offset:4096\n"
"v_mfma_f32_16x16x32_bf16 v[68:71],a[180:183],a[20:23],v[68:71]\n"
"ds_read_b64_tr_b16 a[140:141],v227\n"
"v_mfma_f32_16x16x32_bf16 v[72:75],a[184:187],a[12:15],v[72:75]\n"
"ds_read_b64_tr_b16 a[142:143],v227 offset:4096\n"
"ds_read_b64_tr_b16 a[144:145],v228\n"
"v_mfma_f32_16x16x32_bf16 v[76:79],a[184:187],a[16:19],v[76:79]\n"
"ds_read_b64_tr_b16 a[146:147],v228 offset:4096\n"
"v_mfma_f32_16x16x32_bf16 v[80:83],a[184:187],a[20:23],v[80:83]\n"
"ds_read_b64_tr_b16 a[148:149],v229\n"
"ds_read_b64_tr_b16 a[150:151],v229 offset:4096\n"
"v_mfma_f32_16x16x32_bf16 v[84:87],a[188:191],a[12:15],v[84:87]\n"
"s_waitcnt lgkmcnt(13)\n"
"ds_read_b64_tr_b16 a[152:153],v230\n"
"v_mfma_f32_16x16x32_bf16 v[88:91],a[188:191],a[16:19],v[88:91]\n"
"ds_read_b64_tr_b16 a[154:155],v230 offset:4096\n"
"s_waitcnt lgkmcnt(13)\n"
"ds_read_b64_tr_b16 a[156:157],v231\n"
"v_mfma_f32_16x16x32_bf16 v[92:95],a[188:191],a[20:23],v[92:95]\n"
"ds_read_b64_tr_b16 a[158:159],v231 offset:4096\n"
"s_waitcnt lgkmcnt(13)\n"
"v_mfma_f32_16x16x32_bf16 v[0:3],a[128:131],a[24:27],v[0:3]\n"
"v_mfma_f32_16x16x32_bf16 v[4:7],a[128:131],a[28:31],v[4:7]\n"
"v_mfma_f32_16x16x32_bf16 v[8:11],a[128:131],a[32:35],v[8:11]\n"
"s_waitcnt lgkmcnt(10)\n"
"v_mfma_f32_16x16x32_bf16 v[12:15],a[132:135],a[24:27],v[12:15]\n"
"s_waitcnt lgkmcnt(0)\n"
"v_mfma_f32_16x16x32_bf16 v[16:19],a[132:135],a[28:31],v[16:19]\n"
"s_add_u32 s92,s92,0x8000\n"
"s_and_b32 s92,s92,0x7ffff\n"
"s_add_u32 m0,s66,0\n"
"s_nop 0\n"
"buffer_load_dwordx4 v238,s[40:43],s92 offen lds\n"
"v_mfma_f32_16x16x32_bf16 v[20:23],a[132:135],a[32:35],v[20:23]\n"
"buffer_load_dwordx4 v239,s[40:43],s92 offen offset:1024 lds\n"
"v_mfma_f32_16x16x32_bf16 v[24:27],a[136:139],a[24:27],v[24:27]\n"
"buffer_load_dwordx4 v240,s[40:43],s92 offen offset:2048 lds\n"
"buffer_load_dwordx4 v241,s[40:43],s92 offen offset:3072 lds\n"
"v_mfma_f32_16x16x32_bf16 v[28:31],a[136:139],a[28:31],v[28:31]\n"
"v_mfma_f32_16x16x32_bf16 v[32:35],a[136:139],a[32:35],v[32:35]\n"
"s_add_u32 s67,s92,0x4000\n"
"s_add_u32 m0,s66,0x1000\n"
"s_nop 0\n"
"buffer_load_dwordx4 v238,s[40:43],s67 offen lds\n"
"v_mfma_f32_16x16x32_bf16 v[36:39],a[140:143],a[24:27],v[36:39]\n"
"buffer_load_dwordx4 v239,s[40:43],s67 offen offset:1024 lds\n"
"buffer_load_dwordx4 v240,s[40:43],s67 offen offset:2048 lds\n"
"v_mfma_f32_16x16x32_bf16 v[40:43],a[140:143],a[28:31],v[40:43]\n"
"buffer_load_dwordx4 v241,s[40:43],s67 offen offset:3072 lds\n"
"v_mfma_f32_16x16x32_bf16 v[44:47],a[140:143],a[32:35],v[44:47]\n"
"ds_read_b128 a[36:39],v127\n"
"ds_read_b128 a[40:43],v127 offset:1024\n"
"v_mfma_f32_16x16x32_bf16 v[48:51],a[144:147],a[24:27],v[48:51]\n"
"ds_read_b128 a[44:47],v127 offset:2048\n"
"v_mfma_f32_16x16x32_bf16 v[52:55],a[144:147],a[28:31],v[52:55]\n"
"s_waitcnt vmcnt(16)\n"
"ds_read_b64_tr_b16 a[160:161],v224 offset:8192\n"
"v_mfma_f32_16x16x32_bf16 v[56:59],a[144:147],a[32:35],v[56:59]\n"
"ds_read_b64_tr_b16 a[162:163],v224 offset:12288\n"
"ds_read_b64_tr_b16 a[164:165],v225 offset:8192\n"
"v_mfma_f32_16x16x32_bf16 v[60:63],a[148:151],a[24:27],v[60:63]\n"
"ds_read_b64_tr_b16 a[166:167],v225 offset:12288\n"
"v_mfma_f32_16x16x32_bf16 v[64:67],a[148:151],a[28:31],v[64:67]\n"
"ds_read_b64_tr_b16 a[168:169],v226 offset:8192\n"
"ds_read_b64_tr_b16 a[170:171],v226 offset:12288\n"
"v_mfma_f32_16x16x32_bf16 v[68:71],a[148:151],a[32:35],v[68:71]\n"
"ds_read_b64_tr_b16 a[172:173],v227 offset:8192\n"
"v_mfma_f32_16x16x32_bf16 v[72:75],a[152:155],a[24:27],v[72:75]\n"
"ds_read_b64_tr_b16 a[174:175],v227 offset:12288\n"
"ds_read_b64_tr_b16 a[176:177],v228 offset:8192\n"
"v_mfma_f32_16x16x32_bf16 v[76:79],a[152:155],a[28:31],v[76:79]\n"
"ds_read_b64_tr_b16 a[178:179],v228 offset:12288\n"
"v_mfma_f32_16x16x32_bf16 v[80:83],a[152:155],a[32:35],v[80:83]\n"
"ds_read_b64_tr_b16 a[180:181],v229 offset:8192\n"
"ds_read_b64_tr_b16 a[182:183],v229 offset:12288\n"
"v_mfma_f32_16x16x32_bf16 v[84:87],a[156:159],a[24:27],v[84:87]\n"
"s_waitcnt lgkmcnt(13)\n"
"ds_read_b64_tr_b16 a[184:185],v230 offset:8192\n"
"v_mfma_f32_16x16x32_bf16 v[88:91],a[156:159],a[28:31],v[88:91]\n"
"ds_read_b64_tr_b16 a[186:187],v230 offset:12288\n"
"s_waitcnt lgkmcnt(13)\n"
"ds_read_b64_tr_b16 a[188:189],v231 offset:8192\n"
"v_mfma_f32_16x16x32_bf16 v[92:95],a[156:159],a[32:35],v[92:95]\n"
"ds_read_b64_tr_b16 a[190:191],v231 offset:12288\n"
"s_waitcnt lgkmcnt(13)\n"
"v_mfma_f32_16x16x32_bf16 v[0:3],a[160:163],a[36:39],v[0:3]\n"
"v_mfma_f32_16x16x32_bf16 v[4:7],a[160:163],a[40:43],v[4:7]\n"
"v_mfma_f32_16x16x32_bf16 v[8:11],a[160:163],a[44:47],v[8:11]\n"
"s_waitcnt lgkmcnt(10)\n"
"v_mfma_f32_16x16x32_bf16 v[12:15],a[164:167],a[36:39],v[12:15]\n"
"s_waitcnt lgkmcnt(0)\n"
"v_mfma_f32_16x16x32_bf16 v[16:19],a[164:167],a[40:43],v[16:19]\n"
"s_add_u32 s92,s92,0x8000\n"
"s_and_b32 s92,s92,0x7ffff\n"
"s_add_u32 m0,s66,0x2000\n"
"s_nop 0\n"
"buffer_load_dwordx4 v238,s[40:43],s92 offen lds\n"
"v_mfma_f32_16x16x32_bf16 v[20:23],a[164:167],a[44:47],v[20:23]\n"
"buffer_load_dwordx4 v239,s[40:43],s92 offen offset:1024 lds\n"
"v_mfma_f32_16x16x32_bf16 v[24:27],a[168:171],a[36:39],v[24:27]\n"
"buffer_load_dwordx4 v240,s[40:43],s92 offen offset:2048 lds\n"
"buffer_load_dwordx4 v241,s[40:43],s92 offen offset:3072 lds\n"
"v_mfma_f32_16x16x32_bf16 v[28:31],a[168:171],a[40:43],v[28:31]\n"
"v_mfma_f32_16x16x32_bf16 v[32:35],a[168:171],a[44:47],v[32:35]\n"
"s_add_u32 s67,s92,0x4000\n"
"s_add_u32 m0,s66,0x3000\n"
"s_nop 0\n"
"buffer_load_dwordx4 v238,s[40:43],s67 offen lds\n"
"v_mfma_f32_16x16x32_bf16 v[36:39],a[172:175],a[36:39],v[36:39]\n"
"buffer_load_dwordx4 v239,s[40:43],s67 offen offset:1024 lds\n"
"buffer_load_dwordx4 v240,s[40:43],s67 offen offset:2048 lds\n"
"v_mfma_f32_16x16x32_bf16 v[40:43],a[172:175],a[40:43],v[40:43]\n"
"buffer_load_dwordx4 v241,s[40:43],s67 offen offset:3072 lds\n"
"v_mfma_f32_16x16x32_bf16 v[44:47],a[172:175],a[44:47],v[44:47]\n"
"ds_read_b128 a[0:3],v128\n"
"ds_read_b128 a[4:7],v128 offset:1024\n"
"v_mfma_f32_16x16x32_bf16 v[48:51],a[176:179],a[36:39],v[48:51]\n"
"ds_read_b128 a[8:11],v128 offset:2048\n"
"v_mfma_f32_16x16x32_bf16 v[52:55],a[176:179],a[40:43],v[52:55]\n"
"s_waitcnt vmcnt(16)\n"
"ds_read_b64_tr_b16 a[128:129],v224 offset:16384\n"
"v_mfma_f32_16x16x32_bf16 v[56:59],a[176:179],a[44:47],v[56:59]\n"
"ds_read_b64_tr_b16 a[130:131],v224 offset:20480\n"
"ds_read_b64_tr_b16 a[132:133],v225 offset:16384\n"
"v_mfma_f32_16x16x32_bf16 v[60:63],a[180:183],a[36:39],v[60:63]\n"
"ds_read_b64_tr_b16 a[134:135],v225 offset:20480\n"
"v_mfma_f32_16x16x32_bf16 v[64:67],a[180:183],a[40:43],v[64:67]\n"
"ds_read_b64_tr_b16 a[136:137],v226 offset:16384\n"
"ds_read_b64_tr_b16 a[138:139],v226 offset:20480\n"
"v_mfma_f32_16x16x32_bf16 v[68:71],a[180:183],a[44:47],v[68:71]\n"
"ds_read_b64_tr_b16 a[140:141],v227 offset:16384\n"
"v_mfma_f32_16x16x32_bf16 v[72:75],a[184:187],a[36:39],v[72:75]\n"
"ds_read_b64_tr_b16 a[142:143],v227 offset:20480\n"
"ds_read_b64_tr_b16 a[144:145],v228 offset:16384\n"
"v_mfma_f32_16x16x32_bf16 v[76:79],a[184:187],a[40:43],v[76:79]\n"
"ds_read_b64_tr_b16 a[146:147],v228 offset:20480\n"
"v_mfma_f32_16x16x32_bf16 v[80:83],a[184:187],a[44:47],v[80:83]\n"
"ds_read_b64_tr_b16 a[148:149],v229 offset:16384\n"
"ds_read_b64_tr_b16 a[150:151],v229 offset:20480\n"
"v_mfma_f32_16x16x32_bf16 v[84:87],a[188:191],a[36:39],v[84:87]\n"
"s_waitcnt lgkmcnt(13)\n"
"ds_read_b64_tr_b16 a[152:153],v230 offset:16384\n"
"v_mfma_f32_16x16x32_bf16 v[88:91],a[188:191],a[40:43],v[88:91]\n"
"ds_read_b64_tr_b16 a[154:155],v230 offset:20480\n"
"s_waitcnt lgkmcnt(13)\n"
"ds_read_b64_tr_b16 a[156:157],v231 offset:16384\n"
"v_mfma_f32_16x16x32_bf16 v[92:95],a[188:191],a[44:47],v[92:95]\n"
"ds_read_b64_tr_b16 a[158:159],v231 offset:20480\n"
"s_waitcnt lgkmcnt(13)\n"
"v_mfma_f32_16x16x32_bf16 v[0:3],a[128:131],a[0:3],v[0:3]\n"
"v_mfma_f32_16x16x32_bf16 v[4:7],a[128:131],a[4:7],v[4:7]\n"
"v_mfma_f32_16x16x32_bf16 v[8:11],a[128:131],a[8:11],v[8:11]\n"
"s_waitcnt lgkmcnt(10)\n"
"v_mfma_f32_16x16x32_bf16 v[12:15],a[132:135],a[0:3],v[12:15]\n"
"s_waitcnt lgkmcnt(0)\n"
"v_mfma_f32_16x16x32_bf16 v[16:19],a[132:135],a[4:7],v[16:19]\n"
"s_add_u32 s92,s92,0x8000\n"
"s_and_b32 s92,s92,0x7ffff\n"
"s_add_u32 m0,s66,0x4000\n"
"s_nop 0\n"
"buffer_load_dwordx4 v238,s[40:43],s92 offen lds\n"
"v_mfma_f32_16x16x32_bf16 v[20:23],a[132:135],a[8:11],v[20:23]\n"
"buffer_load_dwordx4 v239,s[40:43],s92 offen offset:1024 lds\n"
"v_mfma_f32_16x16x32_bf16 v[24:27],a[136:139],a[0:3],v[24:27]\n"
"buffer_load_dwordx4 v240,s[40:43],s92 offen offset:2048 lds\n"
"buffer_load_dwordx4 v241,s[40:43],s92 offen offset:3072 lds\n"
"v_mfma_f32_16x16x32_bf16 v[28:31],a[136:139],a[4:7],v[28:31]\n"
"v_mfma_f32_16x16x32_bf16 v[32:35],a[136:139],a[8:11],v[32:35]\n"
"s_add_u32 s67,s92,0x4000\n"
"s_add_u32 m0,s66,0x5000\n"
"s_nop 0\n"
"buffer_load_dwordx4 v238,s[40:43],s67 offen lds\n"
"v_mfma_f32_16x16x32_bf16 v[36:39],a[140:143],a[0:3],v[36:39]\n"
"buffer_load_dwordx4 v239,s[40:43],s67 offen offset:1024 lds\n"
"buffer_load_dwordx4 v240,s[40:43],s67 offen offset:2048 lds\n"
"v_mfma_f32_16x16x32_bf16 v[40:43],a[140:143],a[4:7],v[40:43]\n"
"buffer_load_dwordx4 v241,s[40:43],s67 offen offset:3072 lds\n"
"v_mfma_f32_16x16x32_bf16 v[44:47],a[140:143],a[8:11],v[44:47]\n"
"ds_read_b128 a[12:15],v129\n"
"ds_read_b128 a[16:19],v129 offset:1024\n"
"v_mfma_f32_16x16x32_bf16 v[48:51],a[144:147],a[0:3],v[48:51]\n"
"ds_read_b128 a[20:23],v129 offset:2048\n"
"v_mfma_f32_16x16x32_bf16 v[52:55],a[144:147],a[4:7],v[52:55]\n"
"s_waitcnt vmcnt(16)\n"
"ds_read_b64_tr_b16 a[160:161],v224\n"
"v_mfma_f32_16x16x32_bf16 v[56:59],a[144:147],a[8:11],v[56:59]\n"
"ds_read_b64_tr_b16 a[162:163],v224 offset:4096\n"
"ds_read_b64_tr_b16 a[164:165],v225\n"
"v_mfma_f32_16x16x32_bf16 v[60:63],a[148:151],a[0:3],v[60:63]\n"
"ds_read_b64_tr_b16 a[166:167],v225 offset:4096\n"
"v_mfma_f32_16x16x32_bf16 v[64:67],a[148:151],a[4:7],v[64:67]\n"
"ds_read_b64_tr_b16 a[168:169],v226\n"
"ds_read_b64_tr_b16 a[170:171],v226 offset:4096\n"
"v_mfma_f32_16x16x32_bf16 v[68:71],a[148:151],a[8:11],v[68:71]\n"
"ds_read_b64_tr_b16 a[172:173],v227\n"
"v_mfma_f32_16x16x32_bf16 v[72:75],a[152:155],a[0:3],v[72:75]\n"
"ds_read_b64_tr_b16 a[174:175],v227 offset:4096\n"
"ds_read_b64_tr_b16 a[176:177],v228\n"
"v_mfma_f32_16x16x32_bf16 v[76:79],a[152:155],a[4:7],v[76:79]\n"
"ds_read_b64_tr_b16 a[178:179],v228 offset:4096\n"
"v_mfma_f32_16x16x32_bf16 v[80:83],a[152:155],a[8:11],v[80:83]\n"
"ds_read_b64_tr_b16 a[180:181],v229\n"
"ds_read_b64_tr_b16 a[182:183],v229 offset:4096\n"
"v_mfma_f32_16x16x32_bf16 v[84:87],a[156:159],a[0:3],v[84:87]\n"
"s_waitcnt lgkmcnt(13)\n"
"ds_read_b64_tr_b16 a[184:185],v230\n"
"v_mfma_f32_16x16x32_bf16 v[88:91],a[156:159],a[4:7],v[88:91]\n"
"ds_read_b64_tr_b16 a[186:187],v230 offset:4096\n"
"s_waitcnt lgkmcnt(13)\n"
"ds_read_b64_tr_b16 a[188:189],v231\n"
"v_mfma_f32_16x16x32_bf16 v[92:95],a[156:159],a[8:11],v[92:95]\n"
"ds_read_b64_tr_b16 a[190:191],v231 offset:4096\n"
"s_waitcnt lgkmcnt(13)\n"
"v_mfma_f32_16x16x32_bf16 v[0:3],a[160:163],a[12:15],v[0:3]\n"
"v_mfma_f32_16x16x32_bf16 v[4:7],a[160:163],a[16:19],v[4:7]\n"
"v_mfma_f32_16x16x32_bf16 v[8:11],a[160:163],a[20:23],v[8:11]\n"
"s_waitcnt lgkmcnt(10)\n"
"v_mfma_f32_16x16x32_bf16 v[12:15],a[164:167],a[12:15],v[12:15]\n"
"s_waitcnt lgkmcnt(0)\n"
"v_mfma_f32_16x16x32_bf16 v[16:19],a[164:167],a[16:19],v[16:19]\n"
"s_add_u32 s92,s92,0x8000\n"
"s_and_b32 s92,s92,0x7ffff\n"
"s_add_u32 m0,s66,0\n"
"s_nop 0\n"
"buffer_load_dwordx4 v238,s[40:43],s92 offen lds\n"
"v_mfma_f32_16x16x32_bf16 v[20:23],a[164:167],a[20:23],v[20:23]\n"
"buffer_load_dwordx4 v239,s[40:43],s92 offen offset:1024 lds\n"
"v_mfma_f32_16x16x32_bf16 v[24:27],a[168:171],a[12:15],v[24:27]\n"
"buffer_load_dwordx4 v240,s[40:43],s92 offen offset:2048 lds\n"
"buffer_load_dwordx4 v241,s[40:43],s92 offen offset:3072 lds\n"
"v_mfma_f32_16x16x32_bf16 v[28:31],a[168:171],a[16:19],v[28:31]\n"
"v_mfma_f32_16x16x32_bf16 v[32:35],a[168:171],a[20:23],v[32:35]\n"
"s_add_u32 s67,s92,0x4000\n"
"s_add_u32 m0,s66,0x1000\n"
"s_nop 0\n"
"buffer_load_dwordx4 v238,s[40:43],s67 offen lds\n"
"v_mfma_f32_16x16x32_bf16 v[36:39],a[172:175],a[12:15],v[36:39]\n"
"buffer_load_dwordx4 v239,s[40:43],s67 offen offset:1024 lds\n"
"buffer_load_dwordx4 v240,s[40:43],s67 offen offset:2048 lds\n"
"v_mfma_f32_16x16x32_bf16 v[40:43],a[172:175],a[16:19],v[40:43]\n"
"buffer_load_dwordx4 v241,s[40:43],s67 offen offset:3072 lds\n"
"v_mfma_f32_16x16x32_bf16 v[44:47],a[172:175],a[20:23],v[44:47]\n"
"ds_read_b128 a[24:27],v130\n"
"ds_read_b128 a[28:31],v130 offset:1024\n"
"v_mfma_f32_16x16x32_bf16 v[48:51],a[176:179],a[12:15],v[48:51]\n"
"ds_read_b128 a[32:35],v130 offset:2048\n"
"v_mfma_f32_16x16x32_bf16 v[52:55],a[176:179],a[16:19],v[52:55]\n"
"s_waitcnt vmcnt(16)\n"
"ds_read_b64_tr_b16 a[128:129],v224 offset:8192\n"
"v_mfma_f32_16x16x32_bf16 v[56:59],a[176:179],a[20:23],v[56:59]\n"
"ds_read_b64_tr_b16 a[130:131],v224 offset:12288\n"
"ds_read_b64_tr_b16 a[132:133],v225 offset:8192\n"
"v_mfma_f32_16x16x32_bf16 v[60:63],a[180:183],a[12:15],v[60:63]\n"
"ds_read_b64_tr_b16 a[134:135],v225 offset:12288\n"
"v_mfma_f32_16x16x32_bf16 v[64:67],a[180:183],a[16:19],v[64:67]\n"
"ds_read_b64_tr_b16 a[136:137],v226 offset:8192\n"
"ds_read_b64_tr_b16 a[138:139],v226 offset:12288\n"
"v_mfma_f32_16x16x32_bf16 v[68:71],a[180:183],a[20:23],v[68:71]\n"
"ds_read_b64_tr_b16 a[140:141],v227 offset:8192\n"
"v_mfma_f32_16x16x32_bf16 v[72:75],a[184:187],a[12:15],v[72:75]\n"
"ds_read_b64_tr_b16 a[142:143],v227 offset:12288\n"
"ds_read_b64_tr_b16 a[144:145],v228 offset:8192\n"
"v_mfma_f32_16x16x32_bf16 v[76:79],a[184:187],a[16:19],v[76:79]\n"
"ds_read_b64_tr_b16 a[146:147],v228 offset:12288\n"
"v_mfma_f32_16x16x32_bf16 v[80:83],a[184:187],a[20:23],v[80:83]\n"
"ds_read_b64_tr_b16 a[148:149],v229 offset:8192\n"
"ds_read_b64_tr_b16 a[150:151],v229 offset:12288\n"
"v_mfma_f32_16x16x32_bf16 v[84:87],a[188:191],a[12:15],v[84:87]\n"
"s_waitcnt lgkmcnt(13)\n"
"ds_read_b64_tr_b16 a[152:153],v230 offset:8192\n"
"v_mfma_f32_16x16x32_bf16 v[88:91],a[188:191],a[16:19],v[88:91]\n"
"ds_read_b64_tr_b16 a[154:155],v230 offset:12288\n"
"s_waitcnt lgkmcnt(13)\n"
"ds_read_b64_tr_b16 a[156:157],v231 offset:8192\n"
"v_mfma_f32_16x16x32_bf16 v[92:95],a[188:191],a[20:23],v[92:95]\n"
"ds_read_b64_tr_b16 a[158:159],v231 offset:12288\n"
"s_waitcnt lgkmcnt(13)\n"
"v_mfma_f32_16x16x32_bf16 v[0:3],a[128:131],a[24:27],v[0:3]\n"
"v_mfma_f32_16x16x32_bf16 v[4:7],a[128:131],a[28:31],v[4:7]\n"
"v_mfma_f32_16x16x32_bf16 v[8:11],a[128:131],a[32:35],v[8:11]\n"
"s_waitcnt lgkmcnt(10)\n"
"v_mfma_f32_16x16x32_bf16 v[12:15],a[132:135],a[24:27],v[12:15]\n"
"s_waitcnt lgkmcnt(0)\n"
"v_mfma_f32_16x16x32_bf16 v[16:19],a[132:135],a[28:31],v[16:19]\n"
"s_add_u32 s92,s92,0x8000\n"
"s_and_b32 s92,s92,0x7ffff\n"
"s_add_u32 m0,s66,0x2000\n"
"s_nop 0\n"
"buffer_load_dwordx4 v238,s[40:43],s92 offen lds\n"
"v_mfma_f32_16x16x32_bf16 v[20:23],a[132:135],a[32:35],v[20:23]\n"
"buffer_load_dwordx4 v239,s[40:43],s92 offen offset:1024 lds\n"
"v_mfma_f32_16x16x32_bf16 v[24:27],a[136:139],a[24:27],v[24:27]\n"
"buffer_load_dwordx4 v240,s[40:43],s92 offen offset:2048 lds\n"
"buffer_load_dwordx4 v241,s[40:43],s92 offen offset:3072 lds\n"
"v_mfma_f32_16x16x32_bf16 v[28:31],a[136:139],a[28:31],v[28:31]\n"
"v_mfma_f32_16x16x32_bf16 v[32:35],a[136:139],a[32:35],v[32:35]\n"
"s_add_u32 s67,s92,0x4000\n"
"s_add_u32 m0,s66,0x3000\n"
"s_nop 0\n"
"buffer_load_dwordx4 v238,s[40:43],s67 offen lds\n"
"v_mfma_f32_16x16x32_bf16 v[36:39],a[140:143],a[24:27],v[36:39]\n"
"buffer_load_dwordx4 v239,s[40:43],s67 offen offset:1024 lds\n"
"buffer_load_dwordx4 v240,s[40:43],s67 offen offset:2048 lds\n"
"v_mfma_f32_16x16x32_bf16 v[40:43],a[140:143],a[28:31],v[40:43]\n"
"buffer_load_dwordx4 v241,s[40:43],s67 offen offset:3072 lds\n"
"v_mfma_f32_16x16x32_bf16 v[44:47],a[140:143],a[32:35],v[44:47]\n"
"ds_read_b128 a[36:39],v131\n"
"ds_read_b128 a[40:43],v131 offset:1024\n"
"v_mfma_f32_16x16x32_bf16 v[48:51],a[144:147],a[24:27],v[48:51]\n"
"ds_read_b128 a[44:47],v131 offset:2048\n"
"v_mfma_f32_16x16x32_bf16 v[52:55],a[144:147],a[28:31],v[52:55]\n"
"s_waitcnt vmcnt(16)\n"
"ds_read_b64_tr_b16 a[160:161],v224 offset:16384\n"
"v_mfma_f32_16x16x32_bf16 v[56:59],a[144:147],a[32:35],v[56:59]\n"
"ds_read_b64_tr_b16 a[162:163],v224 offset:20480\n"
"ds_read_b64_tr_b16 a[164:165],v225 offset:16384\n"
"v_mfma_f32_16x16x32_bf16 v[60:63],a[148:151],a[24:27],v[60:63]\n"
"ds_read_b64_tr_b16 a[166:167],v225 offset:20480\n"
"v_mfma_f32_16x16x32_bf16 v[64:67],a[148:151],a[28:31],v[64:67]\n"
"ds_read_b64_tr_b16 a[168:169],v226 offset:16384\n"
"ds_read_b64_tr_b16 a[170:171],v226 offset:20480\n"
"v_mfma_f32_16x16x32_bf16 v[68:71],a[148:151],a[32:35],v[68:71]\n"
"ds_read_b64_tr_b16 a[172:173],v227 offset:16384\n"
"v_mfma_f32_16x16x32_bf16 v[72:75],a[152:155],a[24:27],v[72:75]\n"
"ds_read_b64_tr_b16 a[174:175],v227 offset:20480\n"
"ds_read_b64_tr_b16 a[176:177],v228 offset:16384\n"
"v_mfma_f32_16x16x32_bf16 v[76:79],a[152:155],a[28:31],v[76:79]\n"
"ds_read_b64_tr_b16 a[178:179],v228 offset:20480\n"
"v_mfma_f32_16x16x32_bf16 v[80:83],a[152:155],a[32:35],v[80:83]\n"
"ds_read_b64_tr_b16 a[180:181],v229 offset:16384\n"
"ds_read_b64_tr_b16 a[182:183],v229 offset:20480\n"
"v_mfma_f32_16x16x32_bf16 v[84:87],a[156:159],a[24:27],v[84:87]\n"
"s_waitcnt lgkmcnt(13)\n"
"ds_read_b64_tr_b16 a[184:185],v230 offset:16384\n"
"v_mfma_f32_16x16x32_bf16 v[88:91],a[156:159],a[28:31],v[88:91]\n"
"ds_read_b64_tr_b16 a[186:187],v230 offset:20480\n"
"s_waitcnt lgkmcnt(13)\n"
"ds_read_b64_tr_b16 a[188:189],v231 offset:16384\n"
"v_mfma_f32_16x16x32_bf16 v[92:95],a[156:159],a[32:35],v[92:95]\n"
"ds_read_b64_tr_b16 a[190:191],v231 offset:20480\n"
"s_waitcnt lgkmcnt(13)\n"
"v_mfma_f32_16x16x32_bf16 v[0:3],a[160:163],a[36:39],v[0:3]\n"
"v_mfma_f32_16x16x32_bf16 v[4:7],a[160:163],a[40:43],v[4:7]\n"
"v_mfma_f32_16x16x32_bf16 v[8:11],a[160:163],a[44:47],v[8:11]\n"
"s_waitcnt lgkmcnt(10)\n"
"v_mfma_f32_16x16x32_bf16 v[12:15],a[164:167],a[36:39],v[12:15]\n"
"s_waitcnt lgkmcnt(0)\n"
"v_mfma_f32_16x16x32_bf16 v[16:19],a[164:167],a[40:43],v[16:19]\n"
"s_add_u32 s92,s92,0x8000\n"
"s_and_b32 s92,s92,0x7ffff\n"
"s_add_u32 m0,s66,0x4000\n"
"s_nop 0\n"
"buffer_load_dwordx4 v238,s[40:43],s92 offen lds\n"
"v_mfma_f32_16x16x32_bf16 v[20:23],a[164:167],a[44:47],v[20:23]\n"
"buffer_load_dwordx4 v239,s[40:43],s92 offen offset:1024 lds\n"
"v_mfma_f32_16x16x32_bf16 v[24:27],a[168:171],a[36:39],v[24:27]\n"
"buffer_load_dwordx4 v240,s[40:43],s92 offen offset:2048 lds\n"
"buffer_load_dwordx4 v241,s[40:43],s92 offen offset:3072 lds\n"
"v_mfma_f32_16x16x32_bf16 v[28:31],a[168:171],a[40:43],v[28:31]\n"
"v_mfma_f32_16x16x32_bf16 v[32:35],a[168:171],a[44:47],v[32:35]\n"
"s_add_u32 s67,s92,0x4000\n"
"s_add_u32 m0,s66,0x5000\n"
"s_nop 0\n"
"buffer_load_dwordx4 v238,s[40:43],s67 offen lds\n"
"v_mfma_f32_16x16x32_bf16 v[36:39],a[172:175],a[36:39],v[36:39]\n"
"buffer_load_dwordx4 v239,s[40:43],s67 offen offset:1024 lds\n"
"buffer_load_dwordx4 v240,s[40:43],s67 offen offset:2048 lds\n"
"v_mfma_f32_16x16x32_bf16 v[40:43],a[172:175],a[40:43],v[40:43]\n"
"buffer_load_dwordx4 v241,s[40:43],s67 offen offset:3072 lds\n"
"v_mfma_f32_16x16x32_bf16 v[44:47],a[172:175],a[44:47],v[44:47]\n"
"ds_read_b128 a[0:3],v132\n"
"ds_read_b128 a[4:7],v132 offset:1024\n"
"v_mfma_f32_16x16x32_bf16 v[48:51],a[176:179],a[36:39],v[48:51]\n"
"ds_read_b128 a[8:11],v132 offset:2048\n"
"v_mfma_f32_16x16x32_bf16 v[52:55],a[176:179],a[40:43],v[52:55]\n"
"s_waitcnt vmcnt(16)\n"
"ds_read_b64_tr_b16 a[128:129],v224\n"
"v_mfma_f32_16x16x32_bf16 v[56:59],a[176:179],a[44:47],v[56:59]\n"
"ds_read_b64_tr_b16 a[130:131],v224 offset:4096\n"
"ds_read_b64_tr_b16 a[132:133],v225\n"
"v_mfma_f32_16x16x32_bf16 v[60:63],a[180:183],a[36:39],v[60:63]\n"
"ds_read_b64_tr_b16 a[134:135],v225 offset:4096\n"
"v_mfma_f32_16x16x32_bf16 v[64:67],a[180:183],a[40:43],v[64:67]\n"
"ds_read_b64_tr_b16 a[136:137],v226\n"
"ds_read_b64_tr_b16 a[138:139],v226 offset:4096\n"
"v_mfma_f32_16x16x32_bf16 v[68:71],a[180:183],a[44:47],v[68:71]\n"
"ds_read_b64_tr_b16 a[140:141],v227\n"
"v_mfma_f32_16x16x32_bf16 v[72:75],a[184:187],a[36:39],v[72:75]\n"
"ds_read_b64_tr_b16 a[142:143],v227 offset:4096\n"
"ds_read_b64_tr_b16 a[144:145],v228\n"
"v_mfma_f32_16x16x32_bf16 v[76:79],a[184:187],a[40:43],v[76:79]\n"
"ds_read_b64_tr_b16 a[146:147],v228 offset:4096\n"
"v_mfma_f32_16x16x32_bf16 v[80:83],a[184:187],a[44:47],v[80:83]\n"
"ds_read_b64_tr_b16 a[148:149],v229\n"
"ds_read_b64_tr_b16 a[150:151],v229 offset:4096\n"
"v_mfma_f32_16x16x32_bf16 v[84:87],a[188:191],a[36:39],v[84:87]\n"
"s_waitcnt lgkmcnt(13)\n"
"ds_read_b64_tr_b16 a[152:153],v230\n"
"v_mfma_f32_16x16x32_bf16 v[88:91],a[188:191],a[40:43],v[88:91]\n"
"ds_read_b64_tr_b16 a[154:155],v230 offset:4096\n"
"s_waitcnt lgkmcnt(13)\n"
"ds_read_b64_tr_b16 a[156:157],v231\n"
"v_mfma_f32_16x16x32_bf16 v[92:95],a[188:191],a[44:47],v[92:95]\n"
"ds_read_b64_tr_b16 a[158:159],v231 offset:4096\n"
"s_waitcnt lgkmcnt(13)\n"
"v_mfma_f32_16x16x32_bf16 v[0:3],a[128:131],a[0:3],v[0:3]\n"
"v_mfma_f32_16x16x32_bf16 v[4:7],a[128:131],a[4:7],v[4:7]\n"
"v_mfma_f32_16x16x32_bf16 v[8:11],a[128:131],a[8:11],v[8:11]\n"
"s_waitcnt lgkmcnt(10)\n"
"v_mfma_f32_16x16x32_bf16 v[12:15],a[132:135],a[0:3],v[12:15]\n"
"s_waitcnt lgkmcnt(0)\n"
"v_mfma_f32_16x16x32_bf16 v[16:19],a[132:135],a[4:7],v[16:19]\n"
"s_add_u32 s92,s92,0x8000\n"
"s_and_b32 s92,s92,0x7ffff\n"
"s_add_u32 m0,s66,0\n"
"s_nop 0\n"
"buffer_load_dwordx4 v238,s[40:43],s92 offen lds\n"
"v_mfma_f32_16x16x32_bf16 v[20:23],a[132:135],a[8:11],v[20:23]\n"
"buffer_load_dwordx4 v239,s[40:43],s92 offen offset:1024 lds\n"
"v_mfma_f32_16x16x32_bf16 v[24:27],a[136:139],a[0:3],v[24:27]\n"
"buffer_load_dwordx4 v240,s[40:43],s92 offen offset:2048 lds\n"
"v_mfma_f32_16x16x32_bf16 v[28:31],a[136:139],a[4:7],v[28:31]\n"
"buffer_load_dwordx4 v241,s[40:43],s92 offen offset:3072 lds\n"
"v_mfma_f32_16x16x32_bf16 v[32:35],a[136:139],a[8:11],v[32:35]\n"
"s_add_u32 s67,s92,0x4000\n"
"s_add_u32 m0,s66,0x1000\n"
"s_nop 0\n"
"buffer_load_dwordx4 v238,s[40:43],s67 offen lds\n"
"v_mfma_f32_16x16x32_bf16 v[36:39],a[140:143],a[0:3],v[36:39]\n"
"buffer_load_dwordx4 v239,s[40:43],s67 offen offset:1024 lds\n"
"v_mfma_f32_16x16x32_bf16 v[40:43],a[140:143],a[4:7],v[40:43]\n"
"buffer_load_dwordx4 v240,s[40:43],s67 offen offset:2048 lds\n"
"v_mfma_f32_16x16x32_bf16 v[44:47],a[140:143],a[8:11],v[44:47]\n"
"buffer_load_dwordx4 v241,s[40:43],s67 offen offset:3072 lds\n"
"ds_read_b128 a[12:15],v133\n"
"v_mfma_f32_16x16x32_bf16 v[48:51],a[144:147],a[0:3],v[48:51]\n"
"ds_read_b128 a[16:19],v133 offset:1024\n"
"v_mfma_f32_16x16x32_bf16 v[52:55],a[144:147],a[4:7],v[52:55]\n"
"ds_read_b128 a[20:23],v133 offset:2048\n"
"v_mfma_f32_16x16x32_bf16 v[56:59],a[144:147],a[8:11],v[56:59]\n"
"ds_read_b128 a[24:27],v134\n"
"ds_read_b128 a[28:31],v134 offset:1024\n"
"v_mfma_f32_16x16x32_bf16 v[60:63],a[148:151],a[0:3],v[60:63]\n"
"ds_read_b128 a[32:35],v134 offset:2048\n"
"v_mfma_f32_16x16x32_bf16 v[64:67],a[148:151],a[4:7],v[64:67]\n"
"ds_read_b128 a[36:39],v135\n"
"ds_read_b128 a[40:43],v135 offset:1024\n"
"v_mfma_f32_16x16x32_bf16 v[68:71],a[148:151],a[8:11],v[68:71]\n"
"ds_read_b128 a[44:47],v135 offset:2048\n"
"v_mfma_f32_16x16x32_bf16 v[72:75],a[152:155],a[0:3],v[72:75]\n"
"s_waitcnt vmcnt(8)\n"
"ds_read_b64_tr_b16 a[160:161],v224 offset:8192\n"
"v_mfma_f32_16x16x32_bf16 v[76:79],a[152:155],a[4:7],v[76:79]\n"
"ds_read_b64_tr_b16 a[162:163],v224 offset:12288\n"
"ds_read_b64_tr_b16 a[164:165],v225 offset:8192\n"
"v_mfma_f32_16x16x32_bf16 v[80:83],a[152:155],a[8:11],v[80:83]\n"
"ds_read_b64_tr_b16 a[166:167],v225 offset:12288\n"
"v_mfma_f32_16x16x32_bf16 v[84:87],a[156:159],a[0:3],v[84:87]\n"
"ds_read_b64_tr_b16 a[168:169],v224 offset:16384\n"
"v_mfma_f32_16x16x32_bf16 v[88:91],a[156:159],a[4:7],v[88:91]\n"
"ds_read_b64_tr_b16 a[170:171],v224 offset:20480\n"
"s_waitcnt lgkmcnt(13)\n"
"ds_read_b64_tr_b16 a[172:173],v225 offset:16384\n"
"v_mfma_f32_16x16x32_bf16 v[92:95],a[156:159],a[8:11],v[92:95]\n"
"ds_read_b64_tr_b16 a[174:175],v225 offset:20480\n"
"s_waitcnt vmcnt(0)\n"
"s_waitcnt lgkmcnt(12)\n"
"ds_read_b64_tr_b16 a[176:177],v224\n"
"ds_read_b64_tr_b16 a[178:179],v224 offset:4096\n"
"ds_read_b64_tr_b16 a[180:181],v225\n"
"s_waitcnt lgkmcnt(14)\n"
"ds_read_b64_tr_b16 a[182:183],v225 offset:4096\n"
"s_waitcnt lgkmcnt(10)\n"
"v_mfma_f32_16x16x32_bf16 v[0:3],a[160:163],a[12:15],v[0:3]\n"
"ds_read_b64_tr_b16 a[128:129],v226 offset:8192\n"
"v_mfma_f32_16x16x32_bf16 v[4:7],a[160:163],a[16:19],v[4:7]\n"
"v_mfma_f32_16x16x32_bf16 v[8:11],a[160:163],a[20:23],v[8:11]\n"
"ds_read_b64_tr_b16 a[130:131],v226 offset:12288\n"
"s_waitcnt lgkmcnt(10)\n"
"v_mfma_f32_16x16x32_bf16 v[12:15],a[164:167],a[12:15],v[12:15]\n"
"ds_read_b64_tr_b16 a[132:133],v227 offset:8192\n"
"v_mfma_f32_16x16x32_bf16 v[16:19],a[164:167],a[16:19],v[16:19]\n"
"v_mfma_f32_16x16x32_bf16 v[20:23],a[164:167],a[20:23],v[20:23]\n"
"ds_read_b64_tr_b16 a[134:135],v227 offset:12288\n"
"s_waitcnt lgkmcnt(10)\n"
"v_mfma_f32_16x16x32_bf16 v[0:3],a[168:171],a[24:27],v[0:3]\n"
"ds_read_b64_tr_b16 a[136:137],v226 offset:16384\n"
"v_mfma_f32_16x16x32_bf16 v[4:7],a[168:171],a[28:31],v[4:7]\n"
"v_mfma_f32_16x16x32_bf16 v[8:11],a[168:171],a[32:35],v[8:11]\n"
"ds_read_b64_tr_b16 a[138:139],v226 offset:20480\n"
"s_waitcnt lgkmcnt(10)\n"
"v_mfma_f32_16x16x32_bf16 v[12:15],a[172:175],a[24:27],v[12:15]\n"
"ds_read_b64_tr_b16 a[140:141],v227 offset:16384\n"
"v_mfma_f32_16x16x32_bf16 v[16:19],a[172:175],a[28:31],v[16:19]\n"
"v_mfma_f32_16x16x32_bf16 v[20:23],a[172:175],a[32:35],v[20:23]\n"
"ds_read_b64_tr_b16 a[142:143],v227 offset:20480\n"
"s_waitcnt lgkmcnt(7)\n"
"v_mfma_f32_16x16x32_bf16 v[0:3],a[176:179],a[36:39],v[0:3]\n"
"ds_read_b64_tr_b16 a[144:145],v226\n"
"v_mfma_f32_16x16x32_bf16 v[4:7],a[176:179],a[40:43],v[4:7]\n"
"v_mfma_f32_16x16x32_bf16 v[8:11],a[176:179],a[44:47],v[8:11]\n"
"ds_read_b64_tr_b16 a[146:147],v226 offset:4096\n"
"v_mfma_f32_16x16x32_bf16 v[12:15],a[180:183],a[36:39],v[12:15]\n"
"ds_read_b64_tr_b16 a[148:149],v227\n"
"v_mfma_f32_16x16x32_bf16 v[16:19],a[180:183],a[40:43],v[16:19]\n"
"v_mfma_f32_16x16x32_bf16 v[20:23],a[180:183],a[44:47],v[20:23]\n"
"ds_read_b64_tr_b16 a[150:151],v227 offset:4096\n"
"s_waitcnt lgkmcnt(7)\n"
"v_mfma_f32_16x16x32_bf16 v[24:27],a[128:131],a[12:15],v[24:27]\n"
"ds_read_b64_tr_b16 a[160:161],v228 offset:8192\n"
"ds_read_b64_tr_b16 a[162:163],v228 offset:12288\n"
"v_mfma_f32_16x16x32_bf16 v[28:31],a[128:131],a[16:19],v[28:31]\n"
"ds_read_b64_tr_b16 a[164:165],v229 offset:8192\n"
"v_mfma_f32_16x16x32_bf16 v[32:35],a[128:131],a[20:23],v[32:35]\n"
"ds_read_b64_tr_b16 a[166:167],v229 offset:12288\n"
"ds_read_b64_tr_b16 a[168:169],v228 offset:16384\n"
"v_mfma_f32_16x16x32_bf16 v[36:39],a[132:135],a[12:15],v[36:39]\n"
"ds_read_b64_tr_b16 a[170:171],v228 offset:20480\n"
"v_mfma_f32_16x16x32_bf16 v[40:43],a[132:135],a[16:19],v[40:43]\n"
"ds_read_b64_tr_b16 a[172:173],v229 offset:16384\n"
"ds_read_b64_tr_b16 a[174:175],v229 offset:20480\n"
"v_mfma_f32_16x16x32_bf16 v[44:47],a[132:135],a[20:23],v[44:47]\n"
"s_waitcnt lgkmcnt(11)\n"
"ds_read_b64_tr_b16 a[176:177],v228\n"
"v_mfma_f32_16x16x32_bf16 v[24:27],a[136:139],a[24:27],v[24:27]\n"
"ds_read_b64_tr_b16 a[178:179],v228 offset:4096\n"
"ds_read_b64_tr_b16 a[180:181],v229\n"
"v_mfma_f32_16x16x32_bf16 v[28:31],a[136:139],a[28:31],v[28:31]\n"
"ds_read_b64_tr_b16 a[182:183],v229 offset:4096\n"
"v_mfma_f32_16x16x32_bf16 v[32:35],a[136:139],a[32:35],v[32:35]\n"
"v_cvt_pk_bf16_f32 v140,v0,v1\n"
"v_cvt_pk_bf16_f32 v141,v2,v3\n"
"v_mfma_f32_16x16x32_bf16 v[36:39],a[140:143],a[24:27],v[36:39]\n"
"v_cvt_pk_bf16_f32 v142,v12,v13\n"
"v_mfma_f32_16x16x32_bf16 v[40:43],a[140:143],a[28:31],v[40:43]\n"
"v_cvt_pk_bf16_f32 v143,v14,v15\n"
"buffer_store_dwordx4 v[140:143],v242,s[44:47],s84 offen sc1\n"
"v_mfma_f32_16x16x32_bf16 v[44:47],a[140:143],a[32:35],v[44:47]\n"
"v_cvt_pk_bf16_f32 v144,v4,v5\n"
"s_waitcnt lgkmcnt(7)\n"
"v_mfma_f32_16x16x32_bf16 v[24:27],a[144:147],a[36:39],v[24:27]\n"
"v_cvt_pk_bf16_f32 v145,v6,v7\n"
"v_cvt_pk_bf16_f32 v146,v16,v17\n"
"v_mfma_f32_16x16x32_bf16 v[28:31],a[144:147],a[40:43],v[28:31]\n"
"v_cvt_pk_bf16_f32 v147,v18,v19\n"
"v_mfma_f32_16x16x32_bf16 v[32:35],a[144:147],a[44:47],v[32:35]\n"
"buffer_store_dwordx4 v[144:147],v242,s[44:47],s85 offen sc1\n"
"v_cvt_pk_bf16_f32 v148,v8,v9\n"
"v_mfma_f32_16x16x32_bf16 v[36:39],a[148:151],a[36:39],v[36:39]\n"
"v_cvt_pk_bf16_f32 v149,v10,v11\n"
"v_mfma_f32_16x16x32_bf16 v[40:43],a[148:151],a[40:43],v[40:43]\n"
"v_cvt_pk_bf16_f32 v150,v20,v21\n"
"v_cvt_pk_bf16_f32 v151,v22,v23\n"
"v_mfma_f32_16x16x32_bf16 v[44:47],a[148:151],a[44:47],v[44:47]\n"
"buffer_store_dwordx4 v[148:151],v242,s[44:47],s86 offen sc1\n"
"v_mfma_f32_16x16x32_bf16 v[48:51],a[160:163],a[12:15],v[48:51]\n"
"ds_read_b64_tr_b16 a[128:129],v230 offset:8192\n"
"ds_read_b64_tr_b16 a[130:131],v230 offset:12288\n"
"v_mfma_f32_16x16x32_bf16 v[52:55],a[160:163],a[16:19],v[52:55]\n"
"ds_read_b64_tr_b16 a[132:133],v231 offset:8192\n"
"v_mfma_f32_16x16x32_bf16 v[56:59],a[160:163],a[20:23],v[56:59]\n"
"ds_read_b64_tr_b16 a[134:135],v231 offset:12288\n"
"ds_read_b64_tr_b16 a[136:137],v230 offset:16384\n"
"v_mfma_f32_16x16x32_bf16 v[60:63],a[164:167],a[12:15],v[60:63]\n"
"ds_read_b64_tr_b16 a[138:139],v230 offset:20480\n"
"v_mfma_f32_16x16x32_bf16 v[64:67],a[164:167],a[16:19],v[64:67]\n"
"ds_read_b64_tr_b16 a[140:141],v231 offset:16384\n"
"ds_read_b64_tr_b16 a[142:143],v231 offset:20480\n"
"v_mfma_f32_16x16x32_bf16 v[68:71],a[164:167],a[20:23],v[68:71]\n"
"s_waitcnt lgkmcnt(8)\n"
"ds_read_b64_tr_b16 a[144:145],v230\n"
"v_mfma_f32_16x16x32_bf16 v[48:51],a[168:171],a[24:27],v[48:51]\n"
"ds_read_b64_tr_b16 a[146:147],v230 offset:4096\n"
"ds_read_b64_tr_b16 a[148:149],v231\n"
"v_mfma_f32_16x16x32_bf16 v[52:55],a[168:171],a[28:31],v[52:55]\n"
"ds_read_b64_tr_b16 a[150:151],v231 offset:4096\n"
"v_mfma_f32_16x16x32_bf16 v[56:59],a[168:171],a[32:35],v[56:59]\n"
"v_cvt_pk_bf16_f32 v140,v24,v25\n"
"v_cvt_pk_bf16_f32 v141,v26,v27\n"
"v_mfma_f32_16x16x32_bf16 v[60:63],a[172:175],a[24:27],v[60:63]\n"
"v_cvt_pk_bf16_f32 v142,v36,v37\n"
"v_mfma_f32_16x16x32_bf16 v[64:67],a[172:175],a[28:31],v[64:67]\n"
"v_cvt_pk_bf16_f32 v143,v38,v39\n"
"buffer_store_dwordx4 v[140:143],v242,s[44:47],s84 offen offset:1024 sc1\n"
"v_mfma_f32_16x16x32_bf16 v[68:71],a[172:175],a[32:35],v[68:71]\n"
"v_cvt_pk_bf16_f32 v144,v28,v29\n"
"v_mfma_f32_16x16x32_bf16 v[48:51],a[176:179],a[36:39],v[48:51]\n"
"v_cvt_pk_bf16_f32 v145,v30,v31\n"
"v_cvt_pk_bf16_f32 v146,v40,v41\n"
"v_mfma_f32_16x16x32_bf16 v[52:55],a[176:179],a[40:43],v[52:55]\n"
"v_cvt_pk_bf16_f32 v147,v42,v43\n"
"v_mfma_f32_16x16x32_bf16 v[56:59],a[176:179],a[44:47],v[56:59]\n"
"buffer_store_dwordx4 v[144:147],v242,s[44:47],s85 offen offset:1024 sc1\n"
"v_cvt_pk_bf16_f32 v148,v32,v33\n"
"v_mfma_f32_16x16x32_bf16 v[60:63],a[180:183],a[36:39],v[60:63]\n"
"v_cvt_pk_bf16_f32 v149,v34,v35\n"
"v_mfma_f32_16x16x32_bf16 v[64:67],a[180:183],a[40:43],v[64:67]\n"
"v_cvt_pk_bf16_f32 v150,v44,v45\n"
"v_cvt_pk_bf16_f32 v151,v46,v47\n"
"v_mfma_f32_16x16x32_bf16 v[68:71],a[180:183],a[44:47],v[68:71]\n"
"buffer_store_dwordx4 v[148:151],v242,s[44:47],s86 offen offset:1024 sc1\n"
"s_waitcnt lgkmcnt(0)\n"
"v_mfma_f32_16x16x32_bf16 v[72:75],a[128:131],a[12:15],v[72:75]\n"
"v_cvt_pk_bf16_f32 v140,v48,v49\n"
"v_mfma_f32_16x16x32_bf16 v[76:79],a[128:131],a[16:19],v[76:79]\n"
"v_cvt_pk_bf16_f32 v141,v50,v51\n"
"v_mfma_f32_16x16x32_bf16 v[80:83],a[128:131],a[20:23],v[80:83]\n"
"v_cvt_pk_bf16_f32 v142,v60,v61\n"
"v_mfma_f32_16x16x32_bf16 v[84:87],a[132:135],a[12:15],v[84:87]\n"
"v_mfma_f32_16x16x32_bf16 v[88:91],a[132:135],a[16:19],v[88:91]\n"
"v_cvt_pk_bf16_f32 v143,v62,v63\n"
"v_mfma_f32_16x16x32_bf16 v[92:95],a[132:135],a[20:23],v[92:95]\n"
"buffer_store_dwordx4 v[140:143],v242,s[44:47],s84 offen offset:2048 sc1\n"
"v_mfma_f32_16x16x32_bf16 v[72:75],a[136:139],a[24:27],v[72:75]\n"
"v_cvt_pk_bf16_f32 v144,v52,v53\n"
"v_mfma_f32_16x16x32_bf16 v[76:79],a[136:139],a[28:31],v[76:79]\n"
"v_cvt_pk_bf16_f32 v145,v54,v55\n"
"v_mfma_f32_16x16x32_bf16 v[80:83],a[136:139],a[32:35],v[80:83]\n"
"v_cvt_pk_bf16_f32 v146,v64,v65\n"
"v_mfma_f32_16x16x32_bf16 v[84:87],a[140:143],a[24:27],v[84:87]\n"
"v_mfma_f32_16x16x32_bf16 v[88:91],a[140:143],a[28:31],v[88:91]\n"
"v_cvt_pk_bf16_f32 v147,v66,v67\n"
"v_mfma_f32_16x16x32_bf16 v[92:95],a[140:143],a[32:35],v[92:95]\n"
"buffer_store_dwordx4 v[144:147],v242,s[44:47],s85 offen offset:2048 sc1\n"
"v_mfma_f32_16x16x32_bf16 v[72:75],a[144:147],a[36:39],v[72:75]\n"
"v_cvt_pk_bf16_f32 v148,v56,v57\n"
"v_mfma_f32_16x16x32_bf16 v[76:79],a[144:147],a[40:43],v[76:79]\n"
"v_cvt_pk_bf16_f32 v149,v58,v59\n"
"v_mfma_f32_16x16x32_bf16 v[80:83],a[144:147],a[44:47],v[80:83]\n"
"v_cvt_pk_bf16_f32 v150,v68,v69\n"
"v_mfma_f32_16x16x32_bf16 v[84:87],a[148:151],a[36:39],v[84:87]\n"
"v_mfma_f32_16x16x32_bf16 v[88:91],a[148:151],a[40:43],v[88:91]\n"
"v_cvt_pk_bf16_f32 v151,v70,v71\n"
"v_mfma_f32_16x16x32_bf16 v[92:95],a[148:151],a[44:47],v[92:95]\n"
"buffer_store_dwordx4 v[148:151],v242,s[44:47],s86 offen offset:2048 sc1\n"
"v_cvt_pk_bf16_f32 v140,v72,v73\n"
"v_cvt_pk_bf16_f32 v141,v74,v75\n"
"s_nop 1\n"
"v_cvt_pk_bf16_f32 v142,v84,v85\n"
"v_cvt_pk_bf16_f32 v143,v86,v87\n"
"buffer_store_dwordx4 v[140:143],v242,s[44:47],s84 offen offset:3072 sc1\n"
"v_cvt_pk_bf16_f32 v144,v76,v77\n"
"v_cvt_pk_bf16_f32 v145,v78,v79\n"
"v_cvt_pk_bf16_f32 v146,v88,v89\n"
"v_cvt_pk_bf16_f32 v147,v90,v91\n"
"buffer_store_dwordx4 v[144:147],v242,s[44:47],s85 offen offset:3072 sc1\n"
"v_cvt_pk_bf16_f32 v148,v80,v81\n"
"v_cvt_pk_bf16_f32 v149,v82,v83\n"
"v_cvt_pk_bf16_f32 v150,v92,v93\n"
"v_cvt_pk_bf16_f32 v151,v94,v95\n"
"buffer_store_dwordx4 v[148:151],v242,s[44:47],s86 offen offset:3072 sc1\n"
"v_and_b32 v246,7,v243\n"
"v_lshlrev_b32 v248,2,v246\n"
"v_lshlrev_b32 v246,7,v246\n"
"buffer_load_dword v245,v246,s[52:55],0 offen sc1\n"
"v_and_b32 v251,1,v244\n"
"v_lshlrev_b32 v251,5,v251\n"
"v_lshrrev_b32 v253,1,v244\n"
"v_lshl_or_b32 v251,v253,4,v251\n"
"v_lshl_or_b32 v170,v243,11,v251\n"
"s_mul_i32 s2,s56,6\n"
"s_lshl_b32 s60,s7,1\n"
"s_add_u32 s2,s2,s60\n"
"s_add_u32 s2,s2,0\n"
"s_lshl_b32 s60,s2,10\n"
"s_mov_b32 s8,s60\n"
"s_mul_i32 s62,s2,43\n"
"s_lshr_b32 s62,s62,9\n"
"s_mul_i32 s63,s62,12\n"
"s_sub_u32 s63,s2,s63\n"
"s_lshl_b32 s61,s62,8\n"
"s_and_b32 s62,s63,3\n"
"s_lshl_b32 s62,s62,6\n"
"s_add_u32 s61,s61,s62\n"
"s_lshr_b32 s63,s63,2\n"
"s_lshl_b32 s63,s63,15\n"
"s_add_u32 s61,s61,s63\n"
"s_mul_i32 s63,s57,0x18000\n"
"s_add_u32 s61,s61,s63\n"
"s_add_u32 s61,s61,0x400\n"
"s_mov_b32 s10,s61\n"
"s_mul_i32 s3,s56,6\n"
"s_lshl_b32 s60,s7,1\n"
"s_add_u32 s3,s3,s60\n"
"s_add_u32 s3,s3,1\n"
"s_lshl_b32 s60,s3,10\n"
"s_mov_b32 s9,s60\n"
"s_mul_i32 s62,s3,43\n"
"s_lshr_b32 s62,s62,9\n"
"s_mul_i32 s63,s62,12\n"
"s_sub_u32 s63,s3,s63\n"
"s_lshl_b32 s61,s62,8\n"
"s_and_b32 s62,s63,3\n"
"s_lshl_b32 s62,s62,6\n"
"s_add_u32 s61,s61,s62\n"
"s_lshr_b32 s63,s63,2\n"
"s_lshl_b32 s63,s63,15\n"
"s_add_u32 s61,s61,s63\n"
"s_mul_i32 s63,s57,0x18000\n"
"s_add_u32 s61,s61,s63\n"
"s_add_u32 s61,s61,0x400\n"
"s_mov_b32 s11,s61\n"
"v_lshrrev_b32 v251,2,v243\n"
"v_cmp_eq_u32 vcc,v251,v244\n"
"v_and_b32 v251,1,v243\n"
"v_lshlrev_b32 v251,4,v251\n"
"v_mov_b32 v253,0x3f80\n"
"v_lshlrev_b32 v251,v251,v253\n"
"v_cndmask_b32 v251,0,v251,vcc\n"
"v_and_b32 v253,2,v243\n"
"v_cmp_eq_u32 vcc,0,v253\n"
"v_mov_b32 v180,0\n"
"v_mov_b32 v181,0\n"
"v_mov_b32 v184,0\n"
"v_mov_b32 v185,0\n"
"v_cndmask_b32 v182,0,v251,vcc\n"
"v_cndmask_b32 v183,v251,0,vcc\n"
"s_lshl_b32 s61,s68,2\n"
"s_add_u32 s61,s61,0x1000\n"
"s_lshl_b32 s62,s68,7\n"
"s_add_u32 s62,s62,0x1000\n"
"s_waitcnt vmcnt(0)\n"
"s_barrier\n"
"s_cmp_lg_u32 s7,3\n"
"s_cbranch_scc1 Ls_104\n"
"s_mov_b64 exec,0xff\n"
"buffer_store_dword v245,v246,s[52:55],s61 offen sc1\n"
"s_mov_b64 exec,-1\n"
"s_mov_b32 s69,0\n"
"Ll_105:\n"
"buffer_load_dword v250,v248,s[52:55],s62 offen sc1\n"
"s_waitcnt vmcnt(0)\n"
"v_cmp_ne_u32 vcc,s65,v250\n"
"s_cbranch_vccz Ld_106\n"
"s_add_u32 s69,s69,1\n"
"s_and_b32 s60,s69,15\n"
"s_cbranch_scc1 Ll_105\n"
"buffer_load_dword v249,v246,s[52:55],0 offen sc1\n"
"s_waitcnt vmcnt(0)\n"
"v_cmp_ne_u32 vcc,v249,v245\n"
"s_cbranch_vccz Lo_107\n"
"s_mov_b64 exec,0xff\n"
"buffer_store_dword v249,v246,s[52:55],s61 offen sc1\n"
"s_mov_b64 exec,-1\n"
"v_mov_b32 v245,v249\n"
"Lo_107:\n"
"s_cmp_lt_u32 s69,0x80000\n"
"s_cbranch_scc1 Ll_105\n"
"Ld_106:\n"
"s_waitcnt vmcnt(0)\n"
"Ls_104:\n"
"s_barrier\n"
"s_cmp_lg_u32 s7,3\n"
"s_cbranch_scc1 Lr_108\n"
"buffer_load_dword v249,v246,s[52:55],0 offen sc1\n"
"s_waitcnt vmcnt(0)\n"
"v_cmp_ne_u32 vcc,v249,v245\n"
"s_cbranch_vccz Lo_110\n"
"s_mov_b64 exec,0xff\n"
"buffer_store_dword v249,v246,s[52:55],s61 offen sc1\n"
"s_mov_b64 exec,-1\n"
"v_mov_b32 v245,v249\n"
"Lo_110:\n"
"s_waitcnt vmcnt(0)\n"
"s_branch Le_109\n"
"Lr_108:\n"
"buffer_load_dwordx4 v[0:3],v242,s[44:47],s8 offen sc1\n"
"s_add_u32 s8,s8,0xc400\n"
"buffer_load_dwordx4 v[4:7],v242,s[44:47],s8 offen sc1\n"
"s_add_u32 s8,s8,0xc400\n"
"buffer_load_dwordx4 v[8:11],v242,s[44:47],s8 offen sc1\n"
"s_add_u32 s8,s8,0xc400\n"
"buffer_load_dwordx4 v[12:15],v242,s[44:47],s8 offen sc1\n"
"s_add_u32 s8,s8,0xc400\n"
"buffer_load_dwordx4 v[16:19],v242,s[44:47],s8 offen sc1\n"
"s_add_u32 s8,s8,0xc400\n"
"buffer_load_dwordx4 v[20:23],v242,s[44:47],s8 offen sc1\n"
"s_add_u32 s8,s8,0xc400\n"
"buffer_load_dwordx4 v[24:27],v242,s[44:47],s8 offen sc1\n"
"s_add_u32 s8,s8,0xc400\n"
"buffer_load_dwordx4 v[28:31],v242,s[44:47],s8 offen sc1\n"
"buffer_load_dwordx4 v[32:35],v242,s[44:47],s9 offen sc1\n"
"s_add_u32 s9,s9,0xc400\n"
"buffer_load_dwordx4 v[36:39],v242,s[44:47],s9 offen sc1\n"
"s_add_u32 s9,s9,0xc400\n"
"buffer_load_dwordx4 v[40:43],v242,s[44:47],s9 offen sc1\n"
"s_add_u32 s9,s9,0xc400\n"
"buffer_load_dwordx4 v[44:47],v242,s[44:47],s9 offen sc1\n"
"s_add_u32 s9,s9,0xc400\n"
"buffer_load_dwordx4 v[48:51],v242,s[44:47],s9 offen sc1\n"
"s_add_u32 s9,s9,0xc400\n"
"buffer_load_dwordx4 v[52:55],v242,s[44:47],s9 offen sc1\n"
"s_add_u32 s9,s9,0xc400\n"
"buffer_load_dwordx4 v[56:59],v242,s[44:47],s9 offen sc1\n"
"s_add_u32 s9,s9,0xc400\n"
"buffer_load_dwordx4 v[60:63],v242,s[44:47],s9 offen sc1\n"
"s_waitcnt vmcnt(15)\n"
"v_mfma_f32_16x16x32_bf16 v[186:189],v[182:185],v[0:3],0\n"
"v_mfma_f32_16x16x32_bf16 v[190:193],v[180:183],v[0:3],0\n"
"s_waitcnt vmcnt(14)\n"
"v_mfma_f32_16x16x32_bf16 v[186:189],v[182:185],v[4:7],v[186:189]\n"
"v_mfma_f32_16x16x32_bf16 v[190:193],v[180:183],v[4:7],v[190:193]\n"
"s_waitcnt vmcnt(13)\n"
"v_mfma_f32_16x16x32_bf16 v[186:189],v[182:185],v[8:11],v[186:189]\n"
"v_mfma_f32_16x16x32_bf16 v[190:193],v[180:183],v[8:11],v[190:193]\n"
"s_waitcnt vmcnt(12)\n"
"v_mfma_f32_16x16x32_bf16 v[186:189],v[182:185],v[12:15],v[186:189]\n"
"v_mfma_f32_16x16x32_bf16 v[190:193],v[180:183],v[12:15],v[190:193]\n"
"s_waitcnt vmcnt(11)\n"
"v_mfma_f32_16x16x32_bf16 v[186:189],v[182:185],v[16:19],v[186:189]\n"
"v_mfma_f32_16x16x32_bf16 v[190:193],v[180:183],v[16:19],v[190:193]\n"
"s_waitcnt vmcnt(10)\n"
"v_mfma_f32_16x16x32_bf16 v[186:189],v[182:185],v[20:23],v[186:189]\n"
"v_mfma_f32_16x16x32_bf16 v[190:193],v[180:183],v[20:23],v[190:193]\n"
"s_waitcnt vmcnt(9)\n"
"v_mfma_f32_16x16x32_bf16 v[186:189],v[182:185],v[24:27],v[186:189]\n"
"v_mfma_f32_16x16x32_bf16 v[190:193],v[180:183],v[24:27],v[190:193]\n"
"s_waitcnt vmcnt(8)\n"
"v_mfma_f32_16x16x32_bf16 v[186:189],v[182:185],v[28:31],v[186:189]\n"
"v_mfma_f32_16x16x32_bf16 v[190:193],v[180:183],v[28:31],v[190:193]\n"
"s_nop 6\n"
"v_cvt_pk_bf16_f32 v172,v186,v187\n"
"v_cvt_pk_bf16_f32 v173,v188,v189\n"
"v_cvt_pk_bf16_f32 v174,v190,v191\n"
"v_cvt_pk_bf16_f32 v175,v192,v193\n"
"s_nop 0\n"
"v_permlane16_swap_b32 v172,v174\n"
"v_permlane16_swap_b32 v173,v175\n"
"buffer_store_dwordx4 v[172:175],v170,s[48:51],s10 offen sc0 sc1\n"
"s_waitcnt vmcnt(8)\n"
"v_mfma_f32_16x16x32_bf16 v[194:197],v[182:185],v[32:35],0\n"
"v_mfma_f32_16x16x32_bf16 v[198:201],v[180:183],v[32:35],0\n"
"s_waitcnt vmcnt(7)\n"
"v_mfma_f32_16x16x32_bf16 v[194:197],v[182:185],v[36:39],v[194:197]\n"
"v_mfma_f32_16x16x32_bf16 v[198:201],v[180:183],v[36:39],v[198:201]\n"
"s_waitcnt vmcnt(6)\n"
"v_mfma_f32_16x16x32_bf16 v[194:197],v[182:185],v[40:43],v[194:197]\n"
"v_mfma_f32_16x16x32_bf16 v[198:201],v[180:183],v[40:43],v[198:201]\n"
"s_waitcnt vmcnt(5)\n"
"v_mfma_f32_16x16x32_bf16 v[194:197],v[182:185],v[44:47],v[194:197]\n"
"v_mfma_f32_16x16x32_bf16 v[198:201],v[180:183],v[44:47],v[198:201]\n"
"s_waitcnt vmcnt(4)\n"
"v_mfma_f32_16x16x32_bf16 v[194:197],v[182:185],v[48:51],v[194:197]\n"
"v_mfma_f32_16x16x32_bf16 v[198:201],v[180:183],v[48:51],v[198:201]\n"
"s_waitcnt vmcnt(3)\n"
"v_mfma_f32_16x16x32_bf16 v[194:197],v[182:185],v[52:55],v[194:197]\n"
"v_mfma_f32_16x16x32_bf16 v[198:201],v[180:183],v[52:55],v[198:201]\n"
"s_waitcnt vmcnt(2)\n"
"v_mfma_f32_16x16x32_bf16 v[194:197],v[182:185],v[56:59],v[194:197]\n"
"v_mfma_f32_16x16x32_bf16 v[198:201],v[180:183],v[56:59],v[198:201]\n"
"s_waitcnt vmcnt(1)\n"
"v_mfma_f32_16x16x32_bf16 v[194:197],v[182:185],v[60:63],v[194:197]\n"
"v_mfma_f32_16x16x32_bf16 v[198:201],v[180:183],v[60:63],v[198:201]\n"
"s_nop 6\n"
"v_cvt_pk_bf16_f32 v176,v194,v195\n"
"v_cvt_pk_bf16_f32 v177,v196,v197\n"
"v_cvt_pk_bf16_f32 v178,v198,v199\n"
"v_cvt_pk_bf16_f32 v179,v200,v201\n"
"s_nop 0\n"
"v_permlane16_swap_b32 v176,v178\n"
"v_permlane16_swap_b32 v177,v179\n"
"buffer_store_dwordx4 v[176:179],v170,s[48:51],s11 offen sc0 sc1\n"
"s_waitcnt vmcnt(0)\n"
"Le_109:\n"
"s_endpgm\n"
"Lk_59x:\n"
"s_swappc_b64 s[70:71],s[72:73]\n"
"s_branch Lk_59\n"
"Lk_60x:\n"
"s_swappc_b64 s[70:71],s[74:75]\n"
"s_branch Lk_60\n"
"Lk_61x:\n"
"s_swappc_b64 s[70:71],s[76:77]\n"
"s_branch Lk_61\n"
"Lk_62x:\n"
"s_swappc_b64 s[70:71],s[72:73]\n"
"s_branch Lk_62\n"
"Lk_63x:\n"
"s_swappc_b64 s[70:71],s[74:75]\n"
"s_branch Lk_63\n"
"Lk_64x:\n"
"s_swappc_b64 s[70:71],s[76:77]\n"
"s_branch Lk_64\n"
"Lk_65x:\n"
"s_swappc_b64 s[70:71],s[72:73]\n"
"s_branch Lk_65\n"
"Lk_66x:\n"
"s_swappc_b64 s[70:71],s[74:75]\n"
"s_branch Lk_66\n"
"Lk_67x:\n"
"s_swappc_b64 s[70:71],s[76:77]\n"
"s_branch Lk_67\n"
"Lk_68x:\n"
"s_swappc_b64 s[70:71],s[72:73]\n"
"s_branch Lk_68\n"
"Lk_69x:\n"
"s_swappc_b64 s[70:71],s[74:75]\n"
"s_branch Lk_69\n"
"Lk_70x:\n"
"s_swappc_b64 s[70:71],s[76:77]\n"
"s_branch Lk_70\n"
"Lk_71x:\n"
"s_swappc_b64 s[70:71],s[72:73]\n"
"s_branch Lk_71\n"
"Lk_72x:\n"
"s_swappc_b64 s[70:71],s[74:75]\n"
"s_branch Lk_72\n"
"Lk_73x:\n"
"s_swappc_b64 s[70:71],s[76:77]\n"
"s_branch Lk_73\n"
"Lk_74x:\n"
"s_swappc_b64 s[70:71],s[72:73]\n"
"s_branch Lk_74\n"
"Lk_75x:\n"
"s_swappc_b64 s[70:71],s[74:75]\n"
"s_branch Lk_75\n"
"Lk_76x:\n"
"s_swappc_b64 s[70:71],s[76:77]\n"
"s_branch Lk_76\n"
"Lk_77x:\n"
"s_swappc_b64 s[70:71],s[72:73]\n"
"s_branch Lk_77\n"
"Lk_78x:\n"
"s_swappc_b64 s[70:71],s[74:75]\n"
"s_branch Lk_78\n"
"Lk_79x:\n"
"s_swappc_b64 s[70:71],s[76:77]\n"
"s_branch Lk_79\n"
"Lk_80x:\n"
"s_swappc_b64 s[70:71],s[72:73]\n"
"s_branch Lk_80\n"
"Lk_81x:\n"
"s_swappc_b64 s[70:71],s[74:75]\n"
"s_branch Lk_81\n"
"Lk_82x:\n"
"s_swappc_b64 s[70:71],s[76:77]\n"
"s_branch Lk_82\n"
"Lk_83x:\n"
"s_swappc_b64 s[70:71],s[72:73]\n"
"s_branch Lk_83\n"
"Lk_84x:\n"
"s_swappc_b64 s[70:71],s[74:75]\n"
"s_branch Lk_84\n"
"Lk_85x:\n"
"s_swappc_b64 s[70:71],s[76:77]\n"
"s_branch Lk_85\n"
"Lk_86x:\n"
"s_swappc_b64 s[70:71],s[72:73]\n"
"s_branch Lk_86\n"
"Lk_87x:\n"
"s_swappc_b64 s[70:71],s[74:75]\n"
"s_branch Lk_87\n"
"Lk_88x:\n"
"s_swappc_b64 s[70:71],s[76:77]\n"
"s_branch Lk_88\n"
"Lk_89x:\n"
"s_swappc_b64 s[70:71],s[72:73]\n"
"s_branch Lk_89\n"
"Lk_90x:\n"
"s_swappc_b64 s[70:71],s[74:75]\n"
"s_branch Lk_90\n"
"Lk_91x:\n"
"s_swappc_b64 s[70:71],s[76:77]\n"
"s_branch Lk_91\n"
"Lk_92x:\n"
"s_swappc_b64 s[70:71],s[72:73]\n"
"s_branch Lk_92\n"
"Lk_93x:\n"
"s_swappc_b64 s[70:71],s[74:75]\n"
"s_branch Lk_93\n"
"Lk_94x:\n"
"s_swappc_b64 s[70:71],s[76:77]\n"
"s_branch Lk_94\n"
"Lk_95x:\n"
"s_swappc_b64 s[70:71],s[72:73]\n"
"s_branch Lk_95\n"
"Lk_96x:\n"
"s_swappc_b64 s[70:71],s[74:75]\n"
"s_branch Lk_96\n"
"Lk_97x:\n"
"s_swappc_b64 s[70:71],s[76:77]\n"
"s_branch Lk_97\n"
"Lk_98x:\n"
"s_swappc_b64 s[70:71],s[72:73]\n"
"s_branch Lk_98\n"
"Lk_99x:\n"
"s_swappc_b64 s[70:71],s[74:75]\n"
"s_branch Lk_99\n"
"Lk_100x:\n"
"s_swappc_b64 s[70:71],s[76:77]\n"
"s_branch Lk_100\n"
"Lk_101x:\n"
"s_swappc_b64 s[70:71],s[72:73]\n"
"s_branch Lk_101\n"
"Lk_102x:\n"
"s_swappc_b64 s[70:71],s[74:75]\n"
"s_branch Lk_102\n"
"Lk_103x:\n"
"s_swappc_b64 s[70:71],s[76:77]\n"
"s_branch Lk_103\n"
"LsubB0:\n"
"s_nop 7\n"
"v_mov_b32 v249,v246\n"
"s_nop 1\n"
"v_permlane32_swap_b32 v246,v249\n"
"v_max_f32 v246,v246,v249\n"
"v_mov_b32 v249,v246\n"
"s_nop 1\n"
"v_permlane16_swap_b32 v246,v249\n"
"v_max_f32 v246,v246,v249\n"
"v_max_f32 v246,0,v246\n"
"v_sub_f32 v250,0,v246\n"
"v_exp_f32 v250,v250\n"
"v_sub_f32 v132,v132,v246\n"
"v_sub_f32 v133,v133,v246\n"
"v_sub_f32 v134,v134,v246\n"
"v_sub_f32 v135,v135,v246\n"
"s_nop 1\n"
"v_mfma_f32_16x16x32_bf16 v[108:111],v[156:159],a[0:3],v[132:135]\n"
"v_mfma_f32_16x16x32_bf16 v[112:115],v[172:175],a[0:3],v[132:135]\n"
"v_mfma_f32_16x16x32_bf16 v[108:111],v[160:163],a[12:15],v[108:111]\n"
"v_mfma_f32_16x16x32_bf16 v[112:115],v[176:179],a[12:15],v[112:115]\n"
"v_mfma_f32_16x16x32_bf16 v[108:111],v[164:167],a[24:27],v[108:111]\n"
"v_mfma_f32_16x16x32_bf16 v[112:115],v[180:183],a[24:27],v[112:115]\n"
"v_mfma_f32_16x16x32_bf16 v[108:111],v[168:171],a[36:39],v[108:111]\n"
"v_mfma_f32_16x16x32_bf16 v[112:115],v[184:187],a[36:39],v[112:115]\n"
"v_pk_mul_f32 v[0:1],v[0:1],v[250:251] op_sel_hi:[1,0]\n"
"v_pk_mul_f32 v[2:3],v[2:3],v[250:251] op_sel_hi:[1,0]\n"
"v_pk_mul_f32 v[4:5],v[4:5],v[250:251] op_sel_hi:[1,0]\n"
"v_pk_mul_f32 v[6:7],v[6:7],v[250:251] op_sel_hi:[1,0]\n"
"v_pk_mul_f32 v[8:9],v[8:9],v[250:251] op_sel_hi:[1,0]\n"
"v_pk_mul_f32 v[10:11],v[10:11],v[250:251] op_sel_hi:[1,0]\n"
"v_pk_mul_f32 v[12:13],v[12:13],v[250:251] op_sel_hi:[1,0]\n"
"v_pk_mul_f32 v[14:15],v[14:15],v[250:251] op_sel_hi:[1,0]\n"
"v_pk_mul_f32 v[16:17],v[16:17],v[250:251] op_sel_hi:[1,0]\n"
"v_pk_mul_f32 v[18:19],v[18:19],v[250:251] op_sel_hi:[1,0]\n"
"v_pk_mul_f32 v[20:21],v[20:21],v[250:251] op_sel_hi:[1,0]\n"
"v_pk_mul_f32 v[22:23],v[22:23],v[250:251] op_sel_hi:[1,0]\n"
"v_pk_mul_f32 v[24:25],v[24:25],v[250:251] op_sel_hi:[1,0]\n"
"v_pk_mul_f32 v[26:27],v[26:27],v[250:251] op_sel_hi:[1,0]\n"
"v_pk_mul_f32 v[28:29],v[28:29],v[250:251] op_sel_hi:[1,0]\n"
"v_pk_mul_f32 v[30:31],v[30:31],v[250:251] op_sel_hi:[1,0]\n"
"v_pk_mul_f32 v[96:97],v[96:97],v[250:251] op_sel_hi:[1,0]\n"
"v_pk_mul_f32 v[98:99],v[98:99],v[250:251] op_sel_hi:[1,0]\n"
"v_exp_f32 v108,v108\n"
"v_exp_f32 v109,v109\n"
"v_exp_f32 v110,v110\n"
"v_exp_f32 v111,v111\n"
"v_cvt_pk_bf16_f32 v144,v108,v109\n"
"v_exp_f32 v112,v112\n"
"v_exp_f32 v113,v113\n"
"v_cvt_pk_bf16_f32 v145,v110,v111\n"
"v_exp_f32 v114,v114\n"
"v_exp_f32 v115,v115\n"
"v_cvt_pk_bf16_f32 v146,v112,v113\n"
"v_cvt_pk_bf16_f32 v147,v114,v115\n"
"s_nop 1\n"
"s_setpc_b64 s[70:71]\n"
"LsubB1:\n"
"s_nop 7\n"
"v_mov_b32 v249,v247\n"
"s_nop 1\n"
"v_permlane32_swap_b32 v247,v249\n"
"v_max_f32 v247,v247,v249\n"
"v_mov_b32 v249,v247\n"
"s_nop 1\n"
"v_permlane16_swap_b32 v247,v249\n"
"v_max_f32 v247,v247,v249\n"
"v_max_f32 v247,0,v247\n"
"v_sub_f32 v250,0,v247\n"
"v_exp_f32 v250,v250\n"
"v_sub_f32 v136,v136,v247\n"
"v_sub_f32 v137,v137,v247\n"
"v_sub_f32 v138,v138,v247\n"
"v_sub_f32 v139,v139,v247\n"
"s_nop 1\n"
"v_mfma_f32_16x16x32_bf16 v[116:119],v[156:159],a[4:7],v[136:139]\n"
"v_mfma_f32_16x16x32_bf16 v[120:123],v[172:175],a[4:7],v[136:139]\n"
"v_mfma_f32_16x16x32_bf16 v[116:119],v[160:163],a[16:19],v[116:119]\n"
"v_mfma_f32_16x16x32_bf16 v[120:123],v[176:179],a[16:19],v[120:123]\n"
"v_mfma_f32_16x16x32_bf16 v[116:119],v[164:167],a[28:31],v[116:119]\n"
"v_mfma_f32_16x16x32_bf16 v[120:123],v[180:183],a[28:31],v[120:123]\n"
"v_mfma_f32_16x16x32_bf16 v[116:119],v[168:171],a[40:43],v[116:119]\n"
"v_mfma_f32_16x16x32_bf16 v[120:123],v[184:187],a[40:43],v[120:123]\n"
"v_pk_mul_f32 v[32:33],v[32:33],v[250:251] op_sel_hi:[1,0]\n"
"v_pk_mul_f32 v[34:35],v[34:35],v[250:251] op_sel_hi:[1,0]\n"
"v_pk_mul_f32 v[36:37],v[36:37],v[250:251] op_sel_hi:[1,0]\n"
"v_pk_mul_f32 v[38:39],v[38:39],v[250:251] op_sel_hi:[1,0]\n"
"v_pk_mul_f32 v[40:41],v[40:41],v[250:251] op_sel_hi:[1,0]\n"
"v_pk_mul_f32 v[42:43],v[42:43],v[250:251] op_sel_hi:[1,0]\n"
"v_pk_mul_f32 v[44:45],v[44:45],v[250:251] op_sel_hi:[1,0]\n"
"v_pk_mul_f32 v[46:47],v[46:47],v[250:251] op_sel_hi:[1,0]\n"
"v_pk_mul_f32 v[48:49],v[48:49],v[250:251] op_sel_hi:[1,0]\n"
"v_pk_mul_f32 v[50:51],v[50:51],v[250:251] op_sel_hi:[1,0]\n"
"v_pk_mul_f32 v[52:53],v[52:53],v[250:251] op_sel_hi:[1,0]\n"
"v_pk_mul_f32 v[54:55],v[54:55],v[250:251] op_sel_hi:[1,0]\n"
"v_pk_mul_f32 v[56:57],v[56:57],v[250:251] op_sel_hi:[1,0]\n"
"v_pk_mul_f32 v[58:59],v[58:59],v[250:251] op_sel_hi:[1,0]\n"
"v_pk_mul_f32 v[60:61],v[60:61],v[250:251] op_sel_hi:[1,0]\n"
"v_pk_mul_f32 v[62:63],v[62:63],v[250:251] op_sel_hi:[1,0]\n"
"v_pk_mul_f32 v[100:101],v[100:101],v[250:251] op_sel_hi:[1,0]\n"
"v_pk_mul_f32 v[102:103],v[102:103],v[250:251] op_sel_hi:[1,0]\n"
"v_exp_f32 v116,v116\n"
"v_exp_f32 v117,v117\n"
"v_exp_f32 v118,v118\n"
"v_exp_f32 v119,v119\n"
"v_cvt_pk_bf16_f32 v148,v116,v117\n"
"v_exp_f32 v120,v120\n"
"v_exp_f32 v121,v121\n"
"v_cvt_pk_bf16_f32 v149,v118,v119\n"
"v_exp_f32 v122,v122\n"
"v_exp_f32 v123,v123\n"
"v_cvt_pk_bf16_f32 v150,v120,v121\n"
"v_cvt_pk_bf16_f32 v151,v122,v123\n"
"s_nop 1\n"
"s_setpc_b64 s[70:71]\n"
"LsubB2:\n"
"s_nop 7\n"
"v_mov_b32 v249,v248\n"
"s_nop 1\n"
"v_permlane32_swap_b32 v248,v249\n"
"v_max_f32 v248,v248,v249\n"
"v_mov_b32 v249,v248\n"
"s_nop 1\n"
"v_permlane16_swap_b32 v248,v249\n"
"v_max_f32 v248,v248,v249\n"
"v_max_f32 v248,0,v248\n"
"v_sub_f32 v250,0,v248\n"
"v_exp_f32 v250,v250\n"
"v_sub_f32 v140,v140,v248\n"
"v_sub_f32 v141,v141,v248\n"
"v_sub_f32 v142,v142,v248\n"
"v_sub_f32 v143,v143,v248\n"
"s_nop 1\n"
"v_mfma_f32_16x16x32_bf16 v[124:127],v[156:159],a[8:11],v[140:143]\n"
"v_mfma_f32_16x16x32_bf16 v[128:131],v[172:175],a[8:11],v[140:143]\n"
"v_mfma_f32_16x16x32_bf16 v[124:127],v[160:163],a[20:23],v[124:127]\n"
"v_mfma_f32_16x16x32_bf16 v[128:131],v[176:179],a[20:23],v[128:131]\n"
"v_mfma_f32_16x16x32_bf16 v[124:127],v[164:167],a[32:35],v[124:127]\n"
"v_mfma_f32_16x16x32_bf16 v[128:131],v[180:183],a[32:35],v[128:131]\n"
"v_mfma_f32_16x16x32_bf16 v[124:127],v[168:171],a[44:47],v[124:127]\n"
"v_mfma_f32_16x16x32_bf16 v[128:131],v[184:187],a[44:47],v[128:131]\n"
"v_pk_mul_f32 v[64:65],v[64:65],v[250:251] op_sel_hi:[1,0]\n"
"v_pk_mul_f32 v[66:67],v[66:67],v[250:251] op_sel_hi:[1,0]\n"
"v_pk_mul_f32 v[68:69],v[68:69],v[250:251] op_sel_hi:[1,0]\n"
"v_pk_mul_f32 v[70:71],v[70:71],v[250:251] op_sel_hi:[1,0]\n"
"v_pk_mul_f32 v[72:73],v[72:73],v[250:251] op_sel_hi:[1,0]\n"
"v_pk_mul_f32 v[74:75],v[74:75],v[250:251] op_sel_hi:[1,0]\n"
"v_pk_mul_f32 v[76:77],v[76:77],v[250:251] op_sel_hi:[1,0]\n"
"v_pk_mul_f32 v[78:79],v[78:79],v[250:251] op_sel_hi:[1,0]\n"
"v_pk_mul_f32 v[80:81],v[80:81],v[250:251] op_sel_hi:[1,0]\n"
"v_pk_mul_f32 v[82:83],v[82:83],v[250:251] op_sel_hi:[1,0]\n"
"v_pk_mul_f32 v[84:85],v[84:85],v[250:251] op_sel_hi:[1,0]\n"
"v_pk_mul_f32 v[86:87],v[86:87],v[250:251] op_sel_hi:[1,0]\n"
"v_pk_mul_f32 v[88:89],v[88:89],v[250:251] op_sel_hi:[1,0]\n"
"v_pk_mul_f32 v[90:91],v[90:91],v[250:251] op_sel_hi:[1,0]\n"
"v_pk_mul_f32 v[92:93],v[92:93],v[250:251] op_sel_hi:[1,0]\n"
"v_pk_mul_f32 v[94:95],v[94:95],v[250:251] op_sel_hi:[1,0]\n"
"v_pk_mul_f32 v[104:105],v[104:105],v[250:251] op_sel_hi:[1,0]\n"
"v_pk_mul_f32 v[106:107],v[106:107],v[250:251] op_sel_hi:[1,0]\n"
"v_exp_f32 v124,v124\n"
"v_exp_f32 v125,v125\n"
"v_exp_f32 v126,v126\n"
"v_exp_f32 v127,v127\n"
"v_cvt_pk_bf16_f32 v152,v124,v125\n"
"v_exp_f32 v128,v128\n"
"v_exp_f32 v129,v129\n"
"v_cvt_pk_bf16_f32 v153,v126,v127\n"
"v_exp_f32 v130,v130\n"
"v_exp_f32 v131,v131\n"
"v_cvt_pk_bf16_f32 v154,v128,v129\n"
"v_cvt_pk_bf16_f32 v155,v130,v131\n"
"s_nop 1\n"
"s_setpc_b64 s[70:71]\n"
"Lcode1:\n"
      :
      : "s"(__builtin_amdgcn_kernarg_segment_ptr()), "s"(blockIdx.x), "v"(threadIdx.x)
      : "v1","v2","v3","v4","v5","v6","v7","v8","v9","v10","v11","v12","v13","v14","v15","v16","v17","v18","v19","v20","v21","v22","v23","v24","v25","v26","v27","v28","v29","v30","v31","v32","v33","v34","v35","v36","v37","v38","v39","v40","v41","v42","v43","v44","v45","v46","v47","v48","v49","v50","v51","v52","v53","v54","v55","v56","v57","v58","v59","v60","v61","v62","v63","v64","v65","v66","v67","v68","v69","v70","v71","v72","v73","v74","v75","v76","v77","v78","v79","v80","v81","v82","v83","v84","v85","v86","v87","v88","v89","v90","v91","v92","v93","v94","v95","v96","v97","v98","v99","v100","v101","v102","v103","v104","v105","v106","v107","v108","v109","v110","v111","v112","v113","v114","v115","v116","v117","v118","v119","v120","v121","v122","v123","v124","v125","v126","v127","v128","v129","v130","v131","v132","v133","v134","v135","v136","v137","v138","v139","v140","v141","v142","v143","v144","v145","v146","v147","v148","v149","v150","v151","v152","v153","v154","v155","v156","v157","v158","v159","v160","v161","v162","v163","v164","v165","v166","v167","v168","v169","v170","v171","v172","v173","v174","v175","v176","v177","v178","v179","v180","v181","v182","v183","v184","v185","v186","v187","v188","v189","v190","v191","v192","v193","v194","v195","v196","v197","v198","v199","v200","v201","v202","v203","v204","v205","v206","v207","v208","v209","v210","v211","v212","v213","v214","v215","v216","v217","v218","v219","v220","v221","v222","v223","v224","v225","v226","v227","v228","v229","v230","v231","v232","v233","v234","v235","v236","v237","v238","v239","v240","v241","v242","v243","v244","v245","v246","v247","v248","v249","v250","v251","v252","v253","v254","v255","a0","a1","a2","a3","a4","a5","a6","a7","a8","a9","a10","a11","a12","a13","a14","a15","a16","a17","a18","a19","a20","a21","a22","a23","a24","a25","a26","a27","a28","a29","a30","a31","a32","a33","a34","a35","a36","a37","a38","a39","a40","a41","a42","a43","a44","a45","a46","a47","a48","a49","a50","a51","a52","a53","a54","a55","a56","a57","a58","a59","a60","a61","a62","a63","a64","a65","a66","a67","a68","a69","a70","a71","a72","a73","a74","a75","a76","a77","a78","a79","a80","a81","a82","a83","a84","a85","a86","a87","a88","a89","a90","a91","a92","a93","a94","a95","a96","a97","a98","a99","a100","a101","a102","a103","a104","a105","a106","a107","a108","a109","a110","a111","a112","a113","a114","a115","a116","a117","a118","a119","a120","a121","a122","a123","a124","a125","a126","a127","a128","a129","a130","a131","a132","a133","a134","a135","a136","a137","a138","a139","a140","a141","a142","a143","a144","a145","a146","a147","a148","a149","a150","a151","a152","a153","a154","a155","a156","a157","a158","a159","a160","a161","a162","a163","a164","a165","a166","a167","a168","a169","a170","a171","a172","a173","a174","a175","a176","a177","a178","a179","a180","a181","a182","a183","a184","a185","a186","a187","a188","a189","a190","a191","a192","a193","a194","a195","a196","a197","a198","a199","a200","a201","a202","a203","a204","a205","a206","a207","a208","a209","a210","a211","a212","a213","a214","a215","a216","a217","a218","a219","a220","a221","a222","a223","a224","a225","a226","a227","a228","a229","a230","a231","a232","a233","a234","a235","a236","a237","a238","a239","a240","a241","a242","a243","a244","a245","a246","a247","a248","a249","a250","a251","a252","a253","a254","a255","s4","s5","s6","s7","s8","s9","s10","s11","s12","s13","s14","s15","s16","s17","s18","s19","s20","s21","s22","s23","s24","s25","s26","s27","s28","s29","s30","s31","s33","s34","s35","s36","s37","s38","s39","s40","s41","s42","s43","s44","s45","s46","s47","s48","s49","s50","s51","s52","s53","s54","s55","s56","s57","s58","s59","s60","s61","s62","s63","s64","s65","s66","s67","s68","s69","s70","s71","s72","s73","s74","s75","s76","s77","s78","s79","s80","s81","s82","s83","s84","s85","s86","s87","s88","s89","s90","s91","s92","s93","s94","s95","vcc","scc","memory");
}

extern "C" void kernel_launch(void* const* d_in, const int* in_sizes, int n_in, void* d_out, int out_size, void* d_ws, size_t ws_size,
                              hipStream_t stream) {
  (void)hipFuncSetAttribute((const void*)concat_heads_fused, hipFuncAttributeMaxDynamicSharedMemorySize, 160 * 1024);
  concat_heads_fused<<<256, 256, 160 * 1024, stream>>>(d_in[0], d_in[1], d_in[2], d_in[3], d_in[4], d_in[5], d_in[6], d_in[7], d_in[8],
                                                       d_in[9], d_ws, d_out);
}
```
